```python
import torch

def _softmax(x, axis=-1):
    return torch.softmax(x, dim=axis)
def _gelu(x):
    return torch.nn.functional.gelu(x, approximate="tanh")
def _silu(x):
    return torch.nn.functional.silu(x)
def _sigmoid(x):
    return torch.sigmoid(x)
def _layernorm(x, axis=-1, eps=1e-5):
    m = x.mean(dim=axis, keepdim=True)
    v = x.var(dim=axis, keepdim=True, unbiased=False)
    return (x - m) / torch.sqrt(v + eps)
def _scatter_add(tgt, idx, src, axis=0):
    return tgt.clone().index_add_(axis, idx, src)
def _segment_sum(src, seg, n):
    out = torch.zeros((n,) + src.shape[1:], dtype=src.dtype)
    return out.index_add_(0, seg, src)
def _pad(x, pw):
    flat = tuple(v for pair in reversed(pw) for v in pair)
    return torch.nn.functional.pad(x, flat)

kernel_name = 'recurrent_scan__grouped_kv_mqa__dense_activation_squared_relu__prompt'

def setup_inputs():

    _w = (lambda a: (lambda t: t.to(torch.bfloat16) if t.is_floating_point() else t)(torch.as_tensor(a)))
    def _A_setup():
        B, S, D, H = 1, 2048, 1024, 32
        Dh = D // H
        return {
            "x": _w((lambda *s: torch.randn(*s, dtype=torch.bfloat16) * 0.05)(B, S, D)),
            "wq": _w((lambda *s: torch.randn(*s, dtype=torch.bfloat16) * 0.05)(D, H * Dh)),
            "wk": _w((lambda *s: torch.randn(*s, dtype=torch.bfloat16) * 0.05)(D, Dh)),
            "wv": _w((lambda *s: torch.randn(*s, dtype=torch.bfloat16) * 0.05)(D, Dh)),
            "wo": _w((lambda *s: torch.randn(*s, dtype=torch.bfloat16) * 0.05)(H * Dh, D)),
        }
    _A_in = _A_setup()

    def _B_setup():
        B, S, D, F = 1, 2048, 1024, 3584
        return {
            "x": _w((lambda *s: torch.randn(*s, dtype=torch.bfloat16) * 0.05)(B, S, D)),
            "w1": _w((lambda *s: torch.randn(*s, dtype=torch.bfloat16) * 0.05)(D, F)),
            "b1": _w((lambda *s: torch.randn(*s, dtype=torch.bfloat16) * 0.8)(F)),
            "w2": _w((lambda *s: torch.randn(*s, dtype=torch.bfloat16) * 0.05)(F, D)),
            "b2": _w((lambda *s: torch.randn(*s, dtype=torch.bfloat16) * 6.4)(D)),
        }
    _B_in = _B_setup()

    return {
        "A_x": _A_in["x"],
        "A_wq": _A_in["wq"],
        "A_wk": _A_in["wk"],
        "A_wv": _A_in["wv"],
        "A_wo": _A_in["wo"],
        "B_x": _B_in["x"],
        "B_w1": _B_in["w1"],
        "B_b1": _B_in["b1"],
        "B_w2": _B_in["w2"],
        "B_b2": _B_in["b2"],
    }

def reference(A_x, A_wq, A_wk, A_wv, A_wo, B_x, B_w1, B_b1, B_w2, B_b2):
    def _A_ref(x, wq, wk, wv, wo):
        B, S, D = x.shape
        H = 32
        Dh = D // H
        q = (lambda x, *d: x.permute(*d))((x @ wq).reshape(B, S, H, Dh), 0, 2, 1, 3)
        k = x @ wk
        v = x @ wv
        scores = torch.einsum("bhqd,bkd->bhqk", q, k) * (Dh ** -0.5)
        p = _softmax(scores, axis=-1)
        o = torch.einsum("bhqk,bkd->bhqd", p, v)
        return (lambda x, *d: x.permute(*d))(o, 0, 2, 1, 3).reshape(B, S, H * Dh) @ wo

    def _B_ref(x, w1, b1, w2, b2):
        h = torch.einsum("bsd,df->bsf", x, w1) + b1
        zero = torch.zeros_like(h)
        r = torch.where(h > zero, h, zero)
        h = r * r
        out = torch.einsum("bsf,fd->bsd", h, w2) + b2
        return out

    x = A_x
    xn = _layernorm(x, axis=-1)
    s = _A_ref(xn, A_wq, A_wk, A_wv, A_wo)
    g = _silu(_B_ref(xn, B_w1, B_b1, B_w2, B_b2))
    return x + s * g
```

```cpp
#include <hip/hip_runtime.h>
#include <hip/hip_bf16.h>
#include <stdint.h>

typedef unsigned short u16;
typedef __attribute__((ext_vector_type(8))) short bf16x8;
typedef __attribute__((ext_vector_type(4))) short s16x4;
typedef __attribute__((ext_vector_type(2))) float f32x2;
typedef __attribute__((ext_vector_type(4))) float f32x4;
typedef __attribute__((ext_vector_type(16))) float f32x16;
typedef __attribute__((ext_vector_type(2))) unsigned u32x2;
typedef __attribute__((ext_vector_type(4))) unsigned u32x4;
typedef __attribute__((ext_vector_type(2))) __bf16 bf16x2_t;
#define LAS __attribute__((address_space(3)))
#define DI __device__ __forceinline__
#define SBAR() __builtin_amdgcn_sched_barrier(0)
#define MFMA16(a, b, c) __builtin_amdgcn_mfma_f32_16x16x32_bf16((a), (b), (c), 0, 0, 0)
#define MFMA32(a, b, c) __builtin_amdgcn_mfma_f32_32x32x16_bf16((a), (b), (c), 0, 0, 0)

constexpr int SEQ = 2048, DM = 1024, NH = 32, FF = 3584;
constexpr size_t WS_XN = 0;
constexpr size_t WS_QF = WS_XN + (4u << 20);
constexpr size_t WS_KV = WS_QF + (4u << 20);
constexpr size_t WS_HF = WS_KV + (1u << 20);
constexpr size_t WS_OF = WS_HF + (14u << 20);
constexpr size_t WS_G  = WS_OF + (4u << 20);

DI unsigned cvtpk(float lo, float hi) { f32x2 v = {lo, hi}; bf16x2_t b = __builtin_convertvector(v, bf16x2_t); return __builtin_bit_cast(unsigned, b); }
DI float bf_lo(unsigned u) { return __uint_as_float(u << 16); }
DI float bf_hi(unsigned u) { return __uint_as_float(u & 0xffff0000u); }
DI unsigned rfl(unsigned x) { return __builtin_amdgcn_readfirstlane(x); }
DI void glds16(const void* sbase, unsigned voff, unsigned lds_addr) {
  asm volatile("s_mov_b32 m0, %2\n\ts_nop 0\n\tglobal_load_lds_dwordx4 %0, %1" ::"v"(voff), "s"(sbase), "s"(lds_addr) : "memory");
}
#define WAITV(n) asm volatile("s_waitcnt vmcnt(" #n ")" ::: "memory")
#define BARRIER() asm volatile("s_waitcnt lgkmcnt(0)\n\ts_barrier" ::: "memory")
DI s16x4 trread(LAS const char* p) { return __builtin_amdgcn_ds_read_tr16_b64_v4i16((LAS s16x4*)p); }
DI bf16x8 cat8(s16x4 lo, s16x4 hi) { return __builtin_shufflevector(lo, hi, 0, 1, 2, 3, 4, 5, 6, 7); }

__global__ __launch_bounds__(512, 1) void k0_ln_kv(const u16* __restrict__ x, const u16* __restrict__ wk, const u16* __restrict__ wv,
                                                   char* __restrict__ ws) {
  __shared__ __attribute__((aligned(16))) char smem[65536 + 16384 + 1024];
  LAS char* lds = (LAS char*)smem;
  const int tid = threadIdx.x, lane = tid & 63, w = rfl(tid >> 6);
  const int T = blockIdx.x >> 1, which = blockIdx.x & 1;
  const int g = lane >> 4, l15 = lane & 15;
  const unsigned ldsb = (unsigned)(size_t)lds;
  const char* wsrc = (const char*)(which ? wv : wk) + w * 8192;
#pragma unroll
  for (int i = 0; i < 8; ++i) glds16(wsrc + i * 1024, lane * 16, ldsb + w * 8192 + i * 1024);
  u32x4 xr[4];
  const u16* xp = x + (size_t)(T * 16 + l15) * DM + w * 128 + g * 8;
#pragma unroll
  for (int j = 0; j < 4; ++j) xr[j] = *(const u32x4*)(xp + j * 32);
  float xv[32];
#pragma unroll
  for (int j = 0; j < 4; ++j)
#pragma unroll
    for (int e = 0; e < 4; ++e) { xv[j * 8 + 2 * e] = bf_lo(xr[j][e]); xv[j * 8 + 2 * e + 1] = bf_hi(xr[j][e]); }
  LAS float* red = (LAS float*)(lds + 65536 + 16384);
  float s = 0.f;
#pragma unroll
  for (int i = 0; i < 32; ++i) s += xv[i];
  s += __shfl_xor(s, 16); s += __shfl_xor(s, 32);
  if (g == 0) red[w * 16 + l15] = s;
  BARRIER();
  float mean = 0.f;
#pragma unroll
  for (int i = 0; i < 8; ++i) mean += red[i * 16 + l15];
  mean *= (1.0f / DM);
  float q = 0.f;
#pragma unroll
  for (int i = 0; i < 32; ++i) { xv[i] -= mean; q += xv[i] * xv[i]; }
  q += __shfl_xor(q, 16); q += __shfl_xor(q, 32);
  if (g == 0) red[128 + w * 16 + l15] = q;
  BARRIER();
  float var = 0.f;
#pragma unroll
  for (int i = 0; i < 8; ++i) var += red[128 + i * 16 + l15];
  const float rstd = 1.0f / sqrtf(var * (1.0f / DM) + 1e-5f);
  u32x4 xf[4];
#pragma unroll
  for (int j = 0; j < 4; ++j)
#pragma unroll
    for (int e = 0; e < 4; ++e) xf[j][e] = cvtpk(xv[j * 8 + 2 * e] * rstd, xv[j * 8 + 2 * e + 1] * rstd);
  char* xn = ws + WS_XN + ((size_t)(T * 32 + w * 4) * 64 + lane) * 16;
  if (which == 0) { *(u32x4*)(xn) = xf[0]; *(u32x4*)(xn + 1024) = xf[1]; }
  else { *(u32x4*)(xn + 2048) = xf[2]; *(u32x4*)(xn + 3072) = xf[3]; }
  WAITV(0);
  f32x4 acc[2] = {};
  LAS const char* wp = lds + w * 8192 + (8 * g + (l15 >> 2)) * 64 + (lane & 3) * 8;
#pragma unroll
  for (int j = 0; j < 4; ++j)
#pragma unroll
    for (int nt = 0; nt < 2; ++nt) {
      bf16x8 wf = cat8(trread(wp + (32 * j) * 64 + nt * 32), trread(wp + (32 * j + 4) * 64 + nt * 32));
      acc[nt] = MFMA16(wf, __builtin_bit_cast(bf16x8, xf[j]), acc[nt]);
    }
  LAS float* part = (LAS float*)(lds + 65536);
#pragma unroll
  for (int nt = 0; nt < 2; ++nt)
#pragma unroll
    for (int r = 0; r < 4; ++r) part[w * 512 + (nt * 16 + 4 * g + r) * 16 + l15] = acc[nt][r];
  BARRIER();
  {
    float v = 0.f;
#pragma unroll
    for (int i = 0; i < 8; ++i) v += part[i * 512 + tid];
    const int d = tid >> 4, key = T * 16 + (tid & 15), kk = key & 31;
    unsigned off = (key >> 5) * 4096;
    if (which == 0) off += (d >> 4) * 1024 + (((d >> 3) & 1) * 32 + kk) * 16 + (d & 7) * 2;
    else off += 2048 + (kk >> 4) * 1024 + (((kk >> 2) & 1) * 32 + d) * 16 + (((kk >> 3) & 1) * 4 + (kk & 3)) * 2;
    *(u16*)(ws + WS_KV + off) = (u16)(cvtpk(v, 0.f) & 0xffffu);
  }
}

constexpr int K1_STAGE = 36864;
DI int swap23(int r) { return (r & ~12) | ((r & 4) << 1) | ((r & 8) >> 1); }

__global__ __launch_bounds__(256, 1) void k1_gemm1(const u16* __restrict__ wq, const u16* __restrict__ w1, const u16* __restrict__ b1,
                                                   char* __restrict__ ws) {
  __shared__ __attribute__((aligned(16))) char smem[4 * K1_STAGE];
  LAS char* lds = (LAS char*)smem;
  const unsigned ldsb = (unsigned)(size_t)lds;
  const int tid = threadIdx.x, lane = tid & 63, w = rfl(tid >> 6);
  const int xcd = blockIdx.x & 7, idx = blockIdx.x >> 3;
  const int rp = (xcd & 1) * 4 + (idx & 3), c = (xcd >> 1) * 8 + (idx >> 2);
  const int g = lane >> 4, l15 = lane & 15;
  unsigned voq[2], voh[7];
#pragma unroll
  for (int i = 0; i < 2; ++i) { const int q = (i * 4 + w) * 64 + lane, rho = q >> 2, cc = (q & 3) ^ (((rho >> 2) & 1) << 1); voq[i] = swap23(rho) * 2048 + (c * 32 + 8 * cc) * 2; }
#pragma unroll
  for (int i = 0; i < 7; ++i) { const int q = (i * 4 + w) * 64 + lane, rho = q / 14, cc = q % 14; voh[i] = swap23(rho) * 7168 + (c * 112 + 8 * cc) * 2; }
  const char* wqb = (const char*)wq; const char* w1b = (const char*)w1;
#define K1_DMA(i, st) do { const unsigned sl_ = ldsb + ((st) & 3) * K1_STAGE + w * 1024; \
    if ((i) < 2) glds16(wqb + (size_t)(st) * (128 * 2048), voq[(i) < 2 ? (i) : 0], sl_ + (i) * 4096); \
    else glds16(w1b + (size_t)(st) * (128 * 7168), voh[(i) >= 2 ? (i) - 2 : 0], sl_ + 8192 + ((i) - 2) * 4096); } while (0)
  const char* xnp = ws + WS_XN + ((size_t)((rp * 16 + w * 4) * 32) * 64 + lane) * 16;
  bf16x8 xf[4][4];
#define K1_LDX(j, st) do { _Pragma("unroll") for (int tt = 0; tt < 4; ++tt) xf[j][tt] = *(const bf16x8*)(xnp + (size_t)tt * 32768 + ((st) * 4 + (j)) * 1024); } while (0)
#pragma unroll
  for (int i = 0; i < 9; ++i) K1_DMA(i, 0);
  SBAR();
  K1_LDX(0, 0); K1_LDX(1, 0); K1_LDX(2, 0); K1_LDX(3, 0);
  SBAR();
#pragma unroll
  for (int i = 0; i < 9; ++i) K1_DMA(i, 1);
#pragma unroll
  for (int i = 0; i < 9; ++i) K1_DMA(i, 2);
  f32x4 acc[4][9];
#pragma unroll
  for (int tt = 0; tt < 4; ++tt)
#pragma unroll
    for (int i = 0; i < 9; ++i) acc[tt][i] = (f32x4){0.f, 0.f, 0.f, 0.f};
  const int rl = 16 * (g >> 1) + 4 * (g & 1) + (l15 >> 2);
  const int qoff0 = rl * 64 + ((g & 1) << 5) + (lane & 3) * 8;
  const int hoff = 8192 + rl * 224 + (lane & 3) * 8;
  WAITV(34);
  BARRIER();
  bf16x8 wf[2][3];
#define K1_LDW(dst, sb, j, i) do { if ((i) < 2) { LAS const char* p_ = (sb) + (qoff0 ^ ((i) << 5)) + (32 * (j)) * 64; dst = cat8(trread(p_), trread(p_ + 8 * 64)); } \
    else { LAS const char* p_ = (sb) + hoff + (32 * (j)) * 224 + ((i) - 2) * 32; dst = cat8(trread(p_), trread(p_ + 8 * 224)); } } while (0)
  {
    LAS const char* sb = lds;
#pragma unroll
    for (int u = 0; u < 3; ++u) K1_LDW(wf[0][u], sb, 0, u);
  }
  for (int st = 0; st < 8; ++st) {
    LAS const char* sb = lds + (st & 3) * K1_STAGE;
    LAS const char* sn = lds + ((st + 1) & 3) * K1_STAGE;
#pragma unroll
    for (int gi = 0; gi < 12; ++gi) {
      const int j = gi / 3, gg = gi % 3;
      if (gi == 6) { WAITV(8); BARRIER(); }
      if (gi < 11) {
#pragma unroll
        for (int u = 0; u < 3; ++u) K1_LDW(wf[(gi + 1) & 1][u], sb, (gi + 1) / 3, ((gi + 1) % 3) * 3 + u);
      } else {
#pragma unroll
        for (int u = 0; u < 3; ++u) K1_LDW(wf[0][u], sn, 0, u);
      }
      if (gi >= 6 && st + 3 < 8) { K1_DMA(gi - 6, st + 3); if (gi >= 9) K1_DMA(gi - 3, st + 3); }
      SBAR();
#pragma unroll
      for (int u = 0; u < 3; ++u)
#pragma unroll
        for (int tt = 0; tt < 4; ++tt) acc[tt][gg * 3 + u] = MFMA16(wf[gi & 1][u], xf[j][tt], acc[tt][gg * 3 + u]);
      SBAR();
      if (gg == 2 && st < 7) { K1_LDX(j, st + 1); SBAR(); }
    }
  }
  const float qscale = 0.17677669529663687f * 1.4426950408889634f;
  char* qf = ws + WS_QF; char* hf = ws + WS_HF;
#pragma unroll
  for (int i = 0; i < 9; ++i) {
    f32x4 bv = {0.f, 0.f, 0.f, 0.f};
    if (i >= 2) { const u32x2 b = *(const u32x2*)(b1 + c * 112 + (i - 2) * 16 + 4 * g); bv = (f32x4){bf_lo(b[0]), bf_hi(b[0]), bf_lo(b[1]), bf_hi(b[1])}; }
#pragma unroll
    for (int tp = 0; tp < 2; ++tp) {
      f32x4 a = acc[2 * tp][i], b = acc[2 * tp + 1][i];
      if (i < 2) { a *= qscale; b *= qscale; }
      else {
#pragma unroll
        for (int r = 0; r < 4; ++r) { float t = fmaxf(a[r] + bv[r], 0.f); a[r] = t * t; t = fmaxf(b[r] + bv[r], 0.f); b[r] = t * t; }
      }
      unsigned x0 = cvtpk(a[0], a[1]), x1 = cvtpk(a[2], a[3]), y0 = cvtpk(b[0], b[1]), y1 = cvtpk(b[2], b[3]);
      auto s0 = __builtin_amdgcn_permlane16_swap(x0, y0, false, false);
      auto s1 = __builtin_amdgcn_permlane16_swap(x1, y1, false, false);
      const u32x4 o = {s0[0], s1[0], s0[1], s1[1]};
      const int tt32 = rp * 8 + w * 2 + tp;
      if (i < 2) *(u32x4*)(qf + ((size_t)(tt32 * 64 + 2 * c + i) * 64 + lane) * 16) = o;
      else *(u32x4*)(hf + ((size_t)(tt32 * 224 + 7 * c + (i - 2)) * 64 + lane) * 16) = o;
    }
  }
}

constexpr int AT_CHUNK = 16384;
constexpr int AT_RING = 4;
DI float exp2f_(float x) { return __builtin_amdgcn_exp2f(x); }
DI bf16x8 pack8(const f32x16& s, int o) {
  u32x4 p = {cvtpk(s[o], s[o + 1]), cvtpk(s[o + 2], s[o + 3]), cvtpk(s[o + 4], s[o + 5]), cvtpk(s[o + 6], s[o + 7])};
  return __builtin_bit_cast(bf16x8, p);
}
DI float rowmax16(const f32x16& s) {
  float m = s[0];
#pragma unroll
  for (int i = 1; i < 16; ++i) m = fmaxf(m, s[i]);
  return fmaxf(m, __shfl_xor(m, 32));
}

DI void attn_exact_tile(const char* kv, bf16x8 q0, bf16x8 q1, int lane, char* dst) {
  f32x16 o = {}; float m = -INFINITY, l = 0.f;
  for (int kt = 0; kt < 64; ++kt) {
    const char* p = kv + kt * 4096 + lane * 16;
    const bf16x8 k0 = *(const bf16x8*)p, k1 = *(const bf16x8*)(p + 1024), v0 = *(const bf16x8*)(p + 2048), v1 = *(const bf16x8*)(p + 3072);
    f32x16 s = {};
    s = MFMA32(k0, q0, s); s = MFMA32(k1, q1, s);
    const float mn = fmaxf(m, rowmax16(s));
    const float a = exp2f_(m - mn);
    m = mn;
    float ps = 0.f;
#pragma unroll
    for (int i = 0; i < 16; ++i) s[i] = exp2f_(s[i] - m);
    const bf16x8 p0 = pack8(s, 0), p1 = pack8(s, 8);
#pragma unroll
    for (int i = 0; i < 4; ++i) { const unsigned u = __builtin_bit_cast(u32x4, p0)[i], v = __builtin_bit_cast(u32x4, p1)[i]; ps += bf_lo(u) + bf_hi(u) + bf_lo(v) + bf_hi(v); }
    l = l * a + ps;
#pragma unroll
    for (int i = 0; i < 16; ++i) o[i] *= a;
    o = MFMA32(v0, p0, o); o = MFMA32(v1, p1, o);
  }
  l += __shfl_xor(l, 32);
  const float r = 1.0f / l;
#pragma unroll
  for (int i = 0; i < 16; ++i) o[i] *= r;
  *(bf16x8*)dst = pack8(o, 0); *(bf16x8*)(dst + 1024) = pack8(o, 8);
}

__global__ __launch_bounds__(256, 1) void k2_attn(char* __restrict__ ws) {
  __shared__ __attribute__((aligned(16))) char smem[AT_RING * AT_CHUNK];
  LAS char* lds = (LAS char*)smem;
  const unsigned ldsb = (unsigned)(size_t)lds;
  const int tid = threadIdx.x, lane = tid & 63, w = rfl(tid >> 6);
  const int head = blockIdx.x >> 3, tt0 = (blockIdx.x & 7) * 8 + 2 * w;
  const char* kv = ws + WS_KV;
#define AT_DMA(ch) do { _Pragma("unroll") for (int i_ = 0; i_ < 4; ++i_) glds16(kv + (size_t)(ch) * AT_CHUNK + w * 4096 + i_ * 1024, lane * 16, ldsb + ((ch) % AT_RING) * AT_CHUNK + w * 4096 + i_ * 1024); } while (0)
  AT_DMA(0); AT_DMA(1); AT_DMA(2);
  bf16x8 qf[2][2];
#pragma unroll
  for (int t = 0; t < 2; ++t)
#pragma unroll
    for (int s = 0; s < 2; ++s) qf[t][s] = *(const bf16x8*)(ws + WS_QF + ((size_t)((tt0 + t) * 64 + 2 * head + s) * 64 + lane) * 16);
  const bool match = ((lane >> 4) & 1) == (((lane & 15) >> 2) & 1);
  bf16x8 sel[2];
#pragma unroll
  for (int t = 0; t < 2; ++t) { const short v = (match && (lane & 3) == t) ? (short)0x3f80 : (short)0; sel[t] = (bf16x8){v, v, v, v, v, v, v, v}; }
  f32x16 o[2] = {}; f32x4 lsum = {};
  f32x16 negm[2];
  WAITV(0);
  BARRIER();
  {
    const bf16x8 k0 = *(LAS const bf16x8*)(lds + lane * 16), k1 = *(LAS const bf16x8*)(lds + 1024 + lane * 16);
#pragma unroll
    for (int t = 0; t < 2; ++t) {
      f32x16 s = {};
      s = MFMA32(k0, qf[t][0], s); s = MFMA32(k1, qf[t][1], s);
      const float nm = -rowmax16(s);
#pragma unroll
      for (int i = 0; i < 16; ++i) negm[t][i] = nm;
    }
  }
  for (int ch = 0; ch < 16; ++ch) {
    LAS const char* sb = lds + (ch % AT_RING) * AT_CHUNK + lane * 16;
#pragma unroll
    for (int kt = 0; kt < 4; ++kt) {
      if (kt == 2) { if (ch < 14) WAITV(4); else WAITV(0); BARRIER(); if (ch + 3 < 16) AT_DMA(ch + 3); }
      const bf16x8 k0 = *(LAS const bf16x8*)(sb + kt * 4096), k1 = *(LAS const bf16x8*)(sb + kt * 4096 + 1024);
      const bf16x8 v0 = *(LAS const bf16x8*)(sb + kt * 4096 + 2048), v1 = *(LAS const bf16x8*)(sb + kt * 4096 + 3072);
#pragma unroll
      for (int t = 0; t < 2; ++t) {
        f32x16 s = MFMA32(k0, qf[t][0], negm[t]);
        s = MFMA32(k1, qf[t][1], s);
#pragma unroll
        for (int i = 0; i < 16; ++i) s[i] = exp2f_(s[i]);
        const bf16x8 p0 = pack8(s, 0), p1 = pack8(s, 8);
        o[t] = MFMA32(v0, p0, o[t]); o[t] = MFMA32(v1, p1, o[t]);
        lsum = MFMA16(sel[t], p0, lsum); lsum = MFMA16(sel[t], p1, lsum);
      }
    }
  }
#pragma unroll
  for (int t = 0; t < 2; ++t) {
    char* dst = ws + WS_OF + ((size_t)((tt0 + t) * 64 + 2 * head) * 64 + lane) * 16;
    const float l = lsum[t];
    float amax = 0.f;
#pragma unroll
    for (int i = 0; i < 16; ++i) amax = fmaxf(amax, fabsf(o[t][i]));
    bool ok = (l < 1e30f) && (amax < 1e37f);
#pragma unroll
    for (int i = 0; i < 16; ++i) ok = ok && (o[t][i] == o[t][i]);
    if (__builtin_expect(__all(ok), 1)) {
      const float r = 1.0f / l;
      f32x16 y;
#pragma unroll
      for (int i = 0; i < 16; ++i) y[i] = o[t][i] * r;
      *(bf16x8*)dst = pack8(y, 0); *(bf16x8*)(dst + 1024) = pack8(y, 8);
    } else {
      attn_exact_tile(kv, qf[t][0], qf[t][1], lane, dst);
    }
  }
}

template <int MODE>
__global__ __launch_bounds__(256, 1) void k_gemm2(const u16* __restrict__ wgt, const u16* __restrict__ b2, const u16* __restrict__ x,
                                                  u16* __restrict__ out, char* __restrict__ ws) {
  constexpr int KS16 = MODE == 0 ? 224 : 64, NSTEP = KS16 / 4, NSTAGE = NSTEP / 2;
  __shared__ __attribute__((aligned(16))) char smem[98304];
  LAS char* lds = (LAS char*)smem;
  const unsigned ldsb = (unsigned)(size_t)lds;
  const int tid = threadIdx.x, lane = tid & 63, w = rfl(tid >> 6);
  const int xcd = blockIdx.x & 7, idx = blockIdx.x >> 3;
  const int mt = (xcd & 3) * 4 + (idx & 3), nt64 = (xcd >> 2) * 8 + (idx >> 2);
  const int h = lane >> 5, q4 = (lane & 15) >> 2;
  const int rho0 = lane >> 3;
  const unsigned vo = rho0 * 2048 + (nt64 * 64 + 8 * ((lane & 7) ^ (((rho0 >> 1) & 1) << 2))) * 2;
  const char* wb = (const char*)wgt + (size_t)w * NSTEP * 16 * 2048;
#define G2_DMA(st) do { _Pragma("unroll") for (int i_ = 0; i_ < 4; ++i_) glds16(wb + (size_t)(st) * 65536 + i_ * 16384, vo, ldsb + w * 16384 + ((st) & 3) * 4096 + i_ * 1024); } while (0)
  const char* ap = ws + (MODE == 0 ? WS_HF : WS_OF) + ((size_t)(mt * 4 * KS16 + w * NSTEP) * 64 + lane) * 16;
  bf16x8 af[4][4];
#define G2_LDA(j, step) do { _Pragma("unroll") for (int tt = 0; tt < 4; ++tt) af[j][tt] = *(const bf16x8*)(ap + (size_t)tt * KS16 * 1024 + (step) * 1024); } while (0)
  G2_DMA(0); SBAR();
  G2_LDA(0, 0); G2_LDA(1, 1); SBAR();
  G2_DMA(1); SBAR();
  G2_LDA(2, 2); G2_LDA(3, 3); SBAR();
  G2_DMA(2);
  f32x16 acc[4][2] = {};
  const int woff = w * 16384 + (MODE == 0 ? 8 * h + q4 : 4 * h + q4) * 128 + 64 * ((q4 >> 1) & 1) + 32 * ((lane >> 4) & 1) + 8 * (lane & 3);
  constexpr int TSTR = (MODE == 0 ? 4 : 8) * 128;
  for (int s2 = 0; s2 < NSTAGE; s2 += 2) {
#pragma unroll
    for (int ss = 0; ss < 2; ++ss) {
      const int st = s2 + ss;
      WAITV(8);
      LAS const char* sb = lds + woff + (st & 3) * 4096;
      bf16x8 wf[2][2];
#pragma unroll
      for (int s = 0; s < 2; ++s)
#pragma unroll
        for (int n = 0; n < 2; ++n) { LAS const char* p = (LAS const char*)((unsigned)(size_t)sb ^ (n << 6)) + s * 2048; wf[s][n] = cat8(trread(p), trread(p + TSTR)); }
      asm volatile("s_waitcnt lgkmcnt(0)" : "+v"(wf[0][0]), "+v"(wf[0][1]), "+v"(wf[1][0]), "+v"(wf[1][1])::"memory");
      if (st + 3 < NSTAGE) G2_DMA(st + 3);
      SBAR();
#pragma unroll
      for (int s = 0; s < 2; ++s) {
        const int j = ss * 2 + s;
#pragma unroll
        for (int tt = 0; tt < 4; ++tt)
#pragma unroll
          for (int n = 0; n < 2; ++n) acc[tt][n] = MFMA32(wf[s][n], af[j][tt], acc[tt][n]);
        SBAR();
        if (st * 2 + s + 4 < NSTEP) { G2_LDA(j, st * 2 + s + 4); }
        SBAR();
      }
    }
  }
  WAITV(0);
  BARRIER();
#pragma unroll
  for (int tt = 0; tt < 4; ++tt) {
    if (tt != w) {
      const int slot = tt * 3 + (w > tt ? w - 1 : w);
#pragma unroll
      for (int n = 0; n < 2; ++n)
#pragma unroll
        for (int rg = 0; rg < 4; ++rg)
          *(LAS f32x4*)(lds + slot * 8192 + (n * 4 + rg) * 1024 + lane * 16) = (f32x4){acc[tt][n][4 * rg], acc[tt][n][4 * rg + 1], acc[tt][n][4 * rg + 2], acc[tt][n][4 * rg + 3]};
    }
  }
  BARRIER();
  f32x16 r[2];
#pragma unroll
  for (int tt = 0; tt < 4; ++tt) if (tt == w) { r[0] = acc[tt][0]; r[1] = acc[tt][1]; }
#pragma unroll
  for (int sl = 0; sl < 3; ++sl)
#pragma unroll
    for (int n = 0; n < 2; ++n)
#pragma unroll
      for (int rg = 0; rg < 4; ++rg) {
        const f32x4 v = *(LAS const f32x4*)(lds + (w * 3 + sl) * 8192 + (n * 4 + rg) * 1024 + lane * 16);
#pragma unroll
        for (int e = 0; e < 4; ++e) r[n][4 * rg + e] += v[e];
      }
  char* gp = ws + WS_G + ((size_t)((mt * 16 + nt64) * 4 + w) * 4 * 64 + lane) * 16;
  if (MODE == 0) {
#pragma unroll
    for (int n = 0; n < 2; ++n) {
#pragma unroll
      for (int rg = 0; rg < 4; ++rg) {
        const u32x2 b = *(const u32x2*)(b2 + nt64 * 64 + n * 32 + rg * 8 + 4 * h);
        const float bb[4] = {bf_lo(b[0]), bf_hi(b[0]), bf_lo(b[1]), bf_hi(b[1])};
#pragma unroll
        for (int e = 0; e < 4; ++e) { const float z = r[n][4 * rg + e] + bb[e]; r[n][4 * rg + e] = z / (1.0f + __expf(-z)); }
      }
      *(bf16x8*)(gp + (n * 2) * 1024) = pack8(r[n], 0);
      *(bf16x8*)(gp + (n * 2 + 1) * 1024) = pack8(r[n], 8);
    }
  } else {
    const size_t row = (size_t)(mt * 128 + w * 32 + (lane & 31)) * DM + nt64 * 64 + 4 * h;
#pragma unroll
    for (int n = 0; n < 2; ++n)
#pragma unroll
      for (int hh = 0; hh < 2; ++hh) {
        const u32x4 gv = *(const u32x4*)(gp + (n * 2 + hh) * 1024);
#pragma unroll
        for (int e2 = 0; e2 < 2; ++e2) {
          const int rg = hh * 2 + e2;
          const u32x2 xv = *(const u32x2*)(x + row + n * 32 + rg * 8);
          const float y0 = bf_lo(xv[0]) + r[n][4 * rg] * bf_lo(gv[2 * e2]), y1 = bf_hi(xv[0]) + r[n][4 * rg + 1] * bf_hi(gv[2 * e2]);
          const float y2 = bf_lo(xv[1]) + r[n][4 * rg + 2] * bf_lo(gv[2 * e2 + 1]), y3 = bf_hi(xv[1]) + r[n][4 * rg + 3] * bf_hi(gv[2 * e2 + 1]);
          *(u32x2*)(out + row + n * 32 + rg * 8) = (u32x2){cvtpk(y0, y1), cvtpk(y2, y3)};
        }
      }
  }
}

extern "C" void kernel_launch(void* const* d_in, const int* in_sizes, int n_in, void* d_out, int out_size, void* d_ws, size_t ws_size,
                              hipStream_t stream) {
  const u16* x = (const u16*)d_in[0];
  const u16* wq = (const u16*)d_in[1];
  const u16* wk = (const u16*)d_in[2];
  const u16* wv = (const u16*)d_in[3];
  const u16* wo = (const u16*)d_in[4];
  const u16* w1 = (const u16*)d_in[6];
  const u16* b1 = (const u16*)d_in[7];
  const u16* w2 = (const u16*)d_in[8];
  const u16* b2 = (const u16*)d_in[9];
  char* ws = (char*)d_ws;
  k0_ln_kv<<<256, 512, 0, stream>>>(x, wk, wv, ws);
  k1_gemm1<<<256, 256, 0, stream>>>(wq, w1, b1, ws);
  k2_attn<<<256, 256, 0, stream>>>(ws);
  k_gemm2<0><<<256, 256, 0, stream>>>(w2, b2, x, (u16*)d_out, ws);
  k_gemm2<1><<<256, 256, 0, stream>>>(wo, b2, x, (u16*)d_out, ws);
}
```

```cpp
#include <hip/hip_runtime.h>
#include <hip/hip_bf16.h>
#include <stdint.h>

typedef unsigned short u16;
typedef __attribute__((ext_vector_type(8))) short bf16x8;
typedef __attribute__((ext_vector_type(4))) short s16x4;
typedef __attribute__((ext_vector_type(2))) float f32x2;
typedef __attribute__((ext_vector_type(4))) float f32x4;
typedef __attribute__((ext_vector_type(16))) float f32x16;
typedef __attribute__((ext_vector_type(2))) unsigned u32x2;
typedef __attribute__((ext_vector_type(4))) unsigned u32x4;
typedef __attribute__((ext_vector_type(2))) __bf16 bf16x2_t;
#define LAS __attribute__((address_space(3)))
#define DI __device__ __forceinline__
#define MFMA16(a, b, c) __builtin_amdgcn_mfma_f32_16x16x32_bf16((a), (b), (c), 0, 0, 0)

constexpr int DM = 1024;
constexpr unsigned XT = 33, HT = 225, OT = 65;
constexpr unsigned WS_CNT = 0;
constexpr unsigned WS_REQ = 8192;
constexpr unsigned WS_KV = 16384;
constexpr unsigned WS_XN = WS_KV + 262144, XN_B = 128 * XT * 1024;
constexpr unsigned WS_OF = WS_XN + XN_B, OF_B = 64 * OT * 1024;
constexpr unsigned WS_HF = WS_OF + OF_B, HF_B = 64 * HT * 1024;

DI unsigned cvtpk(float lo, float hi) { f32x2 v = {lo, hi}; bf16x2_t b = __builtin_convertvector(v, bf16x2_t); return __builtin_bit_cast(unsigned, b); }
DI float bf_lo(unsigned u) { return __uint_as_float(u << 16); }
DI float bf_hi(unsigned u) { return __uint_as_float(u & 0xffff0000u); }
DI unsigned rfl(unsigned x) { return __builtin_amdgcn_readfirstlane(x); }
#define WAITV(n) asm volatile("s_waitcnt vmcnt(" #n ")" ::: "memory")
#define BARRIER() asm volatile("s_waitcnt lgkmcnt(0)\n\ts_barrier" ::: "memory")
DI s16x4 trread(LAS const char* p) { return __builtin_amdgcn_ds_read_tr16_b64_v4i16((LAS s16x4*)p); }
DI bf16x8 cat8(s16x4 lo, s16x4 hi) { return __builtin_shufflevector(lo, hi, 0, 1, 2, 3, 4, 5, 6, 7); }

DI void touch4(const void* sbase, unsigned voff, unsigned lds_addr) {
  asm volatile("s_mov_b32 m0, %2\n\ts_nop 0\n\tglobal_load_lds_dword %0, %1" ::"v"(voff), "s"(sbase), "s"(lds_addr) : "memory");
}
DI float xsum16_32(float v) {
  auto a = __builtin_amdgcn_permlane16_swap(__float_as_uint(v), __float_as_uint(v), false, false);
  v = __uint_as_float(a[0]) + __uint_as_float(a[1]);
  auto b = __builtin_amdgcn_permlane32_swap(__float_as_uint(v), __float_as_uint(v), false, false);
  return __uint_as_float(b[0]) + __uint_as_float(b[1]);
}

#define P2_ASM \
  "s_waitcnt lgkmcnt(0)\n\t" \
  "v_accvgpr_write_b32 a160, 0\n\t" \
  "v_accvgpr_write_b32 a161, 0\n\t" \
  "v_accvgpr_write_b32 a162, 0\n\t" \
  "v_accvgpr_write_b32 a163, 0\n\t" \
  "v_exp_f32 v32, v32\n\t" \
  "v_exp_f32 v33, v33\n\t" \
  "v_mfma_f32_32x32x16_bf16 v[48:63], v[88:91], v[72:75], v[16:31]\n\t" \
  "ds_read_b128 v[88:91], v145 offset:4096\n\t" \
  "v_exp_f32 v34, v34\n\t" \
  "v_cvt_pk_bf16_f32 v104, v32, v33\n\t" \
  "v_exp_f32 v35, v35\n\t" \
  "v_exp_f32 v36, v36\n\t" \
  "v_mfma_f32_32x32x16_bf16 v[48:63], v[92:95], v[76:79], v[48:63]\n\t" \
  "ds_read_b128 v[92:95], v145 offset:5120\n\t" \
  "v_cvt_pk_bf16_f32 v105, v34, v35\n\t" \
  "v_exp_f32 v37, v37\n\t" \
  "v_exp_f32 v38, v38\n\t" \
  "v_cvt_pk_bf16_f32 v106, v36, v37\n\t" \
  "v_exp_f32 v39, v39\n\t" \
  "ds_read_b128 v[96:99], v145 offset:2048\n\t" \
  "v_exp_f32 v40, v40\n\t" \
  "v_cvt_pk_bf16_f32 v107, v38, v39\n\t" \
  "v_exp_f32 v41, v41\n\t" \
  "v_exp_f32 v42, v42\n\t" \
  "v_cvt_pk_bf16_f32 v108, v40, v41\n\t" \
  "v_exp_f32 v43, v43\n\t" \
  "ds_read_b128 v[100:103], v145 offset:3072\n\t" \
  "v_exp_f32 v44, v44\n\t" \
  "v_cvt_pk_bf16_f32 v109, v42, v43\n\t" \
  "v_exp_f32 v45, v45\n\t" \
  "v_exp_f32 v46, v46\n\t" \
  "v_cvt_pk_bf16_f32 v110, v44, v45\n\t" \
  "v_exp_f32 v47, v47\n\t" \
  "v_exp_f32 v48, v48\n\t" \
  "v_cvt_pk_bf16_f32 v111, v46, v47\n\t" \
  "v_exp_f32 v49, v49\n\t" \
  "s_waitcnt lgkmcnt(1)\n\t" \
  "v_mfma_f32_32x32x16_bf16 v[32:47], v[88:91], v[64:67], v[0:15]\n\t" \
  "v_exp_f32 v50, v50\n\t" \
  "v_cvt_pk_bf16_f32 v112, v48, v49\n\t" \
  "v_exp_f32 v51, v51\n\t" \
  "v_exp_f32 v52, v52\n\t" \
  "v_mfma_f32_32x32x16_bf16 v[32:47], v[92:95], v[68:71], v[32:47]\n\t" \
  "v_cvt_pk_bf16_f32 v113, v50, v51\n\t" \
  "v_exp_f32 v53, v53\n\t" \
  "v_exp_f32 v54, v54\n\t" \
  "v_cvt_pk_bf16_f32 v114, v52, v53\n\t" \
  "v_exp_f32 v55, v55\n\t" \
  "v_mfma_f32_32x32x16_bf16 a[128:143], v[96:99], v[104:107], 0\n\t" \
  "v_exp_f32 v56, v56\n\t" \
  "v_cvt_pk_bf16_f32 v115, v54, v55\n\t" \
  "v_exp_f32 v57, v57\n\t" \
  "v_exp_f32 v58, v58\n\t" \
  "v_cvt_pk_bf16_f32 v116, v56, v57\n\t" \
  "v_exp_f32 v59, v59\n\t" \
  "s_waitcnt lgkmcnt(0)\n\t" \
  "v_mfma_f32_32x32x16_bf16 a[128:143], v[100:103], v[108:111], a[128:143]\n\t" \
  "v_exp_f32 v60, v60\n\t" \
  "v_cvt_pk_bf16_f32 v117, v58, v59\n\t" \
  "v_exp_f32 v61, v61\n\t" \
  "v_exp_f32 v62, v62\n\t" \
  "v_smfmac_f32_16x16x64_bf16 a[160:163], v[80:83], v[104:111], v154\n\t" \
  "v_cvt_pk_bf16_f32 v118, v60, v61\n\t" \
  "v_exp_f32 v63, v63\n\t" \
  "s_waitcnt vmcnt(0)\n\t" \
  "s_barrier\n\t" \
  "s_cmp_lg_u32 s75, 0\n\t" \
  "s_cbranch_scc1 LPUB%=\n\t" \
  "s_mov_b64 exec, 1\n\t" \
  "v_mov_b32 v136, 0\n\t" \
  "v_mov_b32 v137, 1\n\t" \
  "global_atomic_add v136, v137, s[76:77]\n\t" \
  "s_mov_b64 exec, -1\n\t" \
  "LPUB%=:\n\t" \
  "v_exp_f32 v32, v32\n\t" \
  "v_cvt_pk_bf16_f32 v119, v62, v63\n\t" \
  "v_exp_f32 v33, v33\n\t" \
  "v_mfma_f32_32x32x16_bf16 v[48:63], v[88:91], v[72:75], v[16:31]\n\t" \
  "ds_read_b128 v[88:91], v145 offset:8192\n\t" \
  "v_exp_f32 v34, v34\n\t" \
  "v_cvt_pk_bf16_f32 v104, v32, v33\n\t" \
  "v_exp_f32 v35, v35\n\t" \
  "v_exp_f32 v36, v36\n\t" \
  "v_mfma_f32_32x32x16_bf16 v[48:63], v[92:95], v[76:79], v[48:63]\n\t" \
  "ds_read_b128 v[92:95], v145 offset:9216\n\t" \
  "v_cvt_pk_bf16_f32 v105, v34, v35\n\t" \
  "v_exp_f32 v37, v37\n\t" \
  "v_exp_f32 v38, v38\n\t" \
  "v_cvt_pk_bf16_f32 v106, v36, v37\n\t" \
  "v_exp_f32 v39, v39\n\t" \
  "v_mfma_f32_32x32x16_bf16 a[144:159], v[96:99], v[112:115], 0\n\t" \
  "ds_read_b128 v[96:99], v145 offset:6144\n\t" \
  "v_exp_f32 v40, v40\n\t" \
  "v_cvt_pk_bf16_f32 v107, v38, v39\n\t" \
  "v_exp_f32 v41, v41\n\t" \
  "v_exp_f32 v42, v42\n\t" \
  "v_cvt_pk_bf16_f32 v108, v40, v41\n\t" \
  "v_exp_f32 v43, v43\n\t" \
  "v_mfma_f32_32x32x16_bf16 a[144:159], v[100:103], v[116:119], a[144:159]\n\t" \
  "ds_read_b128 v[100:103], v145 offset:7168\n\t" \
  "v_exp_f32 v44, v44\n\t" \
  "v_cvt_pk_bf16_f32 v109, v42, v43\n\t" \
  "v_exp_f32 v45, v45\n\t" \
  "v_exp_f32 v46, v46\n\t" \
  "v_smfmac_f32_16x16x64_bf16 a[160:163], v[84:87], v[112:119], v154\n\t" \
  "v_cvt_pk_bf16_f32 v110, v44, v45\n\t" \
  "v_exp_f32 v47, v47\n\t" \
  "v_exp_f32 v48, v48\n\t" \
  "v_cvt_pk_bf16_f32 v111, v46, v47\n\t" \
  "v_exp_f32 v49, v49\n\t" \
  "s_waitcnt lgkmcnt(1)\n\t" \
  "v_mfma_f32_32x32x16_bf16 v[32:47], v[88:91], v[64:67], v[0:15]\n\t" \
  "v_exp_f32 v50, v50\n\t" \
  "v_cvt_pk_bf16_f32 v112, v48, v49\n\t" \
  "v_exp_f32 v51, v51\n\t" \
  "v_exp_f32 v52, v52\n\t" \
  "v_mfma_f32_32x32x16_bf16 v[32:47], v[92:95], v[68:71], v[32:47]\n\t" \
  "v_cvt_pk_bf16_f32 v113, v50, v51\n\t" \
  "v_exp_f32 v53, v53\n\t" \
  "v_exp_f32 v54, v54\n\t" \
  "v_cvt_pk_bf16_f32 v114, v52, v53\n\t" \
  "v_exp_f32 v55, v55\n\t" \
  "v_mfma_f32_32x32x16_bf16 a[128:143], v[96:99], v[104:107], a[128:143]\n\t" \
  "v_exp_f32 v56, v56\n\t" \
  "v_cvt_pk_bf16_f32 v115, v54, v55\n\t" \
  "v_exp_f32 v57, v57\n\t" \
  "v_exp_f32 v58, v58\n\t" \
  "v_cvt_pk_bf16_f32 v116, v56, v57\n\t" \
  "v_exp_f32 v59, v59\n\t" \
  "s_waitcnt lgkmcnt(0)\n\t" \
  "v_mfma_f32_32x32x16_bf16 a[128:143], v[100:103], v[108:111], a[128:143]\n\t" \
  "v_exp_f32 v60, v60\n\t" \
  "v_cvt_pk_bf16_f32 v117, v58, v59\n\t" \
  "v_exp_f32 v61, v61\n\t" \
  "v_exp_f32 v62, v62\n\t" \
  "v_smfmac_f32_16x16x64_bf16 a[160:163], v[80:83], v[104:111], v154\n\t" \
  "global_load_dword v136, v152, s[48:49]\n\t" \
  "v_cvt_pk_bf16_f32 v118, v60, v61\n\t" \
  "v_exp_f32 v63, v63\n\t" \
  "v_exp_f32 v32, v32\n\t" \
  "v_cvt_pk_bf16_f32 v119, v62, v63\n\t" \
  "v_exp_f32 v33, v33\n\t" \
  "v_mfma_f32_32x32x16_bf16 v[48:63], v[88:91], v[72:75], v[16:31]\n\t" \
  "ds_read_b128 v[88:91], v145 offset:12288\n\t" \
  "v_exp_f32 v34, v34\n\t" \
  "v_cvt_pk_bf16_f32 v104, v32, v33\n\t" \
  "v_exp_f32 v35, v35\n\t" \
  "v_exp_f32 v36, v36\n\t" \
  "v_mfma_f32_32x32x16_bf16 v[48:63], v[92:95], v[76:79], v[48:63]\n\t" \
  "ds_read_b128 v[92:95], v145 offset:13312\n\t" \
  "v_cvt_pk_bf16_f32 v105, v34, v35\n\t" \
  "v_exp_f32 v37, v37\n\t" \
  "v_exp_f32 v38, v38\n\t" \
  "v_cvt_pk_bf16_f32 v106, v36, v37\n\t" \
  "v_exp_f32 v39, v39\n\t" \
  "v_mfma_f32_32x32x16_bf16 a[144:159], v[96:99], v[112:115], a[144:159]\n\t" \
  "ds_read_b128 v[96:99], v145 offset:10240\n\t" \
  "v_exp_f32 v40, v40\n\t" \
  "v_cvt_pk_bf16_f32 v107, v38, v39\n\t" \
  "v_exp_f32 v41, v41\n\t" \
  "s_add_u32 s48, s48, 458752\n\t" \
  "s_addc_u32 s49, s49, 0\n\t" \
  "v_exp_f32 v42, v42\n\t" \
  "v_cvt_pk_bf16_f32 v108, v40, v41\n\t" \
  "v_exp_f32 v43, v43\n\t" \
  "v_mfma_f32_32x32x16_bf16 a[144:159], v[100:103], v[116:119], a[144:159]\n\t" \
  "ds_read_b128 v[100:103], v145 offset:11264\n\t" \
  "v_exp_f32 v44, v44\n\t" \
  "v_cvt_pk_bf16_f32 v109, v42, v43\n\t" \
  "v_exp_f32 v45, v45\n\t" \
  "v_exp_f32 v46, v46\n\t" \
  "v_smfmac_f32_16x16x64_bf16 a[160:163], v[84:87], v[112:119], v154\n\t" \
  "v_cvt_pk_bf16_f32 v110, v44, v45\n\t" \
  "v_exp_f32 v47, v47\n\t" \
  "v_exp_f32 v48, v48\n\t" \
  "v_cvt_pk_bf16_f32 v111, v46, v47\n\t" \
  "v_exp_f32 v49, v49\n\t" \
  "s_waitcnt lgkmcnt(1)\n\t" \
  "v_mfma_f32_32x32x16_bf16 v[32:47], v[88:91], v[64:67], v[0:15]\n\t" \
  "v_exp_f32 v50, v50\n\t" \
  "v_cvt_pk_bf16_f32 v112, v48, v49\n\t" \
  "v_exp_f32 v51, v51\n\t" \
  "v_exp_f32 v52, v52\n\t" \
  "v_mfma_f32_32x32x16_bf16 v[32:47], v[92:95], v[68:71], v[32:47]\n\t" \
  "v_cvt_pk_bf16_f32 v113, v50, v51\n\t" \
  "v_exp_f32 v53, v53\n\t" \
  "v_exp_f32 v54, v54\n\t" \
  "v_cvt_pk_bf16_f32 v114, v52, v53\n\t" \
  "v_exp_f32 v55, v55\n\t" \
  "v_mfma_f32_32x32x16_bf16 a[128:143], v[96:99], v[104:107], a[128:143]\n\t" \
  "v_exp_f32 v56, v56\n\t" \
  "v_cvt_pk_bf16_f32 v115, v54, v55\n\t" \
  "v_exp_f32 v57, v57\n\t" \
  "v_exp_f32 v58, v58\n\t" \
  "v_cvt_pk_bf16_f32 v116, v56, v57\n\t" \
  "v_exp_f32 v59, v59\n\t" \
  "s_waitcnt lgkmcnt(0)\n\t" \
  "v_mfma_f32_32x32x16_bf16 a[128:143], v[100:103], v[108:111], a[128:143]\n\t" \
  "v_exp_f32 v60, v60\n\t" \
  "v_cvt_pk_bf16_f32 v117, v58, v59\n\t" \
  "v_exp_f32 v61, v61\n\t" \
  "v_exp_f32 v62, v62\n\t" \
  "v_smfmac_f32_16x16x64_bf16 a[160:163], v[80:83], v[104:111], v154\n\t" \
  "global_load_dword v136, v152, s[48:49]\n\t" \
  "v_cvt_pk_bf16_f32 v118, v60, v61\n\t" \
  "v_exp_f32 v63, v63\n\t" \
  "v_exp_f32 v32, v32\n\t" \
  "v_cvt_pk_bf16_f32 v119, v62, v63\n\t" \
  "v_exp_f32 v33, v33\n\t" \
  "v_mfma_f32_32x32x16_bf16 v[48:63], v[88:91], v[72:75], v[16:31]\n\t" \
  "ds_read_b128 v[88:91], v145 offset:16384\n\t" \
  "v_exp_f32 v34, v34\n\t" \
  "v_cvt_pk_bf16_f32 v104, v32, v33\n\t" \
  "v_exp_f32 v35, v35\n\t" \
  "v_exp_f32 v36, v36\n\t" \
  "v_mfma_f32_32x32x16_bf16 v[48:63], v[92:95], v[76:79], v[48:63]\n\t" \
  "ds_read_b128 v[92:95], v145 offset:17408\n\t" \
  "v_cvt_pk_bf16_f32 v105, v34, v35\n\t" \
  "v_exp_f32 v37, v37\n\t" \
  "v_exp_f32 v38, v38\n\t" \
  "v_cvt_pk_bf16_f32 v106, v36, v37\n\t" \
  "v_exp_f32 v39, v39\n\t" \
  "v_mfma_f32_32x32x16_bf16 a[144:159], v[96:99], v[112:115], a[144:159]\n\t" \
  "ds_read_b128 v[96:99], v145 offset:14336\n\t" \
  "v_exp_f32 v40, v40\n\t" \
  "v_cvt_pk_bf16_f32 v107, v38, v39\n\t" \
  "v_exp_f32 v41, v41\n\t" \
  "v_exp_f32 v42, v42\n\t" \
  "v_cvt_pk_bf16_f32 v108, v40, v41\n\t" \
  "v_exp_f32 v43, v43\n\t" \
  "v_mfma_f32_32x32x16_bf16 a[144:159], v[100:103], v[116:119], a[144:159]\n\t" \
  "ds_read_b128 v[100:103], v145 offset:15360\n\t" \
  "v_exp_f32 v44, v44\n\t" \
  "v_cvt_pk_bf16_f32 v109, v42, v43\n\t" \
  "v_exp_f32 v45, v45\n\t" \
  "v_exp_f32 v46, v46\n\t" \
  "v_smfmac_f32_16x16x64_bf16 a[160:163], v[84:87], v[112:119], v154\n\t" \
  "v_cvt_pk_bf16_f32 v110, v44, v45\n\t" \
  "v_exp_f32 v47, v47\n\t" \
  "v_exp_f32 v48, v48\n\t" \
  "v_cvt_pk_bf16_f32 v111, v46, v47\n\t" \
  "v_exp_f32 v49, v49\n\t" \
  "s_waitcnt lgkmcnt(1)\n\t" \
  "v_mfma_f32_32x32x16_bf16 v[32:47], v[88:91], v[64:67], v[0:15]\n\t" \
  "v_exp_f32 v50, v50\n\t" \
  "v_cvt_pk_bf16_f32 v112, v48, v49\n\t" \
  "v_exp_f32 v51, v51\n\t" \
  "v_exp_f32 v52, v52\n\t" \
  "v_mfma_f32_32x32x16_bf16 v[32:47], v[92:95], v[68:71], v[32:47]\n\t" \
  "v_cvt_pk_bf16_f32 v113, v50, v51\n\t" \
  "v_exp_f32 v53, v53\n\t" \
  "v_exp_f32 v54, v54\n\t" \
  "v_cvt_pk_bf16_f32 v114, v52, v53\n\t" \
  "v_exp_f32 v55, v55\n\t" \
  "v_mfma_f32_32x32x16_bf16 a[128:143], v[96:99], v[104:107], a[128:143]\n\t" \
  "v_exp_f32 v56, v56\n\t" \
  "v_cvt_pk_bf16_f32 v115, v54, v55\n\t" \
  "v_exp_f32 v57, v57\n\t" \
  "v_exp_f32 v58, v58\n\t" \
  "v_cvt_pk_bf16_f32 v116, v56, v57\n\t" \
  "v_exp_f32 v59, v59\n\t" \
  "s_waitcnt lgkmcnt(0)\n\t" \
  "v_mfma_f32_32x32x16_bf16 a[128:143], v[100:103], v[108:111], a[128:143]\n\t" \
  "v_exp_f32 v60, v60\n\t" \
  "v_cvt_pk_bf16_f32 v117, v58, v59\n\t" \
  "v_exp_f32 v61, v61\n\t" \
  "v_exp_f32 v62, v62\n\t" \
  "v_smfmac_f32_16x16x64_bf16 a[160:163], v[80:83], v[104:111], v154\n\t" \
  "v_cvt_pk_bf16_f32 v118, v60, v61\n\t" \
  "v_exp_f32 v63, v63\n\t" \
  "v_exp_f32 v32, v32\n\t" \
  "v_cvt_pk_bf16_f32 v119, v62, v63\n\t" \
  "v_exp_f32 v33, v33\n\t" \
  "v_mfma_f32_32x32x16_bf16 v[48:63], v[88:91], v[72:75], v[16:31]\n\t" \
  "ds_read_b128 v[88:91], v145 offset:20480\n\t" \
  "v_exp_f32 v34, v34\n\t" \
  "v_cvt_pk_bf16_f32 v104, v32, v33\n\t" \
  "v_exp_f32 v35, v35\n\t" \
  "v_exp_f32 v36, v36\n\t" \
  "v_mfma_f32_32x32x16_bf16 v[48:63], v[92:95], v[76:79], v[48:63]\n\t" \
  "ds_read_b128 v[92:95], v145 offset:21504\n\t" \
  "v_cvt_pk_bf16_f32 v105, v34, v35\n\t" \
  "v_exp_f32 v37, v37\n\t" \
  "v_exp_f32 v38, v38\n\t" \
  "v_cvt_pk_bf16_f32 v106, v36, v37\n\t" \
  "v_exp_f32 v39, v39\n\t" \
  "v_mfma_f32_32x32x16_bf16 a[144:159], v[96:99], v[112:115], a[144:159]\n\t" \
  "ds_read_b128 v[96:99], v145 offset:18432\n\t" \
  "v_exp_f32 v40, v40\n\t" \
  "v_cvt_pk_bf16_f32 v107, v38, v39\n\t" \
  "v_exp_f32 v41, v41\n\t" \
  "s_add_u32 s48, s48, 458752\n\t" \
  "s_addc_u32 s49, s49, 0\n\t" \
  "v_exp_f32 v42, v42\n\t" \
  "v_cvt_pk_bf16_f32 v108, v40, v41\n\t" \
  "v_exp_f32 v43, v43\n\t" \
  "v_mfma_f32_32x32x16_bf16 a[144:159], v[100:103], v[116:119], a[144:159]\n\t" \
  "ds_read_b128 v[100:103], v145 offset:19456\n\t" \
  "v_exp_f32 v44, v44\n\t" \
  "v_cvt_pk_bf16_f32 v109, v42, v43\n\t" \
  "v_exp_f32 v45, v45\n\t" \
  "v_exp_f32 v46, v46\n\t" \
  "v_smfmac_f32_16x16x64_bf16 a[160:163], v[84:87], v[112:119], v154\n\t" \
  "v_cvt_pk_bf16_f32 v110, v44, v45\n\t" \
  "v_exp_f32 v47, v47\n\t" \
  "v_exp_f32 v48, v48\n\t" \
  "v_cvt_pk_bf16_f32 v111, v46, v47\n\t" \
  "v_exp_f32 v49, v49\n\t" \
  "s_waitcnt lgkmcnt(1)\n\t" \
  "v_mfma_f32_32x32x16_bf16 v[32:47], v[88:91], v[64:67], v[0:15]\n\t" \
  "v_exp_f32 v50, v50\n\t" \
  "v_cvt_pk_bf16_f32 v112, v48, v49\n\t" \
  "v_exp_f32 v51, v51\n\t" \
  "v_exp_f32 v52, v52\n\t" \
  "v_mfma_f32_32x32x16_bf16 v[32:47], v[92:95], v[68:71], v[32:47]\n\t" \
  "v_cvt_pk_bf16_f32 v113, v50, v51\n\t" \
  "v_exp_f32 v53, v53\n\t" \
  "v_exp_f32 v54, v54\n\t" \
  "v_cvt_pk_bf16_f32 v114, v52, v53\n\t" \
  "v_exp_f32 v55, v55\n\t" \
  "v_mfma_f32_32x32x16_bf16 a[128:143], v[96:99], v[104:107], a[128:143]\n\t" \
  "v_exp_f32 v56, v56\n\t" \
  "v_cvt_pk_bf16_f32 v115, v54, v55\n\t" \
  "v_exp_f32 v57, v57\n\t" \
  "v_exp_f32 v58, v58\n\t" \
  "v_cvt_pk_bf16_f32 v116, v56, v57\n\t" \
  "v_exp_f32 v59, v59\n\t" \
  "s_waitcnt lgkmcnt(0)\n\t" \
  "v_mfma_f32_32x32x16_bf16 a[128:143], v[100:103], v[108:111], a[128:143]\n\t" \
  "v_exp_f32 v60, v60\n\t" \
  "v_cvt_pk_bf16_f32 v117, v58, v59\n\t" \
  "v_exp_f32 v61, v61\n\t" \
  "v_exp_f32 v62, v62\n\t" \
  "v_smfmac_f32_16x16x64_bf16 a[160:163], v[80:83], v[104:111], v154\n\t" \
  "global_load_dword v136, v152, s[48:49]\n\t" \
  "v_cvt_pk_bf16_f32 v118, v60, v61\n\t" \
  "v_exp_f32 v63, v63\n\t" \
  "v_exp_f32 v32, v32\n\t" \
  "v_cvt_pk_bf16_f32 v119, v62, v63\n\t" \
  "v_exp_f32 v33, v33\n\t" \
  "v_mfma_f32_32x32x16_bf16 v[48:63], v[88:91], v[72:75], v[16:31]\n\t" \
  "ds_read_b128 v[88:91], v145 offset:24576\n\t" \
  "v_exp_f32 v34, v34\n\t" \
  "v_cvt_pk_bf16_f32 v104, v32, v33\n\t" \
  "v_exp_f32 v35, v35\n\t" \
  "v_exp_f32 v36, v36\n\t" \
  "v_mfma_f32_32x32x16_bf16 v[48:63], v[92:95], v[76:79], v[48:63]\n\t" \
  "ds_read_b128 v[92:95], v145 offset:25600\n\t" \
  "v_cvt_pk_bf16_f32 v105, v34, v35\n\t" \
  "v_exp_f32 v37, v37\n\t" \
  "v_exp_f32 v38, v38\n\t" \
  "v_cvt_pk_bf16_f32 v106, v36, v37\n\t" \
  "v_exp_f32 v39, v39\n\t" \
  "v_mfma_f32_32x32x16_bf16 a[144:159], v[96:99], v[112:115], a[144:159]\n\t" \
  "ds_read_b128 v[96:99], v145 offset:22528\n\t" \
  "v_exp_f32 v40, v40\n\t" \
  "v_cvt_pk_bf16_f32 v107, v38, v39\n\t" \
  "v_exp_f32 v41, v41\n\t" \
  "s_add_u32 s48, s48, 458752\n\t" \
  "s_addc_u32 s49, s49, 0\n\t" \
  "v_exp_f32 v42, v42\n\t" \
  "v_cvt_pk_bf16_f32 v108, v40, v41\n\t" \
  "v_exp_f32 v43, v43\n\t" \
  "v_mfma_f32_32x32x16_bf16 a[144:159], v[100:103], v[116:119], a[144:159]\n\t" \
  "ds_read_b128 v[100:103], v145 offset:23552\n\t" \
  "v_exp_f32 v44, v44\n\t" \
  "v_cvt_pk_bf16_f32 v109, v42, v43\n\t" \
  "v_exp_f32 v45, v45\n\t" \
  "v_exp_f32 v46, v46\n\t" \
  "v_smfmac_f32_16x16x64_bf16 a[160:163], v[84:87], v[112:119], v154\n\t" \
  "v_cvt_pk_bf16_f32 v110, v44, v45\n\t" \
  "v_exp_f32 v47, v47\n\t" \
  "v_exp_f32 v48, v48\n\t" \
  "v_cvt_pk_bf16_f32 v111, v46, v47\n\t" \
  "v_exp_f32 v49, v49\n\t" \
  "s_waitcnt lgkmcnt(1)\n\t" \
  "v_mfma_f32_32x32x16_bf16 v[32:47], v[88:91], v[64:67], v[0:15]\n\t" \
  "v_exp_f32 v50, v50\n\t" \
  "v_cvt_pk_bf16_f32 v112, v48, v49\n\t" \
  "v_exp_f32 v51, v51\n\t" \
  "v_exp_f32 v52, v52\n\t" \
  "v_mfma_f32_32x32x16_bf16 v[32:47], v[92:95], v[68:71], v[32:47]\n\t" \
  "v_cvt_pk_bf16_f32 v113, v50, v51\n\t" \
  "v_exp_f32 v53, v53\n\t" \
  "v_exp_f32 v54, v54\n\t" \
  "v_cvt_pk_bf16_f32 v114, v52, v53\n\t" \
  "v_exp_f32 v55, v55\n\t" \
  "v_mfma_f32_32x32x16_bf16 a[128:143], v[96:99], v[104:107], a[128:143]\n\t" \
  "v_exp_f32 v56, v56\n\t" \
  "v_cvt_pk_bf16_f32 v115, v54, v55\n\t" \
  "v_exp_f32 v57, v57\n\t" \
  "v_exp_f32 v58, v58\n\t" \
  "v_cvt_pk_bf16_f32 v116, v56, v57\n\t" \
  "v_exp_f32 v59, v59\n\t" \
  "s_waitcnt lgkmcnt(0)\n\t" \
  "v_mfma_f32_32x32x16_bf16 a[128:143], v[100:103], v[108:111], a[128:143]\n\t" \
  "v_exp_f32 v60, v60\n\t" \
  "v_cvt_pk_bf16_f32 v117, v58, v59\n\t" \
  "v_exp_f32 v61, v61\n\t" \
  "v_exp_f32 v62, v62\n\t" \
  "v_smfmac_f32_16x16x64_bf16 a[160:163], v[80:83], v[104:111], v154\n\t" \
  "global_load_dword v136, v152, s[48:49]\n\t" \
  "v_cvt_pk_bf16_f32 v118, v60, v61\n\t" \
  "v_exp_f32 v63, v63\n\t" \
  "v_exp_f32 v32, v32\n\t" \
  "v_cvt_pk_bf16_f32 v119, v62, v63\n\t" \
  "v_exp_f32 v33, v33\n\t" \
  "v_mfma_f32_32x32x16_bf16 v[48:63], v[88:91], v[72:75], v[16:31]\n\t" \
  "ds_read_b128 v[88:91], v145 offset:28672\n\t" \
  "v_exp_f32 v34, v34\n\t" \
  "v_cvt_pk_bf16_f32 v104, v32, v33\n\t" \
  "v_exp_f32 v35, v35\n\t" \
  "v_exp_f32 v36, v36\n\t" \
  "v_mfma_f32_32x32x16_bf16 v[48:63], v[92:95], v[76:79], v[48:63]\n\t" \
  "ds_read_b128 v[92:95], v145 offset:29696\n\t" \
  "v_cvt_pk_bf16_f32 v105, v34, v35\n\t" \
  "v_exp_f32 v37, v37\n\t" \
  "v_exp_f32 v38, v38\n\t" \
  "v_cvt_pk_bf16_f32 v106, v36, v37\n\t" \
  "v_exp_f32 v39, v39\n\t" \
  "v_mfma_f32_32x32x16_bf16 a[144:159], v[96:99], v[112:115], a[144:159]\n\t" \
  "ds_read_b128 v[96:99], v145 offset:26624\n\t" \
  "v_exp_f32 v40, v40\n\t" \
  "v_cvt_pk_bf16_f32 v107, v38, v39\n\t" \
  "v_exp_f32 v41, v41\n\t" \
  "v_exp_f32 v42, v42\n\t" \
  "v_cvt_pk_bf16_f32 v108, v40, v41\n\t" \
  "v_exp_f32 v43, v43\n\t" \
  "v_mfma_f32_32x32x16_bf16 a[144:159], v[100:103], v[116:119], a[144:159]\n\t" \
  "ds_read_b128 v[100:103], v145 offset:27648\n\t" \
  "v_exp_f32 v44, v44\n\t" \
  "v_cvt_pk_bf16_f32 v109, v42, v43\n\t" \
  "v_exp_f32 v45, v45\n\t" \
  "v_exp_f32 v46, v46\n\t" \
  "v_smfmac_f32_16x16x64_bf16 a[160:163], v[84:87], v[112:119], v154\n\t" \
  "v_cvt_pk_bf16_f32 v110, v44, v45\n\t" \
  "v_exp_f32 v47, v47\n\t" \
  "v_exp_f32 v48, v48\n\t" \
  "v_cvt_pk_bf16_f32 v111, v46, v47\n\t" \
  "v_exp_f32 v49, v49\n\t" \
  "s_waitcnt lgkmcnt(1)\n\t" \
  "v_mfma_f32_32x32x16_bf16 v[32:47], v[88:91], v[64:67], v[0:15]\n\t" \
  "v_exp_f32 v50, v50\n\t" \
  "v_cvt_pk_bf16_f32 v112, v48, v49\n\t" \
  "v_exp_f32 v51, v51\n\t" \
  "v_exp_f32 v52, v52\n\t" \
  "v_mfma_f32_32x32x16_bf16 v[32:47], v[92:95], v[68:71], v[32:47]\n\t" \
  "v_cvt_pk_bf16_f32 v113, v50, v51\n\t" \
  "v_exp_f32 v53, v53\n\t" \
  "v_exp_f32 v54, v54\n\t" \
  "v_cvt_pk_bf16_f32 v114, v52, v53\n\t" \
  "v_exp_f32 v55, v55\n\t" \
  "v_mfma_f32_32x32x16_bf16 a[128:143], v[96:99], v[104:107], a[128:143]\n\t" \
  "v_exp_f32 v56, v56\n\t" \
  "v_cvt_pk_bf16_f32 v115, v54, v55\n\t" \
  "v_exp_f32 v57, v57\n\t" \
  "v_exp_f32 v58, v58\n\t" \
  "v_cvt_pk_bf16_f32 v116, v56, v57\n\t" \
  "v_exp_f32 v59, v59\n\t" \
  "s_waitcnt lgkmcnt(0)\n\t" \
  "v_mfma_f32_32x32x16_bf16 a[128:143], v[100:103], v[108:111], a[128:143]\n\t" \
  "v_exp_f32 v60, v60\n\t" \
  "v_cvt_pk_bf16_f32 v117, v58, v59\n\t" \
  "v_exp_f32 v61, v61\n\t" \
  "v_exp_f32 v62, v62\n\t" \
  "v_smfmac_f32_16x16x64_bf16 a[160:163], v[80:83], v[104:111], v154\n\t" \
  "global_load_dword v136, v152, s[50:51]\n\t" \
  "v_cvt_pk_bf16_f32 v118, v60, v61\n\t" \
  "v_exp_f32 v63, v63\n\t" \
  "s_barrier\n\t" \
  "v_exp_f32 v32, v32\n\t" \
  "v_cvt_pk_bf16_f32 v119, v62, v63\n\t" \
  "v_exp_f32 v33, v33\n\t" \
  "v_mfma_f32_32x32x16_bf16 v[48:63], v[88:91], v[72:75], v[16:31]\n\t" \
  "ds_read_b128 v[88:91], v145 offset:32768\n\t" \
  "v_exp_f32 v34, v34\n\t" \
  "v_cvt_pk_bf16_f32 v104, v32, v33\n\t" \
  "v_exp_f32 v35, v35\n\t" \
  "v_exp_f32 v36, v36\n\t" \
  "v_mfma_f32_32x32x16_bf16 v[48:63], v[92:95], v[76:79], v[48:63]\n\t" \
  "ds_read_b128 v[92:95], v145 offset:33792\n\t" \
  "v_cvt_pk_bf16_f32 v105, v34, v35\n\t" \
  "v_exp_f32 v37, v37\n\t" \
  "v_exp_f32 v38, v38\n\t" \
  "v_cvt_pk_bf16_f32 v106, v36, v37\n\t" \
  "v_exp_f32 v39, v39\n\t" \
  "v_mfma_f32_32x32x16_bf16 a[144:159], v[96:99], v[112:115], a[144:159]\n\t" \
  "ds_read_b128 v[96:99], v145 offset:30720\n\t" \
  "v_exp_f32 v40, v40\n\t" \
  "v_cvt_pk_bf16_f32 v107, v38, v39\n\t" \
  "v_exp_f32 v41, v41\n\t" \
  "v_exp_f32 v42, v42\n\t" \
  "v_cvt_pk_bf16_f32 v108, v40, v41\n\t" \
  "v_exp_f32 v43, v43\n\t" \
  "v_mfma_f32_32x32x16_bf16 a[144:159], v[100:103], v[116:119], a[144:159]\n\t" \
  "ds_read_b128 v[100:103], v145 offset:31744\n\t" \
  "v_exp_f32 v44, v44\n\t" \
  "v_cvt_pk_bf16_f32 v109, v42, v43\n\t" \
  "v_exp_f32 v45, v45\n\t" \
  "v_exp_f32 v46, v46\n\t" \
  "v_smfmac_f32_16x16x64_bf16 a[160:163], v[84:87], v[112:119], v154\n\t" \
  "s_add_u32 m0, s73, 0\n\t" \
  "s_mov_b32 s69, 49152\n\t" \
  "v_cvt_pk_bf16_f32 v110, v44, v45\n\t" \
  "v_exp_f32 v47, v47\n\t" \
  "v_exp_f32 v48, v48\n\t" \
  "v_cvt_pk_bf16_f32 v111, v46, v47\n\t" \
  "v_exp_f32 v49, v49\n\t" \
  "s_waitcnt lgkmcnt(1)\n\t" \
  "v_mfma_f32_32x32x16_bf16 v[32:47], v[88:91], v[64:67], v[0:15]\n\t" \
  "v_exp_f32 v50, v50\n\t" \
  "v_cvt_pk_bf16_f32 v112, v48, v49\n\t" \
  "v_exp_f32 v51, v51\n\t" \
  "v_exp_f32 v52, v52\n\t" \
  "v_mfma_f32_32x32x16_bf16 v[32:47], v[92:95], v[68:71], v[32:47]\n\t" \
  "v_cvt_pk_bf16_f32 v113, v50, v51\n\t" \
  "v_exp_f32 v53, v53\n\t" \
  "v_exp_f32 v54, v54\n\t" \
  "v_cvt_pk_bf16_f32 v114, v52, v53\n\t" \
  "v_exp_f32 v55, v55\n\t" \
  "v_mfma_f32_32x32x16_bf16 a[128:143], v[96:99], v[104:107], a[128:143]\n\t" \
  "v_exp_f32 v56, v56\n\t" \
  "v_cvt_pk_bf16_f32 v115, v54, v55\n\t" \
  "v_exp_f32 v57, v57\n\t" \
  "v_exp_f32 v58, v58\n\t" \
  "v_cvt_pk_bf16_f32 v116, v56, v57\n\t" \
  "v_exp_f32 v59, v59\n\t" \
  "s_waitcnt lgkmcnt(0)\n\t" \
  "v_mfma_f32_32x32x16_bf16 a[128:143], v[100:103], v[108:111], a[128:143]\n\t" \
  "buffer_load_dwordx4 v144, s[88:91], s69 offen offset:1024 sc1 lds\n\t" \
  "buffer_load_dwordx4 v144, s[88:91], s69 offen offset:0 sc1 lds\n\t" \
  "v_exp_f32 v60, v60\n\t" \
  "v_cvt_pk_bf16_f32 v117, v58, v59\n\t" \
  "v_exp_f32 v61, v61\n\t" \
  "v_exp_f32 v62, v62\n\t" \
  "v_smfmac_f32_16x16x64_bf16 a[160:163], v[80:83], v[104:111], v154\n\t" \
  "buffer_load_dwordx4 v144, s[88:91], s69 offen offset:2048 sc1 lds\n\t" \
  "buffer_load_dwordx4 v144, s[88:91], s69 offen offset:3072 sc1 lds\n\t" \
  "v_cvt_pk_bf16_f32 v118, v60, v61\n\t" \
  "v_exp_f32 v63, v63\n\t" \
  "v_exp_f32 v32, v32\n\t" \
  "v_cvt_pk_bf16_f32 v119, v62, v63\n\t" \
  "v_exp_f32 v33, v33\n\t" \
  "v_mfma_f32_32x32x16_bf16 v[48:63], v[88:91], v[72:75], v[16:31]\n\t" \
  "ds_read_b128 v[88:91], v145 offset:36864\n\t" \
  "v_exp_f32 v34, v34\n\t" \
  "v_cvt_pk_bf16_f32 v104, v32, v33\n\t" \
  "v_exp_f32 v35, v35\n\t" \
  "v_exp_f32 v36, v36\n\t" \
  "v_mfma_f32_32x32x16_bf16 v[48:63], v[92:95], v[76:79], v[48:63]\n\t" \
  "ds_read_b128 v[92:95], v145 offset:37888\n\t" \
  "v_cvt_pk_bf16_f32 v105, v34, v35\n\t" \
  "v_exp_f32 v37, v37\n\t" \
  "v_exp_f32 v38, v38\n\t" \
  "v_cvt_pk_bf16_f32 v106, v36, v37\n\t" \
  "v_exp_f32 v39, v39\n\t" \
  "v_mfma_f32_32x32x16_bf16 a[144:159], v[96:99], v[112:115], a[144:159]\n\t" \
  "ds_read_b128 v[96:99], v145 offset:34816\n\t" \
  "v_exp_f32 v40, v40\n\t" \
  "v_cvt_pk_bf16_f32 v107, v38, v39\n\t" \
  "v_exp_f32 v41, v41\n\t" \
  "v_exp_f32 v42, v42\n\t" \
  "v_cvt_pk_bf16_f32 v108, v40, v41\n\t" \
  "v_exp_f32 v43, v43\n\t" \
  "v_mfma_f32_32x32x16_bf16 a[144:159], v[100:103], v[116:119], a[144:159]\n\t" \
  "ds_read_b128 v[100:103], v145 offset:35840\n\t" \
  "v_exp_f32 v44, v44\n\t" \
  "v_cvt_pk_bf16_f32 v109, v42, v43\n\t" \
  "v_exp_f32 v45, v45\n\t" \
  "v_exp_f32 v46, v46\n\t" \
  "v_smfmac_f32_16x16x64_bf16 a[160:163], v[84:87], v[112:119], v154\n\t" \
  "v_cvt_pk_bf16_f32 v110, v44, v45\n\t" \
  "v_exp_f32 v47, v47\n\t" \
  "v_exp_f32 v48, v48\n\t" \
  "v_cvt_pk_bf16_f32 v111, v46, v47\n\t" \
  "v_exp_f32 v49, v49\n\t" \
  "s_waitcnt lgkmcnt(1)\n\t" \
  "v_mfma_f32_32x32x16_bf16 v[32:47], v[88:91], v[64:67], v[0:15]\n\t" \
  "v_exp_f32 v50, v50\n\t" \
  "v_cvt_pk_bf16_f32 v112, v48, v49\n\t" \
  "v_exp_f32 v51, v51\n\t" \
  "v_exp_f32 v52, v52\n\t" \
  "v_mfma_f32_32x32x16_bf16 v[32:47], v[92:95], v[68:71], v[32:47]\n\t" \
  "v_cvt_pk_bf16_f32 v113, v50, v51\n\t" \
  "v_exp_f32 v53, v53\n\t" \
  "v_exp_f32 v54, v54\n\t" \
  "v_cvt_pk_bf16_f32 v114, v52, v53\n\t" \
  "v_exp_f32 v55, v55\n\t" \
  "v_mfma_f32_32x32x16_bf16 a[128:143], v[96:99], v[104:107], a[128:143]\n\t" \
  "v_exp_f32 v56, v56\n\t" \
  "v_cvt_pk_bf16_f32 v115, v54, v55\n\t" \
  "v_exp_f32 v57, v57\n\t" \
  "v_exp_f32 v58, v58\n\t" \
  "v_cvt_pk_bf16_f32 v116, v56, v57\n\t" \
  "v_exp_f32 v59, v59\n\t" \
  "s_waitcnt lgkmcnt(0)\n\t" \
  "v_mfma_f32_32x32x16_bf16 a[128:143], v[100:103], v[108:111], a[128:143]\n\t" \
  "v_exp_f32 v60, v60\n\t" \
  "v_cvt_pk_bf16_f32 v117, v58, v59\n\t" \
  "v_exp_f32 v61, v61\n\t" \
  "v_exp_f32 v62, v62\n\t" \
  "v_smfmac_f32_16x16x64_bf16 a[160:163], v[80:83], v[104:111], v154\n\t" \
  "v_cvt_pk_bf16_f32 v118, v60, v61\n\t" \
  "v_exp_f32 v63, v63\n\t" \
  "v_exp_f32 v32, v32\n\t" \
  "v_cvt_pk_bf16_f32 v119, v62, v63\n\t" \
  "v_exp_f32 v33, v33\n\t" \
  "v_mfma_f32_32x32x16_bf16 v[48:63], v[88:91], v[72:75], v[16:31]\n\t" \
  "ds_read_b128 v[88:91], v145 offset:40960\n\t" \
  "v_exp_f32 v34, v34\n\t" \
  "v_cvt_pk_bf16_f32 v104, v32, v33\n\t" \
  "v_exp_f32 v35, v35\n\t" \
  "v_exp_f32 v36, v36\n\t" \
  "v_mfma_f32_32x32x16_bf16 v[48:63], v[92:95], v[76:79], v[48:63]\n\t" \
  "ds_read_b128 v[92:95], v145 offset:41984\n\t" \
  "v_cvt_pk_bf16_f32 v105, v34, v35\n\t" \
  "v_exp_f32 v37, v37\n\t" \
  "v_exp_f32 v38, v38\n\t" \
  "v_cvt_pk_bf16_f32 v106, v36, v37\n\t" \
  "v_exp_f32 v39, v39\n\t" \
  "v_mfma_f32_32x32x16_bf16 a[144:159], v[96:99], v[112:115], a[144:159]\n\t" \
  "ds_read_b128 v[96:99], v145 offset:38912\n\t" \
  "v_exp_f32 v40, v40\n\t" \
  "v_cvt_pk_bf16_f32 v107, v38, v39\n\t" \
  "v_exp_f32 v41, v41\n\t" \
  "v_exp_f32 v42, v42\n\t" \
  "v_cvt_pk_bf16_f32 v108, v40, v41\n\t" \
  "v_exp_f32 v43, v43\n\t" \
  "v_mfma_f32_32x32x16_bf16 a[144:159], v[100:103], v[116:119], a[144:159]\n\t" \
  "ds_read_b128 v[100:103], v145 offset:39936\n\t" \
  "v_exp_f32 v44, v44\n\t" \
  "v_cvt_pk_bf16_f32 v109, v42, v43\n\t" \
  "v_exp_f32 v45, v45\n\t" \
  "v_exp_f32 v46, v46\n\t" \
  "v_smfmac_f32_16x16x64_bf16 a[160:163], v[84:87], v[112:119], v154\n\t" \
  "s_add_u32 m0, s72, 0\n\t" \
  "s_mov_b32 s68, 0\n\t" \
  "v_cvt_pk_bf16_f32 v110, v44, v45\n\t" \
  "v_exp_f32 v47, v47\n\t" \
  "v_exp_f32 v48, v48\n\t" \
  "v_cvt_pk_bf16_f32 v111, v46, v47\n\t" \
  "v_exp_f32 v49, v49\n\t" \
  "s_waitcnt lgkmcnt(1)\n\t" \
  "v_mfma_f32_32x32x16_bf16 v[32:47], v[88:91], v[64:67], v[0:15]\n\t" \
  "v_exp_f32 v50, v50\n\t" \
  "v_cvt_pk_bf16_f32 v112, v48, v49\n\t" \
  "v_exp_f32 v51, v51\n\t" \
  "v_exp_f32 v52, v52\n\t" \
  "v_mfma_f32_32x32x16_bf16 v[32:47], v[92:95], v[68:71], v[32:47]\n\t" \
  "v_cvt_pk_bf16_f32 v113, v50, v51\n\t" \
  "v_exp_f32 v53, v53\n\t" \
  "v_exp_f32 v54, v54\n\t" \
  "v_cvt_pk_bf16_f32 v114, v52, v53\n\t" \
  "v_exp_f32 v55, v55\n\t" \
  "v_mfma_f32_32x32x16_bf16 a[128:143], v[96:99], v[104:107], a[128:143]\n\t" \
  "v_exp_f32 v56, v56\n\t" \
  "v_cvt_pk_bf16_f32 v115, v54, v55\n\t" \
  "v_exp_f32 v57, v57\n\t" \
  "v_exp_f32 v58, v58\n\t" \
  "v_cvt_pk_bf16_f32 v116, v56, v57\n\t" \
  "v_exp_f32 v59, v59\n\t" \
  "s_waitcnt lgkmcnt(0)\n\t" \
  "v_mfma_f32_32x32x16_bf16 a[128:143], v[100:103], v[108:111], a[128:143]\n\t" \
  "buffer_load_dwordx4 v141, s[84:87], s68 offen offset:1024 lds\n\t" \
  "buffer_load_dwordx4 v140, s[84:87], s68 offen offset:0 lds\n\t" \
  "v_exp_f32 v60, v60\n\t" \
  "v_cvt_pk_bf16_f32 v117, v58, v59\n\t" \
  "v_exp_f32 v61, v61\n\t" \
  "v_exp_f32 v62, v62\n\t" \
  "v_smfmac_f32_16x16x64_bf16 a[160:163], v[80:83], v[104:111], v154\n\t" \
  "buffer_load_dwordx4 v142, s[84:87], s68 offen offset:2048 lds\n\t" \
  "buffer_load_dwordx4 v143, s[84:87], s68 offen offset:3072 lds\n\t" \
  "v_cvt_pk_bf16_f32 v118, v60, v61\n\t" \
  "v_exp_f32 v63, v63\n\t" \
  "v_exp_f32 v32, v32\n\t" \
  "v_cvt_pk_bf16_f32 v119, v62, v63\n\t" \
  "v_exp_f32 v33, v33\n\t" \
  "v_mfma_f32_32x32x16_bf16 v[48:63], v[88:91], v[72:75], v[16:31]\n\t" \
  "ds_read_b128 v[88:91], v145 offset:45056\n\t" \
  "v_exp_f32 v34, v34\n\t" \
  "v_cvt_pk_bf16_f32 v104, v32, v33\n\t" \
  "v_exp_f32 v35, v35\n\t" \
  "v_exp_f32 v36, v36\n\t" \
  "v_mfma_f32_32x32x16_bf16 v[48:63], v[92:95], v[76:79], v[48:63]\n\t" \
  "ds_read_b128 v[92:95], v145 offset:46080\n\t" \
  "v_cvt_pk_bf16_f32 v105, v34, v35\n\t" \
  "v_exp_f32 v37, v37\n\t" \
  "v_exp_f32 v38, v38\n\t" \
  "v_cvt_pk_bf16_f32 v106, v36, v37\n\t" \
  "v_exp_f32 v39, v39\n\t" \
  "v_mfma_f32_32x32x16_bf16 a[144:159], v[96:99], v[112:115], a[144:159]\n\t" \
  "ds_read_b128 v[96:99], v145 offset:43008\n\t" \
  "v_exp_f32 v40, v40\n\t" \
  "v_cvt_pk_bf16_f32 v107, v38, v39\n\t" \
  "v_exp_f32 v41, v41\n\t" \
  "v_exp_f32 v42, v42\n\t" \
  "v_cvt_pk_bf16_f32 v108, v40, v41\n\t" \
  "v_exp_f32 v43, v43\n\t" \
  "v_mfma_f32_32x32x16_bf16 a[144:159], v[100:103], v[116:119], a[144:159]\n\t" \
  "ds_read_b128 v[100:103], v145 offset:44032\n\t" \
  "v_exp_f32 v44, v44\n\t" \
  "v_cvt_pk_bf16_f32 v109, v42, v43\n\t" \
  "v_exp_f32 v45, v45\n\t" \
  "v_exp_f32 v46, v46\n\t" \
  "v_smfmac_f32_16x16x64_bf16 a[160:163], v[84:87], v[112:119], v154\n\t" \
  "s_add_u32 m0, s72, 4096\n\t" \
  "s_mov_b32 s68, 65536\n\t" \
  "v_cvt_pk_bf16_f32 v110, v44, v45\n\t" \
  "v_exp_f32 v47, v47\n\t" \
  "v_exp_f32 v48, v48\n\t" \
  "v_cvt_pk_bf16_f32 v111, v46, v47\n\t" \
  "v_exp_f32 v49, v49\n\t" \
  "s_waitcnt lgkmcnt(1)\n\t" \
  "v_mfma_f32_32x32x16_bf16 v[32:47], v[88:91], v[64:67], v[0:15]\n\t" \
  "v_exp_f32 v50, v50\n\t" \
  "v_cvt_pk_bf16_f32 v112, v48, v49\n\t" \
  "v_exp_f32 v51, v51\n\t" \
  "v_exp_f32 v52, v52\n\t" \
  "v_mfma_f32_32x32x16_bf16 v[32:47], v[92:95], v[68:71], v[32:47]\n\t" \
  "v_cvt_pk_bf16_f32 v113, v50, v51\n\t" \
  "v_exp_f32 v53, v53\n\t" \
  "v_exp_f32 v54, v54\n\t" \
  "v_cvt_pk_bf16_f32 v114, v52, v53\n\t" \
  "v_exp_f32 v55, v55\n\t" \
  "v_mfma_f32_32x32x16_bf16 a[128:143], v[96:99], v[104:107], a[128:143]\n\t" \
  "v_exp_f32 v56, v56\n\t" \
  "v_cvt_pk_bf16_f32 v115, v54, v55\n\t" \
  "v_exp_f32 v57, v57\n\t" \
  "v_exp_f32 v58, v58\n\t" \
  "v_cvt_pk_bf16_f32 v116, v56, v57\n\t" \
  "v_exp_f32 v59, v59\n\t" \
  "s_waitcnt lgkmcnt(0)\n\t" \
  "v_mfma_f32_32x32x16_bf16 a[128:143], v[100:103], v[108:111], a[128:143]\n\t" \
  "buffer_load_dwordx4 v141, s[84:87], s68 offen offset:1024 lds\n\t" \
  "buffer_load_dwordx4 v140, s[84:87], s68 offen offset:0 lds\n\t" \
  "v_exp_f32 v60, v60\n\t" \
  "v_cvt_pk_bf16_f32 v117, v58, v59\n\t" \
  "v_exp_f32 v61, v61\n\t" \
  "v_exp_f32 v62, v62\n\t" \
  "v_smfmac_f32_16x16x64_bf16 a[160:163], v[80:83], v[104:111], v154\n\t" \
  "buffer_load_dwordx4 v142, s[84:87], s68 offen offset:2048 lds\n\t" \
  "buffer_load_dwordx4 v143, s[84:87], s68 offen offset:3072 lds\n\t" \
  "v_cvt_pk_bf16_f32 v118, v60, v61\n\t" \
  "v_exp_f32 v63, v63\n\t" \
  "s_waitcnt vmcnt(8)\n\t" \
  "s_barrier\n\t" \
  "v_exp_f32 v32, v32\n\t" \
  "v_cvt_pk_bf16_f32 v119, v62, v63\n\t" \
  "v_exp_f32 v33, v33\n\t" \
  "v_mfma_f32_32x32x16_bf16 v[48:63], v[88:91], v[72:75], v[16:31]\n\t" \
  "ds_read_b128 v[88:91], v145 offset:0\n\t" \
  "v_exp_f32 v34, v34\n\t" \
  "v_cvt_pk_bf16_f32 v104, v32, v33\n\t" \
  "v_exp_f32 v35, v35\n\t" \
  "v_exp_f32 v36, v36\n\t" \
  "v_mfma_f32_32x32x16_bf16 v[48:63], v[92:95], v[76:79], v[48:63]\n\t" \
  "ds_read_b128 v[92:95], v145 offset:1024\n\t" \
  "v_cvt_pk_bf16_f32 v105, v34, v35\n\t" \
  "v_exp_f32 v37, v37\n\t" \
  "v_exp_f32 v38, v38\n\t" \
  "v_cvt_pk_bf16_f32 v106, v36, v37\n\t" \
  "v_exp_f32 v39, v39\n\t" \
  "v_mfma_f32_32x32x16_bf16 a[144:159], v[96:99], v[112:115], a[144:159]\n\t" \
  "ds_read_b128 v[96:99], v145 offset:47104\n\t" \
  "v_exp_f32 v40, v40\n\t" \
  "v_cvt_pk_bf16_f32 v107, v38, v39\n\t" \
  "v_exp_f32 v41, v41\n\t" \
  "v_exp_f32 v42, v42\n\t" \
  "v_cvt_pk_bf16_f32 v108, v40, v41\n\t" \
  "v_exp_f32 v43, v43\n\t" \
  "v_mfma_f32_32x32x16_bf16 a[144:159], v[100:103], v[116:119], a[144:159]\n\t" \
  "ds_read_b128 v[100:103], v145 offset:48128\n\t" \
  "v_exp_f32 v44, v44\n\t" \
  "v_cvt_pk_bf16_f32 v109, v42, v43\n\t" \
  "v_exp_f32 v45, v45\n\t" \
  "v_exp_f32 v46, v46\n\t" \
  "v_smfmac_f32_16x16x64_bf16 a[160:163], v[84:87], v[112:119], v154\n\t" \
  "s_add_u32 m0, s73, 16384\n\t" \
  "s_mov_b32 s69, 65536\n\t" \
  "v_mov_b32 v137, 0\n\t" \
  "global_load_dword v136, v137, s[78:79] sc1\n\t" \
  "v_cvt_pk_bf16_f32 v110, v44, v45\n\t" \
  "v_exp_f32 v47, v47\n\t" \
  "v_exp_f32 v48, v48\n\t" \
  "v_cvt_pk_bf16_f32 v111, v46, v47\n\t" \
  "v_exp_f32 v49, v49\n\t" \
  "s_waitcnt lgkmcnt(1)\n\t" \
  "v_mfma_f32_32x32x16_bf16 v[32:47], v[88:91], v[64:67], v[0:15]\n\t" \
  "v_exp_f32 v50, v50\n\t" \
  "v_cvt_pk_bf16_f32 v112, v48, v49\n\t" \
  "v_exp_f32 v51, v51\n\t" \
  "v_exp_f32 v52, v52\n\t" \
  "v_mfma_f32_32x32x16_bf16 v[32:47], v[92:95], v[68:71], v[32:47]\n\t" \
  "v_cvt_pk_bf16_f32 v113, v50, v51\n\t" \
  "v_exp_f32 v53, v53\n\t" \
  "v_exp_f32 v54, v54\n\t" \
  "v_cvt_pk_bf16_f32 v114, v52, v53\n\t" \
  "v_exp_f32 v55, v55\n\t" \
  "v_mfma_f32_32x32x16_bf16 a[128:143], v[96:99], v[104:107], a[128:143]\n\t" \
  "v_exp_f32 v56, v56\n\t" \
  "v_cvt_pk_bf16_f32 v115, v54, v55\n\t" \
  "v_exp_f32 v57, v57\n\t" \
  "v_exp_f32 v58, v58\n\t" \
  "v_cvt_pk_bf16_f32 v116, v56, v57\n\t" \
  "v_exp_f32 v59, v59\n\t" \
  "s_waitcnt lgkmcnt(0)\n\t" \
  "v_mfma_f32_32x32x16_bf16 a[128:143], v[100:103], v[108:111], a[128:143]\n\t" \
  "buffer_load_dwordx4 v144, s[88:91], s69 offen offset:1024 sc1 lds\n\t" \
  "buffer_load_dwordx4 v144, s[88:91], s69 offen offset:0 sc1 lds\n\t" \
  "v_exp_f32 v60, v60\n\t" \
  "v_cvt_pk_bf16_f32 v117, v58, v59\n\t" \
  "v_exp_f32 v61, v61\n\t" \
  "v_exp_f32 v62, v62\n\t" \
  "v_smfmac_f32_16x16x64_bf16 a[160:163], v[80:83], v[104:111], v154\n\t" \
  "buffer_load_dwordx4 v144, s[88:91], s69 offen offset:2048 sc1 lds\n\t" \
  "buffer_load_dwordx4 v144, s[88:91], s69 offen offset:3072 sc1 lds\n\t" \
  "v_cvt_pk_bf16_f32 v118, v60, v61\n\t" \
  "v_exp_f32 v63, v63\n\t" \
  "s_waitcnt vmcnt(4)\n\t" \
  "v_readfirstlane_b32 s71, v136\n\t" \
  "s_mov_b32 s63, 0x40000\n\t" \
  "LPOLL%=:\n\t" \
  "s_cmp_ge_u32 s71, 32\n\t" \
  "s_cbranch_scc1 LPOK%=\n\t" \
  "s_sleep 2\n\t" \
  "global_load_dword v136, v137, s[78:79] sc1\n\t" \
  "s_waitcnt vmcnt(0)\n\t" \
  "v_readfirstlane_b32 s71, v136\n\t" \
  "s_sub_u32 s63, s63, 1\n\t" \
  "s_cbranch_scc0 LPOLL%=\n\t" \
  "LPOK%=:\n\t" \
  "v_exp_f32 v32, v32\n\t" \
  "v_cvt_pk_bf16_f32 v119, v62, v63\n\t" \
  "v_exp_f32 v33, v33\n\t" \
  "v_mfma_f32_32x32x16_bf16 v[48:63], v[88:91], v[72:75], v[16:31]\n\t" \
  "s_mov_b32 s64, 0\n\t" \
  "v_xor_b32 v137, 96, v146\n\t" \
  "ds_read_b128 v[88:91], v145 offset:4096\n\t" \
  "v_exp_f32 v34, v34\n\t" \
  "v_cvt_pk_bf16_f32 v104, v32, v33\n\t" \
  "v_exp_f32 v35, v35\n\t" \
  "v_exp_f32 v36, v36\n\t" \
  "v_mfma_f32_32x32x16_bf16 v[48:63], v[92:95], v[76:79], v[48:63]\n\t" \
  "ds_read_b128 v[92:95], v145 offset:5120\n\t" \
  "buffer_load_dwordx4 a[176:179], v148, s[80:83], s64 offen offset:0 sc1\n\t" \
  "v_cvt_pk_bf16_f32 v105, v34, v35\n\t" \
  "v_exp_f32 v37, v37\n\t" \
  "v_exp_f32 v38, v38\n\t" \
  "v_cvt_pk_bf16_f32 v106, v36, v37\n\t" \
  "v_exp_f32 v39, v39\n\t" \
  "v_mfma_f32_32x32x16_bf16 a[144:159], v[96:99], v[112:115], a[144:159]\n\t" \
  "ds_read_b128 v[96:99], v145 offset:2048\n\t" \
  "v_exp_f32 v40, v40\n\t" \
  "v_cvt_pk_bf16_f32 v107, v38, v39\n\t" \
  "v_exp_f32 v41, v41\n\t" \
  "v_exp_f32 v42, v42\n\t" \
  "v_cvt_pk_bf16_f32 v108, v40, v41\n\t" \
  "v_exp_f32 v43, v43\n\t" \
  "v_mfma_f32_32x32x16_bf16 a[144:159], v[100:103], v[116:119], a[144:159]\n\t" \
  "ds_read_b128 v[100:103], v145 offset:3072\n\t" \
  "v_exp_f32 v44, v44\n\t" \
  "v_cvt_pk_bf16_f32 v109, v42, v43\n\t" \
  "v_exp_f32 v45, v45\n\t" \
  "v_exp_f32 v46, v46\n\t" \
  "v_smfmac_f32_16x16x64_bf16 a[160:163], v[84:87], v[112:119], v154\n\t" \
  "s_add_u32 m0, s72, 8192\n\t" \
  "s_mov_b32 s68, 131072\n\t" \
  "buffer_load_dwordx4 a[180:183], v148, s[80:83], s64 offen offset:2048 sc1\n\t" \
  "v_cvt_pk_bf16_f32 v110, v44, v45\n\t" \
  "v_exp_f32 v47, v47\n\t" \
  "v_exp_f32 v48, v48\n\t" \
  "v_cvt_pk_bf16_f32 v111, v46, v47\n\t" \
  "v_exp_f32 v49, v49\n\t" \
  "s_waitcnt lgkmcnt(1)\n\t" \
  "v_mfma_f32_32x32x16_bf16 v[32:47], v[88:91], v[64:67], v[0:15]\n\t" \
  "ds_read_b64_tr_b16 v[224:225], v146 offset:0\n\t" \
  "ds_read_b64_tr_b16 v[226:227], v146 offset:512\n\t" \
  "v_exp_f32 v50, v50\n\t" \
  "v_cvt_pk_bf16_f32 v112, v48, v49\n\t" \
  "v_exp_f32 v51, v51\n\t" \
  "v_exp_f32 v52, v52\n\t" \
  "v_mfma_f32_32x32x16_bf16 v[32:47], v[92:95], v[68:71], v[32:47]\n\t" \
  "ds_read_b64_tr_b16 v[232:233], v147 offset:0\n\t" \
  "buffer_load_dwordx4 a[184:187], v148, s[80:83], s64 offen offset:256 sc1\n\t" \
  "v_cvt_pk_bf16_f32 v113, v50, v51\n\t" \
  "v_exp_f32 v53, v53\n\t" \
  "v_exp_f32 v54, v54\n\t" \
  "v_cvt_pk_bf16_f32 v114, v52, v53\n\t" \
  "v_exp_f32 v55, v55\n\t" \
  "v_mfma_f32_32x32x16_bf16 a[128:143], v[96:99], v[104:107], a[128:143]\n\t" \
  "ds_read_b64_tr_b16 v[234:235], v147 offset:512\n\t" \
  "v_exp_f32 v56, v56\n\t" \
  "v_cvt_pk_bf16_f32 v115, v54, v55\n\t" \
  "v_exp_f32 v57, v57\n\t" \
  "v_exp_f32 v58, v58\n\t" \
  "v_cvt_pk_bf16_f32 v116, v56, v57\n\t" \
  "v_exp_f32 v59, v59\n\t" \
  "s_waitcnt lgkmcnt(4)\n\t" \
  "v_mfma_f32_32x32x16_bf16 a[128:143], v[100:103], v[108:111], a[128:143]\n\t" \
  "buffer_load_dwordx4 v141, s[84:87], s68 offen offset:1024 lds\n\t" \
  "buffer_load_dwordx4 v140, s[84:87], s68 offen offset:0 lds\n\t" \
  "v_exp_f32 v60, v60\n\t" \
  "v_cvt_pk_bf16_f32 v117, v58, v59\n\t" \
  "v_exp_f32 v61, v61\n\t" \
  "v_exp_f32 v62, v62\n\t" \
  "v_smfmac_f32_16x16x64_bf16 a[160:163], v[80:83], v[104:111], v154\n\t" \
  "buffer_load_dwordx4 v142, s[84:87], s68 offen offset:2048 lds\n\t" \
  "buffer_load_dwordx4 v143, s[84:87], s68 offen offset:3072 lds\n\t" \
  "buffer_load_dwordx4 a[188:191], v148, s[80:83], s64 offen offset:2304 sc1\n\t" \
  "v_cvt_pk_bf16_f32 v118, v60, v61\n\t" \
  "v_exp_f32 v63, v63\n\t" \
  "v_exp_f32 v32, v32\n\t" \
  "v_cvt_pk_bf16_f32 v119, v62, v63\n\t" \
  "v_exp_f32 v33, v33\n\t" \
  "v_mfma_f32_32x32x16_bf16 v[48:63], v[88:91], v[72:75], v[16:31]\n\t" \
  "ds_read_b128 v[88:91], v145 offset:8192\n\t" \
  "v_exp_f32 v34, v34\n\t" \
  "v_cvt_pk_bf16_f32 v104, v32, v33\n\t" \
  "v_exp_f32 v35, v35\n\t" \
  "v_exp_f32 v36, v36\n\t" \
  "v_mfma_f32_32x32x16_bf16 v[48:63], v[92:95], v[76:79], v[48:63]\n\t" \
  "ds_read_b128 v[92:95], v145 offset:9216\n\t" \
  "buffer_load_dwordx4 a[192:195], v149, s[80:83], s64 offen offset:0 sc1\n\t" \
  "v_cvt_pk_bf16_f32 v105, v34, v35\n\t" \
  "v_exp_f32 v37, v37\n\t" \
  "v_exp_f32 v38, v38\n\t" \
  "v_cvt_pk_bf16_f32 v106, v36, v37\n\t" \
  "v_exp_f32 v39, v39\n\t" \
  "v_mfma_f32_32x32x16_bf16 a[144:159], v[96:99], v[112:115], a[144:159]\n\t" \
  "ds_read_b128 v[96:99], v145 offset:6144\n\t" \
  "v_exp_f32 v40, v40\n\t" \
  "v_cvt_pk_bf16_f32 v107, v38, v39\n\t" \
  "v_exp_f32 v41, v41\n\t" \
  "v_exp_f32 v42, v42\n\t" \
  "v_cvt_pk_bf16_f32 v108, v40, v41\n\t" \
  "v_exp_f32 v43, v43\n\t" \
  "v_mfma_f32_32x32x16_bf16 a[144:159], v[100:103], v[116:119], a[144:159]\n\t" \
  "ds_read_b128 v[100:103], v145 offset:7168\n\t" \
  "v_exp_f32 v44, v44\n\t" \
  "v_cvt_pk_bf16_f32 v109, v42, v43\n\t" \
  "v_exp_f32 v45, v45\n\t" \
  "v_exp_f32 v46, v46\n\t" \
  "v_smfmac_f32_16x16x64_bf16 a[160:163], v[84:87], v[112:119], v154\n\t" \
  "s_add_u32 m0, s72, 12288\n\t" \
  "s_mov_b32 s68, 196608\n\t" \
  "buffer_load_dwordx4 a[196:199], v149, s[80:83], s64 offen offset:2048 sc1\n\t" \
  "v_cvt_pk_bf16_f32 v110, v44, v45\n\t" \
  "v_exp_f32 v47, v47\n\t" \
  "v_exp_f32 v48, v48\n\t" \
  "v_cvt_pk_bf16_f32 v111, v46, v47\n\t" \
  "v_exp_f32 v49, v49\n\t" \
  "s_waitcnt lgkmcnt(1)\n\t" \
  "v_mfma_f32_32x32x16_bf16 v[32:47], v[88:91], v[64:67], v[0:15]\n\t" \
  "ds_read_b64_tr_b16 v[240:241], v155 offset:0\n\t" \
  "ds_read_b64_tr_b16 v[242:243], v155 offset:512\n\t" \
  "v_exp_f32 v50, v50\n\t" \
  "v_cvt_pk_bf16_f32 v112, v48, v49\n\t" \
  "v_exp_f32 v51, v51\n\t" \
  "v_exp_f32 v52, v52\n\t" \
  "v_mfma_f32_32x32x16_bf16 v[32:47], v[92:95], v[68:71], v[32:47]\n\t" \
  "ds_read_b64_tr_b16 v[248:249], v137 offset:0\n\t" \
  "buffer_load_dwordx4 a[200:203], v149, s[80:83], s64 offen offset:256 sc1\n\t" \
  "v_cvt_pk_bf16_f32 v113, v50, v51\n\t" \
  "v_exp_f32 v53, v53\n\t" \
  "v_exp_f32 v54, v54\n\t" \
  "v_cvt_pk_bf16_f32 v114, v52, v53\n\t" \
  "v_exp_f32 v55, v55\n\t" \
  "v_mfma_f32_32x32x16_bf16 a[128:143], v[96:99], v[104:107], a[128:143]\n\t" \
  "ds_read_b64_tr_b16 v[250:251], v137 offset:512\n\t" \
  "v_exp_f32 v56, v56\n\t" \
  "v_cvt_pk_bf16_f32 v115, v54, v55\n\t" \
  "v_exp_f32 v57, v57\n\t" \
  "v_exp_f32 v58, v58\n\t" \
  "v_cvt_pk_bf16_f32 v116, v56, v57\n\t" \
  "v_exp_f32 v59, v59\n\t" \
  "s_waitcnt lgkmcnt(4)\n\t" \
  "v_mfma_f32_32x32x16_bf16 a[128:143], v[100:103], v[108:111], a[128:143]\n\t" \
  "buffer_load_dwordx4 v141, s[84:87], s68 offen offset:1024 lds\n\t" \
  "buffer_load_dwordx4 v140, s[84:87], s68 offen offset:0 lds\n\t" \
  "v_exp_f32 v60, v60\n\t" \
  "v_cvt_pk_bf16_f32 v117, v58, v59\n\t" \
  "v_exp_f32 v61, v61\n\t" \
  "v_exp_f32 v62, v62\n\t" \
  "v_smfmac_f32_16x16x64_bf16 a[160:163], v[80:83], v[104:111], v154\n\t" \
  "buffer_load_dwordx4 v142, s[84:87], s68 offen offset:2048 lds\n\t" \
  "buffer_load_dwordx4 v143, s[84:87], s68 offen offset:3072 lds\n\t" \
  "buffer_load_dwordx4 a[204:207], v149, s[80:83], s64 offen offset:2304 sc1\n\t" \
  "v_cvt_pk_bf16_f32 v118, v60, v61\n\t" \
  "v_exp_f32 v63, v63\n\t" \
  "v_exp_f32 v32, v32\n\t" \
  "v_cvt_pk_bf16_f32 v119, v62, v63\n\t" \
  "v_exp_f32 v33, v33\n\t" \
  "v_mfma_f32_32x32x16_bf16 v[48:63], v[88:91], v[72:75], v[16:31]\n\t" \
  "ds_read_b128 v[88:91], v145 offset:12288\n\t" \
  "v_exp_f32 v34, v34\n\t" \
  "v_cvt_pk_bf16_f32 v104, v32, v33\n\t" \
  "v_exp_f32 v35, v35\n\t" \
  "v_exp_f32 v36, v36\n\t" \
  "v_mfma_f32_32x32x16_bf16 v[48:63], v[92:95], v[76:79], v[48:63]\n\t" \
  "ds_read_b128 v[92:95], v145 offset:13312\n\t" \
  "buffer_load_dwordx4 a[208:211], v150, s[80:83], s64 offen offset:0 sc1\n\t" \
  "v_cvt_pk_bf16_f32 v105, v34, v35\n\t" \
  "v_exp_f32 v37, v37\n\t" \
  "v_exp_f32 v38, v38\n\t" \
  "v_cvt_pk_bf16_f32 v106, v36, v37\n\t" \
  "v_exp_f32 v39, v39\n\t" \
  "v_mfma_f32_32x32x16_bf16 a[144:159], v[96:99], v[112:115], a[144:159]\n\t" \
  "ds_read_b128 v[96:99], v145 offset:10240\n\t" \
  "v_exp_f32 v40, v40\n\t" \
  "v_cvt_pk_bf16_f32 v107, v38, v39\n\t" \
  "v_exp_f32 v41, v41\n\t" \
  "v_exp_f32 v42, v42\n\t" \
  "v_cvt_pk_bf16_f32 v108, v40, v41\n\t" \
  "v_exp_f32 v43, v43\n\t" \
  "v_mfma_f32_32x32x16_bf16 a[144:159], v[100:103], v[116:119], a[144:159]\n\t" \
  "ds_read_b128 v[100:103], v145 offset:11264\n\t" \
  "v_exp_f32 v44, v44\n\t" \
  "v_cvt_pk_bf16_f32 v109, v42, v43\n\t" \
  "v_exp_f32 v45, v45\n\t" \
  "v_exp_f32 v46, v46\n\t" \
  "v_smfmac_f32_16x16x64_bf16 a[160:163], v[84:87], v[112:119], v154\n\t" \
  "buffer_load_dwordx4 a[212:215], v150, s[80:83], s64 offen offset:2048 sc1\n\t" \
  "v_cvt_pk_bf16_f32 v110, v44, v45\n\t" \
  "v_exp_f32 v47, v47\n\t" \
  "v_exp_f32 v48, v48\n\t" \
  "v_cvt_pk_bf16_f32 v111, v46, v47\n\t" \
  "v_exp_f32 v49, v49\n\t" \
  "s_waitcnt lgkmcnt(1)\n\t" \
  "v_mfma_f32_32x32x16_bf16 v[32:47], v[88:91], v[64:67], v[0:15]\n\t" \
  "ds_read_b64_tr_b16 v[228:229], v146 offset:4096\n\t" \
  "ds_read_b64_tr_b16 v[230:231], v146 offset:4608\n\t" \
  "v_exp_f32 v50, v50\n\t" \
  "v_cvt_pk_bf16_f32 v112, v48, v49\n\t" \
  "v_exp_f32 v51, v51\n\t" \
  "v_exp_f32 v52, v52\n\t" \
  "v_mfma_f32_32x32x16_bf16 v[32:47], v[92:95], v[68:71], v[32:47]\n\t" \
  "ds_read_b64_tr_b16 v[236:237], v147 offset:4096\n\t" \
  "buffer_load_dwordx4 a[216:219], v150, s[80:83], s64 offen offset:256 sc1\n\t" \
  "v_cvt_pk_bf16_f32 v113, v50, v51\n\t" \
  "v_exp_f32 v53, v53\n\t" \
  "v_exp_f32 v54, v54\n\t" \
  "v_cvt_pk_bf16_f32 v114, v52, v53\n\t" \
  "v_exp_f32 v55, v55\n\t" \
  "v_mfma_f32_32x32x16_bf16 a[128:143], v[96:99], v[104:107], a[128:143]\n\t" \
  "ds_read_b64_tr_b16 v[238:239], v147 offset:4608\n\t" \
  "v_exp_f32 v56, v56\n\t" \
  "v_cvt_pk_bf16_f32 v115, v54, v55\n\t" \
  "v_exp_f32 v57, v57\n\t" \
  "v_exp_f32 v58, v58\n\t" \
  "v_cvt_pk_bf16_f32 v116, v56, v57\n\t" \
  "v_exp_f32 v59, v59\n\t" \
  "s_waitcnt lgkmcnt(4)\n\t" \
  "v_mfma_f32_32x32x16_bf16 a[128:143], v[100:103], v[108:111], a[128:143]\n\t" \
  "v_exp_f32 v60, v60\n\t" \
  "v_cvt_pk_bf16_f32 v117, v58, v59\n\t" \
  "v_exp_f32 v61, v61\n\t" \
  "v_exp_f32 v62, v62\n\t" \
  "v_smfmac_f32_16x16x64_bf16 a[160:163], v[80:83], v[104:111], v154\n\t" \
  "buffer_load_dwordx4 a[220:223], v150, s[80:83], s64 offen offset:2304 sc1\n\t" \
  "v_cvt_pk_bf16_f32 v118, v60, v61\n\t" \
  "v_exp_f32 v63, v63\n\t" \
  "s_waitcnt vmcnt(20)\n\t" \
  "s_barrier\n\t" \
  "v_exp_f32 v32, v32\n\t" \
  "v_cvt_pk_bf16_f32 v119, v62, v63\n\t" \
  "v_exp_f32 v33, v33\n\t" \
  "v_mfma_f32_32x32x16_bf16 v[48:63], v[88:91], v[72:75], v[16:31]\n\t" \
  "ds_read_b128 v[88:91], v145 offset:16384\n\t" \
  "v_exp_f32 v34, v34\n\t" \
  "v_cvt_pk_bf16_f32 v104, v32, v33\n\t" \
  "v_exp_f32 v35, v35\n\t" \
  "v_exp_f32 v36, v36\n\t" \
  "v_mfma_f32_32x32x16_bf16 v[48:63], v[92:95], v[76:79], v[48:63]\n\t" \
  "ds_read_b128 v[92:95], v145 offset:17408\n\t" \
  "buffer_load_dwordx4 a[224:227], v151, s[80:83], s64 offen offset:0 sc1\n\t" \
  "v_cvt_pk_bf16_f32 v105, v34, v35\n\t" \
  "v_exp_f32 v37, v37\n\t" \
  "v_exp_f32 v38, v38\n\t" \
  "v_cvt_pk_bf16_f32 v106, v36, v37\n\t" \
  "v_exp_f32 v39, v39\n\t" \
  "v_mfma_f32_32x32x16_bf16 a[144:159], v[96:99], v[112:115], a[144:159]\n\t" \
  "ds_read_b128 v[96:99], v145 offset:14336\n\t" \
  "v_exp_f32 v40, v40\n\t" \
  "v_cvt_pk_bf16_f32 v107, v38, v39\n\t" \
  "v_exp_f32 v41, v41\n\t" \
  "v_exp_f32 v42, v42\n\t" \
  "v_cvt_pk_bf16_f32 v108, v40, v41\n\t" \
  "v_exp_f32 v43, v43\n\t" \
  "v_mfma_f32_32x32x16_bf16 a[144:159], v[100:103], v[116:119], a[144:159]\n\t" \
  "ds_read_b128 v[100:103], v145 offset:15360\n\t" \
  "v_exp_f32 v44, v44\n\t" \
  "v_cvt_pk_bf16_f32 v109, v42, v43\n\t" \
  "v_exp_f32 v45, v45\n\t" \
  "v_exp_f32 v46, v46\n\t" \
  "v_smfmac_f32_16x16x64_bf16 a[160:163], v[84:87], v[112:119], v154\n\t" \
  "s_add_u32 m0, s73, 32768\n\t" \
  "s_mov_b32 s69, 81920\n\t" \
  "buffer_load_dwordx4 a[228:231], v151, s[80:83], s64 offen offset:2048 sc1\n\t" \
  "v_cvt_pk_bf16_f32 v110, v44, v45\n\t" \
  "v_exp_f32 v47, v47\n\t" \
  "v_exp_f32 v48, v48\n\t" \
  "v_cvt_pk_bf16_f32 v111, v46, v47\n\t" \
  "v_exp_f32 v49, v49\n\t" \
  "s_waitcnt lgkmcnt(1)\n\t" \
  "v_mfma_f32_32x32x16_bf16 v[32:47], v[88:91], v[64:67], v[0:15]\n\t" \
  "ds_read_b64_tr_b16 v[244:245], v155 offset:4096\n\t" \
  "ds_read_b64_tr_b16 v[246:247], v155 offset:4608\n\t" \
  "v_exp_f32 v50, v50\n\t" \
  "v_cvt_pk_bf16_f32 v112, v48, v49\n\t" \
  "v_exp_f32 v51, v51\n\t" \
  "v_exp_f32 v52, v52\n\t" \
  "v_mfma_f32_32x32x16_bf16 v[32:47], v[92:95], v[68:71], v[32:47]\n\t" \
  "ds_read_b64_tr_b16 v[252:253], v137 offset:4096\n\t" \
  "buffer_load_dwordx4 a[232:235], v151, s[80:83], s64 offen offset:256 sc1\n\t" \
  "v_cvt_pk_bf16_f32 v113, v50, v51\n\t" \
  "v_exp_f32 v53, v53\n\t" \
  "v_exp_f32 v54, v54\n\t" \
  "v_cvt_pk_bf16_f32 v114, v52, v53\n\t" \
  "v_exp_f32 v55, v55\n\t" \
  "v_mfma_f32_32x32x16_bf16 a[128:143], v[96:99], v[104:107], a[128:143]\n\t" \
  "ds_read_b64_tr_b16 v[254:255], v137 offset:4608\n\t" \
  "v_exp_f32 v56, v56\n\t" \
  "v_cvt_pk_bf16_f32 v115, v54, v55\n\t" \
  "v_exp_f32 v57, v57\n\t" \
  "v_exp_f32 v58, v58\n\t" \
  "v_cvt_pk_bf16_f32 v116, v56, v57\n\t" \
  "v_exp_f32 v59, v59\n\t" \
  "s_waitcnt lgkmcnt(4)\n\t" \
  "v_mfma_f32_32x32x16_bf16 a[128:143], v[100:103], v[108:111], a[128:143]\n\t" \
  "buffer_load_dwordx4 v144, s[88:91], s69 offen offset:1024 sc1 lds\n\t" \
  "buffer_load_dwordx4 v144, s[88:91], s69 offen offset:0 sc1 lds\n\t" \
  "v_exp_f32 v60, v60\n\t" \
  "v_cvt_pk_bf16_f32 v117, v58, v59\n\t" \
  "v_exp_f32 v61, v61\n\t" \
  "v_exp_f32 v62, v62\n\t" \
  "v_smfmac_f32_16x16x64_bf16 a[160:163], v[80:83], v[104:111], v154\n\t" \
  "buffer_load_dwordx4 v144, s[88:91], s69 offen offset:2048 sc1 lds\n\t" \
  "buffer_load_dwordx4 v144, s[88:91], s69 offen offset:3072 sc1 lds\n\t" \
  "buffer_load_dwordx4 a[236:239], v151, s[80:83], s64 offen offset:2304 sc1\n\t" \
  "v_cvt_pk_bf16_f32 v118, v60, v61\n\t" \
  "v_exp_f32 v63, v63\n\t" \
  "s_waitcnt vmcnt(26) lgkmcnt(0)\n\t" \
  "v_mfma_f32_16x16x32_bf16 a[0:3], v[224:227], a[176:179], 0\n\t" \
  "v_exp_f32 v32, v32\n\t" \
  "v_mfma_f32_16x16x32_bf16 a[0:3], v[228:231], a[180:183], a[0:3]\n\t" \
  "v_cvt_pk_bf16_f32 v119, v62, v63\n\t" \
  "v_exp_f32 v33, v33\n\t" \
  "v_mfma_f32_16x16x32_bf16 a[8:11], v[232:235], a[176:179], 0\n\t" \
  "v_exp_f32 v34, v34\n\t" \
  "v_mfma_f32_16x16x32_bf16 a[8:11], v[236:239], a[180:183], a[8:11]\n\t" \
  "v_cvt_pk_bf16_f32 v104, v32, v33\n\t" \
  "v_exp_f32 v35, v35\n\t" \
  "v_mfma_f32_16x16x32_bf16 a[16:19], v[240:243], a[176:179], 0\n\t" \
  "s_add_u32 m0, s72, 0\n\t" \
  "v_cvt_pk_bf16_f32 v105, v34, v35\n\t" \
  "v_mfma_f32_16x16x32_bf16 a[16:19], v[244:247], a[180:183], a[16:19]\n\t" \
  "v_exp_f32 v36, v36\n\t" \
  "v_exp_f32 v37, v37\n\t" \
  "v_mfma_f32_16x16x32_bf16 a[24:27], v[248:251], a[176:179], 0\n\t" \
  "s_mov_b32 s68, 262144\n\t" \
  "v_cvt_pk_bf16_f32 v106, v36, v37\n\t" \
  "v_mfma_f32_16x16x32_bf16 a[24:27], v[252:255], a[180:183], a[24:27]\n\t" \
  "v_exp_f32 v38, v38\n\t" \
  "v_exp_f32 v39, v39\n\t" \
  "v_mfma_f32_32x32x16_bf16 v[48:63], v[88:91], v[72:75], v[16:31]\n\t" \
  "ds_read_b128 v[88:91], v145 offset:20480\n\t" \
  "v_exp_f32 v40, v40\n\t" \
  "v_cvt_pk_bf16_f32 v107, v38, v39\n\t" \
  "v_exp_f32 v41, v41\n\t" \
  "v_mfma_f32_32x32x16_bf16 v[48:63], v[92:95], v[76:79], v[48:63]\n\t" \
  "ds_read_b128 v[92:95], v145 offset:21504\n\t" \
  "v_exp_f32 v42, v42\n\t" \
  "v_cvt_pk_bf16_f32 v108, v40, v41\n\t" \
  "v_exp_f32 v43, v43\n\t" \
  "v_mfma_f32_32x32x16_bf16 a[144:159], v[96:99], v[112:115], a[144:159]\n\t" \
  "ds_read_b128 v[96:99], v145 offset:18432\n\t" \
  "v_exp_f32 v44, v44\n\t" \
  "v_cvt_pk_bf16_f32 v109, v42, v43\n\t" \
  "v_exp_f32 v45, v45\n\t" \
  "v_mfma_f32_32x32x16_bf16 a[144:159], v[100:103], v[116:119], a[144:159]\n\t" \
  "ds_read_b128 v[100:103], v145 offset:19456\n\t" \
  "v_exp_f32 v46, v46\n\t" \
  "v_cvt_pk_bf16_f32 v110, v44, v45\n\t" \
  "v_exp_f32 v47, v47\n\t" \
  "v_smfmac_f32_16x16x64_bf16 a[160:163], v[84:87], v[112:119], v154\n\t" \
  "s_mov_b32 s64, 4096\n\t" \
  "s_waitcnt vmcnt(20)\n\t" \
  "v_mfma_f32_16x16x32_bf16 a[4:7], v[224:227], a[184:187], 0\n\t" \
  "buffer_load_dwordx4 a[176:179], v148, s[80:83], s64 offen offset:0 sc1\n\t" \
  "v_cvt_pk_bf16_f32 v111, v46, v47\n\t" \
  "v_mfma_f32_16x16x32_bf16 a[4:7], v[228:231], a[188:191], a[4:7]\n\t" \
  "v_exp_f32 v48, v48\n\t" \
  "v_exp_f32 v49, v49\n\t" \
  "v_mfma_f32_16x16x32_bf16 a[12:15], v[232:235], a[184:187], 0\n\t" \
  "buffer_load_dwordx4 a[180:183], v148, s[80:83], s64 offen offset:2048 sc1\n\t" \
  "v_cvt_pk_bf16_f32 v112, v48, v49\n\t" \
  "v_mfma_f32_16x16x32_bf16 a[12:15], v[236:239], a[188:191], a[12:15]\n\t" \
  "v_exp_f32 v50, v50\n\t" \
  "v_exp_f32 v51, v51\n\t" \
  "v_mfma_f32_16x16x32_bf16 a[20:23], v[240:243], a[184:187], 0\n\t" \
  "buffer_load_dwordx4 v140, s[84:87], s68 offen offset:0 lds\n\t" \
  "v_cvt_pk_bf16_f32 v113, v50, v51\n\t" \
  "v_mfma_f32_16x16x32_bf16 a[20:23], v[244:247], a[188:191], a[20:23]\n\t" \
  "v_exp_f32 v52, v52\n\t" \
  "v_exp_f32 v53, v53\n\t" \
  "v_mfma_f32_16x16x32_bf16 a[28:31], v[248:251], a[184:187], 0\n\t" \
  "buffer_load_dwordx4 v141, s[84:87], s68 offen offset:1024 lds\n\t" \
  "v_cvt_pk_bf16_f32 v114, v52, v53\n\t" \
  "v_mfma_f32_16x16x32_bf16 a[28:31], v[252:255], a[188:191], a[28:31]\n\t" \
  "v_exp_f32 v54, v54\n\t" \
  "v_exp_f32 v55, v55\n\t" \
  "s_waitcnt lgkmcnt(1)\n\t" \
  "v_mfma_f32_32x32x16_bf16 v[32:47], v[88:91], v[64:67], v[0:15]\n\t" \
  "ds_read_b64_tr_b16 v[120:121], v146 offset:8192\n\t" \
  "v_exp_f32 v56, v56\n\t" \
  "v_cvt_pk_bf16_f32 v115, v54, v55\n\t" \
  "v_exp_f32 v57, v57\n\t" \
  "v_mfma_f32_32x32x16_bf16 v[32:47], v[92:95], v[68:71], v[32:47]\n\t" \
  "ds_read_b64_tr_b16 v[122:123], v146 offset:8704\n\t" \
  "v_exp_f32 v58, v58\n\t" \
  "v_cvt_pk_bf16_f32 v116, v56, v57\n\t" \
  "v_exp_f32 v59, v59\n\t" \
  "v_mfma_f32_32x32x16_bf16 a[128:143], v[96:99], v[104:107], a[128:143]\n\t" \
  "ds_read_b64_tr_b16 v[128:129], v147 offset:8192\n\t" \
  "v_exp_f32 v60, v60\n\t" \
  "v_cvt_pk_bf16_f32 v117, v58, v59\n\t" \
  "v_exp_f32 v61, v61\n\t" \
  "s_waitcnt lgkmcnt(3)\n\t" \
  "v_mfma_f32_32x32x16_bf16 a[128:143], v[100:103], v[108:111], a[128:143]\n\t" \
  "ds_read_b64_tr_b16 v[130:131], v147 offset:8704\n\t" \
  "v_exp_f32 v62, v62\n\t" \
  "v_cvt_pk_bf16_f32 v118, v60, v61\n\t" \
  "v_exp_f32 v63, v63\n\t" \
  "v_smfmac_f32_16x16x64_bf16 a[160:163], v[80:83], v[104:111], v154\n\t" \
  "s_waitcnt vmcnt(22)\n\t" \
  "v_mfma_f32_16x16x32_bf16 a[32:35], v[224:227], a[192:195], 0\n\t" \
  "buffer_load_dwordx4 a[184:187], v148, s[80:83], s64 offen offset:256 sc1\n\t" \
  "v_cvt_pk_bf16_f32 v119, v62, v63\n\t" \
  "v_mfma_f32_16x16x32_bf16 a[32:35], v[228:231], a[196:199], a[32:35]\n\t" \
  "v_exp_f32 v32, v32\n\t" \
  "v_exp_f32 v33, v33\n\t" \
  "v_mfma_f32_16x16x32_bf16 a[40:43], v[232:235], a[192:195], 0\n\t" \
  "buffer_load_dwordx4 a[188:191], v148, s[80:83], s64 offen offset:2304 sc1\n\t" \
  "v_cvt_pk_bf16_f32 v104, v32, v33\n\t" \
  "v_mfma_f32_16x16x32_bf16 a[40:43], v[236:239], a[196:199], a[40:43]\n\t" \
  "v_exp_f32 v34, v34\n\t" \
  "v_exp_f32 v35, v35\n\t" \
  "v_mfma_f32_16x16x32_bf16 a[48:51], v[240:243], a[192:195], 0\n\t" \
  "buffer_load_dwordx4 v142, s[84:87], s68 offen offset:2048 lds\n\t" \
  "v_cvt_pk_bf16_f32 v105, v34, v35\n\t" \
  "v_mfma_f32_16x16x32_bf16 a[48:51], v[244:247], a[196:199], a[48:51]\n\t" \
  "v_exp_f32 v36, v36\n\t" \
  "v_exp_f32 v37, v37\n\t" \
  "v_mfma_f32_16x16x32_bf16 a[56:59], v[248:251], a[192:195], 0\n\t" \
  "buffer_load_dwordx4 v143, s[84:87], s68 offen offset:3072 lds\n\t" \
  "v_cvt_pk_bf16_f32 v106, v36, v37\n\t" \
  "v_mfma_f32_16x16x32_bf16 a[56:59], v[252:255], a[196:199], a[56:59]\n\t" \
  "v_exp_f32 v38, v38\n\t" \
  "v_exp_f32 v39, v39\n\t" \
  "v_mfma_f32_32x32x16_bf16 v[48:63], v[88:91], v[72:75], v[16:31]\n\t" \
  "ds_read_b128 v[88:91], v145 offset:24576\n\t" \
  "v_exp_f32 v40, v40\n\t" \
  "v_cvt_pk_bf16_f32 v107, v38, v39\n\t" \
  "v_exp_f32 v41, v41\n\t" \
  "v_mfma_f32_32x32x16_bf16 v[48:63], v[92:95], v[76:79], v[48:63]\n\t" \
  "ds_read_b128 v[92:95], v145 offset:25600\n\t" \
  "v_exp_f32 v42, v42\n\t" \
  "v_cvt_pk_bf16_f32 v108, v40, v41\n\t" \
  "v_exp_f32 v43, v43\n\t" \
  "v_mfma_f32_32x32x16_bf16 a[144:159], v[96:99], v[112:115], a[144:159]\n\t" \
  "ds_read_b128 v[96:99], v145 offset:22528\n\t" \
  "v_exp_f32 v44, v44\n\t" \
  "v_cvt_pk_bf16_f32 v109, v42, v43\n\t" \
  "v_exp_f32 v45, v45\n\t" \
  "v_mfma_f32_32x32x16_bf16 a[144:159], v[100:103], v[116:119], a[144:159]\n\t" \
  "ds_read_b128 v[100:103], v145 offset:23552\n\t" \
  "v_exp_f32 v46, v46\n\t" \
  "v_cvt_pk_bf16_f32 v110, v44, v45\n\t" \
  "v_exp_f32 v47, v47\n\t" \
  "v_smfmac_f32_16x16x64_bf16 a[160:163], v[84:87], v[112:119], v154\n\t" \
  "s_waitcnt vmcnt(20)\n\t" \
  "v_mfma_f32_16x16x32_bf16 a[36:39], v[224:227], a[200:203], 0\n\t" \
  "buffer_load_dwordx4 a[192:195], v149, s[80:83], s64 offen offset:0 sc1\n\t" \
  "v_cvt_pk_bf16_f32 v111, v46, v47\n\t" \
  "v_mfma_f32_16x16x32_bf16 a[36:39], v[228:231], a[204:207], a[36:39]\n\t" \
  "v_exp_f32 v48, v48\n\t" \
  "v_exp_f32 v49, v49\n\t" \
  "v_mfma_f32_16x16x32_bf16 a[44:47], v[232:235], a[200:203], 0\n\t" \
  "buffer_load_dwordx4 a[196:199], v149, s[80:83], s64 offen offset:2048 sc1\n\t" \
  "v_cvt_pk_bf16_f32 v112, v48, v49\n\t" \
  "v_mfma_f32_16x16x32_bf16 a[44:47], v[236:239], a[204:207], a[44:47]\n\t" \
  "v_exp_f32 v50, v50\n\t" \
  "v_exp_f32 v51, v51\n\t" \
  "v_mfma_f32_16x16x32_bf16 a[52:55], v[240:243], a[200:203], 0\n\t" \
  "v_exp_f32 v52, v52\n\t" \
  "v_mfma_f32_16x16x32_bf16 a[52:55], v[244:247], a[204:207], a[52:55]\n\t" \
  "v_cvt_pk_bf16_f32 v113, v50, v51\n\t" \
  "v_exp_f32 v53, v53\n\t" \
  "v_mfma_f32_16x16x32_bf16 a[60:63], v[248:251], a[200:203], 0\n\t" \
  "v_exp_f32 v54, v54\n\t" \
  "v_mfma_f32_16x16x32_bf16 a[60:63], v[252:255], a[204:207], a[60:63]\n\t" \
  "v_cvt_pk_bf16_f32 v114, v52, v53\n\t" \
  "v_exp_f32 v55, v55\n\t" \
  "s_waitcnt lgkmcnt(1)\n\t" \
  "v_mfma_f32_32x32x16_bf16 v[32:47], v[88:91], v[64:67], v[0:15]\n\t" \
  "ds_read_b64_tr_b16 a[164:165], v155 offset:8192\n\t" \
  "v_exp_f32 v56, v56\n\t" \
  "v_cvt_pk_bf16_f32 v115, v54, v55\n\t" \
  "v_exp_f32 v57, v57\n\t" \
  "v_mfma_f32_32x32x16_bf16 v[32:47], v[92:95], v[68:71], v[32:47]\n\t" \
  "ds_read_b64_tr_b16 a[166:167], v155 offset:8704\n\t" \
  "v_exp_f32 v58, v58\n\t" \
  "v_cvt_pk_bf16_f32 v116, v56, v57\n\t" \
  "v_exp_f32 v59, v59\n\t" \
  "v_mfma_f32_32x32x16_bf16 a[128:143], v[96:99], v[104:107], a[128:143]\n\t" \
  "ds_read_b64_tr_b16 a[172:173], v137 offset:8192\n\t" \
  "v_exp_f32 v60, v60\n\t" \
  "v_cvt_pk_bf16_f32 v117, v58, v59\n\t" \
  "v_exp_f32 v61, v61\n\t" \
  "s_waitcnt lgkmcnt(3)\n\t" \
  "v_mfma_f32_32x32x16_bf16 a[128:143], v[100:103], v[108:111], a[128:143]\n\t" \
  "ds_read_b64_tr_b16 a[174:175], v137 offset:8704\n\t" \
  "v_exp_f32 v62, v62\n\t" \
  "v_cvt_pk_bf16_f32 v118, v60, v61\n\t" \
  "v_exp_f32 v63, v63\n\t" \
  "v_smfmac_f32_16x16x64_bf16 a[160:163], v[80:83], v[104:111], v154\n\t" \
  "s_waitcnt vmcnt(20)\n\t" \
  "v_mfma_f32_16x16x32_bf16 a[64:67], v[224:227], a[208:211], 0\n\t" \
  "buffer_load_dwordx4 a[200:203], v149, s[80:83], s64 offen offset:256 sc1\n\t" \
  "v_cvt_pk_bf16_f32 v119, v62, v63\n\t" \
  "v_mfma_f32_16x16x32_bf16 a[64:67], v[228:231], a[212:215], a[64:67]\n\t" \
  "v_exp_f32 v32, v32\n\t" \
  "v_exp_f32 v33, v33\n\t" \
  "v_mfma_f32_16x16x32_bf16 a[72:75], v[232:235], a[208:211], 0\n\t" \
  "buffer_load_dwordx4 a[204:207], v149, s[80:83], s64 offen offset:2304 sc1\n\t" \
  "v_cvt_pk_bf16_f32 v104, v32, v33\n\t" \
  "v_mfma_f32_16x16x32_bf16 a[72:75], v[236:239], a[212:215], a[72:75]\n\t" \
  "v_exp_f32 v34, v34\n\t" \
  "v_exp_f32 v35, v35\n\t" \
  "v_mfma_f32_16x16x32_bf16 a[80:83], v[240:243], a[208:211], 0\n\t" \
  "s_add_u32 m0, s72, 4096\n\t" \
  "v_cvt_pk_bf16_f32 v105, v34, v35\n\t" \
  "v_mfma_f32_16x16x32_bf16 a[80:83], v[244:247], a[212:215], a[80:83]\n\t" \
  "v_exp_f32 v36, v36\n\t" \
  "v_exp_f32 v37, v37\n\t" \
  "v_mfma_f32_16x16x32_bf16 a[88:91], v[248:251], a[208:211], 0\n\t" \
  "s_mov_b32 s68, 327680\n\t" \
  "v_cvt_pk_bf16_f32 v106, v36, v37\n\t" \
  "v_mfma_f32_16x16x32_bf16 a[88:91], v[252:255], a[212:215], a[88:91]\n\t" \
  "v_exp_f32 v38, v38\n\t" \
  "v_exp_f32 v39, v39\n\t" \
  "v_mfma_f32_32x32x16_bf16 v[48:63], v[88:91], v[72:75], v[16:31]\n\t" \
  "ds_read_b128 v[88:91], v145 offset:28672\n\t" \
  "v_exp_f32 v40, v40\n\t" \
  "v_cvt_pk_bf16_f32 v107, v38, v39\n\t" \
  "v_exp_f32 v41, v41\n\t" \
  "v_mfma_f32_32x32x16_bf16 v[48:63], v[92:95], v[76:79], v[48:63]\n\t" \
  "ds_read_b128 v[92:95], v145 offset:29696\n\t" \
  "v_exp_f32 v42, v42\n\t" \
  "v_cvt_pk_bf16_f32 v108, v40, v41\n\t" \
  "v_exp_f32 v43, v43\n\t" \
  "v_mfma_f32_32x32x16_bf16 a[144:159], v[96:99], v[112:115], a[144:159]\n\t" \
  "ds_read_b128 v[96:99], v145 offset:26624\n\t" \
  "v_exp_f32 v44, v44\n\t" \
  "v_cvt_pk_bf16_f32 v109, v42, v43\n\t" \
  "v_exp_f32 v45, v45\n\t" \
  "v_mfma_f32_32x32x16_bf16 a[144:159], v[100:103], v[116:119], a[144:159]\n\t" \
  "ds_read_b128 v[100:103], v145 offset:27648\n\t" \
  "v_exp_f32 v46, v46\n\t" \
  "v_cvt_pk_bf16_f32 v110, v44, v45\n\t" \
  "v_exp_f32 v47, v47\n\t" \
  "v_smfmac_f32_16x16x64_bf16 a[160:163], v[84:87], v[112:119], v154\n\t" \
  "s_waitcnt vmcnt(20)\n\t" \
  "v_mfma_f32_16x16x32_bf16 a[68:71], v[224:227], a[216:219], 0\n\t" \
  "buffer_load_dwordx4 a[208:211], v150, s[80:83], s64 offen offset:0 sc1\n\t" \
  "v_cvt_pk_bf16_f32 v111, v46, v47\n\t" \
  "v_mfma_f32_16x16x32_bf16 a[68:71], v[228:231], a[220:223], a[68:71]\n\t" \
  "v_exp_f32 v48, v48\n\t" \
  "v_exp_f32 v49, v49\n\t" \
  "v_mfma_f32_16x16x32_bf16 a[76:79], v[232:235], a[216:219], 0\n\t" \
  "buffer_load_dwordx4 a[212:215], v150, s[80:83], s64 offen offset:2048 sc1\n\t" \
  "v_cvt_pk_bf16_f32 v112, v48, v49\n\t" \
  "v_mfma_f32_16x16x32_bf16 a[76:79], v[236:239], a[220:223], a[76:79]\n\t" \
  "v_exp_f32 v50, v50\n\t" \
  "v_exp_f32 v51, v51\n\t" \
  "v_mfma_f32_16x16x32_bf16 a[84:87], v[240:243], a[216:219], 0\n\t" \
  "buffer_load_dwordx4 v140, s[84:87], s68 offen offset:0 lds\n\t" \
  "v_cvt_pk_bf16_f32 v113, v50, v51\n\t" \
  "v_mfma_f32_16x16x32_bf16 a[84:87], v[244:247], a[220:223], a[84:87]\n\t" \
  "v_exp_f32 v52, v52\n\t" \
  "v_exp_f32 v53, v53\n\t" \
  "v_mfma_f32_16x16x32_bf16 a[92:95], v[248:251], a[216:219], 0\n\t" \
  "buffer_load_dwordx4 v141, s[84:87], s68 offen offset:1024 lds\n\t" \
  "v_cvt_pk_bf16_f32 v114, v52, v53\n\t" \
  "v_mfma_f32_16x16x32_bf16 a[92:95], v[252:255], a[220:223], a[92:95]\n\t" \
  "v_exp_f32 v54, v54\n\t" \
  "v_exp_f32 v55, v55\n\t" \
  "s_waitcnt lgkmcnt(1)\n\t" \
  "v_mfma_f32_32x32x16_bf16 v[32:47], v[88:91], v[64:67], v[0:15]\n\t" \
  "ds_read_b64_tr_b16 v[124:125], v146 offset:12288\n\t" \
  "v_exp_f32 v56, v56\n\t" \
  "v_cvt_pk_bf16_f32 v115, v54, v55\n\t" \
  "v_exp_f32 v57, v57\n\t" \
  "v_mfma_f32_32x32x16_bf16 v[32:47], v[92:95], v[68:71], v[32:47]\n\t" \
  "ds_read_b64_tr_b16 v[126:127], v146 offset:12800\n\t" \
  "v_exp_f32 v58, v58\n\t" \
  "v_cvt_pk_bf16_f32 v116, v56, v57\n\t" \
  "v_exp_f32 v59, v59\n\t" \
  "v_mfma_f32_32x32x16_bf16 a[128:143], v[96:99], v[104:107], a[128:143]\n\t" \
  "ds_read_b64_tr_b16 v[132:133], v147 offset:12288\n\t" \
  "v_exp_f32 v60, v60\n\t" \
  "v_cvt_pk_bf16_f32 v117, v58, v59\n\t" \
  "v_exp_f32 v61, v61\n\t" \
  "s_waitcnt lgkmcnt(3)\n\t" \
  "v_mfma_f32_32x32x16_bf16 a[128:143], v[100:103], v[108:111], a[128:143]\n\t" \
  "ds_read_b64_tr_b16 v[134:135], v147 offset:12800\n\t" \
  "v_exp_f32 v62, v62\n\t" \
  "v_cvt_pk_bf16_f32 v118, v60, v61\n\t" \
  "v_exp_f32 v63, v63\n\t" \
  "v_smfmac_f32_16x16x64_bf16 a[160:163], v[80:83], v[104:111], v154\n\t" \
  "s_waitcnt vmcnt(17)\n\t" \
  "s_barrier\n\t" \
  "v_mfma_f32_16x16x32_bf16 a[96:99], v[224:227], a[224:227], 0\n\t" \
  "buffer_load_dwordx4 a[216:219], v150, s[80:83], s64 offen offset:256 sc1\n\t" \
  "v_cvt_pk_bf16_f32 v119, v62, v63\n\t" \
  "v_mfma_f32_16x16x32_bf16 a[96:99], v[228:231], a[228:231], a[96:99]\n\t" \
  "v_exp_f32 v32, v32\n\t" \
  "v_exp_f32 v33, v33\n\t" \
  "v_mfma_f32_16x16x32_bf16 a[104:107], v[232:235], a[224:227], 0\n\t" \
  "buffer_load_dwordx4 a[220:223], v150, s[80:83], s64 offen offset:2304 sc1\n\t" \
  "v_cvt_pk_bf16_f32 v104, v32, v33\n\t" \
  "v_mfma_f32_16x16x32_bf16 a[104:107], v[236:239], a[228:231], a[104:107]\n\t" \
  "v_exp_f32 v34, v34\n\t" \
  "v_exp_f32 v35, v35\n\t" \
  "v_mfma_f32_16x16x32_bf16 a[112:115], v[240:243], a[224:227], 0\n\t" \
  "buffer_load_dwordx4 v142, s[84:87], s68 offen offset:2048 lds\n\t" \
  "v_cvt_pk_bf16_f32 v105, v34, v35\n\t" \
  "v_mfma_f32_16x16x32_bf16 a[112:115], v[244:247], a[228:231], a[112:115]\n\t" \
  "v_exp_f32 v36, v36\n\t" \
  "v_exp_f32 v37, v37\n\t" \
  "v_mfma_f32_16x16x32_bf16 a[120:123], v[248:251], a[224:227], 0\n\t" \
  "buffer_load_dwordx4 v143, s[84:87], s68 offen offset:3072 lds\n\t" \
  "v_cvt_pk_bf16_f32 v106, v36, v37\n\t" \
  "v_mfma_f32_16x16x32_bf16 a[120:123], v[252:255], a[228:231], a[120:123]\n\t" \
  "v_exp_f32 v38, v38\n\t" \
  "v_exp_f32 v39, v39\n\t" \
  "v_mfma_f32_32x32x16_bf16 v[48:63], v[88:91], v[72:75], v[16:31]\n\t" \
  "ds_read_b128 v[88:91], v145 offset:32768\n\t" \
  "v_exp_f32 v40, v40\n\t" \
  "v_cvt_pk_bf16_f32 v107, v38, v39\n\t" \
  "v_exp_f32 v41, v41\n\t" \
  "v_mfma_f32_32x32x16_bf16 v[48:63], v[92:95], v[76:79], v[48:63]\n\t" \
  "ds_read_b128 v[92:95], v145 offset:33792\n\t" \
  "v_exp_f32 v42, v42\n\t" \
  "v_cvt_pk_bf16_f32 v108, v40, v41\n\t" \
  "v_exp_f32 v43, v43\n\t" \
  "v_mfma_f32_32x32x16_bf16 a[144:159], v[96:99], v[112:115], a[144:159]\n\t" \
  "ds_read_b128 v[96:99], v145 offset:30720\n\t" \
  "v_exp_f32 v44, v44\n\t" \
  "v_cvt_pk_bf16_f32 v109, v42, v43\n\t" \
  "v_exp_f32 v45, v45\n\t" \
  "v_mfma_f32_32x32x16_bf16 a[144:159], v[100:103], v[116:119], a[144:159]\n\t" \
  "ds_read_b128 v[100:103], v145 offset:31744\n\t" \
  "v_exp_f32 v46, v46\n\t" \
  "v_cvt_pk_bf16_f32 v110, v44, v45\n\t" \
  "v_exp_f32 v47, v47\n\t" \
  "v_smfmac_f32_16x16x64_bf16 a[160:163], v[84:87], v[112:119], v154\n\t" \
  "s_add_u32 m0, s73, 0\n\t" \
  "s_mov_b32 s69, 98304\n\t" \
  "s_waitcnt vmcnt(20)\n\t" \
  "v_mfma_f32_16x16x32_bf16 a[100:103], v[224:227], a[232:235], 0\n\t" \
  "buffer_load_dwordx4 a[224:227], v151, s[80:83], s64 offen offset:0 sc1\n\t" \
  "v_cvt_pk_bf16_f32 v111, v46, v47\n\t" \
  "v_mfma_f32_16x16x32_bf16 a[100:103], v[228:231], a[236:239], a[100:103]\n\t" \
  "v_exp_f32 v48, v48\n\t" \
  "v_exp_f32 v49, v49\n\t" \
  "v_mfma_f32_16x16x32_bf16 a[108:111], v[232:235], a[232:235], 0\n\t" \
  "buffer_load_dwordx4 a[228:231], v151, s[80:83], s64 offen offset:2048 sc1\n\t" \
  "v_cvt_pk_bf16_f32 v112, v48, v49\n\t" \
  "v_mfma_f32_16x16x32_bf16 a[108:111], v[236:239], a[236:239], a[108:111]\n\t" \
  "v_exp_f32 v50, v50\n\t" \
  "v_exp_f32 v51, v51\n\t" \
  "v_mfma_f32_16x16x32_bf16 a[116:119], v[240:243], a[232:235], 0\n\t" \
  "buffer_load_dwordx4 v144, s[88:91], s69 offen offset:0 sc1 lds\n\t" \
  "v_cvt_pk_bf16_f32 v113, v50, v51\n\t" \
  "v_mfma_f32_16x16x32_bf16 a[116:119], v[244:247], a[236:239], a[116:119]\n\t" \
  "v_exp_f32 v52, v52\n\t" \
  "v_exp_f32 v53, v53\n\t" \
  "v_mfma_f32_16x16x32_bf16 a[124:127], v[248:251], a[232:235], 0\n\t" \
  "buffer_load_dwordx4 v144, s[88:91], s69 offen offset:1024 sc1 lds\n\t" \
  "v_cvt_pk_bf16_f32 v114, v52, v53\n\t" \
  "v_mfma_f32_16x16x32_bf16 a[124:127], v[252:255], a[236:239], a[124:127]\n\t" \
  "v_exp_f32 v54, v54\n\t" \
  "v_exp_f32 v55, v55\n\t" \
  "s_waitcnt lgkmcnt(1)\n\t" \
  "v_mfma_f32_32x32x16_bf16 v[32:47], v[88:91], v[64:67], v[0:15]\n\t" \
  "ds_read_b64_tr_b16 a[168:169], v155 offset:12288\n\t" \
  "v_exp_f32 v56, v56\n\t" \
  "v_cvt_pk_bf16_f32 v115, v54, v55\n\t" \
  "v_exp_f32 v57, v57\n\t" \
  "v_mfma_f32_32x32x16_bf16 v[32:47], v[92:95], v[68:71], v[32:47]\n\t" \
  "ds_read_b64_tr_b16 a[170:171], v155 offset:12800\n\t" \
  "v_exp_f32 v58, v58\n\t" \
  "v_cvt_pk_bf16_f32 v116, v56, v57\n\t" \
  "v_exp_f32 v59, v59\n\t" \
  "v_mfma_f32_32x32x16_bf16 a[128:143], v[96:99], v[104:107], a[128:143]\n\t" \
  "ds_read_b64_tr_b16 v[156:157], v137 offset:12288\n\t" \
  "v_exp_f32 v60, v60\n\t" \
  "v_cvt_pk_bf16_f32 v117, v58, v59\n\t" \
  "v_exp_f32 v61, v61\n\t" \
  "s_waitcnt lgkmcnt(3)\n\t" \
  "v_mfma_f32_32x32x16_bf16 a[128:143], v[100:103], v[108:111], a[128:143]\n\t" \
  "ds_read_b64_tr_b16 v[158:159], v137 offset:12800\n\t" \
  "v_exp_f32 v62, v62\n\t" \
  "v_cvt_pk_bf16_f32 v118, v60, v61\n\t" \
  "v_exp_f32 v63, v63\n\t" \
  "v_smfmac_f32_16x16x64_bf16 a[160:163], v[80:83], v[104:111], v154\n\t" \
  "buffer_load_dwordx4 v144, s[88:91], s69 offen offset:2048 sc1 lds\n\t" \
  "buffer_load_dwordx4 v144, s[88:91], s69 offen offset:3072 sc1 lds\n\t" \
  "s_waitcnt vmcnt(24) lgkmcnt(0)\n\t" \
  "v_mfma_f32_16x16x32_bf16 a[0:3], v[120:123], a[176:179], a[0:3]\n\t" \
  "buffer_load_dwordx4 a[232:235], v151, s[80:83], s64 offen offset:256 sc1\n\t" \
  "v_cvt_pk_bf16_f32 v119, v62, v63\n\t" \
  "v_mfma_f32_16x16x32_bf16 a[0:3], v[124:127], a[180:183], a[0:3]\n\t" \
  "v_exp_f32 v32, v32\n\t" \
  "v_exp_f32 v33, v33\n\t" \
  "v_mfma_f32_16x16x32_bf16 a[8:11], v[128:131], a[176:179], a[8:11]\n\t" \
  "buffer_load_dwordx4 a[236:239], v151, s[80:83], s64 offen offset:2304 sc1\n\t" \
  "v_cvt_pk_bf16_f32 v104, v32, v33\n\t" \
  "v_mfma_f32_16x16x32_bf16 a[8:11], v[132:135], a[180:183], a[8:11]\n\t" \
  "v_exp_f32 v34, v34\n\t" \
  "v_exp_f32 v35, v35\n\t" \
  "v_mfma_f32_16x16x32_bf16 a[16:19], a[164:167], a[176:179], a[16:19]\n\t" \
  "s_add_u32 m0, s72, 8192\n\t" \
  "v_cvt_pk_bf16_f32 v105, v34, v35\n\t" \
  "v_mfma_f32_16x16x32_bf16 a[16:19], a[168:171], a[180:183], a[16:19]\n\t" \
  "v_exp_f32 v36, v36\n\t" \
  "v_exp_f32 v37, v37\n\t" \
  "v_mfma_f32_16x16x32_bf16 a[24:27], a[172:175], a[176:179], a[24:27]\n\t" \
  "s_mov_b32 s68, 393216\n\t" \
  "v_cvt_pk_bf16_f32 v106, v36, v37\n\t" \
  "v_mfma_f32_16x16x32_bf16 a[24:27], v[156:159], a[180:183], a[24:27]\n\t" \
  "v_exp_f32 v38, v38\n\t" \
  "v_exp_f32 v39, v39\n\t" \
  "v_mfma_f32_32x32x16_bf16 v[48:63], v[88:91], v[72:75], v[16:31]\n\t" \
  "ds_read_b128 v[88:91], v145 offset:36864\n\t" \
  "v_exp_f32 v40, v40\n\t" \
  "v_cvt_pk_bf16_f32 v107, v38, v39\n\t" \
  "v_exp_f32 v41, v41\n\t" \
  "v_mfma_f32_32x32x16_bf16 v[48:63], v[92:95], v[76:79], v[48:63]\n\t" \
  "ds_read_b128 v[92:95], v145 offset:37888\n\t" \
  "v_exp_f32 v42, v42\n\t" \
  "v_cvt_pk_bf16_f32 v108, v40, v41\n\t" \
  "v_exp_f32 v43, v43\n\t" \
  "v_mfma_f32_32x32x16_bf16 a[144:159], v[96:99], v[112:115], a[144:159]\n\t" \
  "ds_read_b128 v[96:99], v145 offset:34816\n\t" \
  "v_exp_f32 v44, v44\n\t" \
  "v_cvt_pk_bf16_f32 v109, v42, v43\n\t" \
  "v_exp_f32 v45, v45\n\t" \
  "v_mfma_f32_32x32x16_bf16 a[144:159], v[100:103], v[116:119], a[144:159]\n\t" \
  "ds_read_b128 v[100:103], v145 offset:35840\n\t" \
  "v_exp_f32 v46, v46\n\t" \
  "v_cvt_pk_bf16_f32 v110, v44, v45\n\t" \
  "v_exp_f32 v47, v47\n\t" \
  "v_smfmac_f32_16x16x64_bf16 a[160:163], v[84:87], v[112:119], v154\n\t" \
  "s_mov_b32 s64, 8192\n\t" \
  "s_waitcnt vmcnt(20)\n\t" \
  "v_mfma_f32_16x16x32_bf16 a[4:7], v[120:123], a[184:187], a[4:7]\n\t" \
  "buffer_load_dwordx4 a[176:179], v148, s[80:83], s64 offen offset:0 sc1\n\t" \
  "v_cvt_pk_bf16_f32 v111, v46, v47\n\t" \
  "v_mfma_f32_16x16x32_bf16 a[4:7], v[124:127], a[188:191], a[4:7]\n\t" \
  "v_exp_f32 v48, v48\n\t" \
  "v_exp_f32 v49, v49\n\t" \
  "v_mfma_f32_16x16x32_bf16 a[12:15], v[128:131], a[184:187], a[12:15]\n\t" \
  "buffer_load_dwordx4 a[180:183], v148, s[80:83], s64 offen offset:2048 sc1\n\t" \
  "v_cvt_pk_bf16_f32 v112, v48, v49\n\t" \
  "v_mfma_f32_16x16x32_bf16 a[12:15], v[132:135], a[188:191], a[12:15]\n\t" \
  "v_exp_f32 v50, v50\n\t" \
  "v_exp_f32 v51, v51\n\t" \
  "v_mfma_f32_16x16x32_bf16 a[20:23], a[164:167], a[184:187], a[20:23]\n\t" \
  "buffer_load_dwordx4 v140, s[84:87], s68 offen offset:0 lds\n\t" \
  "v_cvt_pk_bf16_f32 v113, v50, v51\n\t" \
  "v_mfma_f32_16x16x32_bf16 a[20:23], a[168:171], a[188:191], a[20:23]\n\t" \
  "v_exp_f32 v52, v52\n\t" \
  "v_exp_f32 v53, v53\n\t" \
  "v_mfma_f32_16x16x32_bf16 a[28:31], a[172:175], a[184:187], a[28:31]\n\t" \
  "buffer_load_dwordx4 v141, s[84:87], s68 offen offset:1024 lds\n\t" \
  "v_cvt_pk_bf16_f32 v114, v52, v53\n\t" \
  "v_mfma_f32_16x16x32_bf16 a[28:31], v[156:159], a[188:191], a[28:31]\n\t" \
  "v_exp_f32 v54, v54\n\t" \
  "v_exp_f32 v55, v55\n\t" \
  "s_waitcnt lgkmcnt(1)\n\t" \
  "v_mfma_f32_32x32x16_bf16 v[32:47], v[88:91], v[64:67], v[0:15]\n\t" \
  "ds_read_b64_tr_b16 v[224:225], v146 offset:0\n\t" \
  "v_exp_f32 v56, v56\n\t" \
  "v_cvt_pk_bf16_f32 v115, v54, v55\n\t" \
  "v_exp_f32 v57, v57\n\t" \
  "v_mfma_f32_32x32x16_bf16 v[32:47], v[92:95], v[68:71], v[32:47]\n\t" \
  "ds_read_b64_tr_b16 v[226:227], v146 offset:512\n\t" \
  "v_exp_f32 v58, v58\n\t" \
  "v_cvt_pk_bf16_f32 v116, v56, v57\n\t" \
  "v_exp_f32 v59, v59\n\t" \
  "v_mfma_f32_32x32x16_bf16 a[128:143], v[96:99], v[104:107], a[128:143]\n\t" \
  "ds_read_b64_tr_b16 v[232:233], v147 offset:0\n\t" \
  "v_exp_f32 v60, v60\n\t" \
  "v_cvt_pk_bf16_f32 v117, v58, v59\n\t" \
  "v_exp_f32 v61, v61\n\t" \
  "s_waitcnt lgkmcnt(3)\n\t" \
  "v_mfma_f32_32x32x16_bf16 a[128:143], v[100:103], v[108:111], a[128:143]\n\t" \
  "ds_read_b64_tr_b16 v[234:235], v147 offset:512\n\t" \
  "v_exp_f32 v62, v62\n\t" \
  "v_cvt_pk_bf16_f32 v118, v60, v61\n\t" \
  "v_exp_f32 v63, v63\n\t" \
  "v_smfmac_f32_16x16x64_bf16 a[160:163], v[80:83], v[104:111], v154\n\t" \
  "s_waitcnt vmcnt(22)\n\t" \
  "v_mfma_f32_16x16x32_bf16 a[32:35], v[120:123], a[192:195], a[32:35]\n\t" \
  "buffer_load_dwordx4 a[184:187], v148, s[80:83], s64 offen offset:256 sc1\n\t" \
  "v_cvt_pk_bf16_f32 v119, v62, v63\n\t" \
  "v_mfma_f32_16x16x32_bf16 a[32:35], v[124:127], a[196:199], a[32:35]\n\t" \
  "v_exp_f32 v32, v32\n\t" \
  "v_exp_f32 v33, v33\n\t" \
  "v_mfma_f32_16x16x32_bf16 a[40:43], v[128:131], a[192:195], a[40:43]\n\t" \
  "buffer_load_dwordx4 a[188:191], v148, s[80:83], s64 offen offset:2304 sc1\n\t" \
  "v_cvt_pk_bf16_f32 v104, v32, v33\n\t" \
  "v_mfma_f32_16x16x32_bf16 a[40:43], v[132:135], a[196:199], a[40:43]\n\t" \
  "v_exp_f32 v34, v34\n\t" \
  "v_exp_f32 v35, v35\n\t" \
  "v_mfma_f32_16x16x32_bf16 a[48:51], a[164:167], a[192:195], a[48:51]\n\t" \
  "buffer_load_dwordx4 v142, s[84:87], s68 offen offset:2048 lds\n\t" \
  "v_cvt_pk_bf16_f32 v105, v34, v35\n\t" \
  "v_mfma_f32_16x16x32_bf16 a[48:51], a[168:171], a[196:199], a[48:51]\n\t" \
  "v_exp_f32 v36, v36\n\t" \
  "v_exp_f32 v37, v37\n\t" \
  "v_mfma_f32_16x16x32_bf16 a[56:59], a[172:175], a[192:195], a[56:59]\n\t" \
  "buffer_load_dwordx4 v143, s[84:87], s68 offen offset:3072 lds\n\t" \
  "v_cvt_pk_bf16_f32 v106, v36, v37\n\t" \
  "v_mfma_f32_16x16x32_bf16 a[56:59], v[156:159], a[196:199], a[56:59]\n\t" \
  "v_exp_f32 v38, v38\n\t" \
  "v_exp_f32 v39, v39\n\t" \
  "v_mfma_f32_32x32x16_bf16 v[48:63], v[88:91], v[72:75], v[16:31]\n\t" \
  "ds_read_b128 v[88:91], v145 offset:40960\n\t" \
  "v_exp_f32 v40, v40\n\t" \
  "v_cvt_pk_bf16_f32 v107, v38, v39\n\t" \
  "v_exp_f32 v41, v41\n\t" \
  "v_mfma_f32_32x32x16_bf16 v[48:63], v[92:95], v[76:79], v[48:63]\n\t" \
  "ds_read_b128 v[92:95], v145 offset:41984\n\t" \
  "v_exp_f32 v42, v42\n\t" \
  "v_cvt_pk_bf16_f32 v108, v40, v41\n\t" \
  "v_exp_f32 v43, v43\n\t" \
  "v_mfma_f32_32x32x16_bf16 a[144:159], v[96:99], v[112:115], a[144:159]\n\t" \
  "ds_read_b128 v[96:99], v145 offset:38912\n\t" \
  "v_exp_f32 v44, v44\n\t" \
  "v_cvt_pk_bf16_f32 v109, v42, v43\n\t" \
  "v_exp_f32 v45, v45\n\t" \
  "v_mfma_f32_32x32x16_bf16 a[144:159], v[100:103], v[116:119], a[144:159]\n\t" \
  "ds_read_b128 v[100:103], v145 offset:39936\n\t" \
  "v_exp_f32 v46, v46\n\t" \
  "v_cvt_pk_bf16_f32 v110, v44, v45\n\t" \
  "v_exp_f32 v47, v47\n\t" \
  "v_smfmac_f32_16x16x64_bf16 a[160:163], v[84:87], v[112:119], v154\n\t" \
  "s_waitcnt vmcnt(24)\n\t" \
  "v_mfma_f32_16x16x32_bf16 a[36:39], v[120:123], a[200:203], a[36:39]\n\t" \
  "buffer_load_dwordx4 a[192:195], v149, s[80:83], s64 offen offset:0 sc1\n\t" \
  "v_cvt_pk_bf16_f32 v111, v46, v47\n\t" \
  "v_mfma_f32_16x16x32_bf16 a[36:39], v[124:127], a[204:207], a[36:39]\n\t" \
  "v_exp_f32 v48, v48\n\t" \
  "v_exp_f32 v49, v49\n\t" \
  "v_mfma_f32_16x16x32_bf16 a[44:47], v[128:131], a[200:203], a[44:47]\n\t" \
  "buffer_load_dwordx4 a[196:199], v149, s[80:83], s64 offen offset:2048 sc1\n\t" \
  "v_cvt_pk_bf16_f32 v112, v48, v49\n\t" \
  "v_mfma_f32_16x16x32_bf16 a[44:47], v[132:135], a[204:207], a[44:47]\n\t" \
  "v_exp_f32 v50, v50\n\t" \
  "v_exp_f32 v51, v51\n\t" \
  "v_mfma_f32_16x16x32_bf16 a[52:55], a[164:167], a[200:203], a[52:55]\n\t" \
  "v_exp_f32 v52, v52\n\t" \
  "v_mfma_f32_16x16x32_bf16 a[52:55], a[168:171], a[204:207], a[52:55]\n\t" \
  "v_cvt_pk_bf16_f32 v113, v50, v51\n\t" \
  "v_exp_f32 v53, v53\n\t" \
  "v_mfma_f32_16x16x32_bf16 a[60:63], a[172:175], a[200:203], a[60:63]\n\t" \
  "v_exp_f32 v54, v54\n\t" \
  "v_mfma_f32_16x16x32_bf16 a[60:63], v[156:159], a[204:207], a[60:63]\n\t" \
  "v_cvt_pk_bf16_f32 v114, v52, v53\n\t" \
  "v_exp_f32 v55, v55\n\t" \
  "s_waitcnt lgkmcnt(1)\n\t" \
  "v_mfma_f32_32x32x16_bf16 v[32:47], v[88:91], v[64:67], v[0:15]\n\t" \
  "ds_read_b64_tr_b16 v[240:241], v155 offset:0\n\t" \
  "v_exp_f32 v56, v56\n\t" \
  "v_cvt_pk_bf16_f32 v115, v54, v55\n\t" \
  "v_exp_f32 v57, v57\n\t" \
  "v_mfma_f32_32x32x16_bf16 v[32:47], v[92:95], v[68:71], v[32:47]\n\t" \
  "ds_read_b64_tr_b16 v[242:243], v155 offset:512\n\t" \
  "v_exp_f32 v58, v58\n\t" \
  "v_cvt_pk_bf16_f32 v116, v56, v57\n\t" \
  "v_exp_f32 v59, v59\n\t" \
  "v_mfma_f32_32x32x16_bf16 a[128:143], v[96:99], v[104:107], a[128:143]\n\t" \
  "ds_read_b64_tr_b16 v[248:249], v137 offset:0\n\t" \
  "v_exp_f32 v60, v60\n\t" \
  "v_cvt_pk_bf16_f32 v117, v58, v59\n\t" \
  "v_exp_f32 v61, v61\n\t" \
  "s_waitcnt lgkmcnt(3)\n\t" \
  "v_mfma_f32_32x32x16_bf16 a[128:143], v[100:103], v[108:111], a[128:143]\n\t" \
  "ds_read_b64_tr_b16 v[250:251], v137 offset:512\n\t" \
  "v_exp_f32 v62, v62\n\t" \
  "v_cvt_pk_bf16_f32 v118, v60, v61\n\t" \
  "v_exp_f32 v63, v63\n\t" \
  "v_smfmac_f32_16x16x64_bf16 a[160:163], v[80:83], v[104:111], v154\n\t" \
  "s_waitcnt vmcnt(24)\n\t" \
  "v_mfma_f32_16x16x32_bf16 a[64:67], v[120:123], a[208:211], a[64:67]\n\t" \
  "buffer_load_dwordx4 a[200:203], v149, s[80:83], s64 offen offset:256 sc1\n\t" \
  "v_cvt_pk_bf16_f32 v119, v62, v63\n\t" \
  "v_mfma_f32_16x16x32_bf16 a[64:67], v[124:127], a[212:215], a[64:67]\n\t" \
  "v_exp_f32 v32, v32\n\t" \
  "v_exp_f32 v33, v33\n\t" \
  "v_mfma_f32_16x16x32_bf16 a[72:75], v[128:131], a[208:211], a[72:75]\n\t" \
  "buffer_load_dwordx4 a[204:207], v149, s[80:83], s64 offen offset:2304 sc1\n\t" \
  "v_cvt_pk_bf16_f32 v104, v32, v33\n\t" \
  "v_mfma_f32_16x16x32_bf16 a[72:75], v[132:135], a[212:215], a[72:75]\n\t" \
  "v_exp_f32 v34, v34\n\t" \
  "v_exp_f32 v35, v35\n\t" \
  "v_mfma_f32_16x16x32_bf16 a[80:83], a[164:167], a[208:211], a[80:83]\n\t" \
  "s_add_u32 m0, s72, 12288\n\t" \
  "v_cvt_pk_bf16_f32 v105, v34, v35\n\t" \
  "v_mfma_f32_16x16x32_bf16 a[80:83], a[168:171], a[212:215], a[80:83]\n\t" \
  "v_exp_f32 v36, v36\n\t" \
  "v_exp_f32 v37, v37\n\t" \
  "v_mfma_f32_16x16x32_bf16 a[88:91], a[172:175], a[208:211], a[88:91]\n\t" \
  "s_mov_b32 s68, 458752\n\t" \
  "v_cvt_pk_bf16_f32 v106, v36, v37\n\t" \
  "v_mfma_f32_16x16x32_bf16 a[88:91], v[156:159], a[212:215], a[88:91]\n\t" \
  "v_exp_f32 v38, v38\n\t" \
  "v_exp_f32 v39, v39\n\t" \
  "v_mfma_f32_32x32x16_bf16 v[48:63], v[88:91], v[72:75], v[16:31]\n\t" \
  "ds_read_b128 v[88:91], v145 offset:45056\n\t" \
  "v_exp_f32 v40, v40\n\t" \
  "v_cvt_pk_bf16_f32 v107, v38, v39\n\t" \
  "v_exp_f32 v41, v41\n\t" \
  "v_mfma_f32_32x32x16_bf16 v[48:63], v[92:95], v[76:79], v[48:63]\n\t" \
  "ds_read_b128 v[92:95], v145 offset:46080\n\t" \
  "v_exp_f32 v42, v42\n\t" \
  "v_cvt_pk_bf16_f32 v108, v40, v41\n\t" \
  "v_exp_f32 v43, v43\n\t" \
  "v_mfma_f32_32x32x16_bf16 a[144:159], v[96:99], v[112:115], a[144:159]\n\t" \
  "ds_read_b128 v[96:99], v145 offset:43008\n\t" \
  "v_exp_f32 v44, v44\n\t" \
  "v_cvt_pk_bf16_f32 v109, v42, v43\n\t" \
  "v_exp_f32 v45, v45\n\t" \
  "v_mfma_f32_32x32x16_bf16 a[144:159], v[100:103], v[116:119], a[144:159]\n\t" \
  "ds_read_b128 v[100:103], v145 offset:44032\n\t" \
  "v_exp_f32 v46, v46\n\t" \
  "v_cvt_pk_bf16_f32 v110, v44, v45\n\t" \
  "v_exp_f32 v47, v47\n\t" \
  "v_smfmac_f32_16x16x64_bf16 a[160:163], v[84:87], v[112:119], v154\n\t" \
  "s_waitcnt vmcnt(20)\n\t" \
  "v_mfma_f32_16x16x32_bf16 a[68:71], v[120:123], a[216:219], a[68:71]\n\t" \
  "buffer_load_dwordx4 a[208:211], v150, s[80:83], s64 offen offset:0 sc1\n\t" \
  "v_cvt_pk_bf16_f32 v111, v46, v47\n\t" \
  "v_mfma_f32_16x16x32_bf16 a[68:71], v[124:127], a[220:223], a[68:71]\n\t" \
  "v_exp_f32 v48, v48\n\t" \
  "v_exp_f32 v49, v49\n\t" \
  "v_mfma_f32_16x16x32_bf16 a[76:79], v[128:131], a[216:219], a[76:79]\n\t" \
  "buffer_load_dwordx4 a[212:215], v150, s[80:83], s64 offen offset:2048 sc1\n\t" \
  "v_cvt_pk_bf16_f32 v112, v48, v49\n\t" \
  "v_mfma_f32_16x16x32_bf16 a[76:79], v[132:135], a[220:223], a[76:79]\n\t" \
  "v_exp_f32 v50, v50\n\t" \
  "v_exp_f32 v51, v51\n\t" \
  "v_mfma_f32_16x16x32_bf16 a[84:87], a[164:167], a[216:219], a[84:87]\n\t" \
  "buffer_load_dwordx4 v140, s[84:87], s68 offen offset:0 lds\n\t" \
  "v_cvt_pk_bf16_f32 v113, v50, v51\n\t" \
  "v_mfma_f32_16x16x32_bf16 a[84:87], a[168:171], a[220:223], a[84:87]\n\t" \
  "v_exp_f32 v52, v52\n\t" \
  "v_exp_f32 v53, v53\n\t" \
  "v_mfma_f32_16x16x32_bf16 a[92:95], a[172:175], a[216:219], a[92:95]\n\t" \
  "buffer_load_dwordx4 v141, s[84:87], s68 offen offset:1024 lds\n\t" \
  "v_cvt_pk_bf16_f32 v114, v52, v53\n\t" \
  "v_mfma_f32_16x16x32_bf16 a[92:95], v[156:159], a[220:223], a[92:95]\n\t" \
  "v_exp_f32 v54, v54\n\t" \
  "v_exp_f32 v55, v55\n\t" \
  "s_waitcnt lgkmcnt(1)\n\t" \
  "v_mfma_f32_32x32x16_bf16 v[32:47], v[88:91], v[64:67], v[0:15]\n\t" \
  "ds_read_b64_tr_b16 v[228:229], v146 offset:4096\n\t" \
  "v_exp_f32 v56, v56\n\t" \
  "v_cvt_pk_bf16_f32 v115, v54, v55\n\t" \
  "v_exp_f32 v57, v57\n\t" \
  "v_mfma_f32_32x32x16_bf16 v[32:47], v[92:95], v[68:71], v[32:47]\n\t" \
  "ds_read_b64_tr_b16 v[230:231], v146 offset:4608\n\t" \
  "v_exp_f32 v58, v58\n\t" \
  "v_cvt_pk_bf16_f32 v116, v56, v57\n\t" \
  "v_exp_f32 v59, v59\n\t" \
  "v_mfma_f32_32x32x16_bf16 a[128:143], v[96:99], v[104:107], a[128:143]\n\t" \
  "ds_read_b64_tr_b16 v[236:237], v147 offset:4096\n\t" \
  "v_exp_f32 v60, v60\n\t" \
  "v_cvt_pk_bf16_f32 v117, v58, v59\n\t" \
  "v_exp_f32 v61, v61\n\t" \
  "s_waitcnt lgkmcnt(3)\n\t" \
  "v_mfma_f32_32x32x16_bf16 a[128:143], v[100:103], v[108:111], a[128:143]\n\t" \
  "ds_read_b64_tr_b16 v[238:239], v147 offset:4608\n\t" \
  "v_exp_f32 v62, v62\n\t" \
  "v_cvt_pk_bf16_f32 v118, v60, v61\n\t" \
  "v_exp_f32 v63, v63\n\t" \
  "v_smfmac_f32_16x16x64_bf16 a[160:163], v[80:83], v[104:111], v154\n\t" \
  "s_waitcnt vmcnt(18)\n\t" \
  "s_barrier\n\t" \
  "v_mfma_f32_16x16x32_bf16 a[96:99], v[120:123], a[224:227], a[96:99]\n\t" \
  "buffer_load_dwordx4 a[216:219], v150, s[80:83], s64 offen offset:256 sc1\n\t" \
  "v_cvt_pk_bf16_f32 v119, v62, v63\n\t" \
  "v_mfma_f32_16x16x32_bf16 a[96:99], v[124:127], a[228:231], a[96:99]\n\t" \
  "v_exp_f32 v32, v32\n\t" \
  "v_exp_f32 v33, v33\n\t" \
  "v_mfma_f32_16x16x32_bf16 a[104:107], v[128:131], a[224:227], a[104:107]\n\t" \
  "buffer_load_dwordx4 a[220:223], v150, s[80:83], s64 offen offset:2304 sc1\n\t" \
  "v_cvt_pk_bf16_f32 v104, v32, v33\n\t" \
  "v_mfma_f32_16x16x32_bf16 a[104:107], v[132:135], a[228:231], a[104:107]\n\t" \
  "v_exp_f32 v34, v34\n\t" \
  "v_exp_f32 v35, v35\n\t" \
  "v_mfma_f32_16x16x32_bf16 a[112:115], a[164:167], a[224:227], a[112:115]\n\t" \
  "buffer_load_dwordx4 v142, s[84:87], s68 offen offset:2048 lds\n\t" \
  "v_cvt_pk_bf16_f32 v105, v34, v35\n\t" \
  "v_mfma_f32_16x16x32_bf16 a[112:115], a[168:171], a[228:231], a[112:115]\n\t" \
  "v_exp_f32 v36, v36\n\t" \
  "v_exp_f32 v37, v37\n\t" \
  "v_mfma_f32_16x16x32_bf16 a[120:123], a[172:175], a[224:227], a[120:123]\n\t" \
  "buffer_load_dwordx4 v143, s[84:87], s68 offen offset:3072 lds\n\t" \
  "v_cvt_pk_bf16_f32 v106, v36, v37\n\t" \
  "v_mfma_f32_16x16x32_bf16 a[120:123], v[156:159], a[228:231], a[120:123]\n\t" \
  "v_exp_f32 v38, v38\n\t" \
  "v_exp_f32 v39, v39\n\t" \
  "v_mfma_f32_32x32x16_bf16 v[48:63], v[88:91], v[72:75], v[16:31]\n\t" \
  "ds_read_b128 v[88:91], v145 offset:0\n\t" \
  "v_exp_f32 v40, v40\n\t" \
  "v_cvt_pk_bf16_f32 v107, v38, v39\n\t" \
  "v_exp_f32 v41, v41\n\t" \
  "v_mfma_f32_32x32x16_bf16 v[48:63], v[92:95], v[76:79], v[48:63]\n\t" \
  "ds_read_b128 v[92:95], v145 offset:1024\n\t" \
  "v_exp_f32 v42, v42\n\t" \
  "v_cvt_pk_bf16_f32 v108, v40, v41\n\t" \
  "v_exp_f32 v43, v43\n\t" \
  "v_mfma_f32_32x32x16_bf16 a[144:159], v[96:99], v[112:115], a[144:159]\n\t" \
  "ds_read_b128 v[96:99], v145 offset:47104\n\t" \
  "v_exp_f32 v44, v44\n\t" \
  "v_cvt_pk_bf16_f32 v109, v42, v43\n\t" \
  "v_exp_f32 v45, v45\n\t" \
  "v_mfma_f32_32x32x16_bf16 a[144:159], v[100:103], v[116:119], a[144:159]\n\t" \
  "ds_read_b128 v[100:103], v145 offset:48128\n\t" \
  "v_exp_f32 v46, v46\n\t" \
  "v_cvt_pk_bf16_f32 v110, v44, v45\n\t" \
  "v_exp_f32 v47, v47\n\t" \
  "v_smfmac_f32_16x16x64_bf16 a[160:163], v[84:87], v[112:119], v154\n\t" \
  "s_add_u32 m0, s73, 16384\n\t" \
  "s_mov_b32 s69, 114688\n\t" \
  "s_waitcnt vmcnt(20)\n\t" \
  "v_mfma_f32_16x16x32_bf16 a[100:103], v[120:123], a[232:235], a[100:103]\n\t" \
  "buffer_load_dwordx4 a[224:227], v151, s[80:83], s64 offen offset:0 sc1\n\t" \
  "v_cvt_pk_bf16_f32 v111, v46, v47\n\t" \
  "v_mfma_f32_16x16x32_bf16 a[100:103], v[124:127], a[236:239], a[100:103]\n\t" \
  "v_exp_f32 v48, v48\n\t" \
  "v_exp_f32 v49, v49\n\t" \
  "v_mfma_f32_16x16x32_bf16 a[108:111], v[128:131], a[232:235], a[108:111]\n\t" \
  "buffer_load_dwordx4 a[228:231], v151, s[80:83], s64 offen offset:2048 sc1\n\t" \
  "v_cvt_pk_bf16_f32 v112, v48, v49\n\t" \
  "v_mfma_f32_16x16x32_bf16 a[108:111], v[132:135], a[236:239], a[108:111]\n\t" \
  "v_exp_f32 v50, v50\n\t" \
  "v_exp_f32 v51, v51\n\t" \
  "v_mfma_f32_16x16x32_bf16 a[116:119], a[164:167], a[232:235], a[116:119]\n\t" \
  "buffer_load_dwordx4 v144, s[88:91], s69 offen offset:0 sc1 lds\n\t" \
  "v_cvt_pk_bf16_f32 v113, v50, v51\n\t" \
  "v_mfma_f32_16x16x32_bf16 a[116:119], a[168:171], a[236:239], a[116:119]\n\t" \
  "v_exp_f32 v52, v52\n\t" \
  "v_exp_f32 v53, v53\n\t" \
  "v_mfma_f32_16x16x32_bf16 a[124:127], a[172:175], a[232:235], a[124:127]\n\t" \
  "buffer_load_dwordx4 v144, s[88:91], s69 offen offset:1024 sc1 lds\n\t" \
  "v_cvt_pk_bf16_f32 v114, v52, v53\n\t" \
  "v_mfma_f32_16x16x32_bf16 a[124:127], v[156:159], a[236:239], a[124:127]\n\t" \
  "v_exp_f32 v54, v54\n\t" \
  "v_exp_f32 v55, v55\n\t" \
  "s_waitcnt lgkmcnt(1)\n\t" \
  "v_mfma_f32_32x32x16_bf16 v[32:47], v[88:91], v[64:67], v[0:15]\n\t" \
  "ds_read_b64_tr_b16 v[244:245], v155 offset:4096\n\t" \
  "v_exp_f32 v56, v56\n\t" \
  "v_cvt_pk_bf16_f32 v115, v54, v55\n\t" \
  "v_exp_f32 v57, v57\n\t" \
  "v_mfma_f32_32x32x16_bf16 v[32:47], v[92:95], v[68:71], v[32:47]\n\t" \
  "ds_read_b64_tr_b16 v[246:247], v155 offset:4608\n\t" \
  "v_exp_f32 v58, v58\n\t" \
  "v_cvt_pk_bf16_f32 v116, v56, v57\n\t" \
  "v_exp_f32 v59, v59\n\t" \
  "v_mfma_f32_32x32x16_bf16 a[128:143], v[96:99], v[104:107], a[128:143]\n\t" \
  "ds_read_b64_tr_b16 v[252:253], v137 offset:4096\n\t" \
  "v_exp_f32 v60, v60\n\t" \
  "v_cvt_pk_bf16_f32 v117, v58, v59\n\t" \
  "v_exp_f32 v61, v61\n\t" \
  "s_waitcnt lgkmcnt(3)\n\t" \
  "v_mfma_f32_32x32x16_bf16 a[128:143], v[100:103], v[108:111], a[128:143]\n\t" \
  "ds_read_b64_tr_b16 v[254:255], v137 offset:4608\n\t" \
  "v_exp_f32 v62, v62\n\t" \
  "v_cvt_pk_bf16_f32 v118, v60, v61\n\t" \
  "v_exp_f32 v63, v63\n\t" \
  "v_smfmac_f32_16x16x64_bf16 a[160:163], v[80:83], v[104:111], v154\n\t" \
  "buffer_load_dwordx4 v144, s[88:91], s69 offen offset:2048 sc1 lds\n\t" \
  "buffer_load_dwordx4 v144, s[88:91], s69 offen offset:3072 sc1 lds\n\t" \
  "s_waitcnt vmcnt(24) lgkmcnt(0)\n\t" \
  "v_mfma_f32_16x16x32_bf16 a[0:3], v[224:227], a[176:179], a[0:3]\n\t" \
  "buffer_load_dwordx4 a[232:235], v151, s[80:83], s64 offen offset:256 sc1\n\t" \
  "v_cvt_pk_bf16_f32 v119, v62, v63\n\t" \
  "v_mfma_f32_16x16x32_bf16 a[0:3], v[228:231], a[180:183], a[0:3]\n\t" \
  "v_exp_f32 v32, v32\n\t" \
  "v_exp_f32 v33, v33\n\t" \
  "v_mfma_f32_16x16x32_bf16 a[8:11], v[232:235], a[176:179], a[8:11]\n\t" \
  "buffer_load_dwordx4 a[236:239], v151, s[80:83], s64 offen offset:2304 sc1\n\t" \
  "v_cvt_pk_bf16_f32 v104, v32, v33\n\t" \
  "v_mfma_f32_16x16x32_bf16 a[8:11], v[236:239], a[180:183], a[8:11]\n\t" \
  "v_exp_f32 v34, v34\n\t" \
  "v_exp_f32 v35, v35\n\t" \
  "v_mfma_f32_16x16x32_bf16 a[16:19], v[240:243], a[176:179], a[16:19]\n\t" \
  "s_add_u32 m0, s72, 0\n\t" \
  "v_cvt_pk_bf16_f32 v105, v34, v35\n\t" \
  "v_mfma_f32_16x16x32_bf16 a[16:19], v[244:247], a[180:183], a[16:19]\n\t" \
  "v_exp_f32 v36, v36\n\t" \
  "v_exp_f32 v37, v37\n\t" \
  "v_mfma_f32_16x16x32_bf16 a[24:27], v[248:251], a[176:179], a[24:27]\n\t" \
  "s_mov_b32 s68, 524288\n\t" \
  "v_cvt_pk_bf16_f32 v106, v36, v37\n\t" \
  "v_mfma_f32_16x16x32_bf16 a[24:27], v[252:255], a[180:183], a[24:27]\n\t" \
  "v_exp_f32 v38, v38\n\t" \
  "v_exp_f32 v39, v39\n\t" \
  "v_mfma_f32_32x32x16_bf16 v[48:63], v[88:91], v[72:75], v[16:31]\n\t" \
  "ds_read_b128 v[88:91], v145 offset:4096\n\t" \
  "v_exp_f32 v40, v40\n\t" \
  "v_cvt_pk_bf16_f32 v107, v38, v39\n\t" \
  "v_exp_f32 v41, v41\n\t" \
  "v_mfma_f32_32x32x16_bf16 v[48:63], v[92:95], v[76:79], v[48:63]\n\t" \
  "ds_read_b128 v[92:95], v145 offset:5120\n\t" \
  "v_exp_f32 v42, v42\n\t" \
  "v_cvt_pk_bf16_f32 v108, v40, v41\n\t" \
  "v_exp_f32 v43, v43\n\t" \
  "v_mfma_f32_32x32x16_bf16 a[144:159], v[96:99], v[112:115], a[144:159]\n\t" \
  "ds_read_b128 v[96:99], v145 offset:2048\n\t" \
  "v_exp_f32 v44, v44\n\t" \
  "v_cvt_pk_bf16_f32 v109, v42, v43\n\t" \
  "v_exp_f32 v45, v45\n\t" \
  "v_mfma_f32_32x32x16_bf16 a[144:159], v[100:103], v[116:119], a[144:159]\n\t" \
  "ds_read_b128 v[100:103], v145 offset:3072\n\t" \
  "v_exp_f32 v46, v46\n\t" \
  "v_cvt_pk_bf16_f32 v110, v44, v45\n\t" \
  "v_exp_f32 v47, v47\n\t" \
  "v_smfmac_f32_16x16x64_bf16 a[160:163], v[84:87], v[112:119], v154\n\t" \
  "s_mov_b32 s64, 12288\n\t" \
  "s_waitcnt vmcnt(20)\n\t" \
  "v_mfma_f32_16x16x32_bf16 a[4:7], v[224:227], a[184:187], a[4:7]\n\t" \
  "buffer_load_dwordx4 a[176:179], v148, s[80:83], s64 offen offset:0 sc1\n\t" \
  "v_cvt_pk_bf16_f32 v111, v46, v47\n\t" \
  "v_mfma_f32_16x16x32_bf16 a[4:7], v[228:231], a[188:191], a[4:7]\n\t" \
  "v_exp_f32 v48, v48\n\t" \
  "v_exp_f32 v49, v49\n\t" \
  "v_mfma_f32_16x16x32_bf16 a[12:15], v[232:235], a[184:187], a[12:15]\n\t" \
  "buffer_load_dwordx4 a[180:183], v148, s[80:83], s64 offen offset:2048 sc1\n\t" \
  "v_cvt_pk_bf16_f32 v112, v48, v49\n\t" \
  "v_mfma_f32_16x16x32_bf16 a[12:15], v[236:239], a[188:191], a[12:15]\n\t" \
  "v_exp_f32 v50, v50\n\t" \
  "v_exp_f32 v51, v51\n\t" \
  "v_mfma_f32_16x16x32_bf16 a[20:23], v[240:243], a[184:187], a[20:23]\n\t" \
  "buffer_load_dwordx4 v140, s[84:87], s68 offen offset:0 lds\n\t" \
  "v_cvt_pk_bf16_f32 v113, v50, v51\n\t" \
  "v_mfma_f32_16x16x32_bf16 a[20:23], v[244:247], a[188:191], a[20:23]\n\t" \
  "v_exp_f32 v52, v52\n\t" \
  "v_exp_f32 v53, v53\n\t" \
  "v_mfma_f32_16x16x32_bf16 a[28:31], v[248:251], a[184:187], a[28:31]\n\t" \
  "buffer_load_dwordx4 v141, s[84:87], s68 offen offset:1024 lds\n\t" \
  "v_cvt_pk_bf16_f32 v114, v52, v53\n\t" \
  "v_mfma_f32_16x16x32_bf16 a[28:31], v[252:255], a[188:191], a[28:31]\n\t" \
  "v_exp_f32 v54, v54\n\t" \
  "v_exp_f32 v55, v55\n\t" \
  "s_waitcnt lgkmcnt(1)\n\t" \
  "v_mfma_f32_32x32x16_bf16 v[32:47], v[88:91], v[64:67], v[0:15]\n\t" \
  "ds_read_b64_tr_b16 v[120:121], v146 offset:8192\n\t" \
  "v_exp_f32 v56, v56\n\t" \
  "v_cvt_pk_bf16_f32 v115, v54, v55\n\t" \
  "v_exp_f32 v57, v57\n\t" \
  "v_mfma_f32_32x32x16_bf16 v[32:47], v[92:95], v[68:71], v[32:47]\n\t" \
  "ds_read_b64_tr_b16 v[122:123], v146 offset:8704\n\t" \
  "v_exp_f32 v58, v58\n\t" \
  "v_cvt_pk_bf16_f32 v116, v56, v57\n\t" \
  "v_exp_f32 v59, v59\n\t" \
  "v_mfma_f32_32x32x16_bf16 a[128:143], v[96:99], v[104:107], a[128:143]\n\t" \
  "ds_read_b64_tr_b16 v[128:129], v147 offset:8192\n\t" \
  "v_exp_f32 v60, v60\n\t" \
  "v_cvt_pk_bf16_f32 v117, v58, v59\n\t" \
  "v_exp_f32 v61, v61\n\t" \
  "s_waitcnt lgkmcnt(3)\n\t" \
  "v_mfma_f32_32x32x16_bf16 a[128:143], v[100:103], v[108:111], a[128:143]\n\t" \
  "ds_read_b64_tr_b16 v[130:131], v147 offset:8704\n\t" \
  "v_exp_f32 v62, v62\n\t" \
  "v_cvt_pk_bf16_f32 v118, v60, v61\n\t" \
  "v_exp_f32 v63, v63\n\t" \
  "v_smfmac_f32_16x16x64_bf16 a[160:163], v[80:83], v[104:111], v154\n\t" \
  "s_waitcnt vmcnt(22)\n\t" \
  "v_mfma_f32_16x16x32_bf16 a[32:35], v[224:227], a[192:195], a[32:35]\n\t" \
  "buffer_load_dwordx4 a[184:187], v148, s[80:83], s64 offen offset:256 sc1\n\t" \
  "v_cvt_pk_bf16_f32 v119, v62, v63\n\t" \
  "v_mfma_f32_16x16x32_bf16 a[32:35], v[228:231], a[196:199], a[32:35]\n\t" \
  "v_exp_f32 v32, v32\n\t" \
  "v_exp_f32 v33, v33\n\t" \
  "v_mfma_f32_16x16x32_bf16 a[40:43], v[232:235], a[192:195], a[40:43]\n\t" \
  "buffer_load_dwordx4 a[188:191], v148, s[80:83], s64 offen offset:2304 sc1\n\t" \
  "v_cvt_pk_bf16_f32 v104, v32, v33\n\t" \
  "v_mfma_f32_16x16x32_bf16 a[40:43], v[236:239], a[196:199], a[40:43]\n\t" \
  "v_exp_f32 v34, v34\n\t" \
  "v_exp_f32 v35, v35\n\t" \
  "v_mfma_f32_16x16x32_bf16 a[48:51], v[240:243], a[192:195], a[48:51]\n\t" \
  "buffer_load_dwordx4 v142, s[84:87], s68 offen offset:2048 lds\n\t" \
  "v_cvt_pk_bf16_f32 v105, v34, v35\n\t" \
  "v_mfma_f32_16x16x32_bf16 a[48:51], v[244:247], a[196:199], a[48:51]\n\t" \
  "v_exp_f32 v36, v36\n\t" \
  "v_exp_f32 v37, v37\n\t" \
  "v_mfma_f32_16x16x32_bf16 a[56:59], v[248:251], a[192:195], a[56:59]\n\t" \
  "buffer_load_dwordx4 v143, s[84:87], s68 offen offset:3072 lds\n\t" \
  "v_cvt_pk_bf16_f32 v106, v36, v37\n\t" \
  "v_mfma_f32_16x16x32_bf16 a[56:59], v[252:255], a[196:199], a[56:59]\n\t" \
  "v_exp_f32 v38, v38\n\t" \
  "v_exp_f32 v39, v39\n\t" \
  "v_mfma_f32_32x32x16_bf16 v[48:63], v[88:91], v[72:75], v[16:31]\n\t" \
  "ds_read_b128 v[88:91], v145 offset:8192\n\t" \
  "v_exp_f32 v40, v40\n\t" \
  "v_cvt_pk_bf16_f32 v107, v38, v39\n\t" \
  "v_exp_f32 v41, v41\n\t" \
  "v_mfma_f32_32x32x16_bf16 v[48:63], v[92:95], v[76:79], v[48:63]\n\t" \
  "ds_read_b128 v[92:95], v145 offset:9216\n\t" \
  "v_exp_f32 v42, v42\n\t" \
  "v_cvt_pk_bf16_f32 v108, v40, v41\n\t" \
  "v_exp_f32 v43, v43\n\t" \
  "v_mfma_f32_32x32x16_bf16 a[144:159], v[96:99], v[112:115], a[144:159]\n\t" \
  "ds_read_b128 v[96:99], v145 offset:6144\n\t" \
  "v_exp_f32 v44, v44\n\t" \
  "v_cvt_pk_bf16_f32 v109, v42, v43\n\t" \
  "v_exp_f32 v45, v45\n\t" \
  "v_mfma_f32_32x32x16_bf16 a[144:159], v[100:103], v[116:119], a[144:159]\n\t" \
  "ds_read_b128 v[100:103], v145 offset:7168\n\t" \
  "v_exp_f32 v46, v46\n\t" \
  "v_cvt_pk_bf16_f32 v110, v44, v45\n\t" \
  "v_exp_f32 v47, v47\n\t" \
  "v_smfmac_f32_16x16x64_bf16 a[160:163], v[84:87], v[112:119], v154\n\t" \
  "s_waitcnt vmcnt(24)\n\t" \
  "v_mfma_f32_16x16x32_bf16 a[36:39], v[224:227], a[200:203], a[36:39]\n\t" \
  "buffer_load_dwordx4 a[192:195], v149, s[80:83], s64 offen offset:0 sc1\n\t" \
  "v_cvt_pk_bf16_f32 v111, v46, v47\n\t" \
  "v_mfma_f32_16x16x32_bf16 a[36:39], v[228:231], a[204:207], a[36:39]\n\t" \
  "v_exp_f32 v48, v48\n\t" \
  "v_exp_f32 v49, v49\n\t" \
  "v_mfma_f32_16x16x32_bf16 a[44:47], v[232:235], a[200:203], a[44:47]\n\t" \
  "buffer_load_dwordx4 a[196:199], v149, s[80:83], s64 offen offset:2048 sc1\n\t" \
  "v_cvt_pk_bf16_f32 v112, v48, v49\n\t" \
  "v_mfma_f32_16x16x32_bf16 a[44:47], v[236:239], a[204:207], a[44:47]\n\t" \
  "v_exp_f32 v50, v50\n\t" \
  "v_exp_f32 v51, v51\n\t" \
  "v_mfma_f32_16x16x32_bf16 a[52:55], v[240:243], a[200:203], a[52:55]\n\t" \
  "v_exp_f32 v52, v52\n\t" \
  "v_mfma_f32_16x16x32_bf16 a[52:55], v[244:247], a[204:207], a[52:55]\n\t" \
  "v_cvt_pk_bf16_f32 v113, v50, v51\n\t" \
  "v_exp_f32 v53, v53\n\t" \
  "v_mfma_f32_16x16x32_bf16 a[60:63], v[248:251], a[200:203], a[60:63]\n\t" \
  "v_exp_f32 v54, v54\n\t" \
  "v_mfma_f32_16x16x32_bf16 a[60:63], v[252:255], a[204:207], a[60:63]\n\t" \
  "v_cvt_pk_bf16_f32 v114, v52, v53\n\t" \
  "v_exp_f32 v55, v55\n\t" \
  "s_waitcnt lgkmcnt(1)\n\t" \
  "v_mfma_f32_32x32x16_bf16 v[32:47], v[88:91], v[64:67], v[0:15]\n\t" \
  "ds_read_b64_tr_b16 a[164:165], v155 offset:8192\n\t" \
  "v_exp_f32 v56, v56\n\t" \
  "v_cvt_pk_bf16_f32 v115, v54, v55\n\t" \
  "v_exp_f32 v57, v57\n\t" \
  "v_mfma_f32_32x32x16_bf16 v[32:47], v[92:95], v[68:71], v[32:47]\n\t" \
  "ds_read_b64_tr_b16 a[166:167], v155 offset:8704\n\t" \
  "v_exp_f32 v58, v58\n\t" \
  "v_cvt_pk_bf16_f32 v116, v56, v57\n\t" \
  "v_exp_f32 v59, v59\n\t" \
  "v_mfma_f32_32x32x16_bf16 a[128:143], v[96:99], v[104:107], a[128:143]\n\t" \
  "ds_read_b64_tr_b16 a[172:173], v137 offset:8192\n\t" \
  "v_exp_f32 v60, v60\n\t" \
  "v_cvt_pk_bf16_f32 v117, v58, v59\n\t" \
  "v_exp_f32 v61, v61\n\t" \
  "s_waitcnt lgkmcnt(3)\n\t" \
  "v_mfma_f32_32x32x16_bf16 a[128:143], v[100:103], v[108:111], a[128:143]\n\t" \
  "ds_read_b64_tr_b16 a[174:175], v137 offset:8704\n\t" \
  "v_exp_f32 v62, v62\n\t" \
  "v_cvt_pk_bf16_f32 v118, v60, v61\n\t" \
  "v_exp_f32 v63, v63\n\t" \
  "v_smfmac_f32_16x16x64_bf16 a[160:163], v[80:83], v[104:111], v154\n\t" \
  "s_waitcnt vmcnt(24)\n\t" \
  "v_mfma_f32_16x16x32_bf16 a[64:67], v[224:227], a[208:211], a[64:67]\n\t" \
  "buffer_load_dwordx4 a[200:203], v149, s[80:83], s64 offen offset:256 sc1\n\t" \
  "v_cvt_pk_bf16_f32 v119, v62, v63\n\t" \
  "v_mfma_f32_16x16x32_bf16 a[64:67], v[228:231], a[212:215], a[64:67]\n\t" \
  "v_exp_f32 v32, v32\n\t" \
  "v_exp_f32 v33, v33\n\t" \
  "v_mfma_f32_16x16x32_bf16 a[72:75], v[232:235], a[208:211], a[72:75]\n\t" \
  "buffer_load_dwordx4 a[204:207], v149, s[80:83], s64 offen offset:2304 sc1\n\t" \
  "v_cvt_pk_bf16_f32 v104, v32, v33\n\t" \
  "v_mfma_f32_16x16x32_bf16 a[72:75], v[236:239], a[212:215], a[72:75]\n\t" \
  "v_exp_f32 v34, v34\n\t" \
  "v_exp_f32 v35, v35\n\t" \
  "v_mfma_f32_16x16x32_bf16 a[80:83], v[240:243], a[208:211], a[80:83]\n\t" \
  "s_add_u32 m0, s72, 4096\n\t" \
  "v_cvt_pk_bf16_f32 v105, v34, v35\n\t" \
  "v_mfma_f32_16x16x32_bf16 a[80:83], v[244:247], a[212:215], a[80:83]\n\t" \
  "v_exp_f32 v36, v36\n\t" \
  "v_exp_f32 v37, v37\n\t" \
  "v_mfma_f32_16x16x32_bf16 a[88:91], v[248:251], a[208:211], a[88:91]\n\t" \
  "s_mov_b32 s68, 589824\n\t" \
  "v_cvt_pk_bf16_f32 v106, v36, v37\n\t" \
  "v_mfma_f32_16x16x32_bf16 a[88:91], v[252:255], a[212:215], a[88:91]\n\t" \
  "v_exp_f32 v38, v38\n\t" \
  "v_exp_f32 v39, v39\n\t" \
  "v_mfma_f32_32x32x16_bf16 v[48:63], v[88:91], v[72:75], v[16:31]\n\t" \
  "ds_read_b128 v[88:91], v145 offset:12288\n\t" \
  "v_exp_f32 v40, v40\n\t" \
  "v_cvt_pk_bf16_f32 v107, v38, v39\n\t" \
  "v_exp_f32 v41, v41\n\t" \
  "v_mfma_f32_32x32x16_bf16 v[48:63], v[92:95], v[76:79], v[48:63]\n\t" \
  "ds_read_b128 v[92:95], v145 offset:13312\n\t" \
  "v_exp_f32 v42, v42\n\t" \
  "v_cvt_pk_bf16_f32 v108, v40, v41\n\t" \
  "v_exp_f32 v43, v43\n\t" \
  "v_mfma_f32_32x32x16_bf16 a[144:159], v[96:99], v[112:115], a[144:159]\n\t" \
  "ds_read_b128 v[96:99], v145 offset:10240\n\t" \
  "v_exp_f32 v44, v44\n\t" \
  "v_cvt_pk_bf16_f32 v109, v42, v43\n\t" \
  "v_exp_f32 v45, v45\n\t" \
  "v_mfma_f32_32x32x16_bf16 a[144:159], v[100:103], v[116:119], a[144:159]\n\t" \
  "ds_read_b128 v[100:103], v145 offset:11264\n\t" \
  "v_exp_f32 v46, v46\n\t" \
  "v_cvt_pk_bf16_f32 v110, v44, v45\n\t" \
  "v_exp_f32 v47, v47\n\t" \
  "v_smfmac_f32_16x16x64_bf16 a[160:163], v[84:87], v[112:119], v154\n\t" \
  "s_waitcnt vmcnt(20)\n\t" \
  "v_mfma_f32_16x16x32_bf16 a[68:71], v[224:227], a[216:219], a[68:71]\n\t" \
  "buffer_load_dwordx4 a[208:211], v150, s[80:83], s64 offen offset:0 sc1\n\t" \
  "v_cvt_pk_bf16_f32 v111, v46, v47\n\t" \
  "v_mfma_f32_16x16x32_bf16 a[68:71], v[228:231], a[220:223], a[68:71]\n\t" \
  "v_exp_f32 v48, v48\n\t" \
  "v_exp_f32 v49, v49\n\t" \
  "v_mfma_f32_16x16x32_bf16 a[76:79], v[232:235], a[216:219], a[76:79]\n\t" \
  "buffer_load_dwordx4 a[212:215], v150, s[80:83], s64 offen offset:2048 sc1\n\t" \
  "v_cvt_pk_bf16_f32 v112, v48, v49\n\t" \
  "v_mfma_f32_16x16x32_bf16 a[76:79], v[236:239], a[220:223], a[76:79]\n\t" \
  "v_exp_f32 v50, v50\n\t" \
  "v_exp_f32 v51, v51\n\t" \
  "v_mfma_f32_16x16x32_bf16 a[84:87], v[240:243], a[216:219], a[84:87]\n\t" \
  "buffer_load_dwordx4 v140, s[84:87], s68 offen offset:0 lds\n\t" \
  "v_cvt_pk_bf16_f32 v113, v50, v51\n\t" \
  "v_mfma_f32_16x16x32_bf16 a[84:87], v[244:247], a[220:223], a[84:87]\n\t" \
  "v_exp_f32 v52, v52\n\t" \
  "v_exp_f32 v53, v53\n\t" \
  "v_mfma_f32_16x16x32_bf16 a[92:95], v[248:251], a[216:219], a[92:95]\n\t" \
  "buffer_load_dwordx4 v141, s[84:87], s68 offen offset:1024 lds\n\t" \
  "v_cvt_pk_bf16_f32 v114, v52, v53\n\t" \
  "v_mfma_f32_16x16x32_bf16 a[92:95], v[252:255], a[220:223], a[92:95]\n\t" \
  "v_exp_f32 v54, v54\n\t" \
  "v_exp_f32 v55, v55\n\t" \
  "s_waitcnt lgkmcnt(1)\n\t" \
  "v_mfma_f32_32x32x16_bf16 v[32:47], v[88:91], v[64:67], v[0:15]\n\t" \
  "ds_read_b64_tr_b16 v[124:125], v146 offset:12288\n\t" \
  "v_exp_f32 v56, v56\n\t" \
  "v_cvt_pk_bf16_f32 v115, v54, v55\n\t" \
  "v_exp_f32 v57, v57\n\t" \
  "v_mfma_f32_32x32x16_bf16 v[32:47], v[92:95], v[68:71], v[32:47]\n\t" \
  "ds_read_b64_tr_b16 v[126:127], v146 offset:12800\n\t" \
  "v_exp_f32 v58, v58\n\t" \
  "v_cvt_pk_bf16_f32 v116, v56, v57\n\t" \
  "v_exp_f32 v59, v59\n\t" \
  "v_mfma_f32_32x32x16_bf16 a[128:143], v[96:99], v[104:107], a[128:143]\n\t" \
  "ds_read_b64_tr_b16 v[132:133], v147 offset:12288\n\t" \
  "v_exp_f32 v60, v60\n\t" \
  "v_cvt_pk_bf16_f32 v117, v58, v59\n\t" \
  "v_exp_f32 v61, v61\n\t" \
  "s_waitcnt lgkmcnt(3)\n\t" \
  "v_mfma_f32_32x32x16_bf16 a[128:143], v[100:103], v[108:111], a[128:143]\n\t" \
  "ds_read_b64_tr_b16 v[134:135], v147 offset:12800\n\t" \
  "v_exp_f32 v62, v62\n\t" \
  "v_cvt_pk_bf16_f32 v118, v60, v61\n\t" \
  "v_exp_f32 v63, v63\n\t" \
  "v_smfmac_f32_16x16x64_bf16 a[160:163], v[80:83], v[104:111], v154\n\t" \
  "s_waitcnt vmcnt(18)\n\t" \
  "s_barrier\n\t" \
  "v_mfma_f32_16x16x32_bf16 a[96:99], v[224:227], a[224:227], a[96:99]\n\t" \
  "buffer_load_dwordx4 a[216:219], v150, s[80:83], s64 offen offset:256 sc1\n\t" \
  "v_cvt_pk_bf16_f32 v119, v62, v63\n\t" \
  "v_mfma_f32_16x16x32_bf16 a[96:99], v[228:231], a[228:231], a[96:99]\n\t" \
  "v_exp_f32 v32, v32\n\t" \
  "v_exp_f32 v33, v33\n\t" \
  "v_mfma_f32_16x16x32_bf16 a[104:107], v[232:235], a[224:227], a[104:107]\n\t" \
  "buffer_load_dwordx4 a[220:223], v150, s[80:83], s64 offen offset:2304 sc1\n\t" \
  "v_cvt_pk_bf16_f32 v104, v32, v33\n\t" \
  "v_mfma_f32_16x16x32_bf16 a[104:107], v[236:239], a[228:231], a[104:107]\n\t" \
  "v_exp_f32 v34, v34\n\t" \
  "v_exp_f32 v35, v35\n\t" \
  "v_mfma_f32_16x16x32_bf16 a[112:115], v[240:243], a[224:227], a[112:115]\n\t" \
  "buffer_load_dwordx4 v142, s[84:87], s68 offen offset:2048 lds\n\t" \
  "v_cvt_pk_bf16_f32 v105, v34, v35\n\t" \
  "v_mfma_f32_16x16x32_bf16 a[112:115], v[244:247], a[228:231], a[112:115]\n\t" \
  "v_exp_f32 v36, v36\n\t" \
  "v_exp_f32 v37, v37\n\t" \
  "v_mfma_f32_16x16x32_bf16 a[120:123], v[248:251], a[224:227], a[120:123]\n\t" \
  "buffer_load_dwordx4 v143, s[84:87], s68 offen offset:3072 lds\n\t" \
  "v_cvt_pk_bf16_f32 v106, v36, v37\n\t" \
  "v_mfma_f32_16x16x32_bf16 a[120:123], v[252:255], a[228:231], a[120:123]\n\t" \
  "v_exp_f32 v38, v38\n\t" \
  "v_exp_f32 v39, v39\n\t" \
  "v_mfma_f32_32x32x16_bf16 v[48:63], v[88:91], v[72:75], v[16:31]\n\t" \
  "ds_read_b128 v[88:91], v145 offset:16384\n\t" \
  "v_exp_f32 v40, v40\n\t" \
  "v_cvt_pk_bf16_f32 v107, v38, v39\n\t" \
  "v_exp_f32 v41, v41\n\t" \
  "v_mfma_f32_32x32x16_bf16 v[48:63], v[92:95], v[76:79], v[48:63]\n\t" \
  "ds_read_b128 v[92:95], v145 offset:17408\n\t" \
  "v_exp_f32 v42, v42\n\t" \
  "v_cvt_pk_bf16_f32 v108, v40, v41\n\t" \
  "v_exp_f32 v43, v43\n\t" \
  "v_mfma_f32_32x32x16_bf16 a[144:159], v[96:99], v[112:115], a[144:159]\n\t" \
  "ds_read_b128 v[96:99], v145 offset:14336\n\t" \
  "v_exp_f32 v44, v44\n\t" \
  "v_cvt_pk_bf16_f32 v109, v42, v43\n\t" \
  "v_exp_f32 v45, v45\n\t" \
  "v_mfma_f32_32x32x16_bf16 a[144:159], v[100:103], v[116:119], a[144:159]\n\t" \
  "ds_read_b128 v[100:103], v145 offset:15360\n\t" \
  "v_exp_f32 v46, v46\n\t" \
  "v_cvt_pk_bf16_f32 v110, v44, v45\n\t" \
  "v_exp_f32 v47, v47\n\t" \
  "v_smfmac_f32_16x16x64_bf16 a[160:163], v[84:87], v[112:119], v154\n\t" \
  "s_add_u32 m0, s73, 32768\n\t" \
  "s_mov_b32 s69, 131072\n\t" \
  "s_waitcnt vmcnt(20)\n\t" \
  "v_mfma_f32_16x16x32_bf16 a[100:103], v[224:227], a[232:235], a[100:103]\n\t" \
  "buffer_load_dwordx4 a[224:227], v151, s[80:83], s64 offen offset:0 sc1\n\t" \
  "v_cvt_pk_bf16_f32 v111, v46, v47\n\t" \
  "v_mfma_f32_16x16x32_bf16 a[100:103], v[228:231], a[236:239], a[100:103]\n\t" \
  "v_exp_f32 v48, v48\n\t" \
  "v_exp_f32 v49, v49\n\t" \
  "v_mfma_f32_16x16x32_bf16 a[108:111], v[232:235], a[232:235], a[108:111]\n\t" \
  "buffer_load_dwordx4 a[228:231], v151, s[80:83], s64 offen offset:2048 sc1\n\t" \
  "v_cvt_pk_bf16_f32 v112, v48, v49\n\t" \
  "v_mfma_f32_16x16x32_bf16 a[108:111], v[236:239], a[236:239], a[108:111]\n\t" \
  "v_exp_f32 v50, v50\n\t" \
  "v_exp_f32 v51, v51\n\t" \
  "v_mfma_f32_16x16x32_bf16 a[116:119], v[240:243], a[232:235], a[116:119]\n\t" \
  "buffer_load_dwordx4 v144, s[88:91], s69 offen offset:0 sc1 lds\n\t" \
  "v_cvt_pk_bf16_f32 v113, v50, v51\n\t" \
  "v_mfma_f32_16x16x32_bf16 a[116:119], v[244:247], a[236:239], a[116:119]\n\t" \
  "v_exp_f32 v52, v52\n\t" \
  "v_exp_f32 v53, v53\n\t" \
  "v_mfma_f32_16x16x32_bf16 a[124:127], v[248:251], a[232:235], a[124:127]\n\t" \
  "buffer_load_dwordx4 v144, s[88:91], s69 offen offset:1024 sc1 lds\n\t" \
  "v_cvt_pk_bf16_f32 v114, v52, v53\n\t" \
  "v_mfma_f32_16x16x32_bf16 a[124:127], v[252:255], a[236:239], a[124:127]\n\t" \
  "v_exp_f32 v54, v54\n\t" \
  "v_exp_f32 v55, v55\n\t" \
  "s_waitcnt lgkmcnt(1)\n\t" \
  "v_mfma_f32_32x32x16_bf16 v[32:47], v[88:91], v[64:67], v[0:15]\n\t" \
  "ds_read_b64_tr_b16 a[168:169], v155 offset:12288\n\t" \
  "v_exp_f32 v56, v56\n\t" \
  "v_cvt_pk_bf16_f32 v115, v54, v55\n\t" \
  "v_exp_f32 v57, v57\n\t" \
  "v_mfma_f32_32x32x16_bf16 v[32:47], v[92:95], v[68:71], v[32:47]\n\t" \
  "ds_read_b64_tr_b16 a[170:171], v155 offset:12800\n\t" \
  "v_exp_f32 v58, v58\n\t" \
  "v_cvt_pk_bf16_f32 v116, v56, v57\n\t" \
  "v_exp_f32 v59, v59\n\t" \
  "v_mfma_f32_32x32x16_bf16 a[128:143], v[96:99], v[104:107], a[128:143]\n\t" \
  "ds_read_b64_tr_b16 v[156:157], v137 offset:12288\n\t" \
  "v_exp_f32 v60, v60\n\t" \
  "v_cvt_pk_bf16_f32 v117, v58, v59\n\t" \
  "v_exp_f32 v61, v61\n\t" \
  "s_waitcnt lgkmcnt(3)\n\t" \
  "v_mfma_f32_32x32x16_bf16 a[128:143], v[100:103], v[108:111], a[128:143]\n\t" \
  "ds_read_b64_tr_b16 v[158:159], v137 offset:12800\n\t" \
  "v_exp_f32 v62, v62\n\t" \
  "v_cvt_pk_bf16_f32 v118, v60, v61\n\t" \
  "v_exp_f32 v63, v63\n\t" \
  "v_smfmac_f32_16x16x64_bf16 a[160:163], v[80:83], v[104:111], v154\n\t" \
  "buffer_load_dwordx4 v144, s[88:91], s69 offen offset:2048 sc1 lds\n\t" \
  "buffer_load_dwordx4 v144, s[88:91], s69 offen offset:3072 sc1 lds\n\t" \
  "s_waitcnt vmcnt(24) lgkmcnt(0)\n\t" \
  "v_mfma_f32_16x16x32_bf16 a[0:3], v[120:123], a[176:179], a[0:3]\n\t" \
  "buffer_load_dwordx4 a[232:235], v151, s[80:83], s64 offen offset:256 sc1\n\t" \
  "v_cvt_pk_bf16_f32 v119, v62, v63\n\t" \
  "v_mfma_f32_16x16x32_bf16 a[0:3], v[124:127], a[180:183], a[0:3]\n\t" \
  "v_exp_f32 v32, v32\n\t" \
  "v_exp_f32 v33, v33\n\t" \
  "v_mfma_f32_16x16x32_bf16 a[8:11], v[128:131], a[176:179], a[8:11]\n\t" \
  "buffer_load_dwordx4 a[236:239], v151, s[80:83], s64 offen offset:2304 sc1\n\t" \
  "v_cvt_pk_bf16_f32 v104, v32, v33\n\t" \
  "v_mfma_f32_16x16x32_bf16 a[8:11], v[132:135], a[180:183], a[8:11]\n\t" \
  "v_exp_f32 v34, v34\n\t" \
  "v_exp_f32 v35, v35\n\t" \
  "v_mfma_f32_16x16x32_bf16 a[16:19], a[164:167], a[176:179], a[16:19]\n\t" \
  "s_add_u32 m0, s72, 8192\n\t" \
  "v_cvt_pk_bf16_f32 v105, v34, v35\n\t" \
  "v_mfma_f32_16x16x32_bf16 a[16:19], a[168:171], a[180:183], a[16:19]\n\t" \
  "v_exp_f32 v36, v36\n\t" \
  "v_exp_f32 v37, v37\n\t" \
  "v_mfma_f32_16x16x32_bf16 a[24:27], a[172:175], a[176:179], a[24:27]\n\t" \
  "s_mov_b32 s68, 655360\n\t" \
  "v_cvt_pk_bf16_f32 v106, v36, v37\n\t" \
  "v_mfma_f32_16x16x32_bf16 a[24:27], v[156:159], a[180:183], a[24:27]\n\t" \
  "v_exp_f32 v38, v38\n\t" \
  "v_exp_f32 v39, v39\n\t" \
  "v_mfma_f32_32x32x16_bf16 v[48:63], v[88:91], v[72:75], v[16:31]\n\t" \
  "ds_read_b128 v[88:91], v145 offset:20480\n\t" \
  "v_exp_f32 v40, v40\n\t" \
  "v_cvt_pk_bf16_f32 v107, v38, v39\n\t" \
  "v_exp_f32 v41, v41\n\t" \
  "v_mfma_f32_32x32x16_bf16 v[48:63], v[92:95], v[76:79], v[48:63]\n\t" \
  "ds_read_b128 v[92:95], v145 offset:21504\n\t" \
  "v_exp_f32 v42, v42\n\t" \
  "v_cvt_pk_bf16_f32 v108, v40, v41\n\t" \
  "v_exp_f32 v43, v43\n\t" \
  "v_mfma_f32_32x32x16_bf16 a[144:159], v[96:99], v[112:115], a[144:159]\n\t" \
  "ds_read_b128 v[96:99], v145 offset:18432\n\t" \
  "v_exp_f32 v44, v44\n\t" \
  "v_cvt_pk_bf16_f32 v109, v42, v43\n\t" \
  "v_exp_f32 v45, v45\n\t" \
  "v_mfma_f32_32x32x16_bf16 a[144:159], v[100:103], v[116:119], a[144:159]\n\t" \
  "ds_read_b128 v[100:103], v145 offset:19456\n\t" \
  "v_exp_f32 v46, v46\n\t" \
  "v_cvt_pk_bf16_f32 v110, v44, v45\n\t" \
  "v_exp_f32 v47, v47\n\t" \
  "v_smfmac_f32_16x16x64_bf16 a[160:163], v[84:87], v[112:119], v154\n\t" \
  "s_mov_b32 s64, 16384\n\t" \
  "s_waitcnt vmcnt(20)\n\t" \
  "v_mfma_f32_16x16x32_bf16 a[4:7], v[120:123], a[184:187], a[4:7]\n\t" \
  "buffer_load_dwordx4 a[176:179], v148, s[80:83], s64 offen offset:0 sc1\n\t" \
  "v_cvt_pk_bf16_f32 v111, v46, v47\n\t" \
  "v_mfma_f32_16x16x32_bf16 a[4:7], v[124:127], a[188:191], a[4:7]\n\t" \
  "v_exp_f32 v48, v48\n\t" \
  "v_exp_f32 v49, v49\n\t" \
  "v_mfma_f32_16x16x32_bf16 a[12:15], v[128:131], a[184:187], a[12:15]\n\t" \
  "buffer_load_dwordx4 a[180:183], v148, s[80:83], s64 offen offset:2048 sc1\n\t" \
  "v_cvt_pk_bf16_f32 v112, v48, v49\n\t" \
  "v_mfma_f32_16x16x32_bf16 a[12:15], v[132:135], a[188:191], a[12:15]\n\t" \
  "v_exp_f32 v50, v50\n\t" \
  "v_exp_f32 v51, v51\n\t" \
  "v_mfma_f32_16x16x32_bf16 a[20:23], a[164:167], a[184:187], a[20:23]\n\t" \
  "buffer_load_dwordx4 v140, s[84:87], s68 offen offset:0 lds\n\t" \
  "v_cvt_pk_bf16_f32 v113, v50, v51\n\t" \
  "v_mfma_f32_16x16x32_bf16 a[20:23], a[168:171], a[188:191], a[20:23]\n\t" \
  "v_exp_f32 v52, v52\n\t" \
  "v_exp_f32 v53, v53\n\t" \
  "v_mfma_f32_16x16x32_bf16 a[28:31], a[172:175], a[184:187], a[28:31]\n\t" \
  "buffer_load_dwordx4 v141, s[84:87], s68 offen offset:1024 lds\n\t" \
  "v_cvt_pk_bf16_f32 v114, v52, v53\n\t" \
  "v_mfma_f32_16x16x32_bf16 a[28:31], v[156:159], a[188:191], a[28:31]\n\t" \
  "v_exp_f32 v54, v54\n\t" \
  "v_exp_f32 v55, v55\n\t" \
  "s_waitcnt lgkmcnt(1)\n\t" \
  "v_mfma_f32_32x32x16_bf16 v[32:47], v[88:91], v[64:67], v[0:15]\n\t" \
  "ds_read_b64_tr_b16 v[224:225], v146 offset:0\n\t" \
  "v_exp_f32 v56, v56\n\t" \
  "v_cvt_pk_bf16_f32 v115, v54, v55\n\t" \
  "v_exp_f32 v57, v57\n\t" \
  "v_mfma_f32_32x32x16_bf16 v[32:47], v[92:95], v[68:71], v[32:47]\n\t" \
  "ds_read_b64_tr_b16 v[226:227], v146 offset:512\n\t" \
  "v_exp_f32 v58, v58\n\t" \
  "v_cvt_pk_bf16_f32 v116, v56, v57\n\t" \
  "v_exp_f32 v59, v59\n\t" \
  "v_mfma_f32_32x32x16_bf16 a[128:143], v[96:99], v[104:107], a[128:143]\n\t" \
  "ds_read_b64_tr_b16 v[232:233], v147 offset:0\n\t" \
  "v_exp_f32 v60, v60\n\t" \
  "v_cvt_pk_bf16_f32 v117, v58, v59\n\t" \
  "v_exp_f32 v61, v61\n\t" \
  "s_waitcnt lgkmcnt(3)\n\t" \
  "v_mfma_f32_32x32x16_bf16 a[128:143], v[100:103], v[108:111], a[128:143]\n\t" \
  "ds_read_b64_tr_b16 v[234:235], v147 offset:512\n\t" \
  "v_exp_f32 v62, v62\n\t" \
  "v_cvt_pk_bf16_f32 v118, v60, v61\n\t" \
  "v_exp_f32 v63, v63\n\t" \
  "v_smfmac_f32_16x16x64_bf16 a[160:163], v[80:83], v[104:111], v154\n\t" \
  "global_load_dword v136, v152, s[52:53]\n\t" \
  "s_waitcnt vmcnt(23)\n\t" \
  "v_mfma_f32_16x16x32_bf16 a[32:35], v[120:123], a[192:195], a[32:35]\n\t" \
  "buffer_load_dwordx4 a[184:187], v148, s[80:83], s64 offen offset:256 sc1\n\t" \
  "v_cvt_pk_bf16_f32 v119, v62, v63\n\t" \
  "v_mfma_f32_16x16x32_bf16 a[32:35], v[124:127], a[196:199], a[32:35]\n\t" \
  "v_exp_f32 v32, v32\n\t" \
  "v_exp_f32 v33, v33\n\t" \
  "v_mfma_f32_16x16x32_bf16 a[40:43], v[128:131], a[192:195], a[40:43]\n\t" \
  "buffer_load_dwordx4 a[188:191], v148, s[80:83], s64 offen offset:2304 sc1\n\t" \
  "v_cvt_pk_bf16_f32 v104, v32, v33\n\t" \
  "v_mfma_f32_16x16x32_bf16 a[40:43], v[132:135], a[196:199], a[40:43]\n\t" \
  "v_exp_f32 v34, v34\n\t" \
  "v_exp_f32 v35, v35\n\t" \
  "v_mfma_f32_16x16x32_bf16 a[48:51], a[164:167], a[192:195], a[48:51]\n\t" \
  "buffer_load_dwordx4 v142, s[84:87], s68 offen offset:2048 lds\n\t" \
  "v_cvt_pk_bf16_f32 v105, v34, v35\n\t" \
  "v_mfma_f32_16x16x32_bf16 a[48:51], a[168:171], a[196:199], a[48:51]\n\t" \
  "v_exp_f32 v36, v36\n\t" \
  "v_exp_f32 v37, v37\n\t" \
  "v_mfma_f32_16x16x32_bf16 a[56:59], a[172:175], a[192:195], a[56:59]\n\t" \
  "buffer_load_dwordx4 v143, s[84:87], s68 offen offset:3072 lds\n\t" \
  "v_cvt_pk_bf16_f32 v106, v36, v37\n\t" \
  "v_mfma_f32_16x16x32_bf16 a[56:59], v[156:159], a[196:199], a[56:59]\n\t" \
  "v_exp_f32 v38, v38\n\t" \
  "v_exp_f32 v39, v39\n\t" \
  "v_mfma_f32_32x32x16_bf16 v[48:63], v[88:91], v[72:75], v[16:31]\n\t" \
  "ds_read_b128 v[88:91], v145 offset:24576\n\t" \
  "v_exp_f32 v40, v40\n\t" \
  "v_cvt_pk_bf16_f32 v107, v38, v39\n\t" \
  "v_exp_f32 v41, v41\n\t" \
  "v_mfma_f32_32x32x16_bf16 v[48:63], v[92:95], v[76:79], v[48:63]\n\t" \
  "ds_read_b128 v[92:95], v145 offset:25600\n\t" \
  "v_exp_f32 v42, v42\n\t" \
  "v_cvt_pk_bf16_f32 v108, v40, v41\n\t" \
  "v_exp_f32 v43, v43\n\t" \
  "v_mfma_f32_32x32x16_bf16 a[144:159], v[96:99], v[112:115], a[144:159]\n\t" \
  "ds_read_b128 v[96:99], v145 offset:22528\n\t" \
  "v_exp_f32 v44, v44\n\t" \
  "v_cvt_pk_bf16_f32 v109, v42, v43\n\t" \
  "v_exp_f32 v45, v45\n\t" \
  "v_mfma_f32_32x32x16_bf16 a[144:159], v[100:103], v[116:119], a[144:159]\n\t" \
  "ds_read_b128 v[100:103], v145 offset:23552\n\t" \
  "v_exp_f32 v46, v46\n\t" \
  "v_cvt_pk_bf16_f32 v110, v44, v45\n\t" \
  "v_exp_f32 v47, v47\n\t" \
  "v_smfmac_f32_16x16x64_bf16 a[160:163], v[84:87], v[112:119], v154\n\t" \
  "s_waitcnt vmcnt(25)\n\t" \
  "v_mfma_f32_16x16x32_bf16 a[36:39], v[120:123], a[200:203], a[36:39]\n\t" \
  "buffer_load_dwordx4 a[192:195], v149, s[80:83], s64 offen offset:0 sc1\n\t" \
  "v_cvt_pk_bf16_f32 v111, v46, v47\n\t" \
  "v_mfma_f32_16x16x32_bf16 a[36:39], v[124:127], a[204:207], a[36:39]\n\t" \
  "v_exp_f32 v48, v48\n\t" \
  "v_exp_f32 v49, v49\n\t" \
  "v_mfma_f32_16x16x32_bf16 a[44:47], v[128:131], a[200:203], a[44:47]\n\t" \
  "buffer_load_dwordx4 a[196:199], v149, s[80:83], s64 offen offset:2048 sc1\n\t" \
  "v_cvt_pk_bf16_f32 v112, v48, v49\n\t" \
  "v_mfma_f32_16x16x32_bf16 a[44:47], v[132:135], a[204:207], a[44:47]\n\t" \
  "v_exp_f32 v50, v50\n\t" \
  "v_exp_f32 v51, v51\n\t" \
  "v_mfma_f32_16x16x32_bf16 a[52:55], a[164:167], a[200:203], a[52:55]\n\t" \
  "v_exp_f32 v52, v52\n\t" \
  "v_mfma_f32_16x16x32_bf16 a[52:55], a[168:171], a[204:207], a[52:55]\n\t" \
  "v_cvt_pk_bf16_f32 v113, v50, v51\n\t" \
  "v_exp_f32 v53, v53\n\t" \
  "v_mfma_f32_16x16x32_bf16 a[60:63], a[172:175], a[200:203], a[60:63]\n\t" \
  "v_exp_f32 v54, v54\n\t" \
  "v_mfma_f32_16x16x32_bf16 a[60:63], v[156:159], a[204:207], a[60:63]\n\t" \
  "v_cvt_pk_bf16_f32 v114, v52, v53\n\t" \
  "v_exp_f32 v55, v55\n\t" \
  "s_waitcnt lgkmcnt(1)\n\t" \
  "v_mfma_f32_32x32x16_bf16 v[32:47], v[88:91], v[64:67], v[0:15]\n\t" \
  "ds_read_b64_tr_b16 v[240:241], v155 offset:0\n\t" \
  "v_exp_f32 v56, v56\n\t" \
  "v_cvt_pk_bf16_f32 v115, v54, v55\n\t" \
  "v_exp_f32 v57, v57\n\t" \
  "v_mfma_f32_32x32x16_bf16 v[32:47], v[92:95], v[68:71], v[32:47]\n\t" \
  "ds_read_b64_tr_b16 v[242:243], v155 offset:512\n\t" \
  "v_exp_f32 v58, v58\n\t" \
  "v_cvt_pk_bf16_f32 v116, v56, v57\n\t" \
  "v_exp_f32 v59, v59\n\t" \
  "v_mfma_f32_32x32x16_bf16 a[128:143], v[96:99], v[104:107], a[128:143]\n\t" \
  "ds_read_b64_tr_b16 v[248:249], v137 offset:0\n\t" \
  "v_exp_f32 v60, v60\n\t" \
  "v_cvt_pk_bf16_f32 v117, v58, v59\n\t" \
  "v_exp_f32 v61, v61\n\t" \
  "s_waitcnt lgkmcnt(3)\n\t" \
  "v_mfma_f32_32x32x16_bf16 a[128:143], v[100:103], v[108:111], a[128:143]\n\t" \
  "ds_read_b64_tr_b16 v[250:251], v137 offset:512\n\t" \
  "v_exp_f32 v62, v62\n\t" \
  "v_cvt_pk_bf16_f32 v118, v60, v61\n\t" \
  "v_exp_f32 v63, v63\n\t" \
  "v_smfmac_f32_16x16x64_bf16 a[160:163], v[80:83], v[104:111], v154\n\t" \
  "s_waitcnt vmcnt(25)\n\t" \
  "v_mfma_f32_16x16x32_bf16 a[64:67], v[120:123], a[208:211], a[64:67]\n\t" \
  "buffer_load_dwordx4 a[200:203], v149, s[80:83], s64 offen offset:256 sc1\n\t" \
  "v_cvt_pk_bf16_f32 v119, v62, v63\n\t" \
  "v_mfma_f32_16x16x32_bf16 a[64:67], v[124:127], a[212:215], a[64:67]\n\t" \
  "v_exp_f32 v32, v32\n\t" \
  "v_exp_f32 v33, v33\n\t" \
  "v_mfma_f32_16x16x32_bf16 a[72:75], v[128:131], a[208:211], a[72:75]\n\t" \
  "buffer_load_dwordx4 a[204:207], v149, s[80:83], s64 offen offset:2304 sc1\n\t" \
  "v_cvt_pk_bf16_f32 v104, v32, v33\n\t" \
  "v_mfma_f32_16x16x32_bf16 a[72:75], v[132:135], a[212:215], a[72:75]\n\t" \
  "v_exp_f32 v34, v34\n\t" \
  "v_exp_f32 v35, v35\n\t" \
  "v_mfma_f32_16x16x32_bf16 a[80:83], a[164:167], a[208:211], a[80:83]\n\t" \
  "s_add_u32 m0, s72, 12288\n\t" \
  "v_cvt_pk_bf16_f32 v105, v34, v35\n\t" \
  "v_mfma_f32_16x16x32_bf16 a[80:83], a[168:171], a[212:215], a[80:83]\n\t" \
  "v_exp_f32 v36, v36\n\t" \
  "v_exp_f32 v37, v37\n\t" \
  "v_mfma_f32_16x16x32_bf16 a[88:91], a[172:175], a[208:211], a[88:91]\n\t" \
  "s_mov_b32 s68, 720896\n\t" \
  "v_cvt_pk_bf16_f32 v106, v36, v37\n\t" \
  "v_mfma_f32_16x16x32_bf16 a[88:91], v[156:159], a[212:215], a[88:91]\n\t" \
  "v_exp_f32 v38, v38\n\t" \
  "v_exp_f32 v39, v39\n\t" \
  "v_mfma_f32_32x32x16_bf16 v[48:63], v[88:91], v[72:75], v[16:31]\n\t" \
  "ds_read_b128 v[88:91], v145 offset:28672\n\t" \
  "v_exp_f32 v40, v40\n\t" \
  "v_cvt_pk_bf16_f32 v107, v38, v39\n\t" \
  "v_exp_f32 v41, v41\n\t" \
  "v_mfma_f32_32x32x16_bf16 v[48:63], v[92:95], v[76:79], v[48:63]\n\t" \
  "ds_read_b128 v[92:95], v145 offset:29696\n\t" \
  "v_exp_f32 v42, v42\n\t" \
  "v_cvt_pk_bf16_f32 v108, v40, v41\n\t" \
  "v_exp_f32 v43, v43\n\t" \
  "v_mfma_f32_32x32x16_bf16 a[144:159], v[96:99], v[112:115], a[144:159]\n\t" \
  "ds_read_b128 v[96:99], v145 offset:26624\n\t" \
  "v_exp_f32 v44, v44\n\t" \
  "v_cvt_pk_bf16_f32 v109, v42, v43\n\t" \
  "v_exp_f32 v45, v45\n\t" \
  "v_mfma_f32_32x32x16_bf16 a[144:159], v[100:103], v[116:119], a[144:159]\n\t" \
  "ds_read_b128 v[100:103], v145 offset:27648\n\t" \
  "v_exp_f32 v46, v46\n\t" \
  "v_cvt_pk_bf16_f32 v110, v44, v45\n\t" \
  "v_exp_f32 v47, v47\n\t" \
  "v_smfmac_f32_16x16x64_bf16 a[160:163], v[84:87], v[112:119], v154\n\t" \
  "s_waitcnt vmcnt(21)\n\t" \
  "v_mfma_f32_16x16x32_bf16 a[68:71], v[120:123], a[216:219], a[68:71]\n\t" \
  "buffer_load_dwordx4 a[208:211], v150, s[80:83], s64 offen offset:0 sc1\n\t" \
  "v_cvt_pk_bf16_f32 v111, v46, v47\n\t" \
  "v_mfma_f32_16x16x32_bf16 a[68:71], v[124:127], a[220:223], a[68:71]\n\t" \
  "v_exp_f32 v48, v48\n\t" \
  "v_exp_f32 v49, v49\n\t" \
  "v_mfma_f32_16x16x32_bf16 a[76:79], v[128:131], a[216:219], a[76:79]\n\t" \
  "buffer_load_dwordx4 a[212:215], v150, s[80:83], s64 offen offset:2048 sc1\n\t" \
  "v_cvt_pk_bf16_f32 v112, v48, v49\n\t" \
  "v_mfma_f32_16x16x32_bf16 a[76:79], v[132:135], a[220:223], a[76:79]\n\t" \
  "v_exp_f32 v50, v50\n\t" \
  "v_exp_f32 v51, v51\n\t" \
  "v_mfma_f32_16x16x32_bf16 a[84:87], a[164:167], a[216:219], a[84:87]\n\t" \
  "buffer_load_dwordx4 v140, s[84:87], s68 offen offset:0 lds\n\t" \
  "v_cvt_pk_bf16_f32 v113, v50, v51\n\t" \
  "v_mfma_f32_16x16x32_bf16 a[84:87], a[168:171], a[220:223], a[84:87]\n\t" \
  "v_exp_f32 v52, v52\n\t" \
  "v_exp_f32 v53, v53\n\t" \
  "v_mfma_f32_16x16x32_bf16 a[92:95], a[172:175], a[216:219], a[92:95]\n\t" \
  "buffer_load_dwordx4 v141, s[84:87], s68 offen offset:1024 lds\n\t" \
  "v_cvt_pk_bf16_f32 v114, v52, v53\n\t" \
  "v_mfma_f32_16x16x32_bf16 a[92:95], v[156:159], a[220:223], a[92:95]\n\t" \
  "v_exp_f32 v54, v54\n\t" \
  "v_exp_f32 v55, v55\n\t" \
  "s_waitcnt lgkmcnt(1)\n\t" \
  "v_mfma_f32_32x32x16_bf16 v[32:47], v[88:91], v[64:67], v[0:15]\n\t" \
  "ds_read_b64_tr_b16 v[228:229], v146 offset:4096\n\t" \
  "v_exp_f32 v56, v56\n\t" \
  "v_cvt_pk_bf16_f32 v115, v54, v55\n\t" \
  "v_exp_f32 v57, v57\n\t" \
  "v_mfma_f32_32x32x16_bf16 v[32:47], v[92:95], v[68:71], v[32:47]\n\t" \
  "ds_read_b64_tr_b16 v[230:231], v146 offset:4608\n\t" \
  "v_exp_f32 v58, v58\n\t" \
  "v_cvt_pk_bf16_f32 v116, v56, v57\n\t" \
  "v_exp_f32 v59, v59\n\t" \
  "v_mfma_f32_32x32x16_bf16 a[128:143], v[96:99], v[104:107], a[128:143]\n\t" \
  "ds_read_b64_tr_b16 v[236:237], v147 offset:4096\n\t" \
  "v_exp_f32 v60, v60\n\t" \
  "v_cvt_pk_bf16_f32 v117, v58, v59\n\t" \
  "v_exp_f32 v61, v61\n\t" \
  "s_waitcnt lgkmcnt(3)\n\t" \
  "v_mfma_f32_32x32x16_bf16 a[128:143], v[100:103], v[108:111], a[128:143]\n\t" \
  "ds_read_b64_tr_b16 v[238:239], v147 offset:4608\n\t" \
  "v_exp_f32 v62, v62\n\t" \
  "v_cvt_pk_bf16_f32 v118, v60, v61\n\t" \
  "v_exp_f32 v63, v63\n\t" \
  "v_smfmac_f32_16x16x64_bf16 a[160:163], v[80:83], v[104:111], v154\n\t" \
  "s_waitcnt vmcnt(19)\n\t" \
  "s_barrier\n\t" \
  "v_mfma_f32_16x16x32_bf16 a[96:99], v[120:123], a[224:227], a[96:99]\n\t" \
  "buffer_load_dwordx4 a[216:219], v150, s[80:83], s64 offen offset:256 sc1\n\t" \
  "v_cvt_pk_bf16_f32 v119, v62, v63\n\t" \
  "v_mfma_f32_16x16x32_bf16 a[96:99], v[124:127], a[228:231], a[96:99]\n\t" \
  "v_exp_f32 v32, v32\n\t" \
  "v_exp_f32 v33, v33\n\t" \
  "v_mfma_f32_16x16x32_bf16 a[104:107], v[128:131], a[224:227], a[104:107]\n\t" \
  "buffer_load_dwordx4 a[220:223], v150, s[80:83], s64 offen offset:2304 sc1\n\t" \
  "v_cvt_pk_bf16_f32 v104, v32, v33\n\t" \
  "v_mfma_f32_16x16x32_bf16 a[104:107], v[132:135], a[228:231], a[104:107]\n\t" \
  "v_exp_f32 v34, v34\n\t" \
  "v_exp_f32 v35, v35\n\t" \
  "v_mfma_f32_16x16x32_bf16 a[112:115], a[164:167], a[224:227], a[112:115]\n\t" \
  "buffer_load_dwordx4 v142, s[84:87], s68 offen offset:2048 lds\n\t" \
  "v_cvt_pk_bf16_f32 v105, v34, v35\n\t" \
  "v_mfma_f32_16x16x32_bf16 a[112:115], a[168:171], a[228:231], a[112:115]\n\t" \
  "v_exp_f32 v36, v36\n\t" \
  "v_exp_f32 v37, v37\n\t" \
  "v_mfma_f32_16x16x32_bf16 a[120:123], a[172:175], a[224:227], a[120:123]\n\t" \
  "buffer_load_dwordx4 v143, s[84:87], s68 offen offset:3072 lds\n\t" \
  "v_cvt_pk_bf16_f32 v106, v36, v37\n\t" \
  "v_mfma_f32_16x16x32_bf16 a[120:123], v[156:159], a[228:231], a[120:123]\n\t" \
  "v_exp_f32 v38, v38\n\t" \
  "v_exp_f32 v39, v39\n\t" \
  "v_mfma_f32_32x32x16_bf16 v[48:63], v[88:91], v[72:75], v[16:31]\n\t" \
  "ds_read_b128 v[88:91], v145 offset:32768\n\t" \
  "v_exp_f32 v40, v40\n\t" \
  "v_cvt_pk_bf16_f32 v107, v38, v39\n\t" \
  "v_exp_f32 v41, v41\n\t" \
  "v_mfma_f32_32x32x16_bf16 v[48:63], v[92:95], v[76:79], v[48:63]\n\t" \
  "ds_read_b128 v[92:95], v145 offset:33792\n\t" \
  "v_exp_f32 v42, v42\n\t" \
  "v_cvt_pk_bf16_f32 v108, v40, v41\n\t" \
  "v_exp_f32 v43, v43\n\t" \
  "v_mfma_f32_32x32x16_bf16 a[144:159], v[96:99], v[112:115], a[144:159]\n\t" \
  "ds_read_b128 v[96:99], v145 offset:30720\n\t" \
  "v_exp_f32 v44, v44\n\t" \
  "v_cvt_pk_bf16_f32 v109, v42, v43\n\t" \
  "v_exp_f32 v45, v45\n\t" \
  "v_mfma_f32_32x32x16_bf16 a[144:159], v[100:103], v[116:119], a[144:159]\n\t" \
  "ds_read_b128 v[100:103], v145 offset:31744\n\t" \
  "v_exp_f32 v46, v46\n\t" \
  "v_cvt_pk_bf16_f32 v110, v44, v45\n\t" \
  "v_exp_f32 v47, v47\n\t" \
  "v_smfmac_f32_16x16x64_bf16 a[160:163], v[84:87], v[112:119], v154\n\t" \
  "s_add_u32 m0, s73, 0\n\t" \
  "s_mov_b32 s69, 147456\n\t" \
  "s_waitcnt vmcnt(21)\n\t" \
  "v_mfma_f32_16x16x32_bf16 a[100:103], v[120:123], a[232:235], a[100:103]\n\t" \
  "buffer_load_dwordx4 a[224:227], v151, s[80:83], s64 offen offset:0 sc1\n\t" \
  "v_cvt_pk_bf16_f32 v111, v46, v47\n\t" \
  "v_mfma_f32_16x16x32_bf16 a[100:103], v[124:127], a[236:239], a[100:103]\n\t" \
  "v_exp_f32 v48, v48\n\t" \
  "v_exp_f32 v49, v49\n\t" \
  "v_mfma_f32_16x16x32_bf16 a[108:111], v[128:131], a[232:235], a[108:111]\n\t" \
  "buffer_load_dwordx4 a[228:231], v151, s[80:83], s64 offen offset:2048 sc1\n\t" \
  "v_cvt_pk_bf16_f32 v112, v48, v49\n\t" \
  "v_mfma_f32_16x16x32_bf16 a[108:111], v[132:135], a[236:239], a[108:111]\n\t" \
  "v_exp_f32 v50, v50\n\t" \
  "v_exp_f32 v51, v51\n\t" \
  "v_mfma_f32_16x16x32_bf16 a[116:119], a[164:167], a[232:235], a[116:119]\n\t" \
  "buffer_load_dwordx4 v144, s[88:91], s69 offen offset:0 sc1 lds\n\t" \
  "v_cvt_pk_bf16_f32 v113, v50, v51\n\t" \
  "v_mfma_f32_16x16x32_bf16 a[116:119], a[168:171], a[236:239], a[116:119]\n\t" \
  "v_exp_f32 v52, v52\n\t" \
  "v_exp_f32 v53, v53\n\t" \
  "v_mfma_f32_16x16x32_bf16 a[124:127], a[172:175], a[232:235], a[124:127]\n\t" \
  "buffer_load_dwordx4 v144, s[88:91], s69 offen offset:1024 sc1 lds\n\t" \
  "v_cvt_pk_bf16_f32 v114, v52, v53\n\t" \
  "v_mfma_f32_16x16x32_bf16 a[124:127], v[156:159], a[236:239], a[124:127]\n\t" \
  "v_exp_f32 v54, v54\n\t" \
  "v_exp_f32 v55, v55\n\t" \
  "s_waitcnt lgkmcnt(1)\n\t" \
  "v_mfma_f32_32x32x16_bf16 v[32:47], v[88:91], v[64:67], v[0:15]\n\t" \
  "ds_read_b64_tr_b16 v[244:245], v155 offset:4096\n\t" \
  "v_exp_f32 v56, v56\n\t" \
  "v_cvt_pk_bf16_f32 v115, v54, v55\n\t" \
  "v_exp_f32 v57, v57\n\t" \
  "v_mfma_f32_32x32x16_bf16 v[32:47], v[92:95], v[68:71], v[32:47]\n\t" \
  "ds_read_b64_tr_b16 v[246:247], v155 offset:4608\n\t" \
  "v_exp_f32 v58, v58\n\t" \
  "v_cvt_pk_bf16_f32 v116, v56, v57\n\t" \
  "v_exp_f32 v59, v59\n\t" \
  "v_mfma_f32_32x32x16_bf16 a[128:143], v[96:99], v[104:107], a[128:143]\n\t" \
  "ds_read_b64_tr_b16 v[252:253], v137 offset:4096\n\t" \
  "v_exp_f32 v60, v60\n\t" \
  "v_cvt_pk_bf16_f32 v117, v58, v59\n\t" \
  "v_exp_f32 v61, v61\n\t" \
  "s_waitcnt lgkmcnt(3)\n\t" \
  "v_mfma_f32_32x32x16_bf16 a[128:143], v[100:103], v[108:111], a[128:143]\n\t" \
  "ds_read_b64_tr_b16 v[254:255], v137 offset:4608\n\t" \
  "v_exp_f32 v62, v62\n\t" \
  "v_cvt_pk_bf16_f32 v118, v60, v61\n\t" \
  "v_exp_f32 v63, v63\n\t" \
  "v_smfmac_f32_16x16x64_bf16 a[160:163], v[80:83], v[104:111], v154\n\t" \
  "buffer_load_dwordx4 v144, s[88:91], s69 offen offset:2048 sc1 lds\n\t" \
  "buffer_load_dwordx4 v144, s[88:91], s69 offen offset:3072 sc1 lds\n\t" \
  "s_waitcnt vmcnt(25) lgkmcnt(0)\n\t" \
  "v_mfma_f32_16x16x32_bf16 a[0:3], v[224:227], a[176:179], a[0:3]\n\t" \
  "buffer_load_dwordx4 a[232:235], v151, s[80:83], s64 offen offset:256 sc1\n\t" \
  "v_cvt_pk_bf16_f32 v119, v62, v63\n\t" \
  "v_mfma_f32_16x16x32_bf16 a[0:3], v[228:231], a[180:183], a[0:3]\n\t" \
  "v_exp_f32 v32, v32\n\t" \
  "v_exp_f32 v33, v33\n\t" \
  "v_mfma_f32_16x16x32_bf16 a[8:11], v[232:235], a[176:179], a[8:11]\n\t" \
  "buffer_load_dwordx4 a[236:239], v151, s[80:83], s64 offen offset:2304 sc1\n\t" \
  "v_cvt_pk_bf16_f32 v104, v32, v33\n\t" \
  "v_mfma_f32_16x16x32_bf16 a[8:11], v[236:239], a[180:183], a[8:11]\n\t" \
  "v_exp_f32 v34, v34\n\t" \
  "v_exp_f32 v35, v35\n\t" \
  "v_mfma_f32_16x16x32_bf16 a[16:19], v[240:243], a[176:179], a[16:19]\n\t" \
  "s_add_u32 m0, s72, 0\n\t" \
  "v_cvt_pk_bf16_f32 v105, v34, v35\n\t" \
  "v_mfma_f32_16x16x32_bf16 a[16:19], v[244:247], a[180:183], a[16:19]\n\t" \
  "v_exp_f32 v36, v36\n\t" \
  "v_exp_f32 v37, v37\n\t" \
  "v_mfma_f32_16x16x32_bf16 a[24:27], v[248:251], a[176:179], a[24:27]\n\t" \
  "s_mov_b32 s68, 786432\n\t" \
  "v_cvt_pk_bf16_f32 v106, v36, v37\n\t" \
  "v_mfma_f32_16x16x32_bf16 a[24:27], v[252:255], a[180:183], a[24:27]\n\t" \
  "v_exp_f32 v38, v38\n\t" \
  "v_exp_f32 v39, v39\n\t" \
  "v_mfma_f32_32x32x16_bf16 v[48:63], v[88:91], v[72:75], v[16:31]\n\t" \
  "ds_read_b128 v[88:91], v145 offset:36864\n\t" \
  "v_exp_f32 v40, v40\n\t" \
  "v_cvt_pk_bf16_f32 v107, v38, v39\n\t" \
  "v_exp_f32 v41, v41\n\t" \
  "v_mfma_f32_32x32x16_bf16 v[48:63], v[92:95], v[76:79], v[48:63]\n\t" \
  "ds_read_b128 v[92:95], v145 offset:37888\n\t" \
  "v_exp_f32 v42, v42\n\t" \
  "v_cvt_pk_bf16_f32 v108, v40, v41\n\t" \
  "v_exp_f32 v43, v43\n\t" \
  "v_mfma_f32_32x32x16_bf16 a[144:159], v[96:99], v[112:115], a[144:159]\n\t" \
  "ds_read_b128 v[96:99], v145 offset:34816\n\t" \
  "v_exp_f32 v44, v44\n\t" \
  "v_cvt_pk_bf16_f32 v109, v42, v43\n\t" \
  "v_exp_f32 v45, v45\n\t" \
  "v_mfma_f32_32x32x16_bf16 a[144:159], v[100:103], v[116:119], a[144:159]\n\t" \
  "ds_read_b128 v[100:103], v145 offset:35840\n\t" \
  "v_exp_f32 v46, v46\n\t" \
  "v_cvt_pk_bf16_f32 v110, v44, v45\n\t" \
  "v_exp_f32 v47, v47\n\t" \
  "v_smfmac_f32_16x16x64_bf16 a[160:163], v[84:87], v[112:119], v154\n\t" \
  "s_mov_b32 s64, 20480\n\t" \
  "s_waitcnt vmcnt(20)\n\t" \
  "v_mfma_f32_16x16x32_bf16 a[4:7], v[224:227], a[184:187], a[4:7]\n\t" \
  "buffer_load_dwordx4 a[176:179], v148, s[80:83], s64 offen offset:0 sc1\n\t" \
  "v_cvt_pk_bf16_f32 v111, v46, v47\n\t" \
  "v_mfma_f32_16x16x32_bf16 a[4:7], v[228:231], a[188:191], a[4:7]\n\t" \
  "v_exp_f32 v48, v48\n\t" \
  "v_exp_f32 v49, v49\n\t" \
  "v_mfma_f32_16x16x32_bf16 a[12:15], v[232:235], a[184:187], a[12:15]\n\t" \
  "buffer_load_dwordx4 a[180:183], v148, s[80:83], s64 offen offset:2048 sc1\n\t" \
  "v_cvt_pk_bf16_f32 v112, v48, v49\n\t" \
  "v_mfma_f32_16x16x32_bf16 a[12:15], v[236:239], a[188:191], a[12:15]\n\t" \
  "v_exp_f32 v50, v50\n\t" \
  "v_exp_f32 v51, v51\n\t" \
  "v_mfma_f32_16x16x32_bf16 a[20:23], v[240:243], a[184:187], a[20:23]\n\t" \
  "buffer_load_dwordx4 v140, s[84:87], s68 offen offset:0 lds\n\t" \
  "v_cvt_pk_bf16_f32 v113, v50, v51\n\t" \
  "v_mfma_f32_16x16x32_bf16 a[20:23], v[244:247], a[188:191], a[20:23]\n\t" \
  "v_exp_f32 v52, v52\n\t" \
  "v_exp_f32 v53, v53\n\t" \
  "v_mfma_f32_16x16x32_bf16 a[28:31], v[248:251], a[184:187], a[28:31]\n\t" \
  "buffer_load_dwordx4 v141, s[84:87], s68 offen offset:1024 lds\n\t" \
  "v_cvt_pk_bf16_f32 v114, v52, v53\n\t" \
  "v_mfma_f32_16x16x32_bf16 a[28:31], v[252:255], a[188:191], a[28:31]\n\t" \
  "v_exp_f32 v54, v54\n\t" \
  "v_exp_f32 v55, v55\n\t" \
  "s_waitcnt lgkmcnt(1)\n\t" \
  "v_mfma_f32_32x32x16_bf16 v[32:47], v[88:91], v[64:67], v[0:15]\n\t" \
  "ds_read_b64_tr_b16 v[120:121], v146 offset:8192\n\t" \
  "v_exp_f32 v56, v56\n\t" \
  "v_cvt_pk_bf16_f32 v115, v54, v55\n\t" \
  "v_exp_f32 v57, v57\n\t" \
  "v_mfma_f32_32x32x16_bf16 v[32:47], v[92:95], v[68:71], v[32:47]\n\t" \
  "ds_read_b64_tr_b16 v[122:123], v146 offset:8704\n\t" \
  "v_exp_f32 v58, v58\n\t" \
  "v_cvt_pk_bf16_f32 v116, v56, v57\n\t" \
  "v_exp_f32 v59, v59\n\t" \
  "v_mfma_f32_32x32x16_bf16 a[128:143], v[96:99], v[104:107], a[128:143]\n\t" \
  "ds_read_b64_tr_b16 v[128:129], v147 offset:8192\n\t" \
  "v_exp_f32 v60, v60\n\t" \
  "v_cvt_pk_bf16_f32 v117, v58, v59\n\t" \
  "v_exp_f32 v61, v61\n\t" \
  "s_waitcnt lgkmcnt(3)\n\t" \
  "v_mfma_f32_32x32x16_bf16 a[128:143], v[100:103], v[108:111], a[128:143]\n\t" \
  "ds_read_b64_tr_b16 v[130:131], v147 offset:8704\n\t" \
  "v_exp_f32 v62, v62\n\t" \
  "v_cvt_pk_bf16_f32 v118, v60, v61\n\t" \
  "v_exp_f32 v63, v63\n\t" \
  "v_smfmac_f32_16x16x64_bf16 a[160:163], v[80:83], v[104:111], v154\n\t" \
  "s_waitcnt vmcnt(22)\n\t" \
  "v_mfma_f32_16x16x32_bf16 a[32:35], v[224:227], a[192:195], a[32:35]\n\t" \
  "buffer_load_dwordx4 a[184:187], v148, s[80:83], s64 offen offset:256 sc1\n\t" \
  "v_cvt_pk_bf16_f32 v119, v62, v63\n\t" \
  "v_mfma_f32_16x16x32_bf16 a[32:35], v[228:231], a[196:199], a[32:35]\n\t" \
  "v_exp_f32 v32, v32\n\t" \
  "v_exp_f32 v33, v33\n\t" \
  "v_mfma_f32_16x16x32_bf16 a[40:43], v[232:235], a[192:195], a[40:43]\n\t" \
  "buffer_load_dwordx4 a[188:191], v148, s[80:83], s64 offen offset:2304 sc1\n\t" \
  "v_cvt_pk_bf16_f32 v104, v32, v33\n\t" \
  "v_mfma_f32_16x16x32_bf16 a[40:43], v[236:239], a[196:199], a[40:43]\n\t" \
  "v_exp_f32 v34, v34\n\t" \
  "v_exp_f32 v35, v35\n\t" \
  "v_mfma_f32_16x16x32_bf16 a[48:51], v[240:243], a[192:195], a[48:51]\n\t" \
  "buffer_load_dwordx4 v142, s[84:87], s68 offen offset:2048 lds\n\t" \
  "v_cvt_pk_bf16_f32 v105, v34, v35\n\t" \
  "v_mfma_f32_16x16x32_bf16 a[48:51], v[244:247], a[196:199], a[48:51]\n\t" \
  "v_exp_f32 v36, v36\n\t" \
  "v_exp_f32 v37, v37\n\t" \
  "v_mfma_f32_16x16x32_bf16 a[56:59], v[248:251], a[192:195], a[56:59]\n\t" \
  "buffer_load_dwordx4 v143, s[84:87], s68 offen offset:3072 lds\n\t" \
  "v_cvt_pk_bf16_f32 v106, v36, v37\n\t" \
  "v_mfma_f32_16x16x32_bf16 a[56:59], v[252:255], a[196:199], a[56:59]\n\t" \
  "v_exp_f32 v38, v38\n\t" \
  "v_exp_f32 v39, v39\n\t" \
  "v_mfma_f32_32x32x16_bf16 v[48:63], v[88:91], v[72:75], v[16:31]\n\t" \
  "ds_read_b128 v[88:91], v145 offset:40960\n\t" \
  "v_exp_f32 v40, v40\n\t" \
  "v_cvt_pk_bf16_f32 v107, v38, v39\n\t" \
  "v_exp_f32 v41, v41\n\t" \
  "v_mfma_f32_32x32x16_bf16 v[48:63], v[92:95], v[76:79], v[48:63]\n\t" \
  "ds_read_b128 v[92:95], v145 offset:41984\n\t" \
  "v_exp_f32 v42, v42\n\t" \
  "v_cvt_pk_bf16_f32 v108, v40, v41\n\t" \
  "v_exp_f32 v43, v43\n\t" \
  "v_mfma_f32_32x32x16_bf16 a[144:159], v[96:99], v[112:115], a[144:159]\n\t" \
  "ds_read_b128 v[96:99], v145 offset:38912\n\t" \
  "v_exp_f32 v44, v44\n\t" \
  "v_cvt_pk_bf16_f32 v109, v42, v43\n\t" \
  "v_exp_f32 v45, v45\n\t" \
  "v_mfma_f32_32x32x16_bf16 a[144:159], v[100:103], v[116:119], a[144:159]\n\t" \
  "ds_read_b128 v[100:103], v145 offset:39936\n\t" \
  "v_exp_f32 v46, v46\n\t" \
  "v_cvt_pk_bf16_f32 v110, v44, v45\n\t" \
  "v_exp_f32 v47, v47\n\t" \
  "v_smfmac_f32_16x16x64_bf16 a[160:163], v[84:87], v[112:119], v154\n\t" \
  "s_lshl_b32 s71, s75, 12\n\t" \
  "s_add_u32 s71, s71, s74\n\t" \
  "s_add_u32 m0, s71, 32768\n\t" \
  "s_mov_b32 s70, 0\n\t" \
  "v_xor_b32 v141, 32, v141\n\t" \
  "v_xor_b32 v143, 32, v143\n\t" \
  "s_waitcnt vmcnt(24)\n\t" \
  "v_mfma_f32_16x16x32_bf16 a[36:39], v[224:227], a[200:203], a[36:39]\n\t" \
  "buffer_load_dwordx4 a[192:195], v149, s[80:83], s64 offen offset:0 sc1\n\t" \
  "v_cvt_pk_bf16_f32 v111, v46, v47\n\t" \
  "v_mfma_f32_16x16x32_bf16 a[36:39], v[228:231], a[204:207], a[36:39]\n\t" \
  "v_exp_f32 v48, v48\n\t" \
  "v_exp_f32 v49, v49\n\t" \
  "v_mfma_f32_16x16x32_bf16 a[44:47], v[232:235], a[200:203], a[44:47]\n\t" \
  "buffer_load_dwordx4 a[196:199], v149, s[80:83], s64 offen offset:2048 sc1\n\t" \
  "v_cvt_pk_bf16_f32 v112, v48, v49\n\t" \
  "v_mfma_f32_16x16x32_bf16 a[44:47], v[236:239], a[204:207], a[44:47]\n\t" \
  "v_exp_f32 v50, v50\n\t" \
  "v_exp_f32 v51, v51\n\t" \
  "v_mfma_f32_16x16x32_bf16 a[52:55], v[240:243], a[200:203], a[52:55]\n\t" \
  "buffer_load_dwordx4 v140, s[92:95], s70 offen offset:0 lds\n\t" \
  "v_cvt_pk_bf16_f32 v113, v50, v51\n\t" \
  "v_mfma_f32_16x16x32_bf16 a[52:55], v[244:247], a[204:207], a[52:55]\n\t" \
  "v_exp_f32 v52, v52\n\t" \
  "v_exp_f32 v53, v53\n\t" \
  "v_mfma_f32_16x16x32_bf16 a[60:63], v[248:251], a[200:203], a[60:63]\n\t" \
  "buffer_load_dwordx4 v141, s[92:95], s70 offen offset:1024 lds\n\t" \
  "v_cvt_pk_bf16_f32 v114, v52, v53\n\t" \
  "v_mfma_f32_16x16x32_bf16 a[60:63], v[252:255], a[204:207], a[60:63]\n\t" \
  "v_exp_f32 v54, v54\n\t" \
  "v_exp_f32 v55, v55\n\t" \
  "s_waitcnt lgkmcnt(1)\n\t" \
  "v_mfma_f32_32x32x16_bf16 v[32:47], v[88:91], v[64:67], v[0:15]\n\t" \
  "ds_read_b64_tr_b16 a[164:165], v155 offset:8192\n\t" \
  "v_exp_f32 v56, v56\n\t" \
  "v_cvt_pk_bf16_f32 v115, v54, v55\n\t" \
  "v_exp_f32 v57, v57\n\t" \
  "v_mfma_f32_32x32x16_bf16 v[32:47], v[92:95], v[68:71], v[32:47]\n\t" \
  "ds_read_b64_tr_b16 a[166:167], v155 offset:8704\n\t" \
  "v_exp_f32 v58, v58\n\t" \
  "v_cvt_pk_bf16_f32 v116, v56, v57\n\t" \
  "v_exp_f32 v59, v59\n\t" \
  "v_mfma_f32_32x32x16_bf16 a[128:143], v[96:99], v[104:107], a[128:143]\n\t" \
  "ds_read_b64_tr_b16 a[172:173], v137 offset:8192\n\t" \
  "v_exp_f32 v60, v60\n\t" \
  "v_cvt_pk_bf16_f32 v117, v58, v59\n\t" \
  "v_exp_f32 v61, v61\n\t" \
  "s_waitcnt lgkmcnt(3)\n\t" \
  "v_mfma_f32_32x32x16_bf16 a[128:143], v[100:103], v[108:111], a[128:143]\n\t" \
  "ds_read_b64_tr_b16 a[174:175], v137 offset:8704\n\t" \
  "v_exp_f32 v62, v62\n\t" \
  "v_cvt_pk_bf16_f32 v118, v60, v61\n\t" \
  "v_exp_f32 v63, v63\n\t" \
  "v_smfmac_f32_16x16x64_bf16 a[160:163], v[80:83], v[104:111], v154\n\t" \
  "buffer_load_dwordx4 v142, s[92:95], s70 offen offset:2048 lds\n\t" \
  "buffer_load_dwordx4 v143, s[92:95], s70 offen offset:3072 lds\n\t" \
  "v_xor_b32 v141, 32, v141\n\t" \
  "v_xor_b32 v143, 32, v143\n\t" \
  "s_waitcnt vmcnt(28)\n\t" \
  "v_mfma_f32_16x16x32_bf16 a[64:67], v[224:227], a[208:211], a[64:67]\n\t" \
  "buffer_load_dwordx4 a[200:203], v149, s[80:83], s64 offen offset:256 sc1\n\t" \
  "v_cvt_pk_bf16_f32 v119, v62, v63\n\t" \
  "v_mfma_f32_16x16x32_bf16 a[64:67], v[228:231], a[212:215], a[64:67]\n\t" \
  "v_exp_f32 v32, v32\n\t" \
  "v_exp_f32 v33, v33\n\t" \
  "v_mfma_f32_16x16x32_bf16 a[72:75], v[232:235], a[208:211], a[72:75]\n\t" \
  "buffer_load_dwordx4 a[204:207], v149, s[80:83], s64 offen offset:2304 sc1\n\t" \
  "v_cvt_pk_bf16_f32 v104, v32, v33\n\t" \
  "v_mfma_f32_16x16x32_bf16 a[72:75], v[236:239], a[212:215], a[72:75]\n\t" \
  "v_exp_f32 v34, v34\n\t" \
  "v_exp_f32 v35, v35\n\t" \
  "v_mfma_f32_16x16x32_bf16 a[80:83], v[240:243], a[208:211], a[80:83]\n\t" \
  "s_add_u32 m0, s72, 4096\n\t" \
  "v_cvt_pk_bf16_f32 v105, v34, v35\n\t" \
  "v_mfma_f32_16x16x32_bf16 a[80:83], v[244:247], a[212:215], a[80:83]\n\t" \
  "v_exp_f32 v36, v36\n\t" \
  "v_exp_f32 v37, v37\n\t" \
  "v_mfma_f32_16x16x32_bf16 a[88:91], v[248:251], a[208:211], a[88:91]\n\t" \
  "s_mov_b32 s68, 851968\n\t" \
  "v_cvt_pk_bf16_f32 v106, v36, v37\n\t" \
  "v_mfma_f32_16x16x32_bf16 a[88:91], v[252:255], a[212:215], a[88:91]\n\t" \
  "v_exp_f32 v38, v38\n\t" \
  "v_exp_f32 v39, v39\n\t" \
  "v_mfma_f32_32x32x16_bf16 v[48:63], v[88:91], v[72:75], v[16:31]\n\t" \
  "ds_read_b128 v[88:91], v145 offset:45056\n\t" \
  "v_exp_f32 v40, v40\n\t" \
  "v_cvt_pk_bf16_f32 v107, v38, v39\n\t" \
  "v_exp_f32 v41, v41\n\t" \
  "v_mfma_f32_32x32x16_bf16 v[48:63], v[92:95], v[76:79], v[48:63]\n\t" \
  "ds_read_b128 v[92:95], v145 offset:46080\n\t" \
  "v_exp_f32 v42, v42\n\t" \
  "v_cvt_pk_bf16_f32 v108, v40, v41\n\t" \
  "v_exp_f32 v43, v43\n\t" \
  "v_mfma_f32_32x32x16_bf16 a[144:159], v[96:99], v[112:115], a[144:159]\n\t" \
  "ds_read_b128 v[96:99], v145 offset:43008\n\t" \
  "v_exp_f32 v44, v44\n\t" \
  "v_cvt_pk_bf16_f32 v109, v42, v43\n\t" \
  "v_exp_f32 v45, v45\n\t" \
  "v_mfma_f32_32x32x16_bf16 a[144:159], v[100:103], v[116:119], a[144:159]\n\t" \
  "ds_read_b128 v[100:103], v145 offset:44032\n\t" \
  "v_exp_f32 v46, v46\n\t" \
  "v_cvt_pk_bf16_f32 v110, v44, v45\n\t" \
  "v_exp_f32 v47, v47\n\t" \
  "v_smfmac_f32_16x16x64_bf16 a[160:163], v[84:87], v[112:119], v154\n\t" \
  "s_waitcnt vmcnt(24)\n\t" \
  "v_mfma_f32_16x16x32_bf16 a[68:71], v[224:227], a[216:219], a[68:71]\n\t" \
  "buffer_load_dwordx4 a[208:211], v150, s[80:83], s64 offen offset:0 sc1\n\t" \
  "v_cvt_pk_bf16_f32 v111, v46, v47\n\t" \
  "v_mfma_f32_16x16x32_bf16 a[68:71], v[228:231], a[220:223], a[68:71]\n\t" \
  "v_exp_f32 v48, v48\n\t" \
  "v_exp_f32 v49, v49\n\t" \
  "v_mfma_f32_16x16x32_bf16 a[76:79], v[232:235], a[216:219], a[76:79]\n\t" \
  "buffer_load_dwordx4 a[212:215], v150, s[80:83], s64 offen offset:2048 sc1\n\t" \
  "v_cvt_pk_bf16_f32 v112, v48, v49\n\t" \
  "v_mfma_f32_16x16x32_bf16 a[76:79], v[236:239], a[220:223], a[76:79]\n\t" \
  "v_exp_f32 v50, v50\n\t" \
  "v_exp_f32 v51, v51\n\t" \
  "v_mfma_f32_16x16x32_bf16 a[84:87], v[240:243], a[216:219], a[84:87]\n\t" \
  "buffer_load_dwordx4 v140, s[84:87], s68 offen offset:0 lds\n\t" \
  "v_cvt_pk_bf16_f32 v113, v50, v51\n\t" \
  "v_mfma_f32_16x16x32_bf16 a[84:87], v[244:247], a[220:223], a[84:87]\n\t" \
  "v_exp_f32 v52, v52\n\t" \
  "v_exp_f32 v53, v53\n\t" \
  "v_mfma_f32_16x16x32_bf16 a[92:95], v[248:251], a[216:219], a[92:95]\n\t" \
  "buffer_load_dwordx4 v141, s[84:87], s68 offen offset:1024 lds\n\t" \
  "v_cvt_pk_bf16_f32 v114, v52, v53\n\t" \
  "v_mfma_f32_16x16x32_bf16 a[92:95], v[252:255], a[220:223], a[92:95]\n\t" \
  "v_exp_f32 v54, v54\n\t" \
  "v_exp_f32 v55, v55\n\t" \
  "s_waitcnt lgkmcnt(1)\n\t" \
  "v_mfma_f32_32x32x16_bf16 v[32:47], v[88:91], v[64:67], v[0:15]\n\t" \
  "ds_read_b64_tr_b16 v[124:125], v146 offset:12288\n\t" \
  "v_exp_f32 v56, v56\n\t" \
  "v_cvt_pk_bf16_f32 v115, v54, v55\n\t" \
  "v_exp_f32 v57, v57\n\t" \
  "v_mfma_f32_32x32x16_bf16 v[32:47], v[92:95], v[68:71], v[32:47]\n\t" \
  "ds_read_b64_tr_b16 v[126:127], v146 offset:12800\n\t" \
  "v_exp_f32 v58, v58\n\t" \
  "v_cvt_pk_bf16_f32 v116, v56, v57\n\t" \
  "v_exp_f32 v59, v59\n\t" \
  "v_mfma_f32_32x32x16_bf16 a[128:143], v[96:99], v[104:107], a[128:143]\n\t" \
  "ds_read_b64_tr_b16 v[132:133], v147 offset:12288\n\t" \
  "v_exp_f32 v60, v60\n\t" \
  "v_cvt_pk_bf16_f32 v117, v58, v59\n\t" \
  "v_exp_f32 v61, v61\n\t" \
  "s_waitcnt lgkmcnt(3)\n\t" \
  "v_mfma_f32_32x32x16_bf16 a[128:143], v[100:103], v[108:111], a[128:143]\n\t" \
  "ds_read_b64_tr_b16 v[134:135], v147 offset:12800\n\t" \
  "v_exp_f32 v62, v62\n\t" \
  "v_cvt_pk_bf16_f32 v118, v60, v61\n\t" \
  "v_exp_f32 v63, v63\n\t" \
  "v_smfmac_f32_16x16x64_bf16 a[160:163], v[80:83], v[104:111], v154\n\t" \
  "s_waitcnt vmcnt(22)\n\t" \
  "s_barrier\n\t" \
  "v_mfma_f32_16x16x32_bf16 a[96:99], v[224:227], a[224:227], a[96:99]\n\t" \
  "buffer_load_dwordx4 a[216:219], v150, s[80:83], s64 offen offset:256 sc1\n\t" \
  "v_cvt_pk_bf16_f32 v119, v62, v63\n\t" \
  "v_mfma_f32_16x16x32_bf16 a[96:99], v[228:231], a[228:231], a[96:99]\n\t" \
  "v_exp_f32 v32, v32\n\t" \
  "v_exp_f32 v33, v33\n\t" \
  "v_mfma_f32_16x16x32_bf16 a[104:107], v[232:235], a[224:227], a[104:107]\n\t" \
  "buffer_load_dwordx4 a[220:223], v150, s[80:83], s64 offen offset:2304 sc1\n\t" \
  "v_cvt_pk_bf16_f32 v104, v32, v33\n\t" \
  "v_mfma_f32_16x16x32_bf16 a[104:107], v[236:239], a[228:231], a[104:107]\n\t" \
  "v_exp_f32 v34, v34\n\t" \
  "v_exp_f32 v35, v35\n\t" \
  "v_mfma_f32_16x16x32_bf16 a[112:115], v[240:243], a[224:227], a[112:115]\n\t" \
  "buffer_load_dwordx4 v142, s[84:87], s68 offen offset:2048 lds\n\t" \
  "v_cvt_pk_bf16_f32 v105, v34, v35\n\t" \
  "v_mfma_f32_16x16x32_bf16 a[112:115], v[244:247], a[228:231], a[112:115]\n\t" \
  "v_exp_f32 v36, v36\n\t" \
  "v_exp_f32 v37, v37\n\t" \
  "v_mfma_f32_16x16x32_bf16 a[120:123], v[248:251], a[224:227], a[120:123]\n\t" \
  "buffer_load_dwordx4 v143, s[84:87], s68 offen offset:3072 lds\n\t" \
  "v_cvt_pk_bf16_f32 v106, v36, v37\n\t" \
  "v_mfma_f32_16x16x32_bf16 a[120:123], v[252:255], a[228:231], a[120:123]\n\t" \
  "v_exp_f32 v38, v38\n\t" \
  "v_exp_f32 v39, v39\n\t" \
  "v_mfma_f32_32x32x16_bf16 v[48:63], v[88:91], v[72:75], v[16:31]\n\t" \
  "ds_read_b128 v[88:91], v145 offset:0\n\t" \
  "v_exp_f32 v40, v40\n\t" \
  "v_cvt_pk_bf16_f32 v107, v38, v39\n\t" \
  "v_exp_f32 v41, v41\n\t" \
  "v_mfma_f32_32x32x16_bf16 v[48:63], v[92:95], v[76:79], v[48:63]\n\t" \
  "ds_read_b128 v[92:95], v145 offset:1024\n\t" \
  "v_exp_f32 v42, v42\n\t" \
  "v_cvt_pk_bf16_f32 v108, v40, v41\n\t" \
  "v_exp_f32 v43, v43\n\t" \
  "v_mfma_f32_32x32x16_bf16 a[144:159], v[96:99], v[112:115], a[144:159]\n\t" \
  "ds_read_b128 v[96:99], v145 offset:47104\n\t" \
  "v_exp_f32 v44, v44\n\t" \
  "v_cvt_pk_bf16_f32 v109, v42, v43\n\t" \
  "v_exp_f32 v45, v45\n\t" \
  "v_mfma_f32_32x32x16_bf16 a[144:159], v[100:103], v[116:119], a[144:159]\n\t" \
  "ds_read_b128 v[100:103], v145 offset:48128\n\t" \
  "v_exp_f32 v46, v46\n\t" \
  "v_cvt_pk_bf16_f32 v110, v44, v45\n\t" \
  "v_exp_f32 v47, v47\n\t" \
  "v_smfmac_f32_16x16x64_bf16 a[160:163], v[84:87], v[112:119], v154\n\t" \
  "s_add_u32 m0, s73, 16384\n\t" \
  "s_mov_b32 s69, 163840\n\t" \
  "s_waitcnt vmcnt(24)\n\t" \
  "v_mfma_f32_16x16x32_bf16 a[100:103], v[224:227], a[232:235], a[100:103]\n\t" \
  "buffer_load_dwordx4 a[224:227], v151, s[80:83], s64 offen offset:0 sc1\n\t" \
  "v_cvt_pk_bf16_f32 v111, v46, v47\n\t" \
  "v_mfma_f32_16x16x32_bf16 a[100:103], v[228:231], a[236:239], a[100:103]\n\t" \
  "v_exp_f32 v48, v48\n\t" \
  "v_exp_f32 v49, v49\n\t" \
  "v_mfma_f32_16x16x32_bf16 a[108:111], v[232:235], a[232:235], a[108:111]\n\t" \
  "buffer_load_dwordx4 a[228:231], v151, s[80:83], s64 offen offset:2048 sc1\n\t" \
  "v_cvt_pk_bf16_f32 v112, v48, v49\n\t" \
  "v_mfma_f32_16x16x32_bf16 a[108:111], v[236:239], a[236:239], a[108:111]\n\t" \
  "v_exp_f32 v50, v50\n\t" \
  "v_exp_f32 v51, v51\n\t" \
  "v_mfma_f32_16x16x32_bf16 a[116:119], v[240:243], a[232:235], a[116:119]\n\t" \
  "buffer_load_dwordx4 v144, s[88:91], s69 offen offset:0 sc1 lds\n\t" \
  "v_cvt_pk_bf16_f32 v113, v50, v51\n\t" \
  "v_mfma_f32_16x16x32_bf16 a[116:119], v[244:247], a[236:239], a[116:119]\n\t" \
  "v_exp_f32 v52, v52\n\t" \
  "v_exp_f32 v53, v53\n\t" \
  "v_mfma_f32_16x16x32_bf16 a[124:127], v[248:251], a[232:235], a[124:127]\n\t" \
  "buffer_load_dwordx4 v144, s[88:91], s69 offen offset:1024 sc1 lds\n\t" \
  "v_cvt_pk_bf16_f32 v114, v52, v53\n\t" \
  "v_mfma_f32_16x16x32_bf16 a[124:127], v[252:255], a[236:239], a[124:127]\n\t" \
  "v_exp_f32 v54, v54\n\t" \
  "v_exp_f32 v55, v55\n\t" \
  "s_waitcnt lgkmcnt(1)\n\t" \
  "v_mfma_f32_32x32x16_bf16 v[32:47], v[88:91], v[64:67], v[0:15]\n\t" \
  "ds_read_b64_tr_b16 a[168:169], v155 offset:12288\n\t" \
  "v_exp_f32 v56, v56\n\t" \
  "v_cvt_pk_bf16_f32 v115, v54, v55\n\t" \
  "v_exp_f32 v57, v57\n\t" \
  "v_mfma_f32_32x32x16_bf16 v[32:47], v[92:95], v[68:71], v[32:47]\n\t" \
  "ds_read_b64_tr_b16 a[170:171], v155 offset:12800\n\t" \
  "v_exp_f32 v58, v58\n\t" \
  "v_cvt_pk_bf16_f32 v116, v56, v57\n\t" \
  "v_exp_f32 v59, v59\n\t" \
  "v_mfma_f32_32x32x16_bf16 a[128:143], v[96:99], v[104:107], a[128:143]\n\t" \
  "ds_read_b64_tr_b16 v[156:157], v137 offset:12288\n\t" \
  "v_exp_f32 v60, v60\n\t" \
  "v_cvt_pk_bf16_f32 v117, v58, v59\n\t" \
  "v_exp_f32 v61, v61\n\t" \
  "s_waitcnt lgkmcnt(3)\n\t" \
  "v_mfma_f32_32x32x16_bf16 a[128:143], v[100:103], v[108:111], a[128:143]\n\t" \
  "ds_read_b64_tr_b16 v[158:159], v137 offset:12800\n\t" \
  "v_exp_f32 v62, v62\n\t" \
  "v_cvt_pk_bf16_f32 v118, v60, v61\n\t" \
  "v_exp_f32 v63, v63\n\t" \
  "v_smfmac_f32_16x16x64_bf16 a[160:163], v[80:83], v[104:111], v154\n\t" \
  "buffer_load_dwordx4 v144, s[88:91], s69 offen offset:2048 sc1 lds\n\t" \
  "buffer_load_dwordx4 v144, s[88:91], s69 offen offset:3072 sc1 lds\n\t" \
  "s_waitcnt vmcnt(28) lgkmcnt(0)\n\t" \
  "v_mfma_f32_16x16x32_bf16 a[0:3], v[120:123], a[176:179], a[0:3]\n\t" \
  "buffer_load_dwordx4 a[232:235], v151, s[80:83], s64 offen offset:256 sc1\n\t" \
  "v_cvt_pk_bf16_f32 v119, v62, v63\n\t" \
  "v_mfma_f32_16x16x32_bf16 a[0:3], v[124:127], a[180:183], a[0:3]\n\t" \
  "v_exp_f32 v32, v32\n\t" \
  "v_exp_f32 v33, v33\n\t" \
  "v_mfma_f32_16x16x32_bf16 a[8:11], v[128:131], a[176:179], a[8:11]\n\t" \
  "buffer_load_dwordx4 a[236:239], v151, s[80:83], s64 offen offset:2304 sc1\n\t" \
  "v_cvt_pk_bf16_f32 v104, v32, v33\n\t" \
  "v_mfma_f32_16x16x32_bf16 a[8:11], v[132:135], a[180:183], a[8:11]\n\t" \
  "v_exp_f32 v34, v34\n\t" \
  "v_exp_f32 v35, v35\n\t" \
  "v_mfma_f32_16x16x32_bf16 a[16:19], a[164:167], a[176:179], a[16:19]\n\t" \
  "s_add_u32 m0, s72, 8192\n\t" \
  "v_cvt_pk_bf16_f32 v105, v34, v35\n\t" \
  "v_mfma_f32_16x16x32_bf16 a[16:19], a[168:171], a[180:183], a[16:19]\n\t" \
  "v_exp_f32 v36, v36\n\t" \
  "v_exp_f32 v37, v37\n\t" \
  "v_mfma_f32_16x16x32_bf16 a[24:27], a[172:175], a[176:179], a[24:27]\n\t" \
  "s_mov_b32 s68, 917504\n\t" \
  "v_cvt_pk_bf16_f32 v106, v36, v37\n\t" \
  "v_mfma_f32_16x16x32_bf16 a[24:27], v[156:159], a[180:183], a[24:27]\n\t" \
  "v_exp_f32 v38, v38\n\t" \
  "v_exp_f32 v39, v39\n\t" \
  "v_mfma_f32_32x32x16_bf16 v[48:63], v[88:91], v[72:75], v[16:31]\n\t" \
  "ds_read_b128 v[88:91], v145 offset:4096\n\t" \
  "v_exp_f32 v40, v40\n\t" \
  "v_cvt_pk_bf16_f32 v107, v38, v39\n\t" \
  "v_exp_f32 v41, v41\n\t" \
  "v_mfma_f32_32x32x16_bf16 v[48:63], v[92:95], v[76:79], v[48:63]\n\t" \
  "ds_read_b128 v[92:95], v145 offset:5120\n\t" \
  "v_exp_f32 v42, v42\n\t" \
  "v_cvt_pk_bf16_f32 v108, v40, v41\n\t" \
  "v_exp_f32 v43, v43\n\t" \
  "v_mfma_f32_32x32x16_bf16 a[144:159], v[96:99], v[112:115], a[144:159]\n\t" \
  "ds_read_b128 v[96:99], v145 offset:2048\n\t" \
  "v_exp_f32 v44, v44\n\t" \
  "v_cvt_pk_bf16_f32 v109, v42, v43\n\t" \
  "v_exp_f32 v45, v45\n\t" \
  "v_mfma_f32_32x32x16_bf16 a[144:159], v[100:103], v[116:119], a[144:159]\n\t" \
  "ds_read_b128 v[100:103], v145 offset:3072\n\t" \
  "v_exp_f32 v46, v46\n\t" \
  "v_cvt_pk_bf16_f32 v110, v44, v45\n\t" \
  "v_exp_f32 v47, v47\n\t" \
  "v_smfmac_f32_16x16x64_bf16 a[160:163], v[84:87], v[112:119], v154\n\t" \
  "s_mov_b32 s64, 24576\n\t" \
  "s_waitcnt vmcnt(24)\n\t" \
  "v_mfma_f32_16x16x32_bf16 a[4:7], v[120:123], a[184:187], a[4:7]\n\t" \
  "buffer_load_dwordx4 a[176:179], v148, s[80:83], s64 offen offset:0 sc1\n\t" \
  "v_cvt_pk_bf16_f32 v111, v46, v47\n\t" \
  "v_mfma_f32_16x16x32_bf16 a[4:7], v[124:127], a[188:191], a[4:7]\n\t" \
  "v_exp_f32 v48, v48\n\t" \
  "v_exp_f32 v49, v49\n\t" \
  "v_mfma_f32_16x16x32_bf16 a[12:15], v[128:131], a[184:187], a[12:15]\n\t" \
  "buffer_load_dwordx4 a[180:183], v148, s[80:83], s64 offen offset:2048 sc1\n\t" \
  "v_cvt_pk_bf16_f32 v112, v48, v49\n\t" \
  "v_mfma_f32_16x16x32_bf16 a[12:15], v[132:135], a[188:191], a[12:15]\n\t" \
  "v_exp_f32 v50, v50\n\t" \
  "v_exp_f32 v51, v51\n\t" \
  "v_mfma_f32_16x16x32_bf16 a[20:23], a[164:167], a[184:187], a[20:23]\n\t" \
  "buffer_load_dwordx4 v140, s[84:87], s68 offen offset:0 lds\n\t" \
  "v_cvt_pk_bf16_f32 v113, v50, v51\n\t" \
  "v_mfma_f32_16x16x32_bf16 a[20:23], a[168:171], a[188:191], a[20:23]\n\t" \
  "v_exp_f32 v52, v52\n\t" \
  "v_exp_f32 v53, v53\n\t" \
  "v_mfma_f32_16x16x32_bf16 a[28:31], a[172:175], a[184:187], a[28:31]\n\t" \
  "buffer_load_dwordx4 v141, s[84:87], s68 offen offset:1024 lds\n\t" \
  "v_cvt_pk_bf16_f32 v114, v52, v53\n\t" \
  "v_mfma_f32_16x16x32_bf16 a[28:31], v[156:159], a[188:191], a[28:31]\n\t" \
  "v_exp_f32 v54, v54\n\t" \
  "v_exp_f32 v55, v55\n\t" \
  "s_waitcnt lgkmcnt(1)\n\t" \
  "v_mfma_f32_32x32x16_bf16 v[32:47], v[88:91], v[64:67], v[0:15]\n\t" \
  "ds_read_b64_tr_b16 v[224:225], v146 offset:0\n\t" \
  "v_exp_f32 v56, v56\n\t" \
  "v_cvt_pk_bf16_f32 v115, v54, v55\n\t" \
  "v_exp_f32 v57, v57\n\t" \
  "v_mfma_f32_32x32x16_bf16 v[32:47], v[92:95], v[68:71], v[32:47]\n\t" \
  "ds_read_b64_tr_b16 v[226:227], v146 offset:512\n\t" \
  "v_exp_f32 v58, v58\n\t" \
  "v_cvt_pk_bf16_f32 v116, v56, v57\n\t" \
  "v_exp_f32 v59, v59\n\t" \
  "v_mfma_f32_32x32x16_bf16 a[128:143], v[96:99], v[104:107], a[128:143]\n\t" \
  "ds_read_b64_tr_b16 v[232:233], v147 offset:0\n\t" \
  "v_exp_f32 v60, v60\n\t" \
  "v_cvt_pk_bf16_f32 v117, v58, v59\n\t" \
  "v_exp_f32 v61, v61\n\t" \
  "s_waitcnt lgkmcnt(3)\n\t" \
  "v_mfma_f32_32x32x16_bf16 a[128:143], v[100:103], v[108:111], a[128:143]\n\t" \
  "ds_read_b64_tr_b16 v[234:235], v147 offset:512\n\t" \
  "v_exp_f32 v62, v62\n\t" \
  "v_cvt_pk_bf16_f32 v118, v60, v61\n\t" \
  "v_exp_f32 v63, v63\n\t" \
  "v_smfmac_f32_16x16x64_bf16 a[160:163], v[80:83], v[104:111], v154\n\t" \
  "s_waitcnt vmcnt(26)\n\t" \
  "v_mfma_f32_16x16x32_bf16 a[32:35], v[120:123], a[192:195], a[32:35]\n\t" \
  "buffer_load_dwordx4 a[184:187], v148, s[80:83], s64 offen offset:256 sc1\n\t" \
  "v_cvt_pk_bf16_f32 v119, v62, v63\n\t" \
  "v_mfma_f32_16x16x32_bf16 a[32:35], v[124:127], a[196:199], a[32:35]\n\t" \
  "v_exp_f32 v32, v32\n\t" \
  "v_exp_f32 v33, v33\n\t" \
  "v_mfma_f32_16x16x32_bf16 a[40:43], v[128:131], a[192:195], a[40:43]\n\t" \
  "buffer_load_dwordx4 a[188:191], v148, s[80:83], s64 offen offset:2304 sc1\n\t" \
  "v_cvt_pk_bf16_f32 v104, v32, v33\n\t" \
  "v_mfma_f32_16x16x32_bf16 a[40:43], v[132:135], a[196:199], a[40:43]\n\t" \
  "v_exp_f32 v34, v34\n\t" \
  "v_exp_f32 v35, v35\n\t" \
  "v_mfma_f32_16x16x32_bf16 a[48:51], a[164:167], a[192:195], a[48:51]\n\t" \
  "buffer_load_dwordx4 v142, s[84:87], s68 offen offset:2048 lds\n\t" \
  "v_cvt_pk_bf16_f32 v105, v34, v35\n\t" \
  "v_mfma_f32_16x16x32_bf16 a[48:51], a[168:171], a[196:199], a[48:51]\n\t" \
  "v_exp_f32 v36, v36\n\t" \
  "v_exp_f32 v37, v37\n\t" \
  "v_mfma_f32_16x16x32_bf16 a[56:59], a[172:175], a[192:195], a[56:59]\n\t" \
  "buffer_load_dwordx4 v143, s[84:87], s68 offen offset:3072 lds\n\t" \
  "v_cvt_pk_bf16_f32 v106, v36, v37\n\t" \
  "v_mfma_f32_16x16x32_bf16 a[56:59], v[156:159], a[196:199], a[56:59]\n\t" \
  "v_exp_f32 v38, v38\n\t" \
  "v_exp_f32 v39, v39\n\t" \
  "v_mfma_f32_32x32x16_bf16 v[48:63], v[88:91], v[72:75], v[16:31]\n\t" \
  "ds_read_b128 v[88:91], v145 offset:8192\n\t" \
  "v_exp_f32 v40, v40\n\t" \
  "v_cvt_pk_bf16_f32 v107, v38, v39\n\t" \
  "v_exp_f32 v41, v41\n\t" \
  "v_mfma_f32_32x32x16_bf16 v[48:63], v[92:95], v[76:79], v[48:63]\n\t" \
  "ds_read_b128 v[92:95], v145 offset:9216\n\t" \
  "v_exp_f32 v42, v42\n\t" \
  "v_cvt_pk_bf16_f32 v108, v40, v41\n\t" \
  "v_exp_f32 v43, v43\n\t" \
  "v_mfma_f32_32x32x16_bf16 a[144:159], v[96:99], v[112:115], a[144:159]\n\t" \
  "ds_read_b128 v[96:99], v145 offset:6144\n\t" \
  "v_exp_f32 v44, v44\n\t" \
  "v_cvt_pk_bf16_f32 v109, v42, v43\n\t" \
  "v_exp_f32 v45, v45\n\t" \
  "v_mfma_f32_32x32x16_bf16 a[144:159], v[100:103], v[116:119], a[144:159]\n\t" \
  "ds_read_b128 v[100:103], v145 offset:7168\n\t" \
  "v_exp_f32 v46, v46\n\t" \
  "v_cvt_pk_bf16_f32 v110, v44, v45\n\t" \
  "v_exp_f32 v47, v47\n\t" \
  "v_smfmac_f32_16x16x64_bf16 a[160:163], v[84:87], v[112:119], v154\n\t" \
  "s_lshl_b32 s71, s75, 12\n\t" \
  "s_add_u32 s71, s71, s74\n\t" \
  "s_add_u32 m0, s71, 49152\n\t" \
  "s_mov_b32 s70, 262144\n\t" \
  "v_xor_b32 v141, 32, v141\n\t" \
  "v_xor_b32 v143, 32, v143\n\t" \
  "s_waitcnt vmcnt(24)\n\t" \
  "v_mfma_f32_16x16x32_bf16 a[36:39], v[120:123], a[200:203], a[36:39]\n\t" \
  "buffer_load_dwordx4 a[192:195], v149, s[80:83], s64 offen offset:0 sc1\n\t" \
  "v_cvt_pk_bf16_f32 v111, v46, v47\n\t" \
  "v_mfma_f32_16x16x32_bf16 a[36:39], v[124:127], a[204:207], a[36:39]\n\t" \
  "v_exp_f32 v48, v48\n\t" \
  "v_exp_f32 v49, v49\n\t" \
  "v_mfma_f32_16x16x32_bf16 a[44:47], v[128:131], a[200:203], a[44:47]\n\t" \
  "buffer_load_dwordx4 a[196:199], v149, s[80:83], s64 offen offset:2048 sc1\n\t" \
  "v_cvt_pk_bf16_f32 v112, v48, v49\n\t" \
  "v_mfma_f32_16x16x32_bf16 a[44:47], v[132:135], a[204:207], a[44:47]\n\t" \
  "v_exp_f32 v50, v50\n\t" \
  "v_exp_f32 v51, v51\n\t" \
  "v_mfma_f32_16x16x32_bf16 a[52:55], a[164:167], a[200:203], a[52:55]\n\t" \
  "buffer_load_dwordx4 v140, s[92:95], s70 offen offset:0 lds\n\t" \
  "v_cvt_pk_bf16_f32 v113, v50, v51\n\t" \
  "v_mfma_f32_16x16x32_bf16 a[52:55], a[168:171], a[204:207], a[52:55]\n\t" \
  "v_exp_f32 v52, v52\n\t" \
  "v_exp_f32 v53, v53\n\t" \
  "v_mfma_f32_16x16x32_bf16 a[60:63], a[172:175], a[200:203], a[60:63]\n\t" \
  "buffer_load_dwordx4 v141, s[92:95], s70 offen offset:1024 lds\n\t" \
  "v_cvt_pk_bf16_f32 v114, v52, v53\n\t" \
  "v_mfma_f32_16x16x32_bf16 a[60:63], v[156:159], a[204:207], a[60:63]\n\t" \
  "v_exp_f32 v54, v54\n\t" \
  "v_exp_f32 v55, v55\n\t" \
  "s_waitcnt lgkmcnt(1)\n\t" \
  "v_mfma_f32_32x32x16_bf16 v[32:47], v[88:91], v[64:67], v[0:15]\n\t" \
  "ds_read_b64_tr_b16 v[240:241], v155 offset:0\n\t" \
  "v_exp_f32 v56, v56\n\t" \
  "v_cvt_pk_bf16_f32 v115, v54, v55\n\t" \
  "v_exp_f32 v57, v57\n\t" \
  "v_mfma_f32_32x32x16_bf16 v[32:47], v[92:95], v[68:71], v[32:47]\n\t" \
  "ds_read_b64_tr_b16 v[242:243], v155 offset:512\n\t" \
  "v_exp_f32 v58, v58\n\t" \
  "v_cvt_pk_bf16_f32 v116, v56, v57\n\t" \
  "v_exp_f32 v59, v59\n\t" \
  "v_mfma_f32_32x32x16_bf16 a[128:143], v[96:99], v[104:107], a[128:143]\n\t" \
  "ds_read_b64_tr_b16 v[248:249], v137 offset:0\n\t" \
  "v_exp_f32 v60, v60\n\t" \
  "v_cvt_pk_bf16_f32 v117, v58, v59\n\t" \
  "v_exp_f32 v61, v61\n\t" \
  "s_waitcnt lgkmcnt(3)\n\t" \
  "v_mfma_f32_32x32x16_bf16 a[128:143], v[100:103], v[108:111], a[128:143]\n\t" \
  "ds_read_b64_tr_b16 v[250:251], v137 offset:512\n\t" \
  "v_exp_f32 v62, v62\n\t" \
  "v_cvt_pk_bf16_f32 v118, v60, v61\n\t" \
  "v_exp_f32 v63, v63\n\t" \
  "v_smfmac_f32_16x16x64_bf16 a[160:163], v[80:83], v[104:111], v154\n\t" \
  "buffer_load_dwordx4 v142, s[92:95], s70 offen offset:2048 lds\n\t" \
  "buffer_load_dwordx4 v143, s[92:95], s70 offen offset:3072 lds\n\t" \
  "v_xor_b32 v141, 32, v141\n\t" \
  "v_xor_b32 v143, 32, v143\n\t" \
  "s_waitcnt vmcnt(28)\n\t" \
  "v_mfma_f32_16x16x32_bf16 a[64:67], v[120:123], a[208:211], a[64:67]\n\t" \
  "buffer_load_dwordx4 a[200:203], v149, s[80:83], s64 offen offset:256 sc1\n\t" \
  "v_cvt_pk_bf16_f32 v119, v62, v63\n\t" \
  "v_mfma_f32_16x16x32_bf16 a[64:67], v[124:127], a[212:215], a[64:67]\n\t" \
  "v_exp_f32 v32, v32\n\t" \
  "v_exp_f32 v33, v33\n\t" \
  "v_mfma_f32_16x16x32_bf16 a[72:75], v[128:131], a[208:211], a[72:75]\n\t" \
  "buffer_load_dwordx4 a[204:207], v149, s[80:83], s64 offen offset:2304 sc1\n\t" \
  "v_cvt_pk_bf16_f32 v104, v32, v33\n\t" \
  "v_mfma_f32_16x16x32_bf16 a[72:75], v[132:135], a[212:215], a[72:75]\n\t" \
  "v_exp_f32 v34, v34\n\t" \
  "v_exp_f32 v35, v35\n\t" \
  "v_mfma_f32_16x16x32_bf16 a[80:83], a[164:167], a[208:211], a[80:83]\n\t" \
  "s_add_u32 m0, s72, 12288\n\t" \
  "v_cvt_pk_bf16_f32 v105, v34, v35\n\t" \
  "v_mfma_f32_16x16x32_bf16 a[80:83], a[168:171], a[212:215], a[80:83]\n\t" \
  "v_exp_f32 v36, v36\n\t" \
  "v_exp_f32 v37, v37\n\t" \
  "v_mfma_f32_16x16x32_bf16 a[88:91], a[172:175], a[208:211], a[88:91]\n\t" \
  "s_mov_b32 s68, 983040\n\t" \
  "v_cvt_pk_bf16_f32 v106, v36, v37\n\t" \
  "v_mfma_f32_16x16x32_bf16 a[88:91], v[156:159], a[212:215], a[88:91]\n\t" \
  "v_exp_f32 v38, v38\n\t" \
  "v_exp_f32 v39, v39\n\t" \
  "v_mfma_f32_32x32x16_bf16 v[48:63], v[88:91], v[72:75], v[16:31]\n\t" \
  "ds_read_b128 v[88:91], v145 offset:12288\n\t" \
  "v_exp_f32 v40, v40\n\t" \
  "v_cvt_pk_bf16_f32 v107, v38, v39\n\t" \
  "v_exp_f32 v41, v41\n\t" \
  "v_mfma_f32_32x32x16_bf16 v[48:63], v[92:95], v[76:79], v[48:63]\n\t" \
  "ds_read_b128 v[92:95], v145 offset:13312\n\t" \
  "v_exp_f32 v42, v42\n\t" \
  "v_cvt_pk_bf16_f32 v108, v40, v41\n\t" \
  "v_exp_f32 v43, v43\n\t" \
  "v_mfma_f32_32x32x16_bf16 a[144:159], v[96:99], v[112:115], a[144:159]\n\t" \
  "ds_read_b128 v[96:99], v145 offset:10240\n\t" \
  "v_exp_f32 v44, v44\n\t" \
  "v_cvt_pk_bf16_f32 v109, v42, v43\n\t" \
  "v_exp_f32 v45, v45\n\t" \
  "v_mfma_f32_32x32x16_bf16 a[144:159], v[100:103], v[116:119], a[144:159]\n\t" \
  "ds_read_b128 v[100:103], v145 offset:11264\n\t" \
  "v_exp_f32 v46, v46\n\t" \
  "v_cvt_pk_bf16_f32 v110, v44, v45\n\t" \
  "v_exp_f32 v47, v47\n\t" \
  "v_smfmac_f32_16x16x64_bf16 a[160:163], v[84:87], v[112:119], v154\n\t" \
  "s_waitcnt vmcnt(24)\n\t" \
  "v_mfma_f32_16x16x32_bf16 a[68:71], v[120:123], a[216:219], a[68:71]\n\t" \
  "buffer_load_dwordx4 a[208:211], v150, s[80:83], s64 offen offset:0 sc1\n\t" \
  "v_cvt_pk_bf16_f32 v111, v46, v47\n\t" \
  "v_mfma_f32_16x16x32_bf16 a[68:71], v[124:127], a[220:223], a[68:71]\n\t" \
  "v_exp_f32 v48, v48\n\t" \
  "v_exp_f32 v49, v49\n\t" \
  "v_mfma_f32_16x16x32_bf16 a[76:79], v[128:131], a[216:219], a[76:79]\n\t" \
  "buffer_load_dwordx4 a[212:215], v150, s[80:83], s64 offen offset:2048 sc1\n\t" \
  "v_cvt_pk_bf16_f32 v112, v48, v49\n\t" \
  "v_mfma_f32_16x16x32_bf16 a[76:79], v[132:135], a[220:223], a[76:79]\n\t" \
  "v_exp_f32 v50, v50\n\t" \
  "v_exp_f32 v51, v51\n\t" \
  "v_mfma_f32_16x16x32_bf16 a[84:87], a[164:167], a[216:219], a[84:87]\n\t" \
  "buffer_load_dwordx4 v140, s[84:87], s68 offen offset:0 lds\n\t" \
  "v_cvt_pk_bf16_f32 v113, v50, v51\n\t" \
  "v_mfma_f32_16x16x32_bf16 a[84:87], a[168:171], a[220:223], a[84:87]\n\t" \
  "v_exp_f32 v52, v52\n\t" \
  "v_exp_f32 v53, v53\n\t" \
  "v_mfma_f32_16x16x32_bf16 a[92:95], a[172:175], a[216:219], a[92:95]\n\t" \
  "buffer_load_dwordx4 v141, s[84:87], s68 offen offset:1024 lds\n\t" \
  "v_cvt_pk_bf16_f32 v114, v52, v53\n\t" \
  "v_mfma_f32_16x16x32_bf16 a[92:95], v[156:159], a[220:223], a[92:95]\n\t" \
  "v_exp_f32 v54, v54\n\t" \
  "v_exp_f32 v55, v55\n\t" \
  "s_waitcnt lgkmcnt(1)\n\t" \
  "v_mfma_f32_32x32x16_bf16 v[32:47], v[88:91], v[64:67], v[0:15]\n\t" \
  "ds_read_b64_tr_b16 v[228:229], v146 offset:4096\n\t" \
  "v_exp_f32 v56, v56\n\t" \
  "v_cvt_pk_bf16_f32 v115, v54, v55\n\t" \
  "v_exp_f32 v57, v57\n\t" \
  "v_mfma_f32_32x32x16_bf16 v[32:47], v[92:95], v[68:71], v[32:47]\n\t" \
  "ds_read_b64_tr_b16 v[230:231], v146 offset:4608\n\t" \
  "v_exp_f32 v58, v58\n\t" \
  "v_cvt_pk_bf16_f32 v116, v56, v57\n\t" \
  "v_exp_f32 v59, v59\n\t" \
  "v_mfma_f32_32x32x16_bf16 a[128:143], v[96:99], v[104:107], a[128:143]\n\t" \
  "ds_read_b64_tr_b16 v[236:237], v147 offset:4096\n\t" \
  "v_exp_f32 v60, v60\n\t" \
  "v_cvt_pk_bf16_f32 v117, v58, v59\n\t" \
  "v_exp_f32 v61, v61\n\t" \
  "s_waitcnt lgkmcnt(3)\n\t" \
  "v_mfma_f32_32x32x16_bf16 a[128:143], v[100:103], v[108:111], a[128:143]\n\t" \
  "ds_read_b64_tr_b16 v[238:239], v147 offset:4608\n\t" \
  "v_exp_f32 v62, v62\n\t" \
  "v_cvt_pk_bf16_f32 v118, v60, v61\n\t" \
  "v_exp_f32 v63, v63\n\t" \
  "v_smfmac_f32_16x16x64_bf16 a[160:163], v[80:83], v[104:111], v154\n\t" \
  "s_waitcnt vmcnt(22)\n\t" \
  "s_barrier\n\t" \
  "v_mfma_f32_16x16x32_bf16 a[96:99], v[120:123], a[224:227], a[96:99]\n\t" \
  "buffer_load_dwordx4 a[216:219], v150, s[80:83], s64 offen offset:256 sc1\n\t" \
  "v_cvt_pk_bf16_f32 v119, v62, v63\n\t" \
  "v_mfma_f32_16x16x32_bf16 a[96:99], v[124:127], a[228:231], a[96:99]\n\t" \
  "v_exp_f32 v32, v32\n\t" \
  "v_exp_f32 v33, v33\n\t" \
  "v_mfma_f32_16x16x32_bf16 a[104:107], v[128:131], a[224:227], a[104:107]\n\t" \
  "buffer_load_dwordx4 a[220:223], v150, s[80:83], s64 offen offset:2304 sc1\n\t" \
  "v_cvt_pk_bf16_f32 v104, v32, v33\n\t" \
  "v_mfma_f32_16x16x32_bf16 a[104:107], v[132:135], a[228:231], a[104:107]\n\t" \
  "v_exp_f32 v34, v34\n\t" \
  "v_exp_f32 v35, v35\n\t" \
  "v_mfma_f32_16x16x32_bf16 a[112:115], a[164:167], a[224:227], a[112:115]\n\t" \
  "buffer_load_dwordx4 v142, s[84:87], s68 offen offset:2048 lds\n\t" \
  "v_cvt_pk_bf16_f32 v105, v34, v35\n\t" \
  "v_mfma_f32_16x16x32_bf16 a[112:115], a[168:171], a[228:231], a[112:115]\n\t" \
  "v_exp_f32 v36, v36\n\t" \
  "v_exp_f32 v37, v37\n\t" \
  "v_mfma_f32_16x16x32_bf16 a[120:123], a[172:175], a[224:227], a[120:123]\n\t" \
  "buffer_load_dwordx4 v143, s[84:87], s68 offen offset:3072 lds\n\t" \
  "v_cvt_pk_bf16_f32 v106, v36, v37\n\t" \
  "v_mfma_f32_16x16x32_bf16 a[120:123], v[156:159], a[228:231], a[120:123]\n\t" \
  "v_exp_f32 v38, v38\n\t" \
  "v_exp_f32 v39, v39\n\t" \
  "v_mfma_f32_32x32x16_bf16 v[48:63], v[88:91], v[72:75], v[16:31]\n\t" \
  "ds_read_b128 v[88:91], v145 offset:16384\n\t" \
  "v_exp_f32 v40, v40\n\t" \
  "v_cvt_pk_bf16_f32 v107, v38, v39\n\t" \
  "v_exp_f32 v41, v41\n\t" \
  "v_mfma_f32_32x32x16_bf16 v[48:63], v[92:95], v[76:79], v[48:63]\n\t" \
  "ds_read_b128 v[92:95], v145 offset:17408\n\t" \
  "v_exp_f32 v42, v42\n\t" \
  "v_cvt_pk_bf16_f32 v108, v40, v41\n\t" \
  "v_exp_f32 v43, v43\n\t" \
  "v_mfma_f32_32x32x16_bf16 a[144:159], v[96:99], v[112:115], a[144:159]\n\t" \
  "ds_read_b128 v[96:99], v145 offset:14336\n\t" \
  "v_exp_f32 v44, v44\n\t" \
  "v_cvt_pk_bf16_f32 v109, v42, v43\n\t" \
  "v_exp_f32 v45, v45\n\t" \
  "v_mfma_f32_32x32x16_bf16 a[144:159], v[100:103], v[116:119], a[144:159]\n\t" \
  "ds_read_b128 v[100:103], v145 offset:15360\n\t" \
  "v_exp_f32 v46, v46\n\t" \
  "v_cvt_pk_bf16_f32 v110, v44, v45\n\t" \
  "v_exp_f32 v47, v47\n\t" \
  "v_smfmac_f32_16x16x64_bf16 a[160:163], v[84:87], v[112:119], v154\n\t" \
  "s_add_u32 m0, s73, 32768\n\t" \
  "s_mov_b32 s69, 180224\n\t" \
  "s_waitcnt vmcnt(24)\n\t" \
  "v_mfma_f32_16x16x32_bf16 a[100:103], v[120:123], a[232:235], a[100:103]\n\t" \
  "buffer_load_dwordx4 a[224:227], v151, s[80:83], s64 offen offset:0 sc1\n\t" \
  "v_cvt_pk_bf16_f32 v111, v46, v47\n\t" \
  "v_mfma_f32_16x16x32_bf16 a[100:103], v[124:127], a[236:239], a[100:103]\n\t" \
  "v_exp_f32 v48, v48\n\t" \
  "v_exp_f32 v49, v49\n\t" \
  "v_mfma_f32_16x16x32_bf16 a[108:111], v[128:131], a[232:235], a[108:111]\n\t" \
  "buffer_load_dwordx4 a[228:231], v151, s[80:83], s64 offen offset:2048 sc1\n\t" \
  "v_cvt_pk_bf16_f32 v112, v48, v49\n\t" \
  "v_mfma_f32_16x16x32_bf16 a[108:111], v[132:135], a[236:239], a[108:111]\n\t" \
  "v_exp_f32 v50, v50\n\t" \
  "v_exp_f32 v51, v51\n\t" \
  "v_mfma_f32_16x16x32_bf16 a[116:119], a[164:167], a[232:235], a[116:119]\n\t" \
  "buffer_load_dwordx4 v144, s[88:91], s69 offen offset:0 sc1 lds\n\t" \
  "v_cvt_pk_bf16_f32 v113, v50, v51\n\t" \
  "v_mfma_f32_16x16x32_bf16 a[116:119], a[168:171], a[236:239], a[116:119]\n\t" \
  "v_exp_f32 v52, v52\n\t" \
  "v_exp_f32 v53, v53\n\t" \
  "v_mfma_f32_16x16x32_bf16 a[124:127], a[172:175], a[232:235], a[124:127]\n\t" \
  "buffer_load_dwordx4 v144, s[88:91], s69 offen offset:1024 sc1 lds\n\t" \
  "v_cvt_pk_bf16_f32 v114, v52, v53\n\t" \
  "v_mfma_f32_16x16x32_bf16 a[124:127], v[156:159], a[236:239], a[124:127]\n\t" \
  "v_exp_f32 v54, v54\n\t" \
  "v_exp_f32 v55, v55\n\t" \
  "s_waitcnt lgkmcnt(1)\n\t" \
  "v_mfma_f32_32x32x16_bf16 v[32:47], v[88:91], v[64:67], v[0:15]\n\t" \
  "ds_read_b64_tr_b16 v[244:245], v155 offset:4096\n\t" \
  "v_exp_f32 v56, v56\n\t" \
  "v_cvt_pk_bf16_f32 v115, v54, v55\n\t" \
  "v_exp_f32 v57, v57\n\t" \
  "v_mfma_f32_32x32x16_bf16 v[32:47], v[92:95], v[68:71], v[32:47]\n\t" \
  "ds_read_b64_tr_b16 v[246:247], v155 offset:4608\n\t" \
  "v_exp_f32 v58, v58\n\t" \
  "v_cvt_pk_bf16_f32 v116, v56, v57\n\t" \
  "v_exp_f32 v59, v59\n\t" \
  "v_mfma_f32_32x32x16_bf16 a[128:143], v[96:99], v[104:107], a[128:143]\n\t" \
  "ds_read_b64_tr_b16 v[252:253], v137 offset:4096\n\t" \
  "v_exp_f32 v60, v60\n\t" \
  "v_cvt_pk_bf16_f32 v117, v58, v59\n\t" \
  "v_exp_f32 v61, v61\n\t" \
  "s_waitcnt lgkmcnt(3)\n\t" \
  "v_mfma_f32_32x32x16_bf16 a[128:143], v[100:103], v[108:111], a[128:143]\n\t" \
  "ds_read_b64_tr_b16 v[254:255], v137 offset:4608\n\t" \
  "v_exp_f32 v62, v62\n\t" \
  "v_cvt_pk_bf16_f32 v118, v60, v61\n\t" \
  "v_exp_f32 v63, v63\n\t" \
  "v_smfmac_f32_16x16x64_bf16 a[160:163], v[80:83], v[104:111], v154\n\t" \
  "buffer_load_dwordx4 v144, s[88:91], s69 offen offset:2048 sc1 lds\n\t" \
  "buffer_load_dwordx4 v144, s[88:91], s69 offen offset:3072 sc1 lds\n\t" \
  "s_waitcnt vmcnt(28) lgkmcnt(0)\n\t" \
  "v_mfma_f32_16x16x32_bf16 a[0:3], v[224:227], a[176:179], a[0:3]\n\t" \
  "buffer_load_dwordx4 a[232:235], v151, s[80:83], s64 offen offset:256 sc1\n\t" \
  "v_cvt_pk_bf16_f32 v119, v62, v63\n\t" \
  "v_mfma_f32_16x16x32_bf16 a[0:3], v[228:231], a[180:183], a[0:3]\n\t" \
  "v_exp_f32 v32, v32\n\t" \
  "v_exp_f32 v33, v33\n\t" \
  "v_mfma_f32_16x16x32_bf16 a[8:11], v[232:235], a[176:179], a[8:11]\n\t" \
  "buffer_load_dwordx4 a[236:239], v151, s[80:83], s64 offen offset:2304 sc1\n\t" \
  "v_cvt_pk_bf16_f32 v104, v32, v33\n\t" \
  "v_mfma_f32_16x16x32_bf16 a[8:11], v[236:239], a[180:183], a[8:11]\n\t" \
  "v_exp_f32 v34, v34\n\t" \
  "v_exp_f32 v35, v35\n\t" \
  "v_mfma_f32_16x16x32_bf16 a[16:19], v[240:243], a[176:179], a[16:19]\n\t" \
  "s_add_u32 m0, s72, 0\n\t" \
  "v_cvt_pk_bf16_f32 v105, v34, v35\n\t" \
  "v_mfma_f32_16x16x32_bf16 a[16:19], v[244:247], a[180:183], a[16:19]\n\t" \
  "v_exp_f32 v36, v36\n\t" \
  "v_exp_f32 v37, v37\n\t" \
  "v_mfma_f32_16x16x32_bf16 a[24:27], v[248:251], a[176:179], a[24:27]\n\t" \
  "s_mov_b32 s68, 1048576\n\t" \
  "v_cvt_pk_bf16_f32 v106, v36, v37\n\t" \
  "v_mfma_f32_16x16x32_bf16 a[24:27], v[252:255], a[180:183], a[24:27]\n\t" \
  "v_exp_f32 v38, v38\n\t" \
  "v_exp_f32 v39, v39\n\t" \
  "v_mfma_f32_32x32x16_bf16 v[48:63], v[88:91], v[72:75], v[16:31]\n\t" \
  "ds_read_b128 v[88:91], v145 offset:20480\n\t" \
  "v_exp_f32 v40, v40\n\t" \
  "v_cvt_pk_bf16_f32 v107, v38, v39\n\t" \
  "v_exp_f32 v41, v41\n\t" \
  "v_mfma_f32_32x32x16_bf16 v[48:63], v[92:95], v[76:79], v[48:63]\n\t" \
  "ds_read_b128 v[92:95], v145 offset:21504\n\t" \
  "v_exp_f32 v42, v42\n\t" \
  "v_cvt_pk_bf16_f32 v108, v40, v41\n\t" \
  "v_exp_f32 v43, v43\n\t" \
  "v_mfma_f32_32x32x16_bf16 a[144:159], v[96:99], v[112:115], a[144:159]\n\t" \
  "ds_read_b128 v[96:99], v145 offset:18432\n\t" \
  "v_exp_f32 v44, v44\n\t" \
  "v_cvt_pk_bf16_f32 v109, v42, v43\n\t" \
  "v_exp_f32 v45, v45\n\t" \
  "v_mfma_f32_32x32x16_bf16 a[144:159], v[100:103], v[116:119], a[144:159]\n\t" \
  "ds_read_b128 v[100:103], v145 offset:19456\n\t" \
  "v_exp_f32 v46, v46\n\t" \
  "v_cvt_pk_bf16_f32 v110, v44, v45\n\t" \
  "v_exp_f32 v47, v47\n\t" \
  "v_smfmac_f32_16x16x64_bf16 a[160:163], v[84:87], v[112:119], v154\n\t" \
  "s_mov_b32 s64, 28672\n\t" \
  "s_waitcnt vmcnt(24)\n\t" \
  "v_mfma_f32_16x16x32_bf16 a[4:7], v[224:227], a[184:187], a[4:7]\n\t" \
  "buffer_load_dwordx4 a[176:179], v148, s[80:83], s64 offen offset:0 sc1\n\t" \
  "v_cvt_pk_bf16_f32 v111, v46, v47\n\t" \
  "v_mfma_f32_16x16x32_bf16 a[4:7], v[228:231], a[188:191], a[4:7]\n\t" \
  "v_exp_f32 v48, v48\n\t" \
  "v_exp_f32 v49, v49\n\t" \
  "v_mfma_f32_16x16x32_bf16 a[12:15], v[232:235], a[184:187], a[12:15]\n\t" \
  "buffer_load_dwordx4 a[180:183], v148, s[80:83], s64 offen offset:2048 sc1\n\t" \
  "v_cvt_pk_bf16_f32 v112, v48, v49\n\t" \
  "v_mfma_f32_16x16x32_bf16 a[12:15], v[236:239], a[188:191], a[12:15]\n\t" \
  "v_exp_f32 v50, v50\n\t" \
  "v_exp_f32 v51, v51\n\t" \
  "v_mfma_f32_16x16x32_bf16 a[20:23], v[240:243], a[184:187], a[20:23]\n\t" \
  "buffer_load_dwordx4 v140, s[84:87], s68 offen offset:0 lds\n\t" \
  "v_cvt_pk_bf16_f32 v113, v50, v51\n\t" \
  "v_mfma_f32_16x16x32_bf16 a[20:23], v[244:247], a[188:191], a[20:23]\n\t" \
  "v_exp_f32 v52, v52\n\t" \
  "v_exp_f32 v53, v53\n\t" \
  "v_mfma_f32_16x16x32_bf16 a[28:31], v[248:251], a[184:187], a[28:31]\n\t" \
  "buffer_load_dwordx4 v141, s[84:87], s68 offen offset:1024 lds\n\t" \
  "v_cvt_pk_bf16_f32 v114, v52, v53\n\t" \
  "v_mfma_f32_16x16x32_bf16 a[28:31], v[252:255], a[188:191], a[28:31]\n\t" \
  "v_exp_f32 v54, v54\n\t" \
  "v_exp_f32 v55, v55\n\t" \
  "s_waitcnt lgkmcnt(1)\n\t" \
  "v_mfma_f32_32x32x16_bf16 v[32:47], v[88:91], v[64:67], v[0:15]\n\t" \
  "ds_read_b64_tr_b16 v[120:121], v146 offset:8192\n\t" \
  "v_exp_f32 v56, v56\n\t" \
  "v_cvt_pk_bf16_f32 v115, v54, v55\n\t" \
  "v_exp_f32 v57, v57\n\t" \
  "v_mfma_f32_32x32x16_bf16 v[32:47], v[92:95], v[68:71], v[32:47]\n\t" \
  "ds_read_b64_tr_b16 v[122:123], v146 offset:8704\n\t" \
  "v_exp_f32 v58, v58\n\t" \
  "v_cvt_pk_bf16_f32 v116, v56, v57\n\t" \
  "v_exp_f32 v59, v59\n\t" \
  "v_mfma_f32_32x32x16_bf16 a[128:143], v[96:99], v[104:107], a[128:143]\n\t" \
  "ds_read_b64_tr_b16 v[128:129], v147 offset:8192\n\t" \
  "v_exp_f32 v60, v60\n\t" \
  "v_cvt_pk_bf16_f32 v117, v58, v59\n\t" \
  "v_exp_f32 v61, v61\n\t" \
  "s_waitcnt lgkmcnt(3)\n\t" \
  "v_mfma_f32_32x32x16_bf16 a[128:143], v[100:103], v[108:111], a[128:143]\n\t" \
  "ds_read_b64_tr_b16 v[130:131], v147 offset:8704\n\t" \
  "v_exp_f32 v62, v62\n\t" \
  "v_cvt_pk_bf16_f32 v118, v60, v61\n\t" \
  "v_exp_f32 v63, v63\n\t" \
  "v_smfmac_f32_16x16x64_bf16 a[160:163], v[80:83], v[104:111], v154\n\t" \
  "s_waitcnt vmcnt(26)\n\t" \
  "v_mfma_f32_16x16x32_bf16 a[32:35], v[224:227], a[192:195], a[32:35]\n\t" \
  "buffer_load_dwordx4 a[184:187], v148, s[80:83], s64 offen offset:256 sc1\n\t" \
  "v_cvt_pk_bf16_f32 v119, v62, v63\n\t" \
  "v_mfma_f32_16x16x32_bf16 a[32:35], v[228:231], a[196:199], a[32:35]\n\t" \
  "v_exp_f32 v32, v32\n\t" \
  "v_exp_f32 v33, v33\n\t" \
  "v_mfma_f32_16x16x32_bf16 a[40:43], v[232:235], a[192:195], a[40:43]\n\t" \
  "buffer_load_dwordx4 a[188:191], v148, s[80:83], s64 offen offset:2304 sc1\n\t" \
  "v_cvt_pk_bf16_f32 v104, v32, v33\n\t" \
  "v_mfma_f32_16x16x32_bf16 a[40:43], v[236:239], a[196:199], a[40:43]\n\t" \
  "v_exp_f32 v34, v34\n\t" \
  "v_exp_f32 v35, v35\n\t" \
  "v_mfma_f32_16x16x32_bf16 a[48:51], v[240:243], a[192:195], a[48:51]\n\t" \
  "buffer_load_dwordx4 v142, s[84:87], s68 offen offset:2048 lds\n\t" \
  "v_cvt_pk_bf16_f32 v105, v34, v35\n\t" \
  "v_mfma_f32_16x16x32_bf16 a[48:51], v[244:247], a[196:199], a[48:51]\n\t" \
  "v_exp_f32 v36, v36\n\t" \
  "v_exp_f32 v37, v37\n\t" \
  "v_mfma_f32_16x16x32_bf16 a[56:59], v[248:251], a[192:195], a[56:59]\n\t" \
  "buffer_load_dwordx4 v143, s[84:87], s68 offen offset:3072 lds\n\t" \
  "v_cvt_pk_bf16_f32 v106, v36, v37\n\t" \
  "v_mfma_f32_16x16x32_bf16 a[56:59], v[252:255], a[196:199], a[56:59]\n\t" \
  "v_exp_f32 v38, v38\n\t" \
  "v_exp_f32 v39, v39\n\t" \
  "v_mfma_f32_32x32x16_bf16 v[48:63], v[88:91], v[72:75], v[16:31]\n\t" \
  "ds_read_b128 v[88:91], v145 offset:24576\n\t" \
  "v_exp_f32 v40, v40\n\t" \
  "v_cvt_pk_bf16_f32 v107, v38, v39\n\t" \
  "v_exp_f32 v41, v41\n\t" \
  "v_mfma_f32_32x32x16_bf16 v[48:63], v[92:95], v[76:79], v[48:63]\n\t" \
  "ds_read_b128 v[92:95], v145 offset:25600\n\t" \
  "v_exp_f32 v42, v42\n\t" \
  "v_cvt_pk_bf16_f32 v108, v40, v41\n\t" \
  "v_exp_f32 v43, v43\n\t" \
  "v_mfma_f32_32x32x16_bf16 a[144:159], v[96:99], v[112:115], a[144:159]\n\t" \
  "ds_read_b128 v[96:99], v145 offset:22528\n\t" \
  "v_exp_f32 v44, v44\n\t" \
  "v_cvt_pk_bf16_f32 v109, v42, v43\n\t" \
  "v_exp_f32 v45, v45\n\t" \
  "v_mfma_f32_32x32x16_bf16 a[144:159], v[100:103], v[116:119], a[144:159]\n\t" \
  "ds_read_b128 v[100:103], v145 offset:23552\n\t" \
  "v_exp_f32 v46, v46\n\t" \
  "v_cvt_pk_bf16_f32 v110, v44, v45\n\t" \
  "v_exp_f32 v47, v47\n\t" \
  "v_smfmac_f32_16x16x64_bf16 a[160:163], v[84:87], v[112:119], v154\n\t" \
  "s_mov_b32 s71, 106496\n\t" \
  "s_cmp_eq_u32 s75, 2\n\t" \
  "s_cselect_b32 s71, s71, 159744\n\t" \
  "s_lshl_b32 s63, s75, 12\n\t" \
  "s_add_u32 s63, s63, 65536\n\t" \
  "s_cmp_lt_u32 s75, 2\n\t" \
  "s_cselect_b32 s71, s63, s71\n\t" \
  "s_add_u32 m0, s71, s74\n\t" \
  "s_mov_b32 s70, 524288\n\t" \
  "v_xor_b32 v141, 32, v141\n\t" \
  "v_xor_b32 v143, 32, v143\n\t" \
  "s_waitcnt vmcnt(24)\n\t" \
  "v_mfma_f32_16x16x32_bf16 a[36:39], v[224:227], a[200:203], a[36:39]\n\t" \
  "buffer_load_dwordx4 a[192:195], v149, s[80:83], s64 offen offset:0 sc1\n\t" \
  "v_cvt_pk_bf16_f32 v111, v46, v47\n\t" \
  "v_mfma_f32_16x16x32_bf16 a[36:39], v[228:231], a[204:207], a[36:39]\n\t" \
  "v_exp_f32 v48, v48\n\t" \
  "v_exp_f32 v49, v49\n\t" \
  "v_mfma_f32_16x16x32_bf16 a[44:47], v[232:235], a[200:203], a[44:47]\n\t" \
  "buffer_load_dwordx4 a[196:199], v149, s[80:83], s64 offen offset:2048 sc1\n\t" \
  "v_cvt_pk_bf16_f32 v112, v48, v49\n\t" \
  "v_mfma_f32_16x16x32_bf16 a[44:47], v[236:239], a[204:207], a[44:47]\n\t" \
  "v_exp_f32 v50, v50\n\t" \
  "v_exp_f32 v51, v51\n\t" \
  "v_mfma_f32_16x16x32_bf16 a[52:55], v[240:243], a[200:203], a[52:55]\n\t" \
  "buffer_load_dwordx4 v140, s[92:95], s70 offen offset:0 lds\n\t" \
  "v_cvt_pk_bf16_f32 v113, v50, v51\n\t" \
  "v_mfma_f32_16x16x32_bf16 a[52:55], v[244:247], a[204:207], a[52:55]\n\t" \
  "v_exp_f32 v52, v52\n\t" \
  "v_exp_f32 v53, v53\n\t" \
  "v_mfma_f32_16x16x32_bf16 a[60:63], v[248:251], a[200:203], a[60:63]\n\t" \
  "buffer_load_dwordx4 v141, s[92:95], s70 offen offset:1024 lds\n\t" \
  "v_cvt_pk_bf16_f32 v114, v52, v53\n\t" \
  "v_mfma_f32_16x16x32_bf16 a[60:63], v[252:255], a[204:207], a[60:63]\n\t" \
  "v_exp_f32 v54, v54\n\t" \
  "v_exp_f32 v55, v55\n\t" \
  "s_waitcnt lgkmcnt(1)\n\t" \
  "v_mfma_f32_32x32x16_bf16 v[32:47], v[88:91], v[64:67], v[0:15]\n\t" \
  "ds_read_b64_tr_b16 a[164:165], v155 offset:8192\n\t" \
  "v_exp_f32 v56, v56\n\t" \
  "v_cvt_pk_bf16_f32 v115, v54, v55\n\t" \
  "v_exp_f32 v57, v57\n\t" \
  "v_mfma_f32_32x32x16_bf16 v[32:47], v[92:95], v[68:71], v[32:47]\n\t" \
  "ds_read_b64_tr_b16 a[166:167], v155 offset:8704\n\t" \
  "v_exp_f32 v58, v58\n\t" \
  "v_cvt_pk_bf16_f32 v116, v56, v57\n\t" \
  "v_exp_f32 v59, v59\n\t" \
  "v_mfma_f32_32x32x16_bf16 a[128:143], v[96:99], v[104:107], a[128:143]\n\t" \
  "ds_read_b64_tr_b16 a[172:173], v137 offset:8192\n\t" \
  "v_exp_f32 v60, v60\n\t" \
  "v_cvt_pk_bf16_f32 v117, v58, v59\n\t" \
  "v_exp_f32 v61, v61\n\t" \
  "s_waitcnt lgkmcnt(3)\n\t" \
  "v_mfma_f32_32x32x16_bf16 a[128:143], v[100:103], v[108:111], a[128:143]\n\t" \
  "ds_read_b64_tr_b16 a[174:175], v137 offset:8704\n\t" \
  "v_exp_f32 v62, v62\n\t" \
  "v_cvt_pk_bf16_f32 v118, v60, v61\n\t" \
  "v_exp_f32 v63, v63\n\t" \
  "v_smfmac_f32_16x16x64_bf16 a[160:163], v[80:83], v[104:111], v154\n\t" \
  "buffer_load_dwordx4 v142, s[92:95], s70 offen offset:2048 lds\n\t" \
  "buffer_load_dwordx4 v143, s[92:95], s70 offen offset:3072 lds\n\t" \
  "v_xor_b32 v141, 32, v141\n\t" \
  "v_xor_b32 v143, 32, v143\n\t" \
  "s_waitcnt vmcnt(28)\n\t" \
  "v_mfma_f32_16x16x32_bf16 a[64:67], v[224:227], a[208:211], a[64:67]\n\t" \
  "buffer_load_dwordx4 a[200:203], v149, s[80:83], s64 offen offset:256 sc1\n\t" \
  "v_cvt_pk_bf16_f32 v119, v62, v63\n\t" \
  "v_mfma_f32_16x16x32_bf16 a[64:67], v[228:231], a[212:215], a[64:67]\n\t" \
  "v_exp_f32 v32, v32\n\t" \
  "v_exp_f32 v33, v33\n\t" \
  "v_mfma_f32_16x16x32_bf16 a[72:75], v[232:235], a[208:211], a[72:75]\n\t" \
  "buffer_load_dwordx4 a[204:207], v149, s[80:83], s64 offen offset:2304 sc1\n\t" \
  "v_cvt_pk_bf16_f32 v104, v32, v33\n\t" \
  "v_mfma_f32_16x16x32_bf16 a[72:75], v[236:239], a[212:215], a[72:75]\n\t" \
  "v_exp_f32 v34, v34\n\t" \
  "v_exp_f32 v35, v35\n\t" \
  "v_mfma_f32_16x16x32_bf16 a[80:83], v[240:243], a[208:211], a[80:83]\n\t" \
  "s_add_u32 m0, s72, 4096\n\t" \
  "v_cvt_pk_bf16_f32 v105, v34, v35\n\t" \
  "v_mfma_f32_16x16x32_bf16 a[80:83], v[244:247], a[212:215], a[80:83]\n\t" \
  "v_exp_f32 v36, v36\n\t" \
  "v_exp_f32 v37, v37\n\t" \
  "v_mfma_f32_16x16x32_bf16 a[88:91], v[248:251], a[208:211], a[88:91]\n\t" \
  "s_mov_b32 s68, 1114112\n\t" \
  "v_cvt_pk_bf16_f32 v106, v36, v37\n\t" \
  "v_mfma_f32_16x16x32_bf16 a[88:91], v[252:255], a[212:215], a[88:91]\n\t" \
  "v_exp_f32 v38, v38\n\t" \
  "v_exp_f32 v39, v39\n\t" \
  "v_mfma_f32_32x32x16_bf16 v[48:63], v[88:91], v[72:75], v[16:31]\n\t" \
  "ds_read_b128 v[88:91], v145 offset:28672\n\t" \
  "v_exp_f32 v40, v40\n\t" \
  "v_cvt_pk_bf16_f32 v107, v38, v39\n\t" \
  "v_exp_f32 v41, v41\n\t" \
  "v_mfma_f32_32x32x16_bf16 v[48:63], v[92:95], v[76:79], v[48:63]\n\t" \
  "ds_read_b128 v[92:95], v145 offset:29696\n\t" \
  "v_exp_f32 v42, v42\n\t" \
  "v_cvt_pk_bf16_f32 v108, v40, v41\n\t" \
  "v_exp_f32 v43, v43\n\t" \
  "v_mfma_f32_32x32x16_bf16 a[144:159], v[96:99], v[112:115], a[144:159]\n\t" \
  "ds_read_b128 v[96:99], v145 offset:26624\n\t" \
  "v_exp_f32 v44, v44\n\t" \
  "v_cvt_pk_bf16_f32 v109, v42, v43\n\t" \
  "v_exp_f32 v45, v45\n\t" \
  "v_mfma_f32_32x32x16_bf16 a[144:159], v[100:103], v[116:119], a[144:159]\n\t" \
  "ds_read_b128 v[100:103], v145 offset:27648\n\t" \
  "v_exp_f32 v46, v46\n\t" \
  "v_cvt_pk_bf16_f32 v110, v44, v45\n\t" \
  "v_exp_f32 v47, v47\n\t" \
  "v_smfmac_f32_16x16x64_bf16 a[160:163], v[84:87], v[112:119], v154\n\t" \
  "s_waitcnt vmcnt(24)\n\t" \
  "v_mfma_f32_16x16x32_bf16 a[68:71], v[224:227], a[216:219], a[68:71]\n\t" \
  "buffer_load_dwordx4 a[208:211], v150, s[80:83], s64 offen offset:0 sc1\n\t" \
  "v_cvt_pk_bf16_f32 v111, v46, v47\n\t" \
  "v_mfma_f32_16x16x32_bf16 a[68:71], v[228:231], a[220:223], a[68:71]\n\t" \
  "v_exp_f32 v48, v48\n\t" \
  "v_exp_f32 v49, v49\n\t" \
  "v_mfma_f32_16x16x32_bf16 a[76:79], v[232:235], a[216:219], a[76:79]\n\t" \
  "buffer_load_dwordx4 a[212:215], v150, s[80:83], s64 offen offset:2048 sc1\n\t" \
  "v_cvt_pk_bf16_f32 v112, v48, v49\n\t" \
  "v_mfma_f32_16x16x32_bf16 a[76:79], v[236:239], a[220:223], a[76:79]\n\t" \
  "v_exp_f32 v50, v50\n\t" \
  "v_exp_f32 v51, v51\n\t" \
  "v_mfma_f32_16x16x32_bf16 a[84:87], v[240:243], a[216:219], a[84:87]\n\t" \
  "buffer_load_dwordx4 v140, s[84:87], s68 offen offset:0 lds\n\t" \
  "v_cvt_pk_bf16_f32 v113, v50, v51\n\t" \
  "v_mfma_f32_16x16x32_bf16 a[84:87], v[244:247], a[220:223], a[84:87]\n\t" \
  "v_exp_f32 v52, v52\n\t" \
  "v_exp_f32 v53, v53\n\t" \
  "v_mfma_f32_16x16x32_bf16 a[92:95], v[248:251], a[216:219], a[92:95]\n\t" \
  "buffer_load_dwordx4 v141, s[84:87], s68 offen offset:1024 lds\n\t" \
  "v_cvt_pk_bf16_f32 v114, v52, v53\n\t" \
  "v_mfma_f32_16x16x32_bf16 a[92:95], v[252:255], a[220:223], a[92:95]\n\t" \
  "v_exp_f32 v54, v54\n\t" \
  "v_exp_f32 v55, v55\n\t" \
  "s_waitcnt lgkmcnt(1)\n\t" \
  "v_mfma_f32_32x32x16_bf16 v[32:47], v[88:91], v[64:67], v[0:15]\n\t" \
  "ds_read_b64_tr_b16 v[124:125], v146 offset:12288\n\t" \
  "v_exp_f32 v56, v56\n\t" \
  "v_cvt_pk_bf16_f32 v115, v54, v55\n\t" \
  "v_exp_f32 v57, v57\n\t" \
  "v_mfma_f32_32x32x16_bf16 v[32:47], v[92:95], v[68:71], v[32:47]\n\t" \
  "ds_read_b64_tr_b16 v[126:127], v146 offset:12800\n\t" \
  "v_exp_f32 v58, v58\n\t" \
  "v_cvt_pk_bf16_f32 v116, v56, v57\n\t" \
  "v_exp_f32 v59, v59\n\t" \
  "v_mfma_f32_32x32x16_bf16 a[128:143], v[96:99], v[104:107], a[128:143]\n\t" \
  "ds_read_b64_tr_b16 v[132:133], v147 offset:12288\n\t" \
  "v_exp_f32 v60, v60\n\t" \
  "v_cvt_pk_bf16_f32 v117, v58, v59\n\t" \
  "v_exp_f32 v61, v61\n\t" \
  "s_waitcnt lgkmcnt(3)\n\t" \
  "v_mfma_f32_32x32x16_bf16 a[128:143], v[100:103], v[108:111], a[128:143]\n\t" \
  "ds_read_b64_tr_b16 v[134:135], v147 offset:12800\n\t" \
  "v_exp_f32 v62, v62\n\t" \
  "v_cvt_pk_bf16_f32 v118, v60, v61\n\t" \
  "v_exp_f32 v63, v63\n\t" \
  "v_smfmac_f32_16x16x64_bf16 a[160:163], v[80:83], v[104:111], v154\n\t" \
  "s_waitcnt vmcnt(22)\n\t" \
  "s_barrier\n\t" \
  "v_mfma_f32_16x16x32_bf16 a[96:99], v[224:227], a[224:227], a[96:99]\n\t" \
  "buffer_load_dwordx4 a[216:219], v150, s[80:83], s64 offen offset:256 sc1\n\t" \
  "v_cvt_pk_bf16_f32 v119, v62, v63\n\t" \
  "v_mfma_f32_16x16x32_bf16 a[96:99], v[228:231], a[228:231], a[96:99]\n\t" \
  "v_exp_f32 v32, v32\n\t" \
  "v_exp_f32 v33, v33\n\t" \
  "v_mfma_f32_16x16x32_bf16 a[104:107], v[232:235], a[224:227], a[104:107]\n\t" \
  "buffer_load_dwordx4 a[220:223], v150, s[80:83], s64 offen offset:2304 sc1\n\t" \
  "v_cvt_pk_bf16_f32 v104, v32, v33\n\t" \
  "v_mfma_f32_16x16x32_bf16 a[104:107], v[236:239], a[228:231], a[104:107]\n\t" \
  "v_exp_f32 v34, v34\n\t" \
  "v_exp_f32 v35, v35\n\t" \
  "v_mfma_f32_16x16x32_bf16 a[112:115], v[240:243], a[224:227], a[112:115]\n\t" \
  "buffer_load_dwordx4 v142, s[84:87], s68 offen offset:2048 lds\n\t" \
  "v_cvt_pk_bf16_f32 v105, v34, v35\n\t" \
  "v_mfma_f32_16x16x32_bf16 a[112:115], v[244:247], a[228:231], a[112:115]\n\t" \
  "v_exp_f32 v36, v36\n\t" \
  "v_exp_f32 v37, v37\n\t" \
  "v_mfma_f32_16x16x32_bf16 a[120:123], v[248:251], a[224:227], a[120:123]\n\t" \
  "buffer_load_dwordx4 v143, s[84:87], s68 offen offset:3072 lds\n\t" \
  "v_cvt_pk_bf16_f32 v106, v36, v37\n\t" \
  "v_mfma_f32_16x16x32_bf16 a[120:123], v[252:255], a[228:231], a[120:123]\n\t" \
  "v_exp_f32 v38, v38\n\t" \
  "v_exp_f32 v39, v39\n\t" \
  "v_mfma_f32_32x32x16_bf16 v[48:63], v[88:91], v[72:75], v[16:31]\n\t" \
  "ds_read_b128 v[88:91], v145 offset:32768\n\t" \
  "v_exp_f32 v40, v40\n\t" \
  "v_cvt_pk_bf16_f32 v107, v38, v39\n\t" \
  "v_exp_f32 v41, v41\n\t" \
  "v_mfma_f32_32x32x16_bf16 v[48:63], v[92:95], v[76:79], v[48:63]\n\t" \
  "ds_read_b128 v[92:95], v145 offset:33792\n\t" \
  "v_exp_f32 v42, v42\n\t" \
  "v_cvt_pk_bf16_f32 v108, v40, v41\n\t" \
  "v_exp_f32 v43, v43\n\t" \
  "v_mfma_f32_32x32x16_bf16 a[144:159], v[96:99], v[112:115], a[144:159]\n\t" \
  "ds_read_b128 v[96:99], v145 offset:30720\n\t" \
  "v_exp_f32 v44, v44\n\t" \
  "v_cvt_pk_bf16_f32 v109, v42, v43\n\t" \
  "v_exp_f32 v45, v45\n\t" \
  "v_mfma_f32_32x32x16_bf16 a[144:159], v[100:103], v[116:119], a[144:159]\n\t" \
  "ds_read_b128 v[100:103], v145 offset:31744\n\t" \
  "v_exp_f32 v46, v46\n\t" \
  "v_cvt_pk_bf16_f32 v110, v44, v45\n\t" \
  "v_exp_f32 v47, v47\n\t" \
  "v_smfmac_f32_16x16x64_bf16 a[160:163], v[84:87], v[112:119], v154\n\t" \
  "s_add_u32 m0, s73, 0\n\t" \
  "s_mov_b32 s69, 196608\n\t" \
  "s_waitcnt vmcnt(24)\n\t" \
  "v_mfma_f32_16x16x32_bf16 a[100:103], v[224:227], a[232:235], a[100:103]\n\t" \
  "buffer_load_dwordx4 a[224:227], v151, s[80:83], s64 offen offset:0 sc1\n\t" \
  "v_cvt_pk_bf16_f32 v111, v46, v47\n\t" \
  "v_mfma_f32_16x16x32_bf16 a[100:103], v[228:231], a[236:239], a[100:103]\n\t" \
  "v_exp_f32 v48, v48\n\t" \
  "v_exp_f32 v49, v49\n\t" \
  "v_mfma_f32_16x16x32_bf16 a[108:111], v[232:235], a[232:235], a[108:111]\n\t" \
  "buffer_load_dwordx4 a[228:231], v151, s[80:83], s64 offen offset:2048 sc1\n\t" \
  "v_cvt_pk_bf16_f32 v112, v48, v49\n\t" \
  "v_mfma_f32_16x16x32_bf16 a[108:111], v[236:239], a[236:239], a[108:111]\n\t" \
  "v_exp_f32 v50, v50\n\t" \
  "v_exp_f32 v51, v51\n\t" \
  "v_mfma_f32_16x16x32_bf16 a[116:119], v[240:243], a[232:235], a[116:119]\n\t" \
  "buffer_load_dwordx4 v144, s[88:91], s69 offen offset:0 sc1 lds\n\t" \
  "v_cvt_pk_bf16_f32 v113, v50, v51\n\t" \
  "v_mfma_f32_16x16x32_bf16 a[116:119], v[244:247], a[236:239], a[116:119]\n\t" \
  "v_exp_f32 v52, v52\n\t" \
  "v_exp_f32 v53, v53\n\t" \
  "v_mfma_f32_16x16x32_bf16 a[124:127], v[248:251], a[232:235], a[124:127]\n\t" \
  "buffer_load_dwordx4 v144, s[88:91], s69 offen offset:1024 sc1 lds\n\t" \
  "v_cvt_pk_bf16_f32 v114, v52, v53\n\t" \
  "v_mfma_f32_16x16x32_bf16 a[124:127], v[252:255], a[236:239], a[124:127]\n\t" \
  "v_exp_f32 v54, v54\n\t" \
  "v_exp_f32 v55, v55\n\t" \
  "s_waitcnt lgkmcnt(1)\n\t" \
  "v_mfma_f32_32x32x16_bf16 v[32:47], v[88:91], v[64:67], v[0:15]\n\t" \
  "ds_read_b64_tr_b16 a[168:169], v155 offset:12288\n\t" \
  "v_exp_f32 v56, v56\n\t" \
  "v_cvt_pk_bf16_f32 v115, v54, v55\n\t" \
  "v_exp_f32 v57, v57\n\t" \
  "v_mfma_f32_32x32x16_bf16 v[32:47], v[92:95], v[68:71], v[32:47]\n\t" \
  "ds_read_b64_tr_b16 a[170:171], v155 offset:12800\n\t" \
  "v_exp_f32 v58, v58\n\t" \
  "v_cvt_pk_bf16_f32 v116, v56, v57\n\t" \
  "v_exp_f32 v59, v59\n\t" \
  "v_mfma_f32_32x32x16_bf16 a[128:143], v[96:99], v[104:107], a[128:143]\n\t" \
  "ds_read_b64_tr_b16 v[156:157], v137 offset:12288\n\t" \
  "v_exp_f32 v60, v60\n\t" \
  "v_cvt_pk_bf16_f32 v117, v58, v59\n\t" \
  "v_exp_f32 v61, v61\n\t" \
  "s_waitcnt lgkmcnt(3)\n\t" \
  "v_mfma_f32_32x32x16_bf16 a[128:143], v[100:103], v[108:111], a[128:143]\n\t" \
  "ds_read_b64_tr_b16 v[158:159], v137 offset:12800\n\t" \
  "v_exp_f32 v62, v62\n\t" \
  "v_cvt_pk_bf16_f32 v118, v60, v61\n\t" \
  "v_exp_f32 v63, v63\n\t" \
  "v_smfmac_f32_16x16x64_bf16 a[160:163], v[80:83], v[104:111], v154\n\t" \
  "buffer_load_dwordx4 v144, s[88:91], s69 offen offset:2048 sc1 lds\n\t" \
  "buffer_load_dwordx4 v144, s[88:91], s69 offen offset:3072 sc1 lds\n\t" \
  "s_waitcnt vmcnt(28) lgkmcnt(0)\n\t" \
  "v_mfma_f32_16x16x32_bf16 a[0:3], v[120:123], a[176:179], a[0:3]\n\t" \
  "buffer_load_dwordx4 a[232:235], v151, s[80:83], s64 offen offset:256 sc1\n\t" \
  "v_cvt_pk_bf16_f32 v119, v62, v63\n\t" \
  "v_mfma_f32_16x16x32_bf16 a[0:3], v[124:127], a[180:183], a[0:3]\n\t" \
  "v_exp_f32 v32, v32\n\t" \
  "v_exp_f32 v33, v33\n\t" \
  "v_mfma_f32_16x16x32_bf16 a[8:11], v[128:131], a[176:179], a[8:11]\n\t" \
  "buffer_load_dwordx4 a[236:239], v151, s[80:83], s64 offen offset:2304 sc1\n\t" \
  "v_cvt_pk_bf16_f32 v104, v32, v33\n\t" \
  "v_mfma_f32_16x16x32_bf16 a[8:11], v[132:135], a[180:183], a[8:11]\n\t" \
  "v_exp_f32 v34, v34\n\t" \
  "v_exp_f32 v35, v35\n\t" \
  "v_mfma_f32_16x16x32_bf16 a[16:19], a[164:167], a[176:179], a[16:19]\n\t" \
  "s_add_u32 m0, s72, 8192\n\t" \
  "v_cvt_pk_bf16_f32 v105, v34, v35\n\t" \
  "v_mfma_f32_16x16x32_bf16 a[16:19], a[168:171], a[180:183], a[16:19]\n\t" \
  "v_exp_f32 v36, v36\n\t" \
  "v_exp_f32 v37, v37\n\t" \
  "v_mfma_f32_16x16x32_bf16 a[24:27], a[172:175], a[176:179], a[24:27]\n\t" \
  "s_mov_b32 s68, 1179648\n\t" \
  "v_cvt_pk_bf16_f32 v106, v36, v37\n\t" \
  "v_mfma_f32_16x16x32_bf16 a[24:27], v[156:159], a[180:183], a[24:27]\n\t" \
  "v_exp_f32 v38, v38\n\t" \
  "v_exp_f32 v39, v39\n\t" \
  "v_mfma_f32_32x32x16_bf16 v[48:63], v[88:91], v[72:75], v[16:31]\n\t" \
  "ds_read_b128 v[88:91], v145 offset:36864\n\t" \
  "v_exp_f32 v40, v40\n\t" \
  "v_cvt_pk_bf16_f32 v107, v38, v39\n\t" \
  "v_exp_f32 v41, v41\n\t" \
  "v_mfma_f32_32x32x16_bf16 v[48:63], v[92:95], v[76:79], v[48:63]\n\t" \
  "ds_read_b128 v[92:95], v145 offset:37888\n\t" \
  "v_exp_f32 v42, v42\n\t" \
  "v_cvt_pk_bf16_f32 v108, v40, v41\n\t" \
  "v_exp_f32 v43, v43\n\t" \
  "v_mfma_f32_32x32x16_bf16 a[144:159], v[96:99], v[112:115], a[144:159]\n\t" \
  "ds_read_b128 v[96:99], v145 offset:34816\n\t" \
  "v_exp_f32 v44, v44\n\t" \
  "v_cvt_pk_bf16_f32 v109, v42, v43\n\t" \
  "v_exp_f32 v45, v45\n\t" \
  "v_mfma_f32_32x32x16_bf16 a[144:159], v[100:103], v[116:119], a[144:159]\n\t" \
  "ds_read_b128 v[100:103], v145 offset:35840\n\t" \
  "v_exp_f32 v46, v46\n\t" \
  "v_cvt_pk_bf16_f32 v110, v44, v45\n\t" \
  "v_exp_f32 v47, v47\n\t" \
  "v_smfmac_f32_16x16x64_bf16 a[160:163], v[84:87], v[112:119], v154\n\t" \
  "s_mov_b32 s64, 32768\n\t" \
  "s_waitcnt vmcnt(24)\n\t" \
  "v_mfma_f32_16x16x32_bf16 a[4:7], v[120:123], a[184:187], a[4:7]\n\t" \
  "buffer_load_dwordx4 a[176:179], v148, s[80:83], s64 offen offset:0 sc1\n\t" \
  "v_cvt_pk_bf16_f32 v111, v46, v47\n\t" \
  "v_mfma_f32_16x16x32_bf16 a[4:7], v[124:127], a[188:191], a[4:7]\n\t" \
  "v_exp_f32 v48, v48\n\t" \
  "v_exp_f32 v49, v49\n\t" \
  "v_mfma_f32_16x16x32_bf16 a[12:15], v[128:131], a[184:187], a[12:15]\n\t" \
  "buffer_load_dwordx4 a[180:183], v148, s[80:83], s64 offen offset:2048 sc1\n\t" \
  "v_cvt_pk_bf16_f32 v112, v48, v49\n\t" \
  "v_mfma_f32_16x16x32_bf16 a[12:15], v[132:135], a[188:191], a[12:15]\n\t" \
  "v_exp_f32 v50, v50\n\t" \
  "v_exp_f32 v51, v51\n\t" \
  "v_mfma_f32_16x16x32_bf16 a[20:23], a[164:167], a[184:187], a[20:23]\n\t" \
  "buffer_load_dwordx4 v140, s[84:87], s68 offen offset:0 lds\n\t" \
  "v_cvt_pk_bf16_f32 v113, v50, v51\n\t" \
  "v_mfma_f32_16x16x32_bf16 a[20:23], a[168:171], a[188:191], a[20:23]\n\t" \
  "v_exp_f32 v52, v52\n\t" \
  "v_exp_f32 v53, v53\n\t" \
  "v_mfma_f32_16x16x32_bf16 a[28:31], a[172:175], a[184:187], a[28:31]\n\t" \
  "buffer_load_dwordx4 v141, s[84:87], s68 offen offset:1024 lds\n\t" \
  "v_cvt_pk_bf16_f32 v114, v52, v53\n\t" \
  "v_mfma_f32_16x16x32_bf16 a[28:31], v[156:159], a[188:191], a[28:31]\n\t" \
  "v_exp_f32 v54, v54\n\t" \
  "v_exp_f32 v55, v55\n\t" \
  "s_waitcnt lgkmcnt(1)\n\t" \
  "v_mfma_f32_32x32x16_bf16 v[32:47], v[88:91], v[64:67], v[0:15]\n\t" \
  "ds_read_b64_tr_b16 v[224:225], v146 offset:0\n\t" \
  "v_exp_f32 v56, v56\n\t" \
  "v_cvt_pk_bf16_f32 v115, v54, v55\n\t" \
  "v_exp_f32 v57, v57\n\t" \
  "v_mfma_f32_32x32x16_bf16 v[32:47], v[92:95], v[68:71], v[32:47]\n\t" \
  "ds_read_b64_tr_b16 v[226:227], v146 offset:512\n\t" \
  "v_exp_f32 v58, v58\n\t" \
  "v_cvt_pk_bf16_f32 v116, v56, v57\n\t" \
  "v_exp_f32 v59, v59\n\t" \
  "v_mfma_f32_32x32x16_bf16 a[128:143], v[96:99], v[104:107], a[128:143]\n\t" \
  "ds_read_b64_tr_b16 v[232:233], v147 offset:0\n\t" \
  "v_exp_f32 v60, v60\n\t" \
  "v_cvt_pk_bf16_f32 v117, v58, v59\n\t" \
  "v_exp_f32 v61, v61\n\t" \
  "s_waitcnt lgkmcnt(3)\n\t" \
  "v_mfma_f32_32x32x16_bf16 a[128:143], v[100:103], v[108:111], a[128:143]\n\t" \
  "ds_read_b64_tr_b16 v[234:235], v147 offset:512\n\t" \
  "v_exp_f32 v62, v62\n\t" \
  "v_cvt_pk_bf16_f32 v118, v60, v61\n\t" \
  "v_exp_f32 v63, v63\n\t" \
  "v_smfmac_f32_16x16x64_bf16 a[160:163], v[80:83], v[104:111], v154\n\t" \
  "s_waitcnt vmcnt(26)\n\t" \
  "v_mfma_f32_16x16x32_bf16 a[32:35], v[120:123], a[192:195], a[32:35]\n\t" \
  "buffer_load_dwordx4 a[184:187], v148, s[80:83], s64 offen offset:256 sc1\n\t" \
  "v_cvt_pk_bf16_f32 v119, v62, v63\n\t" \
  "v_mfma_f32_16x16x32_bf16 a[32:35], v[124:127], a[196:199], a[32:35]\n\t" \
  "v_exp_f32 v32, v32\n\t" \
  "v_exp_f32 v33, v33\n\t" \
  "v_mfma_f32_16x16x32_bf16 a[40:43], v[128:131], a[192:195], a[40:43]\n\t" \
  "buffer_load_dwordx4 a[188:191], v148, s[80:83], s64 offen offset:2304 sc1\n\t" \
  "v_cvt_pk_bf16_f32 v104, v32, v33\n\t" \
  "v_mfma_f32_16x16x32_bf16 a[40:43], v[132:135], a[196:199], a[40:43]\n\t" \
  "v_exp_f32 v34, v34\n\t" \
  "v_exp_f32 v35, v35\n\t" \
  "v_mfma_f32_16x16x32_bf16 a[48:51], a[164:167], a[192:195], a[48:51]\n\t" \
  "buffer_load_dwordx4 v142, s[84:87], s68 offen offset:2048 lds\n\t" \
  "v_cvt_pk_bf16_f32 v105, v34, v35\n\t" \
  "v_mfma_f32_16x16x32_bf16 a[48:51], a[168:171], a[196:199], a[48:51]\n\t" \
  "v_exp_f32 v36, v36\n\t" \
  "v_exp_f32 v37, v37\n\t" \
  "v_mfma_f32_16x16x32_bf16 a[56:59], a[172:175], a[192:195], a[56:59]\n\t" \
  "buffer_load_dwordx4 v143, s[84:87], s68 offen offset:3072 lds\n\t" \
  "v_cvt_pk_bf16_f32 v106, v36, v37\n\t" \
  "v_mfma_f32_16x16x32_bf16 a[56:59], v[156:159], a[196:199], a[56:59]\n\t" \
  "v_exp_f32 v38, v38\n\t" \
  "v_exp_f32 v39, v39\n\t" \
  "v_mfma_f32_32x32x16_bf16 v[48:63], v[88:91], v[72:75], v[16:31]\n\t" \
  "ds_read_b128 v[88:91], v145 offset:40960\n\t" \
  "v_exp_f32 v40, v40\n\t" \
  "v_cvt_pk_bf16_f32 v107, v38, v39\n\t" \
  "v_exp_f32 v41, v41\n\t" \
  "v_mfma_f32_32x32x16_bf16 v[48:63], v[92:95], v[76:79], v[48:63]\n\t" \
  "ds_read_b128 v[92:95], v145 offset:41984\n\t" \
  "v_exp_f32 v42, v42\n\t" \
  "v_cvt_pk_bf16_f32 v108, v40, v41\n\t" \
  "v_exp_f32 v43, v43\n\t" \
  "v_mfma_f32_32x32x16_bf16 a[144:159], v[96:99], v[112:115], a[144:159]\n\t" \
  "ds_read_b128 v[96:99], v145 offset:38912\n\t" \
  "v_exp_f32 v44, v44\n\t" \
  "v_cvt_pk_bf16_f32 v109, v42, v43\n\t" \
  "v_exp_f32 v45, v45\n\t" \
  "v_mfma_f32_32x32x16_bf16 a[144:159], v[100:103], v[116:119], a[144:159]\n\t" \
  "ds_read_b128 v[100:103], v145 offset:39936\n\t" \
  "v_exp_f32 v46, v46\n\t" \
  "v_cvt_pk_bf16_f32 v110, v44, v45\n\t" \
  "v_exp_f32 v47, v47\n\t" \
  "v_smfmac_f32_16x16x64_bf16 a[160:163], v[84:87], v[112:119], v154\n\t" \
  "s_waitcnt vmcnt(24)\n\t" \
  "v_mfma_f32_16x16x32_bf16 a[36:39], v[120:123], a[200:203], a[36:39]\n\t" \
  "buffer_load_dwordx4 a[192:195], v149, s[80:83], s64 offen offset:0 sc1\n\t" \
  "v_cvt_pk_bf16_f32 v111, v46, v47\n\t" \
  "v_mfma_f32_16x16x32_bf16 a[36:39], v[124:127], a[204:207], a[36:39]\n\t" \
  "v_exp_f32 v48, v48\n\t" \
  "v_exp_f32 v49, v49\n\t" \
  "v_mfma_f32_16x16x32_bf16 a[44:47], v[128:131], a[200:203], a[44:47]\n\t" \
  "buffer_load_dwordx4 a[196:199], v149, s[80:83], s64 offen offset:2048 sc1\n\t" \
  "v_cvt_pk_bf16_f32 v112, v48, v49\n\t" \
  "v_mfma_f32_16x16x32_bf16 a[44:47], v[132:135], a[204:207], a[44:47]\n\t" \
  "v_exp_f32 v50, v50\n\t" \
  "v_exp_f32 v51, v51\n\t" \
  "v_mfma_f32_16x16x32_bf16 a[52:55], a[164:167], a[200:203], a[52:55]\n\t" \
  "v_exp_f32 v52, v52\n\t" \
  "v_mfma_f32_16x16x32_bf16 a[52:55], a[168:171], a[204:207], a[52:55]\n\t" \
  "v_cvt_pk_bf16_f32 v113, v50, v51\n\t" \
  "v_exp_f32 v53, v53\n\t" \
  "v_mfma_f32_16x16x32_bf16 a[60:63], a[172:175], a[200:203], a[60:63]\n\t" \
  "v_exp_f32 v54, v54\n\t" \
  "v_mfma_f32_16x16x32_bf16 a[60:63], v[156:159], a[204:207], a[60:63]\n\t" \
  "v_cvt_pk_bf16_f32 v114, v52, v53\n\t" \
  "v_exp_f32 v55, v55\n\t" \
  "s_waitcnt lgkmcnt(1)\n\t" \
  "v_mfma_f32_32x32x16_bf16 v[32:47], v[88:91], v[64:67], v[0:15]\n\t" \
  "ds_read_b64_tr_b16 v[240:241], v155 offset:0\n\t" \
  "v_exp_f32 v56, v56\n\t" \
  "v_cvt_pk_bf16_f32 v115, v54, v55\n\t" \
  "v_exp_f32 v57, v57\n\t" \
  "v_mfma_f32_32x32x16_bf16 v[32:47], v[92:95], v[68:71], v[32:47]\n\t" \
  "ds_read_b64_tr_b16 v[242:243], v155 offset:512\n\t" \
  "v_exp_f32 v58, v58\n\t" \
  "v_cvt_pk_bf16_f32 v116, v56, v57\n\t" \
  "v_exp_f32 v59, v59\n\t" \
  "v_mfma_f32_32x32x16_bf16 a[128:143], v[96:99], v[104:107], a[128:143]\n\t" \
  "ds_read_b64_tr_b16 v[248:249], v137 offset:0\n\t" \
  "v_exp_f32 v60, v60\n\t" \
  "v_cvt_pk_bf16_f32 v117, v58, v59\n\t" \
  "v_exp_f32 v61, v61\n\t" \
  "s_waitcnt lgkmcnt(3)\n\t" \
  "v_mfma_f32_32x32x16_bf16 a[128:143], v[100:103], v[108:111], a[128:143]\n\t" \
  "ds_read_b64_tr_b16 v[250:251], v137 offset:512\n\t" \
  "v_exp_f32 v62, v62\n\t" \
  "v_cvt_pk_bf16_f32 v118, v60, v61\n\t" \
  "v_exp_f32 v63, v63\n\t" \
  "v_smfmac_f32_16x16x64_bf16 a[160:163], v[80:83], v[104:111], v154\n\t" \
  "s_waitcnt vmcnt(24)\n\t" \
  "v_mfma_f32_16x16x32_bf16 a[64:67], v[120:123], a[208:211], a[64:67]\n\t" \
  "buffer_load_dwordx4 a[200:203], v149, s[80:83], s64 offen offset:256 sc1\n\t" \
  "v_cvt_pk_bf16_f32 v119, v62, v63\n\t" \
  "v_mfma_f32_16x16x32_bf16 a[64:67], v[124:127], a[212:215], a[64:67]\n\t" \
  "v_exp_f32 v32, v32\n\t" \
  "v_exp_f32 v33, v33\n\t" \
  "v_mfma_f32_16x16x32_bf16 a[72:75], v[128:131], a[208:211], a[72:75]\n\t" \
  "buffer_load_dwordx4 a[204:207], v149, s[80:83], s64 offen offset:2304 sc1\n\t" \
  "v_cvt_pk_bf16_f32 v104, v32, v33\n\t" \
  "v_mfma_f32_16x16x32_bf16 a[72:75], v[132:135], a[212:215], a[72:75]\n\t" \
  "v_exp_f32 v34, v34\n\t" \
  "v_exp_f32 v35, v35\n\t" \
  "v_mfma_f32_16x16x32_bf16 a[80:83], a[164:167], a[208:211], a[80:83]\n\t" \
  "s_add_u32 m0, s72, 12288\n\t" \
  "v_cvt_pk_bf16_f32 v105, v34, v35\n\t" \
  "v_mfma_f32_16x16x32_bf16 a[80:83], a[168:171], a[212:215], a[80:83]\n\t" \
  "v_exp_f32 v36, v36\n\t" \
  "v_exp_f32 v37, v37\n\t" \
  "v_mfma_f32_16x16x32_bf16 a[88:91], a[172:175], a[208:211], a[88:91]\n\t" \
  "s_mov_b32 s68, 1245184\n\t" \
  "v_cvt_pk_bf16_f32 v106, v36, v37\n\t" \
  "v_mfma_f32_16x16x32_bf16 a[88:91], v[156:159], a[212:215], a[88:91]\n\t" \
  "v_exp_f32 v38, v38\n\t" \
  "v_exp_f32 v39, v39\n\t" \
  "v_mfma_f32_32x32x16_bf16 v[48:63], v[88:91], v[72:75], v[16:31]\n\t" \
  "ds_read_b128 v[88:91], v145 offset:45056\n\t" \
  "v_exp_f32 v40, v40\n\t" \
  "v_cvt_pk_bf16_f32 v107, v38, v39\n\t" \
  "v_exp_f32 v41, v41\n\t" \
  "v_mfma_f32_32x32x16_bf16 v[48:63], v[92:95], v[76:79], v[48:63]\n\t" \
  "ds_read_b128 v[92:95], v145 offset:46080\n\t" \
  "v_exp_f32 v42, v42\n\t" \
  "v_cvt_pk_bf16_f32 v108, v40, v41\n\t" \
  "v_exp_f32 v43, v43\n\t" \
  "v_mfma_f32_32x32x16_bf16 a[144:159], v[96:99], v[112:115], a[144:159]\n\t" \
  "ds_read_b128 v[96:99], v145 offset:43008\n\t" \
  "v_exp_f32 v44, v44\n\t" \
  "v_cvt_pk_bf16_f32 v109, v42, v43\n\t" \
  "v_exp_f32 v45, v45\n\t" \
  "v_mfma_f32_32x32x16_bf16 a[144:159], v[100:103], v[116:119], a[144:159]\n\t" \
  "ds_read_b128 v[100:103], v145 offset:44032\n\t" \
  "v_exp_f32 v46, v46\n\t" \
  "v_cvt_pk_bf16_f32 v110, v44, v45\n\t" \
  "v_exp_f32 v47, v47\n\t" \
  "v_smfmac_f32_16x16x64_bf16 a[160:163], v[84:87], v[112:119], v154\n\t" \
  "s_waitcnt vmcnt(20)\n\t" \
  "v_mfma_f32_16x16x32_bf16 a[68:71], v[120:123], a[216:219], a[68:71]\n\t" \
  "buffer_load_dwordx4 a[208:211], v150, s[80:83], s64 offen offset:0 sc1\n\t" \
  "v_cvt_pk_bf16_f32 v111, v46, v47\n\t" \
  "v_mfma_f32_16x16x32_bf16 a[68:71], v[124:127], a[220:223], a[68:71]\n\t" \
  "v_exp_f32 v48, v48\n\t" \
  "v_exp_f32 v49, v49\n\t" \
  "v_mfma_f32_16x16x32_bf16 a[76:79], v[128:131], a[216:219], a[76:79]\n\t" \
  "buffer_load_dwordx4 a[212:215], v150, s[80:83], s64 offen offset:2048 sc1\n\t" \
  "v_cvt_pk_bf16_f32 v112, v48, v49\n\t" \
  "v_mfma_f32_16x16x32_bf16 a[76:79], v[132:135], a[220:223], a[76:79]\n\t" \
  "v_exp_f32 v50, v50\n\t" \
  "v_exp_f32 v51, v51\n\t" \
  "v_mfma_f32_16x16x32_bf16 a[84:87], a[164:167], a[216:219], a[84:87]\n\t" \
  "buffer_load_dwordx4 v140, s[84:87], s68 offen offset:0 lds\n\t" \
  "v_cvt_pk_bf16_f32 v113, v50, v51\n\t" \
  "v_mfma_f32_16x16x32_bf16 a[84:87], a[168:171], a[220:223], a[84:87]\n\t" \
  "v_exp_f32 v52, v52\n\t" \
  "v_exp_f32 v53, v53\n\t" \
  "v_mfma_f32_16x16x32_bf16 a[92:95], a[172:175], a[216:219], a[92:95]\n\t" \
  "buffer_load_dwordx4 v141, s[84:87], s68 offen offset:1024 lds\n\t" \
  "v_cvt_pk_bf16_f32 v114, v52, v53\n\t" \
  "v_mfma_f32_16x16x32_bf16 a[92:95], v[156:159], a[220:223], a[92:95]\n\t" \
  "v_exp_f32 v54, v54\n\t" \
  "v_exp_f32 v55, v55\n\t" \
  "s_waitcnt lgkmcnt(1)\n\t" \
  "v_mfma_f32_32x32x16_bf16 v[32:47], v[88:91], v[64:67], v[0:15]\n\t" \
  "ds_read_b64_tr_b16 v[228:229], v146 offset:4096\n\t" \
  "v_exp_f32 v56, v56\n\t" \
  "v_cvt_pk_bf16_f32 v115, v54, v55\n\t" \
  "v_exp_f32 v57, v57\n\t" \
  "v_mfma_f32_32x32x16_bf16 v[32:47], v[92:95], v[68:71], v[32:47]\n\t" \
  "ds_read_b64_tr_b16 v[230:231], v146 offset:4608\n\t" \
  "v_exp_f32 v58, v58\n\t" \
  "v_cvt_pk_bf16_f32 v116, v56, v57\n\t" \
  "v_exp_f32 v59, v59\n\t" \
  "v_mfma_f32_32x32x16_bf16 a[128:143], v[96:99], v[104:107], a[128:143]\n\t" \
  "ds_read_b64_tr_b16 v[236:237], v147 offset:4096\n\t" \
  "v_exp_f32 v60, v60\n\t" \
  "v_cvt_pk_bf16_f32 v117, v58, v59\n\t" \
  "v_exp_f32 v61, v61\n\t" \
  "s_waitcnt lgkmcnt(3)\n\t" \
  "v_mfma_f32_32x32x16_bf16 a[128:143], v[100:103], v[108:111], a[128:143]\n\t" \
  "ds_read_b64_tr_b16 v[238:239], v147 offset:4608\n\t" \
  "v_exp_f32 v62, v62\n\t" \
  "v_cvt_pk_bf16_f32 v118, v60, v61\n\t" \
  "v_exp_f32 v63, v63\n\t" \
  "v_smfmac_f32_16x16x64_bf16 a[160:163], v[80:83], v[104:111], v154\n\t" \
  "s_waitcnt vmcnt(18)\n\t" \
  "s_barrier\n\t" \
  "v_mfma_f32_16x16x32_bf16 a[96:99], v[120:123], a[224:227], a[96:99]\n\t" \
  "buffer_load_dwordx4 a[216:219], v150, s[80:83], s64 offen offset:256 sc1\n\t" \
  "v_cvt_pk_bf16_f32 v119, v62, v63\n\t" \
  "v_mfma_f32_16x16x32_bf16 a[96:99], v[124:127], a[228:231], a[96:99]\n\t" \
  "v_exp_f32 v32, v32\n\t" \
  "v_exp_f32 v33, v33\n\t" \
  "v_mfma_f32_16x16x32_bf16 a[104:107], v[128:131], a[224:227], a[104:107]\n\t" \
  "buffer_load_dwordx4 a[220:223], v150, s[80:83], s64 offen offset:2304 sc1\n\t" \
  "v_cvt_pk_bf16_f32 v104, v32, v33\n\t" \
  "v_mfma_f32_16x16x32_bf16 a[104:107], v[132:135], a[228:231], a[104:107]\n\t" \
  "v_exp_f32 v34, v34\n\t" \
  "v_exp_f32 v35, v35\n\t" \
  "v_mfma_f32_16x16x32_bf16 a[112:115], a[164:167], a[224:227], a[112:115]\n\t" \
  "buffer_load_dwordx4 v142, s[84:87], s68 offen offset:2048 lds\n\t" \
  "v_cvt_pk_bf16_f32 v105, v34, v35\n\t" \
  "v_mfma_f32_16x16x32_bf16 a[112:115], a[168:171], a[228:231], a[112:115]\n\t" \
  "v_exp_f32 v36, v36\n\t" \
  "v_exp_f32 v37, v37\n\t" \
  "v_mfma_f32_16x16x32_bf16 a[120:123], a[172:175], a[224:227], a[120:123]\n\t" \
  "buffer_load_dwordx4 v143, s[84:87], s68 offen offset:3072 lds\n\t" \
  "v_cvt_pk_bf16_f32 v106, v36, v37\n\t" \
  "v_mfma_f32_16x16x32_bf16 a[120:123], v[156:159], a[228:231], a[120:123]\n\t" \
  "v_exp_f32 v38, v38\n\t" \
  "v_exp_f32 v39, v39\n\t" \
  "v_mfma_f32_32x32x16_bf16 v[48:63], v[88:91], v[72:75], v[16:31]\n\t" \
  "ds_read_b128 v[88:91], v145 offset:0\n\t" \
  "v_exp_f32 v40, v40\n\t" \
  "v_cvt_pk_bf16_f32 v107, v38, v39\n\t" \
  "v_exp_f32 v41, v41\n\t" \
  "v_mfma_f32_32x32x16_bf16 v[48:63], v[92:95], v[76:79], v[48:63]\n\t" \
  "ds_read_b128 v[92:95], v145 offset:1024\n\t" \
  "v_exp_f32 v42, v42\n\t" \
  "v_cvt_pk_bf16_f32 v108, v40, v41\n\t" \
  "v_exp_f32 v43, v43\n\t" \
  "v_mfma_f32_32x32x16_bf16 a[144:159], v[96:99], v[112:115], a[144:159]\n\t" \
  "ds_read_b128 v[96:99], v145 offset:47104\n\t" \
  "v_exp_f32 v44, v44\n\t" \
  "v_cvt_pk_bf16_f32 v109, v42, v43\n\t" \
  "v_exp_f32 v45, v45\n\t" \
  "v_mfma_f32_32x32x16_bf16 a[144:159], v[100:103], v[116:119], a[144:159]\n\t" \
  "ds_read_b128 v[100:103], v145 offset:48128\n\t" \
  "v_exp_f32 v46, v46\n\t" \
  "v_cvt_pk_bf16_f32 v110, v44, v45\n\t" \
  "v_exp_f32 v47, v47\n\t" \
  "v_smfmac_f32_16x16x64_bf16 a[160:163], v[84:87], v[112:119], v154\n\t" \
  "s_add_u32 m0, s73, 16384\n\t" \
  "s_mov_b32 s69, 212992\n\t" \
  "s_waitcnt vmcnt(20)\n\t" \
  "v_mfma_f32_16x16x32_bf16 a[100:103], v[120:123], a[232:235], a[100:103]\n\t" \
  "buffer_load_dwordx4 a[224:227], v151, s[80:83], s64 offen offset:0 sc1\n\t" \
  "v_cvt_pk_bf16_f32 v111, v46, v47\n\t" \
  "v_mfma_f32_16x16x32_bf16 a[100:103], v[124:127], a[236:239], a[100:103]\n\t" \
  "v_exp_f32 v48, v48\n\t" \
  "v_exp_f32 v49, v49\n\t" \
  "v_mfma_f32_16x16x32_bf16 a[108:111], v[128:131], a[232:235], a[108:111]\n\t" \
  "buffer_load_dwordx4 a[228:231], v151, s[80:83], s64 offen offset:2048 sc1\n\t" \
  "v_cvt_pk_bf16_f32 v112, v48, v49\n\t" \
  "v_mfma_f32_16x16x32_bf16 a[108:111], v[132:135], a[236:239], a[108:111]\n\t" \
  "v_exp_f32 v50, v50\n\t" \
  "v_exp_f32 v51, v51\n\t" \
  "v_mfma_f32_16x16x32_bf16 a[116:119], a[164:167], a[232:235], a[116:119]\n\t" \
  "buffer_load_dwordx4 v144, s[88:91], s69 offen offset:0 sc1 lds\n\t" \
  "v_cvt_pk_bf16_f32 v113, v50, v51\n\t" \
  "v_mfma_f32_16x16x32_bf16 a[116:119], a[168:171], a[236:239], a[116:119]\n\t" \
  "v_exp_f32 v52, v52\n\t" \
  "v_exp_f32 v53, v53\n\t" \
  "v_mfma_f32_16x16x32_bf16 a[124:127], a[172:175], a[232:235], a[124:127]\n\t" \
  "buffer_load_dwordx4 v144, s[88:91], s69 offen offset:1024 sc1 lds\n\t" \
  "v_cvt_pk_bf16_f32 v114, v52, v53\n\t" \
  "v_mfma_f32_16x16x32_bf16 a[124:127], v[156:159], a[236:239], a[124:127]\n\t" \
  "v_exp_f32 v54, v54\n\t" \
  "v_exp_f32 v55, v55\n\t" \
  "s_waitcnt lgkmcnt(1)\n\t" \
  "v_mfma_f32_32x32x16_bf16 v[32:47], v[88:91], v[64:67], v[0:15]\n\t" \
  "ds_read_b64_tr_b16 v[244:245], v155 offset:4096\n\t" \
  "v_exp_f32 v56, v56\n\t" \
  "v_cvt_pk_bf16_f32 v115, v54, v55\n\t" \
  "v_exp_f32 v57, v57\n\t" \
  "v_mfma_f32_32x32x16_bf16 v[32:47], v[92:95], v[68:71], v[32:47]\n\t" \
  "ds_read_b64_tr_b16 v[246:247], v155 offset:4608\n\t" \
  "v_exp_f32 v58, v58\n\t" \
  "v_cvt_pk_bf16_f32 v116, v56, v57\n\t" \
  "v_exp_f32 v59, v59\n\t" \
  "v_mfma_f32_32x32x16_bf16 a[128:143], v[96:99], v[104:107], a[128:143]\n\t" \
  "ds_read_b64_tr_b16 v[252:253], v137 offset:4096\n\t" \
  "v_exp_f32 v60, v60\n\t" \
  "v_cvt_pk_bf16_f32 v117, v58, v59\n\t" \
  "v_exp_f32 v61, v61\n\t" \
  "s_waitcnt lgkmcnt(3)\n\t" \
  "v_mfma_f32_32x32x16_bf16 a[128:143], v[100:103], v[108:111], a[128:143]\n\t" \
  "ds_read_b64_tr_b16 v[254:255], v137 offset:4608\n\t" \
  "v_exp_f32 v62, v62\n\t" \
  "v_cvt_pk_bf16_f32 v118, v60, v61\n\t" \
  "v_exp_f32 v63, v63\n\t" \
  "v_smfmac_f32_16x16x64_bf16 a[160:163], v[80:83], v[104:111], v154\n\t" \
  "buffer_load_dwordx4 v144, s[88:91], s69 offen offset:2048 sc1 lds\n\t" \
  "buffer_load_dwordx4 v144, s[88:91], s69 offen offset:3072 sc1 lds\n\t" \
  "s_waitcnt vmcnt(24) lgkmcnt(0)\n\t" \
  "v_mfma_f32_16x16x32_bf16 a[0:3], v[224:227], a[176:179], a[0:3]\n\t" \
  "buffer_load_dwordx4 a[232:235], v151, s[80:83], s64 offen offset:256 sc1\n\t" \
  "v_cvt_pk_bf16_f32 v119, v62, v63\n\t" \
  "v_mfma_f32_16x16x32_bf16 a[0:3], v[228:231], a[180:183], a[0:3]\n\t" \
  "v_exp_f32 v32, v32\n\t" \
  "v_exp_f32 v33, v33\n\t" \
  "v_mfma_f32_16x16x32_bf16 a[8:11], v[232:235], a[176:179], a[8:11]\n\t" \
  "buffer_load_dwordx4 a[236:239], v151, s[80:83], s64 offen offset:2304 sc1\n\t" \
  "v_cvt_pk_bf16_f32 v104, v32, v33\n\t" \
  "v_mfma_f32_16x16x32_bf16 a[8:11], v[236:239], a[180:183], a[8:11]\n\t" \
  "v_exp_f32 v34, v34\n\t" \
  "v_exp_f32 v35, v35\n\t" \
  "v_mfma_f32_16x16x32_bf16 a[16:19], v[240:243], a[176:179], a[16:19]\n\t" \
  "s_add_u32 m0, s72, 0\n\t" \
  "v_cvt_pk_bf16_f32 v105, v34, v35\n\t" \
  "v_mfma_f32_16x16x32_bf16 a[16:19], v[244:247], a[180:183], a[16:19]\n\t" \
  "v_exp_f32 v36, v36\n\t" \
  "v_exp_f32 v37, v37\n\t" \
  "v_mfma_f32_16x16x32_bf16 a[24:27], v[248:251], a[176:179], a[24:27]\n\t" \
  "s_mov_b32 s68, 1310720\n\t" \
  "v_cvt_pk_bf16_f32 v106, v36, v37\n\t" \
  "v_mfma_f32_16x16x32_bf16 a[24:27], v[252:255], a[180:183], a[24:27]\n\t" \
  "v_exp_f32 v38, v38\n\t" \
  "v_exp_f32 v39, v39\n\t" \
  "v_mfma_f32_32x32x16_bf16 v[48:63], v[88:91], v[72:75], v[16:31]\n\t" \
  "ds_read_b128 v[88:91], v145 offset:4096\n\t" \
  "v_exp_f32 v40, v40\n\t" \
  "v_cvt_pk_bf16_f32 v107, v38, v39\n\t" \
  "v_exp_f32 v41, v41\n\t" \
  "v_mfma_f32_32x32x16_bf16 v[48:63], v[92:95], v[76:79], v[48:63]\n\t" \
  "ds_read_b128 v[92:95], v145 offset:5120\n\t" \
  "v_exp_f32 v42, v42\n\t" \
  "v_cvt_pk_bf16_f32 v108, v40, v41\n\t" \
  "v_exp_f32 v43, v43\n\t" \
  "v_mfma_f32_32x32x16_bf16 a[144:159], v[96:99], v[112:115], a[144:159]\n\t" \
  "ds_read_b128 v[96:99], v145 offset:2048\n\t" \
  "v_exp_f32 v44, v44\n\t" \
  "v_cvt_pk_bf16_f32 v109, v42, v43\n\t" \
  "v_exp_f32 v45, v45\n\t" \
  "v_mfma_f32_32x32x16_bf16 a[144:159], v[100:103], v[116:119], a[144:159]\n\t" \
  "ds_read_b128 v[100:103], v145 offset:3072\n\t" \
  "v_exp_f32 v46, v46\n\t" \
  "v_cvt_pk_bf16_f32 v110, v44, v45\n\t" \
  "v_exp_f32 v47, v47\n\t" \
  "v_smfmac_f32_16x16x64_bf16 a[160:163], v[84:87], v[112:119], v154\n\t" \
  "s_mov_b32 s64, 36864\n\t" \
  "s_waitcnt vmcnt(20)\n\t" \
  "v_mfma_f32_16x16x32_bf16 a[4:7], v[224:227], a[184:187], a[4:7]\n\t" \
  "buffer_load_dwordx4 a[176:179], v148, s[80:83], s64 offen offset:0 sc1\n\t" \
  "v_cvt_pk_bf16_f32 v111, v46, v47\n\t" \
  "v_mfma_f32_16x16x32_bf16 a[4:7], v[228:231], a[188:191], a[4:7]\n\t" \
  "v_exp_f32 v48, v48\n\t" \
  "v_exp_f32 v49, v49\n\t" \
  "v_mfma_f32_16x16x32_bf16 a[12:15], v[232:235], a[184:187], a[12:15]\n\t" \
  "buffer_load_dwordx4 a[180:183], v148, s[80:83], s64 offen offset:2048 sc1\n\t" \
  "v_cvt_pk_bf16_f32 v112, v48, v49\n\t" \
  "v_mfma_f32_16x16x32_bf16 a[12:15], v[236:239], a[188:191], a[12:15]\n\t" \
  "v_exp_f32 v50, v50\n\t" \
  "v_exp_f32 v51, v51\n\t" \
  "v_mfma_f32_16x16x32_bf16 a[20:23], v[240:243], a[184:187], a[20:23]\n\t" \
  "buffer_load_dwordx4 v140, s[84:87], s68 offen offset:0 lds\n\t" \
  "v_cvt_pk_bf16_f32 v113, v50, v51\n\t" \
  "v_mfma_f32_16x16x32_bf16 a[20:23], v[244:247], a[188:191], a[20:23]\n\t" \
  "v_exp_f32 v52, v52\n\t" \
  "v_exp_f32 v53, v53\n\t" \
  "v_mfma_f32_16x16x32_bf16 a[28:31], v[248:251], a[184:187], a[28:31]\n\t" \
  "buffer_load_dwordx4 v141, s[84:87], s68 offen offset:1024 lds\n\t" \
  "v_cvt_pk_bf16_f32 v114, v52, v53\n\t" \
  "v_mfma_f32_16x16x32_bf16 a[28:31], v[252:255], a[188:191], a[28:31]\n\t" \
  "v_exp_f32 v54, v54\n\t" \
  "v_exp_f32 v55, v55\n\t" \
  "s_waitcnt lgkmcnt(1)\n\t" \
  "v_mfma_f32_32x32x16_bf16 v[32:47], v[88:91], v[64:67], v[0:15]\n\t" \
  "ds_read_b64_tr_b16 v[120:121], v146 offset:8192\n\t" \
  "v_exp_f32 v56, v56\n\t" \
  "v_cvt_pk_bf16_f32 v115, v54, v55\n\t" \
  "v_exp_f32 v57, v57\n\t" \
  "v_mfma_f32_32x32x16_bf16 v[32:47], v[92:95], v[68:71], v[32:47]\n\t" \
  "ds_read_b64_tr_b16 v[122:123], v146 offset:8704\n\t" \
  "v_exp_f32 v58, v58\n\t" \
  "v_cvt_pk_bf16_f32 v116, v56, v57\n\t" \
  "v_exp_f32 v59, v59\n\t" \
  "v_mfma_f32_32x32x16_bf16 a[128:143], v[96:99], v[104:107], a[128:143]\n\t" \
  "ds_read_b64_tr_b16 v[128:129], v147 offset:8192\n\t" \
  "v_exp_f32 v60, v60\n\t" \
  "v_cvt_pk_bf16_f32 v117, v58, v59\n\t" \
  "v_exp_f32 v61, v61\n\t" \
  "s_waitcnt lgkmcnt(3)\n\t" \
  "v_mfma_f32_32x32x16_bf16 a[128:143], v[100:103], v[108:111], a[128:143]\n\t" \
  "ds_read_b64_tr_b16 v[130:131], v147 offset:8704\n\t" \
  "v_exp_f32 v62, v62\n\t" \
  "v_cvt_pk_bf16_f32 v118, v60, v61\n\t" \
  "v_exp_f32 v63, v63\n\t" \
  "v_smfmac_f32_16x16x64_bf16 a[160:163], v[80:83], v[104:111], v154\n\t" \
  "global_load_dword v136, v152, s[52:53]\n\t" \
  "s_waitcnt vmcnt(23)\n\t" \
  "v_mfma_f32_16x16x32_bf16 a[32:35], v[224:227], a[192:195], a[32:35]\n\t" \
  "buffer_load_dwordx4 a[184:187], v148, s[80:83], s64 offen offset:256 sc1\n\t" \
  "v_cvt_pk_bf16_f32 v119, v62, v63\n\t" \
  "v_mfma_f32_16x16x32_bf16 a[32:35], v[228:231], a[196:199], a[32:35]\n\t" \
  "v_exp_f32 v32, v32\n\t" \
  "v_exp_f32 v33, v33\n\t" \
  "v_mfma_f32_16x16x32_bf16 a[40:43], v[232:235], a[192:195], a[40:43]\n\t" \
  "buffer_load_dwordx4 a[188:191], v148, s[80:83], s64 offen offset:2304 sc1\n\t" \
  "v_cvt_pk_bf16_f32 v104, v32, v33\n\t" \
  "v_mfma_f32_16x16x32_bf16 a[40:43], v[236:239], a[196:199], a[40:43]\n\t" \
  "v_exp_f32 v34, v34\n\t" \
  "v_exp_f32 v35, v35\n\t" \
  "v_mfma_f32_16x16x32_bf16 a[48:51], v[240:243], a[192:195], a[48:51]\n\t" \
  "buffer_load_dwordx4 v142, s[84:87], s68 offen offset:2048 lds\n\t" \
  "v_cvt_pk_bf16_f32 v105, v34, v35\n\t" \
  "v_mfma_f32_16x16x32_bf16 a[48:51], v[244:247], a[196:199], a[48:51]\n\t" \
  "v_exp_f32 v36, v36\n\t" \
  "v_exp_f32 v37, v37\n\t" \
  "v_mfma_f32_16x16x32_bf16 a[56:59], v[248:251], a[192:195], a[56:59]\n\t" \
  "buffer_load_dwordx4 v143, s[84:87], s68 offen offset:3072 lds\n\t" \
  "v_cvt_pk_bf16_f32 v106, v36, v37\n\t" \
  "v_mfma_f32_16x16x32_bf16 a[56:59], v[252:255], a[196:199], a[56:59]\n\t" \
  "v_exp_f32 v38, v38\n\t" \
  "v_exp_f32 v39, v39\n\t" \
  "v_mfma_f32_32x32x16_bf16 v[48:63], v[88:91], v[72:75], v[16:31]\n\t" \
  "ds_read_b128 v[88:91], v145 offset:8192\n\t" \
  "v_exp_f32 v40, v40\n\t" \
  "v_cvt_pk_bf16_f32 v107, v38, v39\n\t" \
  "v_exp_f32 v41, v41\n\t" \
  "v_mfma_f32_32x32x16_bf16 v[48:63], v[92:95], v[76:79], v[48:63]\n\t" \
  "ds_read_b128 v[92:95], v145 offset:9216\n\t" \
  "v_exp_f32 v42, v42\n\t" \
  "v_cvt_pk_bf16_f32 v108, v40, v41\n\t" \
  "v_exp_f32 v43, v43\n\t" \
  "v_mfma_f32_32x32x16_bf16 a[144:159], v[96:99], v[112:115], a[144:159]\n\t" \
  "ds_read_b128 v[96:99], v145 offset:6144\n\t" \
  "v_exp_f32 v44, v44\n\t" \
  "v_cvt_pk_bf16_f32 v109, v42, v43\n\t" \
  "v_exp_f32 v45, v45\n\t" \
  "v_mfma_f32_32x32x16_bf16 a[144:159], v[100:103], v[116:119], a[144:159]\n\t" \
  "ds_read_b128 v[100:103], v145 offset:7168\n\t" \
  "v_exp_f32 v46, v46\n\t" \
  "v_cvt_pk_bf16_f32 v110, v44, v45\n\t" \
  "v_exp_f32 v47, v47\n\t" \
  "v_smfmac_f32_16x16x64_bf16 a[160:163], v[84:87], v[112:119], v154\n\t" \
  "s_waitcnt vmcnt(25)\n\t" \
  "v_mfma_f32_16x16x32_bf16 a[36:39], v[224:227], a[200:203], a[36:39]\n\t" \
  "buffer_load_dwordx4 a[192:195], v149, s[80:83], s64 offen offset:0 sc1\n\t" \
  "v_cvt_pk_bf16_f32 v111, v46, v47\n\t" \
  "v_mfma_f32_16x16x32_bf16 a[36:39], v[228:231], a[204:207], a[36:39]\n\t" \
  "v_exp_f32 v48, v48\n\t" \
  "v_exp_f32 v49, v49\n\t" \
  "v_mfma_f32_16x16x32_bf16 a[44:47], v[232:235], a[200:203], a[44:47]\n\t" \
  "buffer_load_dwordx4 a[196:199], v149, s[80:83], s64 offen offset:2048 sc1\n\t" \
  "v_cvt_pk_bf16_f32 v112, v48, v49\n\t" \
  "v_mfma_f32_16x16x32_bf16 a[44:47], v[236:239], a[204:207], a[44:47]\n\t" \
  "v_exp_f32 v50, v50\n\t" \
  "v_exp_f32 v51, v51\n\t" \
  "v_mfma_f32_16x16x32_bf16 a[52:55], v[240:243], a[200:203], a[52:55]\n\t" \
  "v_exp_f32 v52, v52\n\t" \
  "v_mfma_f32_16x16x32_bf16 a[52:55], v[244:247], a[204:207], a[52:55]\n\t" \
  "v_cvt_pk_bf16_f32 v113, v50, v51\n\t" \
  "v_exp_f32 v53, v53\n\t" \
  "v_mfma_f32_16x16x32_bf16 a[60:63], v[248:251], a[200:203], a[60:63]\n\t" \
  "v_exp_f32 v54, v54\n\t" \
  "v_mfma_f32_16x16x32_bf16 a[60:63], v[252:255], a[204:207], a[60:63]\n\t" \
  "v_cvt_pk_bf16_f32 v114, v52, v53\n\t" \
  "v_exp_f32 v55, v55\n\t" \
  "s_waitcnt lgkmcnt(1)\n\t" \
  "v_mfma_f32_32x32x16_bf16 v[32:47], v[88:91], v[64:67], v[0:15]\n\t" \
  "ds_read_b64_tr_b16 a[164:165], v155 offset:8192\n\t" \
  "v_exp_f32 v56, v56\n\t" \
  "v_cvt_pk_bf16_f32 v115, v54, v55\n\t" \
  "v_exp_f32 v57, v57\n\t" \
  "v_mfma_f32_32x32x16_bf16 v[32:47], v[92:95], v[68:71], v[32:47]\n\t" \
  "ds_read_b64_tr_b16 a[166:167], v155 offset:8704\n\t" \
  "v_exp_f32 v58, v58\n\t" \
  "v_cvt_pk_bf16_f32 v116, v56, v57\n\t" \
  "v_exp_f32 v59, v59\n\t" \
  "v_mfma_f32_32x32x16_bf16 a[128:143], v[96:99], v[104:107], a[128:143]\n\t" \
  "ds_read_b64_tr_b16 a[172:173], v137 offset:8192\n\t" \
  "v_exp_f32 v60, v60\n\t" \
  "v_cvt_pk_bf16_f32 v117, v58, v59\n\t" \
  "v_exp_f32 v61, v61\n\t" \
  "s_waitcnt lgkmcnt(3)\n\t" \
  "v_mfma_f32_32x32x16_bf16 a[128:143], v[100:103], v[108:111], a[128:143]\n\t" \
  "ds_read_b64_tr_b16 a[174:175], v137 offset:8704\n\t" \
  "v_exp_f32 v62, v62\n\t" \
  "v_cvt_pk_bf16_f32 v118, v60, v61\n\t" \
  "v_exp_f32 v63, v63\n\t" \
  "v_smfmac_f32_16x16x64_bf16 a[160:163], v[80:83], v[104:111], v154\n\t" \
  "s_waitcnt vmcnt(25)\n\t" \
  "v_mfma_f32_16x16x32_bf16 a[64:67], v[224:227], a[208:211], a[64:67]\n\t" \
  "buffer_load_dwordx4 a[200:203], v149, s[80:83], s64 offen offset:256 sc1\n\t" \
  "v_cvt_pk_bf16_f32 v119, v62, v63\n\t" \
  "v_mfma_f32_16x16x32_bf16 a[64:67], v[228:231], a[212:215], a[64:67]\n\t" \
  "v_exp_f32 v32, v32\n\t" \
  "v_exp_f32 v33, v33\n\t" \
  "v_mfma_f32_16x16x32_bf16 a[72:75], v[232:235], a[208:211], a[72:75]\n\t" \
  "buffer_load_dwordx4 a[204:207], v149, s[80:83], s64 offen offset:2304 sc1\n\t" \
  "v_cvt_pk_bf16_f32 v104, v32, v33\n\t" \
  "v_mfma_f32_16x16x32_bf16 a[72:75], v[236:239], a[212:215], a[72:75]\n\t" \
  "v_exp_f32 v34, v34\n\t" \
  "v_exp_f32 v35, v35\n\t" \
  "v_mfma_f32_16x16x32_bf16 a[80:83], v[240:243], a[208:211], a[80:83]\n\t" \
  "s_add_u32 m0, s72, 4096\n\t" \
  "v_cvt_pk_bf16_f32 v105, v34, v35\n\t" \
  "v_mfma_f32_16x16x32_bf16 a[80:83], v[244:247], a[212:215], a[80:83]\n\t" \
  "v_exp_f32 v36, v36\n\t" \
  "v_exp_f32 v37, v37\n\t" \
  "v_mfma_f32_16x16x32_bf16 a[88:91], v[248:251], a[208:211], a[88:91]\n\t" \
  "s_mov_b32 s68, 1376256\n\t" \
  "v_cvt_pk_bf16_f32 v106, v36, v37\n\t" \
  "v_mfma_f32_16x16x32_bf16 a[88:91], v[252:255], a[212:215], a[88:91]\n\t" \
  "v_exp_f32 v38, v38\n\t" \
  "v_exp_f32 v39, v39\n\t" \
  "v_mfma_f32_32x32x16_bf16 v[48:63], v[88:91], v[72:75], v[16:31]\n\t" \
  "ds_read_b128 v[88:91], v145 offset:12288\n\t" \
  "v_exp_f32 v40, v40\n\t" \
  "v_cvt_pk_bf16_f32 v107, v38, v39\n\t" \
  "v_exp_f32 v41, v41\n\t" \
  "v_mfma_f32_32x32x16_bf16 v[48:63], v[92:95], v[76:79], v[48:63]\n\t" \
  "ds_read_b128 v[92:95], v145 offset:13312\n\t" \
  "v_exp_f32 v42, v42\n\t" \
  "v_cvt_pk_bf16_f32 v108, v40, v41\n\t" \
  "v_exp_f32 v43, v43\n\t" \
  "v_mfma_f32_32x32x16_bf16 a[144:159], v[96:99], v[112:115], a[144:159]\n\t" \
  "ds_read_b128 v[96:99], v145 offset:10240\n\t" \
  "v_exp_f32 v44, v44\n\t" \
  "v_cvt_pk_bf16_f32 v109, v42, v43\n\t" \
  "v_exp_f32 v45, v45\n\t" \
  "v_mfma_f32_32x32x16_bf16 a[144:159], v[100:103], v[116:119], a[144:159]\n\t" \
  "ds_read_b128 v[100:103], v145 offset:11264\n\t" \
  "v_exp_f32 v46, v46\n\t" \
  "v_cvt_pk_bf16_f32 v110, v44, v45\n\t" \
  "v_exp_f32 v47, v47\n\t" \
  "v_smfmac_f32_16x16x64_bf16 a[160:163], v[84:87], v[112:119], v154\n\t" \
  "s_waitcnt vmcnt(21)\n\t" \
  "v_mfma_f32_16x16x32_bf16 a[68:71], v[224:227], a[216:219], a[68:71]\n\t" \
  "buffer_load_dwordx4 a[208:211], v150, s[80:83], s64 offen offset:0 sc1\n\t" \
  "v_cvt_pk_bf16_f32 v111, v46, v47\n\t" \
  "v_mfma_f32_16x16x32_bf16 a[68:71], v[228:231], a[220:223], a[68:71]\n\t" \
  "v_exp_f32 v48, v48\n\t" \
  "v_exp_f32 v49, v49\n\t" \
  "v_mfma_f32_16x16x32_bf16 a[76:79], v[232:235], a[216:219], a[76:79]\n\t" \
  "buffer_load_dwordx4 a[212:215], v150, s[80:83], s64 offen offset:2048 sc1\n\t" \
  "v_cvt_pk_bf16_f32 v112, v48, v49\n\t" \
  "v_mfma_f32_16x16x32_bf16 a[76:79], v[236:239], a[220:223], a[76:79]\n\t" \
  "v_exp_f32 v50, v50\n\t" \
  "v_exp_f32 v51, v51\n\t" \
  "v_mfma_f32_16x16x32_bf16 a[84:87], v[240:243], a[216:219], a[84:87]\n\t" \
  "buffer_load_dwordx4 v140, s[84:87], s68 offen offset:0 lds\n\t" \
  "v_cvt_pk_bf16_f32 v113, v50, v51\n\t" \
  "v_mfma_f32_16x16x32_bf16 a[84:87], v[244:247], a[220:223], a[84:87]\n\t" \
  "v_exp_f32 v52, v52\n\t" \
  "v_exp_f32 v53, v53\n\t" \
  "v_mfma_f32_16x16x32_bf16 a[92:95], v[248:251], a[216:219], a[92:95]\n\t" \
  "buffer_load_dwordx4 v141, s[84:87], s68 offen offset:1024 lds\n\t" \
  "v_cvt_pk_bf16_f32 v114, v52, v53\n\t" \
  "v_mfma_f32_16x16x32_bf16 a[92:95], v[252:255], a[220:223], a[92:95]\n\t" \
  "v_exp_f32 v54, v54\n\t" \
  "v_exp_f32 v55, v55\n\t" \
  "s_waitcnt lgkmcnt(1)\n\t" \
  "v_mfma_f32_32x32x16_bf16 v[32:47], v[88:91], v[64:67], v[0:15]\n\t" \
  "ds_read_b64_tr_b16 v[124:125], v146 offset:12288\n\t" \
  "v_exp_f32 v56, v56\n\t" \
  "v_cvt_pk_bf16_f32 v115, v54, v55\n\t" \
  "v_exp_f32 v57, v57\n\t" \
  "v_mfma_f32_32x32x16_bf16 v[32:47], v[92:95], v[68:71], v[32:47]\n\t" \
  "ds_read_b64_tr_b16 v[126:127], v146 offset:12800\n\t" \
  "v_exp_f32 v58, v58\n\t" \
  "v_cvt_pk_bf16_f32 v116, v56, v57\n\t" \
  "v_exp_f32 v59, v59\n\t" \
  "v_mfma_f32_32x32x16_bf16 a[128:143], v[96:99], v[104:107], a[128:143]\n\t" \
  "ds_read_b64_tr_b16 v[132:133], v147 offset:12288\n\t" \
  "v_exp_f32 v60, v60\n\t" \
  "v_cvt_pk_bf16_f32 v117, v58, v59\n\t" \
  "v_exp_f32 v61, v61\n\t" \
  "s_waitcnt lgkmcnt(3)\n\t" \
  "v_mfma_f32_32x32x16_bf16 a[128:143], v[100:103], v[108:111], a[128:143]\n\t" \
  "ds_read_b64_tr_b16 v[134:135], v147 offset:12800\n\t" \
  "v_exp_f32 v62, v62\n\t" \
  "v_cvt_pk_bf16_f32 v118, v60, v61\n\t" \
  "v_exp_f32 v63, v63\n\t" \
  "v_smfmac_f32_16x16x64_bf16 a[160:163], v[80:83], v[104:111], v154\n\t" \
  "s_waitcnt vmcnt(19)\n\t" \
  "s_barrier\n\t" \
  "v_mfma_f32_16x16x32_bf16 a[96:99], v[224:227], a[224:227], a[96:99]\n\t" \
  "buffer_load_dwordx4 a[216:219], v150, s[80:83], s64 offen offset:256 sc1\n\t" \
  "v_cvt_pk_bf16_f32 v119, v62, v63\n\t" \
  "v_mfma_f32_16x16x32_bf16 a[96:99], v[228:231], a[228:231], a[96:99]\n\t" \
  "v_exp_f32 v32, v32\n\t" \
  "v_exp_f32 v33, v33\n\t" \
  "v_mfma_f32_16x16x32_bf16 a[104:107], v[232:235], a[224:227], a[104:107]\n\t" \
  "buffer_load_dwordx4 a[220:223], v150, s[80:83], s64 offen offset:2304 sc1\n\t" \
  "v_cvt_pk_bf16_f32 v104, v32, v33\n\t" \
  "v_mfma_f32_16x16x32_bf16 a[104:107], v[236:239], a[228:231], a[104:107]\n\t" \
  "v_exp_f32 v34, v34\n\t" \
  "v_exp_f32 v35, v35\n\t" \
  "v_mfma_f32_16x16x32_bf16 a[112:115], v[240:243], a[224:227], a[112:115]\n\t" \
  "buffer_load_dwordx4 v142, s[84:87], s68 offen offset:2048 lds\n\t" \
  "v_cvt_pk_bf16_f32 v105, v34, v35\n\t" \
  "v_mfma_f32_16x16x32_bf16 a[112:115], v[244:247], a[228:231], a[112:115]\n\t" \
  "v_exp_f32 v36, v36\n\t" \
  "v_exp_f32 v37, v37\n\t" \
  "v_mfma_f32_16x16x32_bf16 a[120:123], v[248:251], a[224:227], a[120:123]\n\t" \
  "buffer_load_dwordx4 v143, s[84:87], s68 offen offset:3072 lds\n\t" \
  "v_cvt_pk_bf16_f32 v106, v36, v37\n\t" \
  "v_mfma_f32_16x16x32_bf16 a[120:123], v[252:255], a[228:231], a[120:123]\n\t" \
  "v_exp_f32 v38, v38\n\t" \
  "v_exp_f32 v39, v39\n\t" \
  "v_mfma_f32_32x32x16_bf16 v[48:63], v[88:91], v[72:75], v[16:31]\n\t" \
  "ds_read_b128 v[88:91], v145 offset:16384\n\t" \
  "v_exp_f32 v40, v40\n\t" \
  "v_cvt_pk_bf16_f32 v107, v38, v39\n\t" \
  "v_exp_f32 v41, v41\n\t" \
  "v_mfma_f32_32x32x16_bf16 v[48:63], v[92:95], v[76:79], v[48:63]\n\t" \
  "ds_read_b128 v[92:95], v145 offset:17408\n\t" \
  "v_exp_f32 v42, v42\n\t" \
  "v_cvt_pk_bf16_f32 v108, v40, v41\n\t" \
  "v_exp_f32 v43, v43\n\t" \
  "v_mfma_f32_32x32x16_bf16 a[144:159], v[96:99], v[112:115], a[144:159]\n\t" \
  "ds_read_b128 v[96:99], v145 offset:14336\n\t" \
  "v_exp_f32 v44, v44\n\t" \
  "v_cvt_pk_bf16_f32 v109, v42, v43\n\t" \
  "v_exp_f32 v45, v45\n\t" \
  "v_mfma_f32_32x32x16_bf16 a[144:159], v[100:103], v[116:119], a[144:159]\n\t" \
  "ds_read_b128 v[100:103], v145 offset:15360\n\t" \
  "v_exp_f32 v46, v46\n\t" \
  "v_cvt_pk_bf16_f32 v110, v44, v45\n\t" \
  "v_exp_f32 v47, v47\n\t" \
  "v_smfmac_f32_16x16x64_bf16 a[160:163], v[84:87], v[112:119], v154\n\t" \
  "s_add_u32 m0, s73, 32768\n\t" \
  "s_mov_b32 s69, 229376\n\t" \
  "s_waitcnt vmcnt(21)\n\t" \
  "v_mfma_f32_16x16x32_bf16 a[100:103], v[224:227], a[232:235], a[100:103]\n\t" \
  "buffer_load_dwordx4 a[224:227], v151, s[80:83], s64 offen offset:0 sc1\n\t" \
  "v_cvt_pk_bf16_f32 v111, v46, v47\n\t" \
  "v_mfma_f32_16x16x32_bf16 a[100:103], v[228:231], a[236:239], a[100:103]\n\t" \
  "v_exp_f32 v48, v48\n\t" \
  "v_exp_f32 v49, v49\n\t" \
  "v_mfma_f32_16x16x32_bf16 a[108:111], v[232:235], a[232:235], a[108:111]\n\t" \
  "buffer_load_dwordx4 a[228:231], v151, s[80:83], s64 offen offset:2048 sc1\n\t" \
  "v_cvt_pk_bf16_f32 v112, v48, v49\n\t" \
  "v_mfma_f32_16x16x32_bf16 a[108:111], v[236:239], a[236:239], a[108:111]\n\t" \
  "v_exp_f32 v50, v50\n\t" \
  "v_exp_f32 v51, v51\n\t" \
  "v_mfma_f32_16x16x32_bf16 a[116:119], v[240:243], a[232:235], a[116:119]\n\t" \
  "buffer_load_dwordx4 v144, s[88:91], s69 offen offset:0 sc1 lds\n\t" \
  "v_cvt_pk_bf16_f32 v113, v50, v51\n\t" \
  "v_mfma_f32_16x16x32_bf16 a[116:119], v[244:247], a[236:239], a[116:119]\n\t" \
  "v_exp_f32 v52, v52\n\t" \
  "v_exp_f32 v53, v53\n\t" \
  "v_mfma_f32_16x16x32_bf16 a[124:127], v[248:251], a[232:235], a[124:127]\n\t" \
  "buffer_load_dwordx4 v144, s[88:91], s69 offen offset:1024 sc1 lds\n\t" \
  "v_cvt_pk_bf16_f32 v114, v52, v53\n\t" \
  "v_mfma_f32_16x16x32_bf16 a[124:127], v[252:255], a[236:239], a[124:127]\n\t" \
  "v_exp_f32 v54, v54\n\t" \
  "v_exp_f32 v55, v55\n\t" \
  "s_waitcnt lgkmcnt(1)\n\t" \
  "v_mfma_f32_32x32x16_bf16 v[32:47], v[88:91], v[64:67], v[0:15]\n\t" \
  "ds_read_b64_tr_b16 a[168:169], v155 offset:12288\n\t" \
  "v_exp_f32 v56, v56\n\t" \
  "v_cvt_pk_bf16_f32 v115, v54, v55\n\t" \
  "v_exp_f32 v57, v57\n\t" \
  "v_mfma_f32_32x32x16_bf16 v[32:47], v[92:95], v[68:71], v[32:47]\n\t" \
  "ds_read_b64_tr_b16 a[170:171], v155 offset:12800\n\t" \
  "v_exp_f32 v58, v58\n\t" \
  "v_cvt_pk_bf16_f32 v116, v56, v57\n\t" \
  "v_exp_f32 v59, v59\n\t" \
  "v_mfma_f32_32x32x16_bf16 a[128:143], v[96:99], v[104:107], a[128:143]\n\t" \
  "ds_read_b64_tr_b16 v[156:157], v137 offset:12288\n\t" \
  "v_exp_f32 v60, v60\n\t" \
  "v_cvt_pk_bf16_f32 v117, v58, v59\n\t" \
  "v_exp_f32 v61, v61\n\t" \
  "s_waitcnt lgkmcnt(3)\n\t" \
  "v_mfma_f32_32x32x16_bf16 a[128:143], v[100:103], v[108:111], a[128:143]\n\t" \
  "ds_read_b64_tr_b16 v[158:159], v137 offset:12800\n\t" \
  "v_exp_f32 v62, v62\n\t" \
  "v_cvt_pk_bf16_f32 v118, v60, v61\n\t" \
  "v_exp_f32 v63, v63\n\t" \
  "v_smfmac_f32_16x16x64_bf16 a[160:163], v[80:83], v[104:111], v154\n\t" \
  "buffer_load_dwordx4 v144, s[88:91], s69 offen offset:2048 sc1 lds\n\t" \
  "buffer_load_dwordx4 v144, s[88:91], s69 offen offset:3072 sc1 lds\n\t" \
  "s_waitcnt vmcnt(25) lgkmcnt(0)\n\t" \
  "v_mfma_f32_16x16x32_bf16 a[0:3], v[120:123], a[176:179], a[0:3]\n\t" \
  "buffer_load_dwordx4 a[232:235], v151, s[80:83], s64 offen offset:256 sc1\n\t" \
  "v_cvt_pk_bf16_f32 v119, v62, v63\n\t" \
  "v_mfma_f32_16x16x32_bf16 a[0:3], v[124:127], a[180:183], a[0:3]\n\t" \
  "v_exp_f32 v32, v32\n\t" \
  "v_exp_f32 v33, v33\n\t" \
  "v_mfma_f32_16x16x32_bf16 a[8:11], v[128:131], a[176:179], a[8:11]\n\t" \
  "buffer_load_dwordx4 a[236:239], v151, s[80:83], s64 offen offset:2304 sc1\n\t" \
  "v_cvt_pk_bf16_f32 v104, v32, v33\n\t" \
  "v_mfma_f32_16x16x32_bf16 a[8:11], v[132:135], a[180:183], a[8:11]\n\t" \
  "v_exp_f32 v34, v34\n\t" \
  "v_exp_f32 v35, v35\n\t" \
  "v_mfma_f32_16x16x32_bf16 a[16:19], a[164:167], a[176:179], a[16:19]\n\t" \
  "s_add_u32 m0, s72, 8192\n\t" \
  "v_cvt_pk_bf16_f32 v105, v34, v35\n\t" \
  "v_mfma_f32_16x16x32_bf16 a[16:19], a[168:171], a[180:183], a[16:19]\n\t" \
  "v_exp_f32 v36, v36\n\t" \
  "v_exp_f32 v37, v37\n\t" \
  "v_mfma_f32_16x16x32_bf16 a[24:27], a[172:175], a[176:179], a[24:27]\n\t" \
  "s_mov_b32 s68, 1441792\n\t" \
  "v_cvt_pk_bf16_f32 v106, v36, v37\n\t" \
  "v_mfma_f32_16x16x32_bf16 a[24:27], v[156:159], a[180:183], a[24:27]\n\t" \
  "v_exp_f32 v38, v38\n\t" \
  "v_exp_f32 v39, v39\n\t" \
  "v_mfma_f32_32x32x16_bf16 v[48:63], v[88:91], v[72:75], v[16:31]\n\t" \
  "ds_read_b128 v[88:91], v145 offset:20480\n\t" \
  "v_exp_f32 v40, v40\n\t" \
  "v_cvt_pk_bf16_f32 v107, v38, v39\n\t" \
  "v_exp_f32 v41, v41\n\t" \
  "v_mfma_f32_32x32x16_bf16 v[48:63], v[92:95], v[76:79], v[48:63]\n\t" \
  "ds_read_b128 v[92:95], v145 offset:21504\n\t" \
  "v_exp_f32 v42, v42\n\t" \
  "v_cvt_pk_bf16_f32 v108, v40, v41\n\t" \
  "v_exp_f32 v43, v43\n\t" \
  "v_mfma_f32_32x32x16_bf16 a[144:159], v[96:99], v[112:115], a[144:159]\n\t" \
  "ds_read_b128 v[96:99], v145 offset:18432\n\t" \
  "v_exp_f32 v44, v44\n\t" \
  "v_cvt_pk_bf16_f32 v109, v42, v43\n\t" \
  "v_exp_f32 v45, v45\n\t" \
  "v_mfma_f32_32x32x16_bf16 a[144:159], v[100:103], v[116:119], a[144:159]\n\t" \
  "ds_read_b128 v[100:103], v145 offset:19456\n\t" \
  "v_exp_f32 v46, v46\n\t" \
  "v_cvt_pk_bf16_f32 v110, v44, v45\n\t" \
  "v_exp_f32 v47, v47\n\t" \
  "v_smfmac_f32_16x16x64_bf16 a[160:163], v[84:87], v[112:119], v154\n\t" \
  "s_mov_b32 s64, 40960\n\t" \
  "s_mov_b32 s65, 49152\n\t" \
  "s_mov_b32 s66, 53248\n\t" \
  "s_waitcnt vmcnt(20)\n\t" \
  "v_mfma_f32_16x16x32_bf16 a[4:7], v[120:123], a[184:187], a[4:7]\n\t" \
  "buffer_load_dwordx4 a[176:179], v148, s[80:83], s64 offen offset:0 sc1\n\t" \
  "v_cvt_pk_bf16_f32 v111, v46, v47\n\t" \
  "v_mfma_f32_16x16x32_bf16 a[4:7], v[124:127], a[188:191], a[4:7]\n\t" \
  "v_exp_f32 v48, v48\n\t" \
  "v_exp_f32 v49, v49\n\t" \
  "v_mfma_f32_16x16x32_bf16 a[12:15], v[128:131], a[184:187], a[12:15]\n\t" \
  "buffer_load_dwordx4 a[180:183], v148, s[80:83], s64 offen offset:2048 sc1\n\t" \
  "v_cvt_pk_bf16_f32 v112, v48, v49\n\t" \
  "v_mfma_f32_16x16x32_bf16 a[12:15], v[132:135], a[188:191], a[12:15]\n\t" \
  "v_exp_f32 v50, v50\n\t" \
  "v_exp_f32 v51, v51\n\t" \
  "v_mfma_f32_16x16x32_bf16 a[20:23], a[164:167], a[184:187], a[20:23]\n\t" \
  "buffer_load_dwordx4 v140, s[84:87], s68 offen offset:0 lds\n\t" \
  "v_cvt_pk_bf16_f32 v113, v50, v51\n\t" \
  "v_mfma_f32_16x16x32_bf16 a[20:23], a[168:171], a[188:191], a[20:23]\n\t" \
  "v_exp_f32 v52, v52\n\t" \
  "v_exp_f32 v53, v53\n\t" \
  "v_mfma_f32_16x16x32_bf16 a[28:31], a[172:175], a[184:187], a[28:31]\n\t" \
  "buffer_load_dwordx4 v141, s[84:87], s68 offen offset:1024 lds\n\t" \
  "v_cvt_pk_bf16_f32 v114, v52, v53\n\t" \
  "v_mfma_f32_16x16x32_bf16 a[28:31], v[156:159], a[188:191], a[28:31]\n\t" \
  "v_exp_f32 v54, v54\n\t" \
  "v_exp_f32 v55, v55\n\t" \
  "s_waitcnt lgkmcnt(1)\n\t" \
  "v_mfma_f32_32x32x16_bf16 v[32:47], v[88:91], v[64:67], v[0:15]\n\t" \
  "ds_read_b64_tr_b16 v[224:225], v146 offset:0\n\t" \
  "v_exp_f32 v56, v56\n\t" \
  "v_cvt_pk_bf16_f32 v115, v54, v55\n\t" \
  "v_exp_f32 v57, v57\n\t" \
  "v_mfma_f32_32x32x16_bf16 v[32:47], v[92:95], v[68:71], v[32:47]\n\t" \
  "ds_read_b64_tr_b16 v[226:227], v146 offset:512\n\t" \
  "v_exp_f32 v58, v58\n\t" \
  "v_cvt_pk_bf16_f32 v116, v56, v57\n\t" \
  "v_exp_f32 v59, v59\n\t" \
  "v_mfma_f32_32x32x16_bf16 a[128:143], v[96:99], v[104:107], a[128:143]\n\t" \
  "ds_read_b64_tr_b16 v[232:233], v147 offset:0\n\t" \
  "v_exp_f32 v60, v60\n\t" \
  "v_cvt_pk_bf16_f32 v117, v58, v59\n\t" \
  "v_exp_f32 v61, v61\n\t" \
  "s_waitcnt lgkmcnt(3)\n\t" \
  "v_mfma_f32_32x32x16_bf16 a[128:143], v[100:103], v[108:111], a[128:143]\n\t" \
  "ds_read_b64_tr_b16 v[234:235], v147 offset:512\n\t" \
  "v_exp_f32 v62, v62\n\t" \
  "v_cvt_pk_bf16_f32 v118, v60, v61\n\t" \
  "v_exp_f32 v63, v63\n\t" \
  "v_smfmac_f32_16x16x64_bf16 a[160:163], v[80:83], v[104:111], v154\n\t" \
  "buffer_load_dwordx4 v[160:163], v148, s[80:83], s65 offen offset:0 sc1\n\t" \
  "buffer_load_dwordx4 v[164:167], v148, s[80:83], s65 offen offset:2048 sc1\n\t" \
  "s_waitcnt vmcnt(24)\n\t" \
  "v_mfma_f32_16x16x32_bf16 a[32:35], v[120:123], a[192:195], a[32:35]\n\t" \
  "buffer_load_dwordx4 a[184:187], v148, s[80:83], s64 offen offset:256 sc1\n\t" \
  "v_cvt_pk_bf16_f32 v119, v62, v63\n\t" \
  "v_mfma_f32_16x16x32_bf16 a[32:35], v[124:127], a[196:199], a[32:35]\n\t" \
  "v_exp_f32 v32, v32\n\t" \
  "v_exp_f32 v33, v33\n\t" \
  "v_mfma_f32_16x16x32_bf16 a[40:43], v[128:131], a[192:195], a[40:43]\n\t" \
  "buffer_load_dwordx4 a[188:191], v148, s[80:83], s64 offen offset:2304 sc1\n\t" \
  "v_cvt_pk_bf16_f32 v104, v32, v33\n\t" \
  "v_mfma_f32_16x16x32_bf16 a[40:43], v[132:135], a[196:199], a[40:43]\n\t" \
  "v_exp_f32 v34, v34\n\t" \
  "v_exp_f32 v35, v35\n\t" \
  "v_mfma_f32_16x16x32_bf16 a[48:51], a[164:167], a[192:195], a[48:51]\n\t" \
  "buffer_load_dwordx4 v142, s[84:87], s68 offen offset:2048 lds\n\t" \
  "v_cvt_pk_bf16_f32 v105, v34, v35\n\t" \
  "v_mfma_f32_16x16x32_bf16 a[48:51], a[168:171], a[196:199], a[48:51]\n\t" \
  "v_exp_f32 v36, v36\n\t" \
  "v_exp_f32 v37, v37\n\t" \
  "v_mfma_f32_16x16x32_bf16 a[56:59], a[172:175], a[192:195], a[56:59]\n\t" \
  "buffer_load_dwordx4 v143, s[84:87], s68 offen offset:3072 lds\n\t" \
  "v_cvt_pk_bf16_f32 v106, v36, v37\n\t" \
  "v_mfma_f32_16x16x32_bf16 a[56:59], v[156:159], a[196:199], a[56:59]\n\t" \
  "v_exp_f32 v38, v38\n\t" \
  "v_exp_f32 v39, v39\n\t" \
  "v_mfma_f32_32x32x16_bf16 v[48:63], v[88:91], v[72:75], v[16:31]\n\t" \
  "ds_read_b128 v[88:91], v145 offset:24576\n\t" \
  "v_exp_f32 v40, v40\n\t" \
  "v_cvt_pk_bf16_f32 v107, v38, v39\n\t" \
  "v_exp_f32 v41, v41\n\t" \
  "v_mfma_f32_32x32x16_bf16 v[48:63], v[92:95], v[76:79], v[48:63]\n\t" \
  "ds_read_b128 v[92:95], v145 offset:25600\n\t" \
  "v_exp_f32 v42, v42\n\t" \
  "v_cvt_pk_bf16_f32 v108, v40, v41\n\t" \
  "v_exp_f32 v43, v43\n\t" \
  "v_mfma_f32_32x32x16_bf16 a[144:159], v[96:99], v[112:115], a[144:159]\n\t" \
  "ds_read_b128 v[96:99], v145 offset:22528\n\t" \
  "v_exp_f32 v44, v44\n\t" \
  "v_cvt_pk_bf16_f32 v109, v42, v43\n\t" \
  "v_exp_f32 v45, v45\n\t" \
  "v_mfma_f32_32x32x16_bf16 a[144:159], v[100:103], v[116:119], a[144:159]\n\t" \
  "ds_read_b128 v[100:103], v145 offset:23552\n\t" \
  "v_exp_f32 v46, v46\n\t" \
  "v_cvt_pk_bf16_f32 v110, v44, v45\n\t" \
  "v_exp_f32 v47, v47\n\t" \
  "v_smfmac_f32_16x16x64_bf16 a[160:163], v[84:87], v[112:119], v154\n\t" \
  "s_waitcnt vmcnt(26)\n\t" \
  "v_mfma_f32_16x16x32_bf16 a[36:39], v[120:123], a[200:203], a[36:39]\n\t" \
  "buffer_load_dwordx4 a[192:195], v149, s[80:83], s64 offen offset:0 sc1\n\t" \
  "v_cvt_pk_bf16_f32 v111, v46, v47\n\t" \
  "v_mfma_f32_16x16x32_bf16 a[36:39], v[124:127], a[204:207], a[36:39]\n\t" \
  "v_exp_f32 v48, v48\n\t" \
  "v_exp_f32 v49, v49\n\t" \
  "v_mfma_f32_16x16x32_bf16 a[44:47], v[128:131], a[200:203], a[44:47]\n\t" \
  "buffer_load_dwordx4 a[196:199], v149, s[80:83], s64 offen offset:2048 sc1\n\t" \
  "v_cvt_pk_bf16_f32 v112, v48, v49\n\t" \
  "v_mfma_f32_16x16x32_bf16 a[44:47], v[132:135], a[204:207], a[44:47]\n\t" \
  "v_exp_f32 v50, v50\n\t" \
  "v_exp_f32 v51, v51\n\t" \
  "v_mfma_f32_16x16x32_bf16 a[52:55], a[164:167], a[200:203], a[52:55]\n\t" \
  "v_exp_f32 v52, v52\n\t" \
  "v_mfma_f32_16x16x32_bf16 a[52:55], a[168:171], a[204:207], a[52:55]\n\t" \
  "v_cvt_pk_bf16_f32 v113, v50, v51\n\t" \
  "v_exp_f32 v53, v53\n\t" \
  "v_mfma_f32_16x16x32_bf16 a[60:63], a[172:175], a[200:203], a[60:63]\n\t" \
  "v_exp_f32 v54, v54\n\t" \
  "v_mfma_f32_16x16x32_bf16 a[60:63], v[156:159], a[204:207], a[60:63]\n\t" \
  "v_cvt_pk_bf16_f32 v114, v52, v53\n\t" \
  "v_exp_f32 v55, v55\n\t" \
  "s_waitcnt lgkmcnt(1)\n\t" \
  "v_mfma_f32_32x32x16_bf16 v[32:47], v[88:91], v[64:67], v[0:15]\n\t" \
  "ds_read_b64_tr_b16 v[240:241], v155 offset:0\n\t" \
  "v_exp_f32 v56, v56\n\t" \
  "v_cvt_pk_bf16_f32 v115, v54, v55\n\t" \
  "v_exp_f32 v57, v57\n\t" \
  "v_mfma_f32_32x32x16_bf16 v[32:47], v[92:95], v[68:71], v[32:47]\n\t" \
  "ds_read_b64_tr_b16 v[242:243], v155 offset:512\n\t" \
  "v_exp_f32 v58, v58\n\t" \
  "v_cvt_pk_bf16_f32 v116, v56, v57\n\t" \
  "v_exp_f32 v59, v59\n\t" \
  "v_mfma_f32_32x32x16_bf16 a[128:143], v[96:99], v[104:107], a[128:143]\n\t" \
  "ds_read_b64_tr_b16 v[248:249], v137 offset:0\n\t" \
  "v_exp_f32 v60, v60\n\t" \
  "v_cvt_pk_bf16_f32 v117, v58, v59\n\t" \
  "v_exp_f32 v61, v61\n\t" \
  "s_waitcnt lgkmcnt(3)\n\t" \
  "v_mfma_f32_32x32x16_bf16 a[128:143], v[100:103], v[108:111], a[128:143]\n\t" \
  "ds_read_b64_tr_b16 v[250:251], v137 offset:512\n\t" \
  "v_exp_f32 v62, v62\n\t" \
  "v_cvt_pk_bf16_f32 v118, v60, v61\n\t" \
  "v_exp_f32 v63, v63\n\t" \
  "v_smfmac_f32_16x16x64_bf16 a[160:163], v[80:83], v[104:111], v154\n\t" \
  "buffer_load_dwordx4 v[168:171], v148, s[80:83], s65 offen offset:256 sc1\n\t" \
  "buffer_load_dwordx4 v[172:175], v148, s[80:83], s65 offen offset:2304 sc1\n\t" \
  "s_waitcnt vmcnt(28)\n\t" \
  "v_mfma_f32_16x16x32_bf16 a[64:67], v[120:123], a[208:211], a[64:67]\n\t" \
  "buffer_load_dwordx4 a[200:203], v149, s[80:83], s64 offen offset:256 sc1\n\t" \
  "v_cvt_pk_bf16_f32 v119, v62, v63\n\t" \
  "v_mfma_f32_16x16x32_bf16 a[64:67], v[124:127], a[212:215], a[64:67]\n\t" \
  "v_exp_f32 v32, v32\n\t" \
  "v_exp_f32 v33, v33\n\t" \
  "v_mfma_f32_16x16x32_bf16 a[72:75], v[128:131], a[208:211], a[72:75]\n\t" \
  "buffer_load_dwordx4 a[204:207], v149, s[80:83], s64 offen offset:2304 sc1\n\t" \
  "v_cvt_pk_bf16_f32 v104, v32, v33\n\t" \
  "v_mfma_f32_16x16x32_bf16 a[72:75], v[132:135], a[212:215], a[72:75]\n\t" \
  "v_exp_f32 v34, v34\n\t" \
  "v_exp_f32 v35, v35\n\t" \
  "v_mfma_f32_16x16x32_bf16 a[80:83], a[164:167], a[208:211], a[80:83]\n\t" \
  "s_add_u32 m0, s72, 12288\n\t" \
  "v_cvt_pk_bf16_f32 v105, v34, v35\n\t" \
  "v_mfma_f32_16x16x32_bf16 a[80:83], a[168:171], a[212:215], a[80:83]\n\t" \
  "v_exp_f32 v36, v36\n\t" \
  "v_exp_f32 v37, v37\n\t" \
  "v_mfma_f32_16x16x32_bf16 a[88:91], a[172:175], a[208:211], a[88:91]\n\t" \
  "s_mov_b32 s68, 1507328\n\t" \
  "v_cvt_pk_bf16_f32 v106, v36, v37\n\t" \
  "v_mfma_f32_16x16x32_bf16 a[88:91], v[156:159], a[212:215], a[88:91]\n\t" \
  "v_exp_f32 v38, v38\n\t" \
  "v_exp_f32 v39, v39\n\t" \
  "v_mfma_f32_32x32x16_bf16 v[48:63], v[88:91], v[72:75], v[16:31]\n\t" \
  "ds_read_b128 v[88:91], v145 offset:28672\n\t" \
  "v_exp_f32 v40, v40\n\t" \
  "v_cvt_pk_bf16_f32 v107, v38, v39\n\t" \
  "v_exp_f32 v41, v41\n\t" \
  "v_mfma_f32_32x32x16_bf16 v[48:63], v[92:95], v[76:79], v[48:63]\n\t" \
  "ds_read_b128 v[92:95], v145 offset:29696\n\t" \
  "v_exp_f32 v42, v42\n\t" \
  "v_cvt_pk_bf16_f32 v108, v40, v41\n\t" \
  "v_exp_f32 v43, v43\n\t" \
  "v_mfma_f32_32x32x16_bf16 a[144:159], v[96:99], v[112:115], a[144:159]\n\t" \
  "ds_read_b128 v[96:99], v145 offset:26624\n\t" \
  "v_exp_f32 v44, v44\n\t" \
  "v_cvt_pk_bf16_f32 v109, v42, v43\n\t" \
  "v_exp_f32 v45, v45\n\t" \
  "v_mfma_f32_32x32x16_bf16 a[144:159], v[100:103], v[116:119], a[144:159]\n\t" \
  "ds_read_b128 v[100:103], v145 offset:27648\n\t" \
  "v_exp_f32 v46, v46\n\t" \
  "v_cvt_pk_bf16_f32 v110, v44, v45\n\t" \
  "v_exp_f32 v47, v47\n\t" \
  "v_smfmac_f32_16x16x64_bf16 a[160:163], v[84:87], v[112:119], v154\n\t" \
  "s_waitcnt vmcnt(24)\n\t" \
  "v_mfma_f32_16x16x32_bf16 a[68:71], v[120:123], a[216:219], a[68:71]\n\t" \
  "buffer_load_dwordx4 a[208:211], v150, s[80:83], s64 offen offset:0 sc1\n\t" \
  "v_cvt_pk_bf16_f32 v111, v46, v47\n\t" \
  "v_mfma_f32_16x16x32_bf16 a[68:71], v[124:127], a[220:223], a[68:71]\n\t" \
  "v_exp_f32 v48, v48\n\t" \
  "v_exp_f32 v49, v49\n\t" \
  "v_mfma_f32_16x16x32_bf16 a[76:79], v[128:131], a[216:219], a[76:79]\n\t" \
  "buffer_load_dwordx4 a[212:215], v150, s[80:83], s64 offen offset:2048 sc1\n\t" \
  "v_cvt_pk_bf16_f32 v112, v48, v49\n\t" \
  "v_mfma_f32_16x16x32_bf16 a[76:79], v[132:135], a[220:223], a[76:79]\n\t" \
  "v_exp_f32 v50, v50\n\t" \
  "v_exp_f32 v51, v51\n\t" \
  "v_mfma_f32_16x16x32_bf16 a[84:87], a[164:167], a[216:219], a[84:87]\n\t" \
  "buffer_load_dwordx4 v140, s[84:87], s68 offen offset:0 lds\n\t" \
  "v_cvt_pk_bf16_f32 v113, v50, v51\n\t" \
  "v_mfma_f32_16x16x32_bf16 a[84:87], a[168:171], a[220:223], a[84:87]\n\t" \
  "v_exp_f32 v52, v52\n\t" \
  "v_exp_f32 v53, v53\n\t" \
  "v_mfma_f32_16x16x32_bf16 a[92:95], a[172:175], a[216:219], a[92:95]\n\t" \
  "buffer_load_dwordx4 v141, s[84:87], s68 offen offset:1024 lds\n\t" \
  "v_cvt_pk_bf16_f32 v114, v52, v53\n\t" \
  "v_mfma_f32_16x16x32_bf16 a[92:95], v[156:159], a[220:223], a[92:95]\n\t" \
  "v_exp_f32 v54, v54\n\t" \
  "v_exp_f32 v55, v55\n\t" \
  "s_waitcnt lgkmcnt(1)\n\t" \
  "v_mfma_f32_32x32x16_bf16 v[32:47], v[88:91], v[64:67], v[0:15]\n\t" \
  "ds_read_b64_tr_b16 v[228:229], v146 offset:4096\n\t" \
  "v_exp_f32 v56, v56\n\t" \
  "v_cvt_pk_bf16_f32 v115, v54, v55\n\t" \
  "v_exp_f32 v57, v57\n\t" \
  "v_mfma_f32_32x32x16_bf16 v[32:47], v[92:95], v[68:71], v[32:47]\n\t" \
  "ds_read_b64_tr_b16 v[230:231], v146 offset:4608\n\t" \
  "v_exp_f32 v58, v58\n\t" \
  "v_cvt_pk_bf16_f32 v116, v56, v57\n\t" \
  "v_exp_f32 v59, v59\n\t" \
  "v_mfma_f32_32x32x16_bf16 a[128:143], v[96:99], v[104:107], a[128:143]\n\t" \
  "ds_read_b64_tr_b16 v[236:237], v147 offset:4096\n\t" \
  "v_exp_f32 v60, v60\n\t" \
  "v_cvt_pk_bf16_f32 v117, v58, v59\n\t" \
  "v_exp_f32 v61, v61\n\t" \
  "s_waitcnt lgkmcnt(3)\n\t" \
  "v_mfma_f32_32x32x16_bf16 a[128:143], v[100:103], v[108:111], a[128:143]\n\t" \
  "ds_read_b64_tr_b16 v[238:239], v147 offset:4608\n\t" \
  "v_exp_f32 v62, v62\n\t" \
  "v_cvt_pk_bf16_f32 v118, v60, v61\n\t" \
  "v_exp_f32 v63, v63\n\t" \
  "v_smfmac_f32_16x16x64_bf16 a[160:163], v[80:83], v[104:111], v154\n\t" \
  "buffer_load_dwordx4 v[176:179], v149, s[80:83], s65 offen offset:0 sc1\n\t" \
  "buffer_load_dwordx4 v[180:183], v149, s[80:83], s65 offen offset:2048 sc1\n\t" \
  "s_waitcnt vmcnt(24)\n\t" \
  "s_barrier\n\t" \
  "v_mfma_f32_16x16x32_bf16 a[96:99], v[120:123], a[224:227], a[96:99]\n\t" \
  "buffer_load_dwordx4 a[216:219], v150, s[80:83], s64 offen offset:256 sc1\n\t" \
  "v_cvt_pk_bf16_f32 v119, v62, v63\n\t" \
  "v_mfma_f32_16x16x32_bf16 a[96:99], v[124:127], a[228:231], a[96:99]\n\t" \
  "v_exp_f32 v32, v32\n\t" \
  "v_exp_f32 v33, v33\n\t" \
  "v_mfma_f32_16x16x32_bf16 a[104:107], v[128:131], a[224:227], a[104:107]\n\t" \
  "buffer_load_dwordx4 a[220:223], v150, s[80:83], s64 offen offset:2304 sc1\n\t" \
  "v_cvt_pk_bf16_f32 v104, v32, v33\n\t" \
  "v_mfma_f32_16x16x32_bf16 a[104:107], v[132:135], a[228:231], a[104:107]\n\t" \
  "v_exp_f32 v34, v34\n\t" \
  "v_exp_f32 v35, v35\n\t" \
  "v_mfma_f32_16x16x32_bf16 a[112:115], a[164:167], a[224:227], a[112:115]\n\t" \
  "buffer_load_dwordx4 v142, s[84:87], s68 offen offset:2048 lds\n\t" \
  "v_cvt_pk_bf16_f32 v105, v34, v35\n\t" \
  "v_mfma_f32_16x16x32_bf16 a[112:115], a[168:171], a[228:231], a[112:115]\n\t" \
  "v_exp_f32 v36, v36\n\t" \
  "v_exp_f32 v37, v37\n\t" \
  "v_mfma_f32_16x16x32_bf16 a[120:123], a[172:175], a[224:227], a[120:123]\n\t" \
  "buffer_load_dwordx4 v143, s[84:87], s68 offen offset:3072 lds\n\t" \
  "v_cvt_pk_bf16_f32 v106, v36, v37\n\t" \
  "v_mfma_f32_16x16x32_bf16 a[120:123], v[156:159], a[228:231], a[120:123]\n\t" \
  "v_exp_f32 v38, v38\n\t" \
  "v_exp_f32 v39, v39\n\t" \
  "v_mfma_f32_32x32x16_bf16 v[48:63], v[88:91], v[72:75], v[16:31]\n\t" \
  "ds_read_b128 v[88:91], v145 offset:32768\n\t" \
  "v_exp_f32 v40, v40\n\t" \
  "v_cvt_pk_bf16_f32 v107, v38, v39\n\t" \
  "v_exp_f32 v41, v41\n\t" \
  "v_mfma_f32_32x32x16_bf16 v[48:63], v[92:95], v[76:79], v[48:63]\n\t" \
  "ds_read_b128 v[92:95], v145 offset:33792\n\t" \
  "v_exp_f32 v42, v42\n\t" \
  "v_cvt_pk_bf16_f32 v108, v40, v41\n\t" \
  "v_exp_f32 v43, v43\n\t" \
  "v_mfma_f32_32x32x16_bf16 a[144:159], v[96:99], v[112:115], a[144:159]\n\t" \
  "ds_read_b128 v[96:99], v145 offset:30720\n\t" \
  "v_exp_f32 v44, v44\n\t" \
  "v_cvt_pk_bf16_f32 v109, v42, v43\n\t" \
  "v_exp_f32 v45, v45\n\t" \
  "v_mfma_f32_32x32x16_bf16 a[144:159], v[100:103], v[116:119], a[144:159]\n\t" \
  "ds_read_b128 v[100:103], v145 offset:31744\n\t" \
  "v_exp_f32 v46, v46\n\t" \
  "v_cvt_pk_bf16_f32 v110, v44, v45\n\t" \
  "v_exp_f32 v47, v47\n\t" \
  "v_smfmac_f32_16x16x64_bf16 a[160:163], v[84:87], v[112:119], v154\n\t" \
  "buffer_load_dwordx4 v[184:187], v149, s[80:83], s65 offen offset:256 sc1\n\t" \
  "buffer_load_dwordx4 v[188:191], v149, s[80:83], s65 offen offset:2304 sc1\n\t" \
  "s_add_u32 m0, s73, 0\n\t" \
  "s_mov_b32 s69, 245760\n\t" \
  "s_waitcnt vmcnt(28)\n\t" \
  "v_mfma_f32_16x16x32_bf16 a[100:103], v[120:123], a[232:235], a[100:103]\n\t" \
  "buffer_load_dwordx4 a[224:227], v151, s[80:83], s64 offen offset:0 sc1\n\t" \
  "v_cvt_pk_bf16_f32 v111, v46, v47\n\t" \
  "v_mfma_f32_16x16x32_bf16 a[100:103], v[124:127], a[236:239], a[100:103]\n\t" \
  "v_exp_f32 v48, v48\n\t" \
  "v_exp_f32 v49, v49\n\t" \
  "v_mfma_f32_16x16x32_bf16 a[108:111], v[128:131], a[232:235], a[108:111]\n\t" \
  "buffer_load_dwordx4 a[228:231], v151, s[80:83], s64 offen offset:2048 sc1\n\t" \
  "v_cvt_pk_bf16_f32 v112, v48, v49\n\t" \
  "v_mfma_f32_16x16x32_bf16 a[108:111], v[132:135], a[236:239], a[108:111]\n\t" \
  "v_exp_f32 v50, v50\n\t" \
  "v_exp_f32 v51, v51\n\t" \
  "v_mfma_f32_16x16x32_bf16 a[116:119], a[164:167], a[232:235], a[116:119]\n\t" \
  "buffer_load_dwordx4 v144, s[88:91], s69 offen offset:0 sc1 lds\n\t" \
  "v_cvt_pk_bf16_f32 v113, v50, v51\n\t" \
  "v_mfma_f32_16x16x32_bf16 a[116:119], a[168:171], a[236:239], a[116:119]\n\t" \
  "v_exp_f32 v52, v52\n\t" \
  "v_exp_f32 v53, v53\n\t" \
  "v_mfma_f32_16x16x32_bf16 a[124:127], a[172:175], a[232:235], a[124:127]\n\t" \
  "buffer_load_dwordx4 v144, s[88:91], s69 offen offset:1024 sc1 lds\n\t" \
  "v_cvt_pk_bf16_f32 v114, v52, v53\n\t" \
  "v_mfma_f32_16x16x32_bf16 a[124:127], v[156:159], a[236:239], a[124:127]\n\t" \
  "v_exp_f32 v54, v54\n\t" \
  "v_exp_f32 v55, v55\n\t" \
  "s_waitcnt lgkmcnt(1)\n\t" \
  "v_mfma_f32_32x32x16_bf16 v[32:47], v[88:91], v[64:67], v[0:15]\n\t" \
  "ds_read_b64_tr_b16 v[244:245], v155 offset:4096\n\t" \
  "v_exp_f32 v56, v56\n\t" \
  "v_cvt_pk_bf16_f32 v115, v54, v55\n\t" \
  "v_exp_f32 v57, v57\n\t" \
  "v_mfma_f32_32x32x16_bf16 v[32:47], v[92:95], v[68:71], v[32:47]\n\t" \
  "ds_read_b64_tr_b16 v[246:247], v155 offset:4608\n\t" \
  "v_exp_f32 v58, v58\n\t" \
  "v_cvt_pk_bf16_f32 v116, v56, v57\n\t" \
  "v_exp_f32 v59, v59\n\t" \
  "v_mfma_f32_32x32x16_bf16 a[128:143], v[96:99], v[104:107], a[128:143]\n\t" \
  "ds_read_b64_tr_b16 v[252:253], v137 offset:4096\n\t" \
  "v_exp_f32 v60, v60\n\t" \
  "v_cvt_pk_bf16_f32 v117, v58, v59\n\t" \
  "v_exp_f32 v61, v61\n\t" \
  "s_waitcnt lgkmcnt(3)\n\t" \
  "v_mfma_f32_32x32x16_bf16 a[128:143], v[100:103], v[108:111], a[128:143]\n\t" \
  "ds_read_b64_tr_b16 v[254:255], v137 offset:4608\n\t" \
  "v_exp_f32 v62, v62\n\t" \
  "v_cvt_pk_bf16_f32 v118, v60, v61\n\t" \
  "v_exp_f32 v63, v63\n\t" \
  "v_smfmac_f32_16x16x64_bf16 a[160:163], v[80:83], v[104:111], v154\n\t" \
  "buffer_load_dwordx4 v144, s[88:91], s69 offen offset:2048 sc1 lds\n\t" \
  "buffer_load_dwordx4 v144, s[88:91], s69 offen offset:3072 sc1 lds\n\t" \
  "s_waitcnt vmcnt(32) lgkmcnt(0)\n\t" \
  "v_mfma_f32_16x16x32_bf16 a[0:3], v[224:227], a[176:179], a[0:3]\n\t" \
  "buffer_load_dwordx4 a[232:235], v151, s[80:83], s64 offen offset:256 sc1\n\t" \
  "v_cvt_pk_bf16_f32 v119, v62, v63\n\t" \
  "v_mfma_f32_16x16x32_bf16 a[0:3], v[228:231], a[180:183], a[0:3]\n\t" \
  "v_exp_f32 v32, v32\n\t" \
  "v_exp_f32 v33, v33\n\t" \
  "v_mfma_f32_16x16x32_bf16 a[8:11], v[232:235], a[176:179], a[8:11]\n\t" \
  "buffer_load_dwordx4 a[236:239], v151, s[80:83], s64 offen offset:2304 sc1\n\t" \
  "v_cvt_pk_bf16_f32 v104, v32, v33\n\t" \
  "v_mfma_f32_16x16x32_bf16 a[8:11], v[236:239], a[180:183], a[8:11]\n\t" \
  "v_exp_f32 v34, v34\n\t" \
  "v_exp_f32 v35, v35\n\t" \
  "v_mfma_f32_16x16x32_bf16 a[16:19], v[240:243], a[176:179], a[16:19]\n\t" \
  "s_add_u32 m0, s72, 0\n\t" \
  "v_cvt_pk_bf16_f32 v105, v34, v35\n\t" \
  "v_mfma_f32_16x16x32_bf16 a[16:19], v[244:247], a[180:183], a[16:19]\n\t" \
  "v_exp_f32 v36, v36\n\t" \
  "v_exp_f32 v37, v37\n\t" \
  "v_mfma_f32_16x16x32_bf16 a[24:27], v[248:251], a[176:179], a[24:27]\n\t" \
  "s_mov_b32 s68, 1572864\n\t" \
  "v_cvt_pk_bf16_f32 v106, v36, v37\n\t" \
  "v_mfma_f32_16x16x32_bf16 a[24:27], v[252:255], a[180:183], a[24:27]\n\t" \
  "v_exp_f32 v38, v38\n\t" \
  "v_exp_f32 v39, v39\n\t" \
  "v_mfma_f32_32x32x16_bf16 v[48:63], v[88:91], v[72:75], v[16:31]\n\t" \
  "ds_read_b128 v[88:91], v145 offset:36864\n\t" \
  "v_exp_f32 v40, v40\n\t" \
  "v_cvt_pk_bf16_f32 v107, v38, v39\n\t" \
  "v_exp_f32 v41, v41\n\t" \
  "v_mfma_f32_32x32x16_bf16 v[48:63], v[92:95], v[76:79], v[48:63]\n\t" \
  "ds_read_b128 v[92:95], v145 offset:37888\n\t" \
  "v_exp_f32 v42, v42\n\t" \
  "v_cvt_pk_bf16_f32 v108, v40, v41\n\t" \
  "v_exp_f32 v43, v43\n\t" \
  "v_mfma_f32_32x32x16_bf16 a[144:159], v[96:99], v[112:115], a[144:159]\n\t" \
  "ds_read_b128 v[96:99], v145 offset:34816\n\t" \
  "v_exp_f32 v44, v44\n\t" \
  "v_cvt_pk_bf16_f32 v109, v42, v43\n\t" \
  "v_exp_f32 v45, v45\n\t" \
  "v_mfma_f32_32x32x16_bf16 a[144:159], v[100:103], v[116:119], a[144:159]\n\t" \
  "ds_read_b128 v[100:103], v145 offset:35840\n\t" \
  "v_exp_f32 v46, v46\n\t" \
  "v_cvt_pk_bf16_f32 v110, v44, v45\n\t" \
  "v_exp_f32 v47, v47\n\t" \
  "v_smfmac_f32_16x16x64_bf16 a[160:163], v[84:87], v[112:119], v154\n\t" \
  "s_mov_b32 s64, 45056\n\t" \
  "s_waitcnt vmcnt(26)\n\t" \
  "v_mfma_f32_16x16x32_bf16 a[4:7], v[224:227], a[184:187], a[4:7]\n\t" \
  "buffer_load_dwordx4 a[176:179], v148, s[80:83], s64 offen offset:0 sc1\n\t" \
  "v_cvt_pk_bf16_f32 v111, v46, v47\n\t" \
  "v_mfma_f32_16x16x32_bf16 a[4:7], v[228:231], a[188:191], a[4:7]\n\t" \
  "v_exp_f32 v48, v48\n\t" \
  "v_exp_f32 v49, v49\n\t" \
  "v_mfma_f32_16x16x32_bf16 a[12:15], v[232:235], a[184:187], a[12:15]\n\t" \
  "buffer_load_dwordx4 a[180:183], v148, s[80:83], s64 offen offset:2048 sc1\n\t" \
  "v_cvt_pk_bf16_f32 v112, v48, v49\n\t" \
  "v_mfma_f32_16x16x32_bf16 a[12:15], v[236:239], a[188:191], a[12:15]\n\t" \
  "v_exp_f32 v50, v50\n\t" \
  "v_exp_f32 v51, v51\n\t" \
  "v_mfma_f32_16x16x32_bf16 a[20:23], v[240:243], a[184:187], a[20:23]\n\t" \
  "buffer_load_dwordx4 v140, s[84:87], s68 offen offset:0 lds\n\t" \
  "v_cvt_pk_bf16_f32 v113, v50, v51\n\t" \
  "v_mfma_f32_16x16x32_bf16 a[20:23], v[244:247], a[188:191], a[20:23]\n\t" \
  "v_exp_f32 v52, v52\n\t" \
  "v_exp_f32 v53, v53\n\t" \
  "v_mfma_f32_16x16x32_bf16 a[28:31], v[248:251], a[184:187], a[28:31]\n\t" \
  "buffer_load_dwordx4 v141, s[84:87], s68 offen offset:1024 lds\n\t" \
  "v_cvt_pk_bf16_f32 v114, v52, v53\n\t" \
  "v_mfma_f32_16x16x32_bf16 a[28:31], v[252:255], a[188:191], a[28:31]\n\t" \
  "v_exp_f32 v54, v54\n\t" \
  "v_exp_f32 v55, v55\n\t" \
  "s_waitcnt lgkmcnt(1)\n\t" \
  "v_mfma_f32_32x32x16_bf16 v[32:47], v[88:91], v[64:67], v[0:15]\n\t" \
  "ds_read_b64_tr_b16 v[120:121], v146 offset:8192\n\t" \
  "v_exp_f32 v56, v56\n\t" \
  "v_cvt_pk_bf16_f32 v115, v54, v55\n\t" \
  "v_exp_f32 v57, v57\n\t" \
  "v_mfma_f32_32x32x16_bf16 v[32:47], v[92:95], v[68:71], v[32:47]\n\t" \
  "ds_read_b64_tr_b16 v[122:123], v146 offset:8704\n\t" \
  "v_exp_f32 v58, v58\n\t" \
  "v_cvt_pk_bf16_f32 v116, v56, v57\n\t" \
  "v_exp_f32 v59, v59\n\t" \
  "v_mfma_f32_32x32x16_bf16 a[128:143], v[96:99], v[104:107], a[128:143]\n\t" \
  "ds_read_b64_tr_b16 v[128:129], v147 offset:8192\n\t" \
  "v_exp_f32 v60, v60\n\t" \
  "v_cvt_pk_bf16_f32 v117, v58, v59\n\t" \
  "v_exp_f32 v61, v61\n\t" \
  "s_waitcnt lgkmcnt(3)\n\t" \
  "v_mfma_f32_32x32x16_bf16 a[128:143], v[100:103], v[108:111], a[128:143]\n\t" \
  "ds_read_b64_tr_b16 v[130:131], v147 offset:8704\n\t" \
  "v_exp_f32 v62, v62\n\t" \
  "v_cvt_pk_bf16_f32 v118, v60, v61\n\t" \
  "v_exp_f32 v63, v63\n\t" \
  "v_smfmac_f32_16x16x64_bf16 a[160:163], v[80:83], v[104:111], v154\n\t" \
  "buffer_load_dwordx4 v[192:195], v150, s[80:83], s65 offen offset:0 sc1\n\t" \
  "buffer_load_dwordx4 v[196:199], v150, s[80:83], s65 offen offset:2048 sc1\n\t" \
  "s_waitcnt vmcnt(30)\n\t" \
  "v_mfma_f32_16x16x32_bf16 a[32:35], v[224:227], a[192:195], a[32:35]\n\t" \
  "buffer_load_dwordx4 a[184:187], v148, s[80:83], s64 offen offset:256 sc1\n\t" \
  "v_cvt_pk_bf16_f32 v119, v62, v63\n\t" \
  "v_mfma_f32_16x16x32_bf16 a[32:35], v[228:231], a[196:199], a[32:35]\n\t" \
  "v_exp_f32 v32, v32\n\t" \
  "v_exp_f32 v33, v33\n\t" \
  "v_mfma_f32_16x16x32_bf16 a[40:43], v[232:235], a[192:195], a[40:43]\n\t" \
  "buffer_load_dwordx4 a[188:191], v148, s[80:83], s64 offen offset:2304 sc1\n\t" \
  "v_cvt_pk_bf16_f32 v104, v32, v33\n\t" \
  "v_mfma_f32_16x16x32_bf16 a[40:43], v[236:239], a[196:199], a[40:43]\n\t" \
  "v_exp_f32 v34, v34\n\t" \
  "v_exp_f32 v35, v35\n\t" \
  "v_mfma_f32_16x16x32_bf16 a[48:51], v[240:243], a[192:195], a[48:51]\n\t" \
  "buffer_load_dwordx4 v142, s[84:87], s68 offen offset:2048 lds\n\t" \
  "v_cvt_pk_bf16_f32 v105, v34, v35\n\t" \
  "v_mfma_f32_16x16x32_bf16 a[48:51], v[244:247], a[196:199], a[48:51]\n\t" \
  "v_exp_f32 v36, v36\n\t" \
  "v_exp_f32 v37, v37\n\t" \
  "v_mfma_f32_16x16x32_bf16 a[56:59], v[248:251], a[192:195], a[56:59]\n\t" \
  "buffer_load_dwordx4 v143, s[84:87], s68 offen offset:3072 lds\n\t" \
  "v_cvt_pk_bf16_f32 v106, v36, v37\n\t" \
  "v_mfma_f32_16x16x32_bf16 a[56:59], v[252:255], a[196:199], a[56:59]\n\t" \
  "v_exp_f32 v38, v38\n\t" \
  "v_exp_f32 v39, v39\n\t" \
  "v_mfma_f32_32x32x16_bf16 v[48:63], v[88:91], v[72:75], v[16:31]\n\t" \
  "ds_read_b128 v[88:91], v145 offset:40960\n\t" \
  "v_exp_f32 v40, v40\n\t" \
  "v_cvt_pk_bf16_f32 v107, v38, v39\n\t" \
  "v_exp_f32 v41, v41\n\t" \
  "v_mfma_f32_32x32x16_bf16 v[48:63], v[92:95], v[76:79], v[48:63]\n\t" \
  "ds_read_b128 v[92:95], v145 offset:41984\n\t" \
  "v_exp_f32 v42, v42\n\t" \
  "v_cvt_pk_bf16_f32 v108, v40, v41\n\t" \
  "v_exp_f32 v43, v43\n\t" \
  "v_mfma_f32_32x32x16_bf16 a[144:159], v[96:99], v[112:115], a[144:159]\n\t" \
  "ds_read_b128 v[96:99], v145 offset:38912\n\t" \
  "v_exp_f32 v44, v44\n\t" \
  "v_cvt_pk_bf16_f32 v109, v42, v43\n\t" \
  "v_exp_f32 v45, v45\n\t" \
  "v_mfma_f32_32x32x16_bf16 a[144:159], v[100:103], v[116:119], a[144:159]\n\t" \
  "ds_read_b128 v[100:103], v145 offset:39936\n\t" \
  "v_exp_f32 v46, v46\n\t" \
  "v_cvt_pk_bf16_f32 v110, v44, v45\n\t" \
  "v_exp_f32 v47, v47\n\t" \
  "v_smfmac_f32_16x16x64_bf16 a[160:163], v[84:87], v[112:119], v154\n\t" \
  "s_waitcnt vmcnt(30)\n\t" \
  "v_mfma_f32_16x16x32_bf16 a[36:39], v[224:227], a[200:203], a[36:39]\n\t" \
  "buffer_load_dwordx4 a[192:195], v149, s[80:83], s64 offen offset:0 sc1\n\t" \
  "v_cvt_pk_bf16_f32 v111, v46, v47\n\t" \
  "v_mfma_f32_16x16x32_bf16 a[36:39], v[228:231], a[204:207], a[36:39]\n\t" \
  "v_exp_f32 v48, v48\n\t" \
  "v_exp_f32 v49, v49\n\t" \
  "v_mfma_f32_16x16x32_bf16 a[44:47], v[232:235], a[200:203], a[44:47]\n\t" \
  "buffer_load_dwordx4 a[196:199], v149, s[80:83], s64 offen offset:2048 sc1\n\t" \
  "v_cvt_pk_bf16_f32 v112, v48, v49\n\t" \
  "v_mfma_f32_16x16x32_bf16 a[44:47], v[236:239], a[204:207], a[44:47]\n\t" \
  "v_exp_f32 v50, v50\n\t" \
  "v_exp_f32 v51, v51\n\t" \
  "v_mfma_f32_16x16x32_bf16 a[52:55], v[240:243], a[200:203], a[52:55]\n\t" \
  "v_exp_f32 v52, v52\n\t" \
  "v_mfma_f32_16x16x32_bf16 a[52:55], v[244:247], a[204:207], a[52:55]\n\t" \
  "v_cvt_pk_bf16_f32 v113, v50, v51\n\t" \
  "v_exp_f32 v53, v53\n\t" \
  "v_mfma_f32_16x16x32_bf16 a[60:63], v[248:251], a[200:203], a[60:63]\n\t" \
  "v_exp_f32 v54, v54\n\t" \
  "v_mfma_f32_16x16x32_bf16 a[60:63], v[252:255], a[204:207], a[60:63]\n\t" \
  "v_cvt_pk_bf16_f32 v114, v52, v53\n\t" \
  "v_exp_f32 v55, v55\n\t" \
  "s_waitcnt lgkmcnt(1)\n\t" \
  "v_mfma_f32_32x32x16_bf16 v[32:47], v[88:91], v[64:67], v[0:15]\n\t" \
  "ds_read_b64_tr_b16 a[164:165], v155 offset:8192\n\t" \
  "v_exp_f32 v56, v56\n\t" \
  "v_cvt_pk_bf16_f32 v115, v54, v55\n\t" \
  "v_exp_f32 v57, v57\n\t" \
  "v_mfma_f32_32x32x16_bf16 v[32:47], v[92:95], v[68:71], v[32:47]\n\t" \
  "ds_read_b64_tr_b16 a[166:167], v155 offset:8704\n\t" \
  "v_exp_f32 v58, v58\n\t" \
  "v_cvt_pk_bf16_f32 v116, v56, v57\n\t" \
  "v_exp_f32 v59, v59\n\t" \
  "v_mfma_f32_32x32x16_bf16 a[128:143], v[96:99], v[104:107], a[128:143]\n\t" \
  "ds_read_b64_tr_b16 a[172:173], v137 offset:8192\n\t" \
  "v_exp_f32 v60, v60\n\t" \
  "v_cvt_pk_bf16_f32 v117, v58, v59\n\t" \
  "v_exp_f32 v61, v61\n\t" \
  "s_waitcnt lgkmcnt(3)\n\t" \
  "v_mfma_f32_32x32x16_bf16 a[128:143], v[100:103], v[108:111], a[128:143]\n\t" \
  "ds_read_b64_tr_b16 a[174:175], v137 offset:8704\n\t" \
  "v_exp_f32 v62, v62\n\t" \
  "v_cvt_pk_bf16_f32 v118, v60, v61\n\t" \
  "v_exp_f32 v63, v63\n\t" \
  "v_smfmac_f32_16x16x64_bf16 a[160:163], v[80:83], v[104:111], v154\n\t" \
  "buffer_load_dwordx4 v[200:203], v150, s[80:83], s65 offen offset:256 sc1\n\t" \
  "buffer_load_dwordx4 v[204:207], v150, s[80:83], s65 offen offset:2304 sc1\n\t" \
  "s_waitcnt vmcnt(32)\n\t" \
  "v_mfma_f32_16x16x32_bf16 a[64:67], v[224:227], a[208:211], a[64:67]\n\t" \
  "buffer_load_dwordx4 a[200:203], v149, s[80:83], s64 offen offset:256 sc1\n\t" \
  "v_cvt_pk_bf16_f32 v119, v62, v63\n\t" \
  "v_mfma_f32_16x16x32_bf16 a[64:67], v[228:231], a[212:215], a[64:67]\n\t" \
  "v_exp_f32 v32, v32\n\t" \
  "v_exp_f32 v33, v33\n\t" \
  "v_mfma_f32_16x16x32_bf16 a[72:75], v[232:235], a[208:211], a[72:75]\n\t" \
  "buffer_load_dwordx4 a[204:207], v149, s[80:83], s64 offen offset:2304 sc1\n\t" \
  "v_cvt_pk_bf16_f32 v104, v32, v33\n\t" \
  "v_mfma_f32_16x16x32_bf16 a[72:75], v[236:239], a[212:215], a[72:75]\n\t" \
  "v_exp_f32 v34, v34\n\t" \
  "v_exp_f32 v35, v35\n\t" \
  "v_mfma_f32_16x16x32_bf16 a[80:83], v[240:243], a[208:211], a[80:83]\n\t" \
  "s_add_u32 m0, s72, 4096\n\t" \
  "v_cvt_pk_bf16_f32 v105, v34, v35\n\t" \
  "v_mfma_f32_16x16x32_bf16 a[80:83], v[244:247], a[212:215], a[80:83]\n\t" \
  "v_exp_f32 v36, v36\n\t" \
  "v_exp_f32 v37, v37\n\t" \
  "v_mfma_f32_16x16x32_bf16 a[88:91], v[248:251], a[208:211], a[88:91]\n\t" \
  "s_mov_b32 s68, 1638400\n\t" \
  "v_cvt_pk_bf16_f32 v106, v36, v37\n\t" \
  "v_mfma_f32_16x16x32_bf16 a[88:91], v[252:255], a[212:215], a[88:91]\n\t" \
  "v_exp_f32 v38, v38\n\t" \
  "v_exp_f32 v39, v39\n\t" \
  "v_mfma_f32_32x32x16_bf16 v[48:63], v[88:91], v[72:75], v[16:31]\n\t" \
  "ds_read_b128 v[88:91], v145 offset:45056\n\t" \
  "v_exp_f32 v40, v40\n\t" \
  "v_cvt_pk_bf16_f32 v107, v38, v39\n\t" \
  "v_exp_f32 v41, v41\n\t" \
  "v_mfma_f32_32x32x16_bf16 v[48:63], v[92:95], v[76:79], v[48:63]\n\t" \
  "ds_read_b128 v[92:95], v145 offset:46080\n\t" \
  "v_exp_f32 v42, v42\n\t" \
  "v_cvt_pk_bf16_f32 v108, v40, v41\n\t" \
  "v_exp_f32 v43, v43\n\t" \
  "v_mfma_f32_32x32x16_bf16 a[144:159], v[96:99], v[112:115], a[144:159]\n\t" \
  "ds_read_b128 v[96:99], v145 offset:43008\n\t" \
  "v_exp_f32 v44, v44\n\t" \
  "v_cvt_pk_bf16_f32 v109, v42, v43\n\t" \
  "v_exp_f32 v45, v45\n\t" \
  "v_mfma_f32_32x32x16_bf16 a[144:159], v[100:103], v[116:119], a[144:159]\n\t" \
  "ds_read_b128 v[100:103], v145 offset:44032\n\t" \
  "v_exp_f32 v46, v46\n\t" \
  "v_cvt_pk_bf16_f32 v110, v44, v45\n\t" \
  "v_exp_f32 v47, v47\n\t" \
  "v_smfmac_f32_16x16x64_bf16 a[160:163], v[84:87], v[112:119], v154\n\t" \
  "s_waitcnt vmcnt(26)\n\t" \
  "v_mfma_f32_16x16x32_bf16 a[68:71], v[224:227], a[216:219], a[68:71]\n\t" \
  "buffer_load_dwordx4 a[208:211], v150, s[80:83], s64 offen offset:0 sc1\n\t" \
  "v_cvt_pk_bf16_f32 v111, v46, v47\n\t" \
  "v_mfma_f32_16x16x32_bf16 a[68:71], v[228:231], a[220:223], a[68:71]\n\t" \
  "v_exp_f32 v48, v48\n\t" \
  "v_exp_f32 v49, v49\n\t" \
  "v_mfma_f32_16x16x32_bf16 a[76:79], v[232:235], a[216:219], a[76:79]\n\t" \
  "buffer_load_dwordx4 a[212:215], v150, s[80:83], s64 offen offset:2048 sc1\n\t" \
  "v_cvt_pk_bf16_f32 v112, v48, v49\n\t" \
  "v_mfma_f32_16x16x32_bf16 a[76:79], v[236:239], a[220:223], a[76:79]\n\t" \
  "v_exp_f32 v50, v50\n\t" \
  "v_exp_f32 v51, v51\n\t" \
  "v_mfma_f32_16x16x32_bf16 a[84:87], v[240:243], a[216:219], a[84:87]\n\t" \
  "buffer_load_dwordx4 v140, s[84:87], s68 offen offset:0 lds\n\t" \
  "v_cvt_pk_bf16_f32 v113, v50, v51\n\t" \
  "v_mfma_f32_16x16x32_bf16 a[84:87], v[244:247], a[220:223], a[84:87]\n\t" \
  "v_exp_f32 v52, v52\n\t" \
  "v_exp_f32 v53, v53\n\t" \
  "v_mfma_f32_16x16x32_bf16 a[92:95], v[248:251], a[216:219], a[92:95]\n\t" \
  "buffer_load_dwordx4 v141, s[84:87], s68 offen offset:1024 lds\n\t" \
  "v_cvt_pk_bf16_f32 v114, v52, v53\n\t" \
  "v_mfma_f32_16x16x32_bf16 a[92:95], v[252:255], a[220:223], a[92:95]\n\t" \
  "v_exp_f32 v54, v54\n\t" \
  "v_exp_f32 v55, v55\n\t" \
  "s_waitcnt lgkmcnt(1)\n\t" \
  "v_mfma_f32_32x32x16_bf16 v[32:47], v[88:91], v[64:67], v[0:15]\n\t" \
  "ds_read_b64_tr_b16 v[124:125], v146 offset:12288\n\t" \
  "v_exp_f32 v56, v56\n\t" \
  "v_cvt_pk_bf16_f32 v115, v54, v55\n\t" \
  "v_exp_f32 v57, v57\n\t" \
  "v_mfma_f32_32x32x16_bf16 v[32:47], v[92:95], v[68:71], v[32:47]\n\t" \
  "ds_read_b64_tr_b16 v[126:127], v146 offset:12800\n\t" \
  "v_exp_f32 v58, v58\n\t" \
  "v_cvt_pk_bf16_f32 v116, v56, v57\n\t" \
  "v_exp_f32 v59, v59\n\t" \
  "v_mfma_f32_32x32x16_bf16 a[128:143], v[96:99], v[104:107], a[128:143]\n\t" \
  "ds_read_b64_tr_b16 v[132:133], v147 offset:12288\n\t" \
  "v_exp_f32 v60, v60\n\t" \
  "v_cvt_pk_bf16_f32 v117, v58, v59\n\t" \
  "v_exp_f32 v61, v61\n\t" \
  "s_waitcnt lgkmcnt(3)\n\t" \
  "v_mfma_f32_32x32x16_bf16 a[128:143], v[100:103], v[108:111], a[128:143]\n\t" \
  "ds_read_b64_tr_b16 v[134:135], v147 offset:12800\n\t" \
  "v_exp_f32 v62, v62\n\t" \
  "v_cvt_pk_bf16_f32 v118, v60, v61\n\t" \
  "v_exp_f32 v63, v63\n\t" \
  "v_smfmac_f32_16x16x64_bf16 a[160:163], v[80:83], v[104:111], v154\n\t" \
  "buffer_load_dwordx4 v[208:211], v151, s[80:83], s65 offen offset:0 sc1\n\t" \
  "buffer_load_dwordx4 v[212:215], v151, s[80:83], s65 offen offset:2048 sc1\n\t" \
  "s_waitcnt vmcnt(24)\n\t" \
  "s_barrier\n\t" \
  "v_mfma_f32_16x16x32_bf16 a[96:99], v[224:227], a[224:227], a[96:99]\n\t" \
  "buffer_load_dwordx4 a[216:219], v150, s[80:83], s64 offen offset:256 sc1\n\t" \
  "v_cvt_pk_bf16_f32 v119, v62, v63\n\t" \
  "v_mfma_f32_16x16x32_bf16 a[96:99], v[228:231], a[228:231], a[96:99]\n\t" \
  "v_exp_f32 v32, v32\n\t" \
  "v_exp_f32 v33, v33\n\t" \
  "v_mfma_f32_16x16x32_bf16 a[104:107], v[232:235], a[224:227], a[104:107]\n\t" \
  "buffer_load_dwordx4 a[220:223], v150, s[80:83], s64 offen offset:2304 sc1\n\t" \
  "v_cvt_pk_bf16_f32 v104, v32, v33\n\t" \
  "v_mfma_f32_16x16x32_bf16 a[104:107], v[236:239], a[228:231], a[104:107]\n\t" \
  "v_exp_f32 v34, v34\n\t" \
  "v_exp_f32 v35, v35\n\t" \
  "v_mfma_f32_16x16x32_bf16 a[112:115], v[240:243], a[224:227], a[112:115]\n\t" \
  "buffer_load_dwordx4 v142, s[84:87], s68 offen offset:2048 lds\n\t" \
  "v_cvt_pk_bf16_f32 v105, v34, v35\n\t" \
  "v_mfma_f32_16x16x32_bf16 a[112:115], v[244:247], a[228:231], a[112:115]\n\t" \
  "v_exp_f32 v36, v36\n\t" \
  "v_exp_f32 v37, v37\n\t" \
  "v_mfma_f32_16x16x32_bf16 a[120:123], v[248:251], a[224:227], a[120:123]\n\t" \
  "buffer_load_dwordx4 v143, s[84:87], s68 offen offset:3072 lds\n\t" \
  "v_cvt_pk_bf16_f32 v106, v36, v37\n\t" \
  "v_mfma_f32_16x16x32_bf16 a[120:123], v[252:255], a[228:231], a[120:123]\n\t" \
  "v_exp_f32 v38, v38\n\t" \
  "v_exp_f32 v39, v39\n\t" \
  "v_mfma_f32_32x32x16_bf16 v[48:63], v[88:91], v[72:75], v[16:31]\n\t" \
  "ds_read_b128 v[88:91], v145 offset:0\n\t" \
  "v_exp_f32 v40, v40\n\t" \
  "v_cvt_pk_bf16_f32 v107, v38, v39\n\t" \
  "v_exp_f32 v41, v41\n\t" \
  "v_mfma_f32_32x32x16_bf16 v[48:63], v[92:95], v[76:79], v[48:63]\n\t" \
  "ds_read_b128 v[92:95], v145 offset:1024\n\t" \
  "v_exp_f32 v42, v42\n\t" \
  "v_cvt_pk_bf16_f32 v108, v40, v41\n\t" \
  "v_exp_f32 v43, v43\n\t" \
  "v_mfma_f32_32x32x16_bf16 a[144:159], v[96:99], v[112:115], a[144:159]\n\t" \
  "ds_read_b128 v[96:99], v145 offset:47104\n\t" \
  "v_exp_f32 v44, v44\n\t" \
  "v_cvt_pk_bf16_f32 v109, v42, v43\n\t" \
  "v_exp_f32 v45, v45\n\t" \
  "v_mfma_f32_32x32x16_bf16 a[144:159], v[100:103], v[116:119], a[144:159]\n\t" \
  "ds_read_b128 v[100:103], v145 offset:48128\n\t" \
  "v_exp_f32 v46, v46\n\t" \
  "v_cvt_pk_bf16_f32 v110, v44, v45\n\t" \
  "v_exp_f32 v47, v47\n\t" \
  "v_smfmac_f32_16x16x64_bf16 a[160:163], v[84:87], v[112:119], v154\n\t" \
  "buffer_load_dwordx4 v[216:219], v151, s[80:83], s65 offen offset:256 sc1\n\t" \
  "buffer_load_dwordx4 v[220:223], v151, s[80:83], s65 offen offset:2304 sc1\n\t" \
  "s_lshl_b32 s71, s75, 12\n\t" \
  "s_add_u32 s71, s71, s74\n\t" \
  "s_add_u32 m0, s71, 126976\n\t" \
  "s_mov_b32 s70, 786432\n\t" \
  "v_xor_b32 v141, 32, v141\n\t" \
  "v_xor_b32 v143, 32, v143\n\t" \
  "s_waitcnt vmcnt(28)\n\t" \
  "v_mfma_f32_16x16x32_bf16 a[100:103], v[224:227], a[232:235], a[100:103]\n\t" \
  "buffer_load_dwordx4 a[224:227], v151, s[80:83], s64 offen offset:0 sc1\n\t" \
  "v_cvt_pk_bf16_f32 v111, v46, v47\n\t" \
  "v_mfma_f32_16x16x32_bf16 a[100:103], v[228:231], a[236:239], a[100:103]\n\t" \
  "v_exp_f32 v48, v48\n\t" \
  "v_exp_f32 v49, v49\n\t" \
  "v_mfma_f32_16x16x32_bf16 a[108:111], v[232:235], a[232:235], a[108:111]\n\t" \
  "buffer_load_dwordx4 a[228:231], v151, s[80:83], s64 offen offset:2048 sc1\n\t" \
  "v_cvt_pk_bf16_f32 v112, v48, v49\n\t" \
  "v_mfma_f32_16x16x32_bf16 a[108:111], v[236:239], a[236:239], a[108:111]\n\t" \
  "v_exp_f32 v50, v50\n\t" \
  "v_exp_f32 v51, v51\n\t" \
  "v_mfma_f32_16x16x32_bf16 a[116:119], v[240:243], a[232:235], a[116:119]\n\t" \
  "buffer_load_dwordx4 v140, s[92:95], s70 offen offset:0 lds\n\t" \
  "v_cvt_pk_bf16_f32 v113, v50, v51\n\t" \
  "v_mfma_f32_16x16x32_bf16 a[116:119], v[244:247], a[236:239], a[116:119]\n\t" \
  "v_exp_f32 v52, v52\n\t" \
  "v_exp_f32 v53, v53\n\t" \
  "v_mfma_f32_16x16x32_bf16 a[124:127], v[248:251], a[232:235], a[124:127]\n\t" \
  "buffer_load_dwordx4 v141, s[92:95], s70 offen offset:1024 lds\n\t" \
  "v_cvt_pk_bf16_f32 v114, v52, v53\n\t" \
  "v_mfma_f32_16x16x32_bf16 a[124:127], v[252:255], a[236:239], a[124:127]\n\t" \
  "v_exp_f32 v54, v54\n\t" \
  "v_exp_f32 v55, v55\n\t" \
  "s_waitcnt lgkmcnt(1)\n\t" \
  "v_mfma_f32_32x32x16_bf16 v[32:47], v[88:91], v[64:67], v[0:15]\n\t" \
  "ds_read_b64_tr_b16 a[168:169], v155 offset:12288\n\t" \
  "v_exp_f32 v56, v56\n\t" \
  "v_cvt_pk_bf16_f32 v115, v54, v55\n\t" \
  "v_exp_f32 v57, v57\n\t" \
  "v_mfma_f32_32x32x16_bf16 v[32:47], v[92:95], v[68:71], v[32:47]\n\t" \
  "ds_read_b64_tr_b16 a[170:171], v155 offset:12800\n\t" \
  "v_exp_f32 v58, v58\n\t" \
  "v_cvt_pk_bf16_f32 v116, v56, v57\n\t" \
  "v_exp_f32 v59, v59\n\t" \
  "v_mfma_f32_32x32x16_bf16 a[128:143], v[96:99], v[104:107], a[128:143]\n\t" \
  "ds_read_b64_tr_b16 v[156:157], v137 offset:12288\n\t" \
  "v_exp_f32 v60, v60\n\t" \
  "v_cvt_pk_bf16_f32 v117, v58, v59\n\t" \
  "v_exp_f32 v61, v61\n\t" \
  "s_waitcnt lgkmcnt(3)\n\t" \
  "v_mfma_f32_32x32x16_bf16 a[128:143], v[100:103], v[108:111], a[128:143]\n\t" \
  "ds_read_b64_tr_b16 v[158:159], v137 offset:12800\n\t" \
  "v_exp_f32 v62, v62\n\t" \
  "v_cvt_pk_bf16_f32 v118, v60, v61\n\t" \
  "v_exp_f32 v63, v63\n\t" \
  "v_smfmac_f32_16x16x64_bf16 a[160:163], v[80:83], v[104:111], v154\n\t" \
  "buffer_load_dwordx4 v142, s[92:95], s70 offen offset:2048 lds\n\t" \
  "buffer_load_dwordx4 v143, s[92:95], s70 offen offset:3072 lds\n\t" \
  "v_xor_b32 v141, 32, v141\n\t" \
  "v_xor_b32 v143, 32, v143\n\t" \
  "s_waitcnt vmcnt(32) lgkmcnt(0)\n\t" \
  "v_mfma_f32_16x16x32_bf16 a[0:3], v[120:123], a[176:179], a[0:3]\n\t" \
  "buffer_load_dwordx4 a[232:235], v151, s[80:83], s64 offen offset:256 sc1\n\t" \
  "v_cvt_pk_bf16_f32 v119, v62, v63\n\t" \
  "v_mfma_f32_16x16x32_bf16 a[0:3], v[124:127], a[180:183], a[0:3]\n\t" \
  "v_exp_f32 v32, v32\n\t" \
  "v_exp_f32 v33, v33\n\t" \
  "v_mfma_f32_16x16x32_bf16 a[8:11], v[128:131], a[176:179], a[8:11]\n\t" \
  "buffer_load_dwordx4 a[236:239], v151, s[80:83], s64 offen offset:2304 sc1\n\t" \
  "v_cvt_pk_bf16_f32 v104, v32, v33\n\t" \
  "v_mfma_f32_16x16x32_bf16 a[8:11], v[132:135], a[180:183], a[8:11]\n\t" \
  "v_exp_f32 v34, v34\n\t" \
  "v_exp_f32 v35, v35\n\t" \
  "v_mfma_f32_16x16x32_bf16 a[16:19], a[164:167], a[176:179], a[16:19]\n\t" \
  "s_add_u32 m0, s72, 8192\n\t" \
  "v_cvt_pk_bf16_f32 v105, v34, v35\n\t" \
  "v_mfma_f32_16x16x32_bf16 a[16:19], a[168:171], a[180:183], a[16:19]\n\t" \
  "v_exp_f32 v36, v36\n\t" \
  "v_exp_f32 v37, v37\n\t" \
  "v_mfma_f32_16x16x32_bf16 a[24:27], a[172:175], a[176:179], a[24:27]\n\t" \
  "s_mov_b32 s68, 1703936\n\t" \
  "v_cvt_pk_bf16_f32 v106, v36, v37\n\t" \
  "v_mfma_f32_16x16x32_bf16 a[24:27], v[156:159], a[180:183], a[24:27]\n\t" \
  "v_exp_f32 v38, v38\n\t" \
  "v_exp_f32 v39, v39\n\t" \
  "v_mfma_f32_32x32x16_bf16 v[48:63], v[88:91], v[72:75], v[16:31]\n\t" \
  "ds_read_b128 v[88:91], v145 offset:4096\n\t" \
  "v_exp_f32 v40, v40\n\t" \
  "v_cvt_pk_bf16_f32 v107, v38, v39\n\t" \
  "v_exp_f32 v41, v41\n\t" \
  "v_mfma_f32_32x32x16_bf16 v[48:63], v[92:95], v[76:79], v[48:63]\n\t" \
  "ds_read_b128 v[92:95], v145 offset:5120\n\t" \
  "v_exp_f32 v42, v42\n\t" \
  "v_cvt_pk_bf16_f32 v108, v40, v41\n\t" \
  "v_exp_f32 v43, v43\n\t" \
  "v_mfma_f32_32x32x16_bf16 a[144:159], v[96:99], v[112:115], a[144:159]\n\t" \
  "ds_read_b128 v[96:99], v145 offset:2048\n\t" \
  "v_exp_f32 v44, v44\n\t" \
  "v_cvt_pk_bf16_f32 v109, v42, v43\n\t" \
  "v_exp_f32 v45, v45\n\t" \
  "v_mfma_f32_32x32x16_bf16 a[144:159], v[100:103], v[116:119], a[144:159]\n\t" \
  "ds_read_b128 v[100:103], v145 offset:3072\n\t" \
  "v_exp_f32 v46, v46\n\t" \
  "v_cvt_pk_bf16_f32 v110, v44, v45\n\t" \
  "v_exp_f32 v47, v47\n\t" \
  "v_smfmac_f32_16x16x64_bf16 a[160:163], v[84:87], v[112:119], v154\n\t" \
  "s_mov_b32 s64, 53248\n\t" \
  "s_waitcnt vmcnt(28)\n\t" \
  "v_mfma_f32_16x16x32_bf16 a[4:7], v[120:123], a[184:187], a[4:7]\n\t" \
  "buffer_load_dwordx4 a[176:179], v148, s[80:83], s64 offen offset:0 sc1\n\t" \
  "v_cvt_pk_bf16_f32 v111, v46, v47\n\t" \
  "v_mfma_f32_16x16x32_bf16 a[4:7], v[124:127], a[188:191], a[4:7]\n\t" \
  "v_exp_f32 v48, v48\n\t" \
  "v_exp_f32 v49, v49\n\t" \
  "v_mfma_f32_16x16x32_bf16 a[12:15], v[128:131], a[184:187], a[12:15]\n\t" \
  "buffer_load_dwordx4 a[180:183], v148, s[80:83], s64 offen offset:2048 sc1\n\t" \
  "v_cvt_pk_bf16_f32 v112, v48, v49\n\t" \
  "v_mfma_f32_16x16x32_bf16 a[12:15], v[132:135], a[188:191], a[12:15]\n\t" \
  "v_exp_f32 v50, v50\n\t" \
  "v_exp_f32 v51, v51\n\t" \
  "v_mfma_f32_16x16x32_bf16 a[20:23], a[164:167], a[184:187], a[20:23]\n\t" \
  "buffer_load_dwordx4 v140, s[84:87], s68 offen offset:0 lds\n\t" \
  "v_cvt_pk_bf16_f32 v113, v50, v51\n\t" \
  "v_mfma_f32_16x16x32_bf16 a[20:23], a[168:171], a[188:191], a[20:23]\n\t" \
  "v_exp_f32 v52, v52\n\t" \
  "v_exp_f32 v53, v53\n\t" \
  "v_mfma_f32_16x16x32_bf16 a[28:31], a[172:175], a[184:187], a[28:31]\n\t" \
  "buffer_load_dwordx4 v141, s[84:87], s68 offen offset:1024 lds\n\t" \
  "v_cvt_pk_bf16_f32 v114, v52, v53\n\t" \
  "v_mfma_f32_16x16x32_bf16 a[28:31], v[156:159], a[188:191], a[28:31]\n\t" \
  "v_exp_f32 v54, v54\n\t" \
  "v_exp_f32 v55, v55\n\t" \
  "s_waitcnt lgkmcnt(1)\n\t" \
  "v_mfma_f32_32x32x16_bf16 v[32:47], v[88:91], v[64:67], v[0:15]\n\t" \
  "buffer_load_dwordx2 v[224:225], v153, s[56:59], 0 offen offset:0\n\t" \
  "v_exp_f32 v56, v56\n\t" \
  "v_cvt_pk_bf16_f32 v115, v54, v55\n\t" \
  "v_exp_f32 v57, v57\n\t" \
  "v_mfma_f32_32x32x16_bf16 v[32:47], v[92:95], v[68:71], v[32:47]\n\t" \
  "buffer_load_dwordx2 v[226:227], v153, s[56:59], 0 offen offset:16\n\t" \
  "v_exp_f32 v58, v58\n\t" \
  "v_cvt_pk_bf16_f32 v116, v56, v57\n\t" \
  "v_exp_f32 v59, v59\n\t" \
  "v_mfma_f32_32x32x16_bf16 a[128:143], v[96:99], v[104:107], a[128:143]\n\t" \
  "buffer_load_dwordx2 v[228:229], v153, s[56:59], 0 offen offset:32\n\t" \
  "v_exp_f32 v60, v60\n\t" \
  "v_cvt_pk_bf16_f32 v117, v58, v59\n\t" \
  "v_exp_f32 v61, v61\n\t" \
  "s_waitcnt lgkmcnt(0)\n\t" \
  "v_mfma_f32_32x32x16_bf16 a[128:143], v[100:103], v[108:111], a[128:143]\n\t" \
  "buffer_load_dwordx2 v[230:231], v153, s[56:59], 0 offen offset:48\n\t" \
  "v_exp_f32 v62, v62\n\t" \
  "v_cvt_pk_bf16_f32 v118, v60, v61\n\t" \
  "v_exp_f32 v63, v63\n\t" \
  "v_smfmac_f32_16x16x64_bf16 a[160:163], v[80:83], v[104:111], v154\n\t" \
  "buffer_load_dwordx4 a[240:243], v151, s[80:83], s66 offen offset:0 sc1\n\t" \
  "buffer_load_dwordx4 a[244:247], v151, s[80:83], s66 offen offset:2048 sc1\n\t" \
  "s_waitcnt vmcnt(34)\n\t" \
  "v_mfma_f32_16x16x32_bf16 a[32:35], v[120:123], a[192:195], a[32:35]\n\t" \
  "buffer_load_dwordx4 a[184:187], v148, s[80:83], s64 offen offset:256 sc1\n\t" \
  "v_cvt_pk_bf16_f32 v119, v62, v63\n\t" \
  "v_mfma_f32_16x16x32_bf16 a[32:35], v[124:127], a[196:199], a[32:35]\n\t" \
  "v_exp_f32 v32, v32\n\t" \
  "v_exp_f32 v33, v33\n\t" \
  "v_mfma_f32_16x16x32_bf16 a[40:43], v[128:131], a[192:195], a[40:43]\n\t" \
  "buffer_load_dwordx4 a[188:191], v148, s[80:83], s64 offen offset:2304 sc1\n\t" \
  "v_cvt_pk_bf16_f32 v104, v32, v33\n\t" \
  "v_mfma_f32_16x16x32_bf16 a[40:43], v[132:135], a[196:199], a[40:43]\n\t" \
  "v_exp_f32 v34, v34\n\t" \
  "v_exp_f32 v35, v35\n\t" \
  "v_mfma_f32_16x16x32_bf16 a[48:51], a[164:167], a[192:195], a[48:51]\n\t" \
  "buffer_load_dwordx4 v142, s[84:87], s68 offen offset:2048 lds\n\t" \
  "v_cvt_pk_bf16_f32 v105, v34, v35\n\t" \
  "v_mfma_f32_16x16x32_bf16 a[48:51], a[168:171], a[196:199], a[48:51]\n\t" \
  "v_exp_f32 v36, v36\n\t" \
  "v_exp_f32 v37, v37\n\t" \
  "v_mfma_f32_16x16x32_bf16 a[56:59], a[172:175], a[192:195], a[56:59]\n\t" \
  "buffer_load_dwordx4 v143, s[84:87], s68 offen offset:3072 lds\n\t" \
  "v_cvt_pk_bf16_f32 v106, v36, v37\n\t" \
  "v_mfma_f32_16x16x32_bf16 a[56:59], v[156:159], a[196:199], a[56:59]\n\t" \
  "v_exp_f32 v38, v38\n\t" \
  "v_exp_f32 v39, v39\n\t" \
  "v_mfma_f32_32x32x16_bf16 v[48:63], v[88:91], v[72:75], v[16:31]\n\t" \
  "ds_read_b128 v[88:91], v145 offset:8192\n\t" \
  "v_exp_f32 v40, v40\n\t" \
  "v_cvt_pk_bf16_f32 v107, v38, v39\n\t" \
  "v_exp_f32 v41, v41\n\t" \
  "v_mfma_f32_32x32x16_bf16 v[48:63], v[92:95], v[76:79], v[48:63]\n\t" \
  "ds_read_b128 v[92:95], v145 offset:9216\n\t" \
  "v_exp_f32 v42, v42\n\t" \
  "v_cvt_pk_bf16_f32 v108, v40, v41\n\t" \
  "v_exp_f32 v43, v43\n\t" \
  "v_mfma_f32_32x32x16_bf16 a[144:159], v[96:99], v[112:115], a[144:159]\n\t" \
  "ds_read_b128 v[96:99], v145 offset:6144\n\t" \
  "v_exp_f32 v44, v44\n\t" \
  "v_cvt_pk_bf16_f32 v109, v42, v43\n\t" \
  "v_exp_f32 v45, v45\n\t" \
  "v_mfma_f32_32x32x16_bf16 a[144:159], v[100:103], v[116:119], a[144:159]\n\t" \
  "ds_read_b128 v[100:103], v145 offset:7168\n\t" \
  "v_exp_f32 v46, v46\n\t" \
  "v_cvt_pk_bf16_f32 v110, v44, v45\n\t" \
  "v_exp_f32 v47, v47\n\t" \
  "v_smfmac_f32_16x16x64_bf16 a[160:163], v[84:87], v[112:119], v154\n\t" \
  "s_waitcnt vmcnt(34)\n\t" \
  "v_mfma_f32_16x16x32_bf16 a[36:39], v[120:123], a[200:203], a[36:39]\n\t" \
  "buffer_load_dwordx4 a[192:195], v149, s[80:83], s64 offen offset:0 sc1\n\t" \
  "v_cvt_pk_bf16_f32 v111, v46, v47\n\t" \
  "v_mfma_f32_16x16x32_bf16 a[36:39], v[124:127], a[204:207], a[36:39]\n\t" \
  "v_exp_f32 v48, v48\n\t" \
  "v_exp_f32 v49, v49\n\t" \
  "v_mfma_f32_16x16x32_bf16 a[44:47], v[128:131], a[200:203], a[44:47]\n\t" \
  "buffer_load_dwordx4 a[196:199], v149, s[80:83], s64 offen offset:2048 sc1\n\t" \
  "v_cvt_pk_bf16_f32 v112, v48, v49\n\t" \
  "v_mfma_f32_16x16x32_bf16 a[44:47], v[132:135], a[204:207], a[44:47]\n\t" \
  "v_exp_f32 v50, v50\n\t" \
  "v_exp_f32 v51, v51\n\t" \
  "v_mfma_f32_16x16x32_bf16 a[52:55], a[164:167], a[200:203], a[52:55]\n\t" \
  "v_exp_f32 v52, v52\n\t" \
  "v_mfma_f32_16x16x32_bf16 a[52:55], a[168:171], a[204:207], a[52:55]\n\t" \
  "v_cvt_pk_bf16_f32 v113, v50, v51\n\t" \
  "v_exp_f32 v53, v53\n\t" \
  "v_mfma_f32_16x16x32_bf16 a[60:63], a[172:175], a[200:203], a[60:63]\n\t" \
  "v_exp_f32 v54, v54\n\t" \
  "v_mfma_f32_16x16x32_bf16 a[60:63], v[156:159], a[204:207], a[60:63]\n\t" \
  "v_cvt_pk_bf16_f32 v114, v52, v53\n\t" \
  "v_exp_f32 v55, v55\n\t" \
  "s_waitcnt lgkmcnt(1)\n\t" \
  "v_mfma_f32_32x32x16_bf16 v[32:47], v[88:91], v[64:67], v[0:15]\n\t" \
  "buffer_load_dwordx2 v[232:233], v153, s[56:59], 0 offen offset:64\n\t" \
  "v_exp_f32 v56, v56\n\t" \
  "v_cvt_pk_bf16_f32 v115, v54, v55\n\t" \
  "v_exp_f32 v57, v57\n\t" \
  "v_mfma_f32_32x32x16_bf16 v[32:47], v[92:95], v[68:71], v[32:47]\n\t" \
  "buffer_load_dwordx2 v[234:235], v153, s[56:59], 0 offen offset:80\n\t" \
  "v_exp_f32 v58, v58\n\t" \
  "v_cvt_pk_bf16_f32 v116, v56, v57\n\t" \
  "v_exp_f32 v59, v59\n\t" \
  "v_mfma_f32_32x32x16_bf16 a[128:143], v[96:99], v[104:107], a[128:143]\n\t" \
  "buffer_load_dwordx2 v[236:237], v153, s[56:59], 0 offen offset:96\n\t" \
  "v_exp_f32 v60, v60\n\t" \
  "v_cvt_pk_bf16_f32 v117, v58, v59\n\t" \
  "v_exp_f32 v61, v61\n\t" \
  "s_waitcnt lgkmcnt(0)\n\t" \
  "v_mfma_f32_32x32x16_bf16 a[128:143], v[100:103], v[108:111], a[128:143]\n\t" \
  "buffer_load_dwordx2 v[238:239], v153, s[56:59], 0 offen offset:112\n\t" \
  "v_exp_f32 v62, v62\n\t" \
  "v_cvt_pk_bf16_f32 v118, v60, v61\n\t" \
  "v_exp_f32 v63, v63\n\t" \
  "v_smfmac_f32_16x16x64_bf16 a[160:163], v[80:83], v[104:111], v154\n\t" \
  "buffer_load_dwordx4 a[248:251], v151, s[80:83], s66 offen offset:256 sc1\n\t" \
  "buffer_load_dwordx4 a[252:255], v151, s[80:83], s66 offen offset:2304 sc1\n\t" \
  "s_waitcnt vmcnt(40)\n\t" \
  "v_mfma_f32_16x16x32_bf16 a[64:67], v[120:123], a[208:211], a[64:67]\n\t" \
  "buffer_load_dwordx4 a[200:203], v149, s[80:83], s64 offen offset:256 sc1\n\t" \
  "v_cvt_pk_bf16_f32 v119, v62, v63\n\t" \
  "v_mfma_f32_16x16x32_bf16 a[64:67], v[124:127], a[212:215], a[64:67]\n\t" \
  "v_exp_f32 v32, v32\n\t" \
  "v_exp_f32 v33, v33\n\t" \
  "v_mfma_f32_16x16x32_bf16 a[72:75], v[128:131], a[208:211], a[72:75]\n\t" \
  "buffer_load_dwordx4 a[204:207], v149, s[80:83], s64 offen offset:2304 sc1\n\t" \
  "v_cvt_pk_bf16_f32 v104, v32, v33\n\t" \
  "v_mfma_f32_16x16x32_bf16 a[72:75], v[132:135], a[212:215], a[72:75]\n\t" \
  "v_exp_f32 v34, v34\n\t" \
  "v_exp_f32 v35, v35\n\t" \
  "v_mfma_f32_16x16x32_bf16 a[80:83], a[164:167], a[208:211], a[80:83]\n\t" \
  "s_add_u32 m0, s72, 12288\n\t" \
  "v_cvt_pk_bf16_f32 v105, v34, v35\n\t" \
  "v_mfma_f32_16x16x32_bf16 a[80:83], a[168:171], a[212:215], a[80:83]\n\t" \
  "v_exp_f32 v36, v36\n\t" \
  "v_exp_f32 v37, v37\n\t" \
  "v_mfma_f32_16x16x32_bf16 a[88:91], a[172:175], a[208:211], a[88:91]\n\t" \
  "s_mov_b32 s68, 1769472\n\t" \
  "v_cvt_pk_bf16_f32 v106, v36, v37\n\t" \
  "v_mfma_f32_16x16x32_bf16 a[88:91], v[156:159], a[212:215], a[88:91]\n\t" \
  "v_exp_f32 v38, v38\n\t" \
  "v_exp_f32 v39, v39\n\t" \
  "v_mfma_f32_32x32x16_bf16 v[48:63], v[88:91], v[72:75], v[16:31]\n\t" \
  "ds_read_b128 v[88:91], v145 offset:12288\n\t" \
  "v_exp_f32 v40, v40\n\t" \
  "v_cvt_pk_bf16_f32 v107, v38, v39\n\t" \
  "v_exp_f32 v41, v41\n\t" \
  "v_mfma_f32_32x32x16_bf16 v[48:63], v[92:95], v[76:79], v[48:63]\n\t" \
  "ds_read_b128 v[92:95], v145 offset:13312\n\t" \
  "v_exp_f32 v42, v42\n\t" \
  "v_cvt_pk_bf16_f32 v108, v40, v41\n\t" \
  "v_exp_f32 v43, v43\n\t" \
  "v_mfma_f32_32x32x16_bf16 a[144:159], v[96:99], v[112:115], a[144:159]\n\t" \
  "ds_read_b128 v[96:99], v145 offset:10240\n\t" \
  "v_exp_f32 v44, v44\n\t" \
  "v_cvt_pk_bf16_f32 v109, v42, v43\n\t" \
  "v_exp_f32 v45, v45\n\t" \
  "v_mfma_f32_32x32x16_bf16 a[144:159], v[100:103], v[116:119], a[144:159]\n\t" \
  "ds_read_b128 v[100:103], v145 offset:11264\n\t" \
  "v_exp_f32 v46, v46\n\t" \
  "v_cvt_pk_bf16_f32 v110, v44, v45\n\t" \
  "v_exp_f32 v47, v47\n\t" \
  "v_smfmac_f32_16x16x64_bf16 a[160:163], v[84:87], v[112:119], v154\n\t" \
  "s_waitcnt vmcnt(36)\n\t" \
  "v_mfma_f32_16x16x32_bf16 a[68:71], v[120:123], a[216:219], a[68:71]\n\t" \
  "buffer_load_dwordx4 a[208:211], v150, s[80:83], s64 offen offset:0 sc1\n\t" \
  "v_cvt_pk_bf16_f32 v111, v46, v47\n\t" \
  "v_mfma_f32_16x16x32_bf16 a[68:71], v[124:127], a[220:223], a[68:71]\n\t" \
  "v_exp_f32 v48, v48\n\t" \
  "v_exp_f32 v49, v49\n\t" \
  "v_mfma_f32_16x16x32_bf16 a[76:79], v[128:131], a[216:219], a[76:79]\n\t" \
  "buffer_load_dwordx4 a[212:215], v150, s[80:83], s64 offen offset:2048 sc1\n\t" \
  "v_cvt_pk_bf16_f32 v112, v48, v49\n\t" \
  "v_mfma_f32_16x16x32_bf16 a[76:79], v[132:135], a[220:223], a[76:79]\n\t" \
  "v_exp_f32 v50, v50\n\t" \
  "v_exp_f32 v51, v51\n\t" \
  "v_mfma_f32_16x16x32_bf16 a[84:87], a[164:167], a[216:219], a[84:87]\n\t" \
  "buffer_load_dwordx4 v140, s[84:87], s68 offen offset:0 lds\n\t" \
  "v_cvt_pk_bf16_f32 v113, v50, v51\n\t" \
  "v_mfma_f32_16x16x32_bf16 a[84:87], a[168:171], a[220:223], a[84:87]\n\t" \
  "v_exp_f32 v52, v52\n\t" \
  "v_exp_f32 v53, v53\n\t" \
  "v_mfma_f32_16x16x32_bf16 a[92:95], a[172:175], a[216:219], a[92:95]\n\t" \
  "buffer_load_dwordx4 v141, s[84:87], s68 offen offset:1024 lds\n\t" \
  "v_cvt_pk_bf16_f32 v114, v52, v53\n\t" \
  "v_mfma_f32_16x16x32_bf16 a[92:95], v[156:159], a[220:223], a[92:95]\n\t" \
  "v_exp_f32 v54, v54\n\t" \
  "v_exp_f32 v55, v55\n\t" \
  "s_waitcnt lgkmcnt(1)\n\t" \
  "v_mfma_f32_32x32x16_bf16 v[32:47], v[88:91], v[64:67], v[0:15]\n\t" \
  "v_exp_f32 v56, v56\n\t" \
  "v_cvt_pk_bf16_f32 v115, v54, v55\n\t" \
  "v_exp_f32 v57, v57\n\t" \
  "v_mfma_f32_32x32x16_bf16 v[32:47], v[92:95], v[68:71], v[32:47]\n\t" \
  "v_exp_f32 v58, v58\n\t" \
  "v_cvt_pk_bf16_f32 v116, v56, v57\n\t" \
  "v_exp_f32 v59, v59\n\t" \
  "v_mfma_f32_32x32x16_bf16 a[128:143], v[96:99], v[104:107], a[128:143]\n\t" \
  "v_exp_f32 v60, v60\n\t" \
  "v_cvt_pk_bf16_f32 v117, v58, v59\n\t" \
  "v_exp_f32 v61, v61\n\t" \
  "s_waitcnt lgkmcnt(0)\n\t" \
  "v_mfma_f32_32x32x16_bf16 a[128:143], v[100:103], v[108:111], a[128:143]\n\t" \
  "v_exp_f32 v62, v62\n\t" \
  "v_cvt_pk_bf16_f32 v118, v60, v61\n\t" \
  "v_exp_f32 v63, v63\n\t" \
  "v_smfmac_f32_16x16x64_bf16 a[160:163], v[80:83], v[104:111], v154\n\t" \
  "s_barrier\n\t" \
  "s_waitcnt vmcnt(34)\n\t" \
  "v_mfma_f32_16x16x32_bf16 a[96:99], v[120:123], a[224:227], a[96:99]\n\t" \
  "buffer_load_dwordx4 a[216:219], v150, s[80:83], s64 offen offset:256 sc1\n\t" \
  "v_cvt_pk_bf16_f32 v119, v62, v63\n\t" \
  "v_mfma_f32_16x16x32_bf16 a[96:99], v[124:127], a[228:231], a[96:99]\n\t" \
  "v_exp_f32 v32, v32\n\t" \
  "v_exp_f32 v33, v33\n\t" \
  "v_mfma_f32_16x16x32_bf16 a[104:107], v[128:131], a[224:227], a[104:107]\n\t" \
  "buffer_load_dwordx4 a[220:223], v150, s[80:83], s64 offen offset:2304 sc1\n\t" \
  "v_cvt_pk_bf16_f32 v104, v32, v33\n\t" \
  "v_mfma_f32_16x16x32_bf16 a[104:107], v[132:135], a[228:231], a[104:107]\n\t" \
  "v_exp_f32 v34, v34\n\t" \
  "v_exp_f32 v35, v35\n\t" \
  "v_mfma_f32_16x16x32_bf16 a[112:115], a[164:167], a[224:227], a[112:115]\n\t" \
  "buffer_load_dwordx4 v142, s[84:87], s68 offen offset:2048 lds\n\t" \
  "v_cvt_pk_bf16_f32 v105, v34, v35\n\t" \
  "v_mfma_f32_16x16x32_bf16 a[112:115], a[168:171], a[228:231], a[112:115]\n\t" \
  "v_exp_f32 v36, v36\n\t" \
  "v_exp_f32 v37, v37\n\t" \
  "v_mfma_f32_16x16x32_bf16 a[120:123], a[172:175], a[224:227], a[120:123]\n\t" \
  "buffer_load_dwordx4 v143, s[84:87], s68 offen offset:3072 lds\n\t" \
  "v_cvt_pk_bf16_f32 v106, v36, v37\n\t" \
  "v_mfma_f32_16x16x32_bf16 a[120:123], v[156:159], a[228:231], a[120:123]\n\t" \
  "v_exp_f32 v38, v38\n\t" \
  "v_exp_f32 v39, v39\n\t" \
  "v_mfma_f32_32x32x16_bf16 v[48:63], v[88:91], v[72:75], v[16:31]\n\t" \
  "v_exp_f32 v40, v40\n\t" \
  "v_cvt_pk_bf16_f32 v107, v38, v39\n\t" \
  "v_exp_f32 v41, v41\n\t" \
  "v_mfma_f32_32x32x16_bf16 v[48:63], v[92:95], v[76:79], v[48:63]\n\t" \
  "v_exp_f32 v42, v42\n\t" \
  "v_cvt_pk_bf16_f32 v108, v40, v41\n\t" \
  "v_exp_f32 v43, v43\n\t" \
  "v_mfma_f32_32x32x16_bf16 a[144:159], v[96:99], v[112:115], a[144:159]\n\t" \
  "ds_read_b128 v[96:99], v145 offset:14336\n\t" \
  "v_exp_f32 v44, v44\n\t" \
  "v_cvt_pk_bf16_f32 v109, v42, v43\n\t" \
  "v_exp_f32 v45, v45\n\t" \
  "v_mfma_f32_32x32x16_bf16 a[144:159], v[100:103], v[116:119], a[144:159]\n\t" \
  "ds_read_b128 v[100:103], v145 offset:15360\n\t" \
  "v_exp_f32 v46, v46\n\t" \
  "v_cvt_pk_bf16_f32 v110, v44, v45\n\t" \
  "v_exp_f32 v47, v47\n\t" \
  "v_smfmac_f32_16x16x64_bf16 a[160:163], v[84:87], v[112:119], v154\n\t" \
  "s_lshl_b32 s71, s75, 12\n\t" \
  "s_add_u32 s71, s71, s74\n\t" \
  "s_add_u32 m0, s71, 143360\n\t" \
  "s_mov_b32 s70, 1048576\n\t" \
  "v_xor_b32 v141, 32, v141\n\t" \
  "v_xor_b32 v143, 32, v143\n\t" \
  "s_waitcnt vmcnt(32)\n\t" \
  "v_mfma_f32_16x16x32_bf16 a[100:103], v[120:123], a[232:235], a[100:103]\n\t" \
  "v_exp_f32 v48, v48\n\t" \
  "v_mfma_f32_16x16x32_bf16 a[100:103], v[124:127], a[236:239], a[100:103]\n\t" \
  "v_cvt_pk_bf16_f32 v111, v46, v47\n\t" \
  "v_exp_f32 v49, v49\n\t" \
  "v_mfma_f32_16x16x32_bf16 a[108:111], v[128:131], a[232:235], a[108:111]\n\t" \
  "v_exp_f32 v50, v50\n\t" \
  "v_mfma_f32_16x16x32_bf16 a[108:111], v[132:135], a[236:239], a[108:111]\n\t" \
  "v_cvt_pk_bf16_f32 v112, v48, v49\n\t" \
  "v_exp_f32 v51, v51\n\t" \
  "v_mfma_f32_16x16x32_bf16 a[116:119], a[164:167], a[232:235], a[116:119]\n\t" \
  "buffer_load_dwordx4 v140, s[92:95], s70 offen offset:0 lds\n\t" \
  "v_cvt_pk_bf16_f32 v113, v50, v51\n\t" \
  "v_mfma_f32_16x16x32_bf16 a[116:119], a[168:171], a[236:239], a[116:119]\n\t" \
  "v_exp_f32 v52, v52\n\t" \
  "v_exp_f32 v53, v53\n\t" \
  "v_mfma_f32_16x16x32_bf16 a[124:127], a[172:175], a[232:235], a[124:127]\n\t" \
  "buffer_load_dwordx4 v141, s[92:95], s70 offen offset:1024 lds\n\t" \
  "v_cvt_pk_bf16_f32 v114, v52, v53\n\t" \
  "v_mfma_f32_16x16x32_bf16 a[124:127], v[156:159], a[236:239], a[124:127]\n\t" \
  "v_exp_f32 v54, v54\n\t" \
  "v_exp_f32 v55, v55\n\t" \
  "v_exp_f32 v56, v56\n\t" \
  "v_cvt_pk_bf16_f32 v115, v54, v55\n\t" \
  "v_exp_f32 v57, v57\n\t" \
  "v_exp_f32 v58, v58\n\t" \
  "v_cvt_pk_bf16_f32 v116, v56, v57\n\t" \
  "v_exp_f32 v59, v59\n\t" \
  "s_waitcnt lgkmcnt(1)\n\t" \
  "v_mfma_f32_32x32x16_bf16 a[128:143], v[96:99], v[104:107], a[128:143]\n\t" \
  "v_exp_f32 v60, v60\n\t" \
  "v_cvt_pk_bf16_f32 v117, v58, v59\n\t" \
  "v_exp_f32 v61, v61\n\t" \
  "s_waitcnt lgkmcnt(0)\n\t" \
  "v_mfma_f32_32x32x16_bf16 a[128:143], v[100:103], v[108:111], a[128:143]\n\t" \
  "v_exp_f32 v62, v62\n\t" \
  "v_cvt_pk_bf16_f32 v118, v60, v61\n\t" \
  "v_exp_f32 v63, v63\n\t" \
  "v_smfmac_f32_16x16x64_bf16 a[160:163], v[80:83], v[104:111], v154\n\t" \
  "buffer_load_dwordx4 v142, s[92:95], s70 offen offset:2048 lds\n\t" \
  "buffer_load_dwordx4 v143, s[92:95], s70 offen offset:3072 lds\n\t" \
  "v_mfma_f32_32x32x16_bf16 a[144:159], v[96:99], v[112:115], a[144:159]\n\t" \
  "v_cvt_pk_bf16_f32 v119, v62, v63\n\t" \
  "s_nop 0\n\t" \
  "s_nop 0\n\t" \
  "v_mfma_f32_32x32x16_bf16 a[144:159], v[100:103], v[116:119], a[144:159]\n\t" \
  "v_smfmac_f32_16x16x64_bf16 a[160:163], v[84:87], v[112:119], v154\n\t" \
  "s_waitcnt vmcnt(6)\n\t" \
  "s_waitcnt lgkmcnt(0)\n\t" \
  "v_accvgpr_read_b32 v0, a128\n\t" \
  "v_accvgpr_read_b32 v1, a129\n\t" \
  "v_accvgpr_read_b32 v2, a130\n\t" \
  "v_accvgpr_read_b32 v3, a131\n\t" \
  "v_accvgpr_read_b32 v4, a132\n\t" \
  "v_accvgpr_read_b32 v5, a133\n\t" \
  "v_accvgpr_read_b32 v6, a134\n\t" \
  "v_accvgpr_read_b32 v7, a135\n\t" \
  "v_accvgpr_read_b32 v8, a136\n\t" \
  "v_accvgpr_read_b32 v9, a137\n\t" \
  "v_accvgpr_read_b32 v10, a138\n\t" \
  "v_accvgpr_read_b32 v11, a139\n\t" \
  "v_accvgpr_read_b32 v12, a140\n\t" \
  "v_accvgpr_read_b32 v13, a141\n\t" \
  "v_accvgpr_read_b32 v14, a142\n\t" \
  "v_accvgpr_read_b32 v15, a143\n\t" \
  "v_accvgpr_read_b32 v32, a160\n\t" \
  "v_accvgpr_read_b32 v33, a161\n\t" \
  "v_accvgpr_read_b32 v34, a162\n\t" \
  "v_accvgpr_read_b32 v35, a163\n\t" \
  "v_accvgpr_read_b32 v16, a144\n\t" \
  "v_accvgpr_read_b32 v17, a145\n\t" \
  "v_accvgpr_read_b32 v18, a146\n\t" \
  "v_accvgpr_read_b32 v19, a147\n\t" \
  "v_accvgpr_read_b32 v20, a148\n\t" \
  "v_accvgpr_read_b32 v21, a149\n\t" \
  "v_accvgpr_read_b32 v22, a150\n\t" \
  "v_accvgpr_read_b32 v23, a151\n\t" \
  "v_accvgpr_read_b32 v24, a152\n\t" \
  "v_accvgpr_read_b32 v25, a153\n\t" \
  "v_accvgpr_read_b32 v26, a154\n\t" \
  "v_accvgpr_read_b32 v27, a155\n\t" \
  "v_accvgpr_read_b32 v28, a156\n\t" \
  "v_accvgpr_read_b32 v29, a157\n\t" \
  "v_accvgpr_read_b32 v30, a158\n\t" \
  "v_accvgpr_read_b32 v31, a159\n\t"
#define TAIL_ASM \
  "s_waitcnt lgkmcnt(0)\n\t" \
  "v_xor_b32 v70, 64, v146\n\t" \
  "v_xor_b32 v71, 96, v146\n\t" \
  "ds_read_b64_tr_b16 v[104:105], v146 offset:0\n\t" \
  "ds_read_b64_tr_b16 v[106:107], v146 offset:512\n\t" \
  "ds_read_b64_tr_b16 v[108:109], v146 offset:4096\n\t" \
  "ds_read_b64_tr_b16 v[110:111], v146 offset:4608\n\t" \
  "ds_read_b64_tr_b16 v[112:113], v147 offset:0\n\t" \
  "ds_read_b64_tr_b16 v[114:115], v147 offset:512\n\t" \
  "ds_read_b64_tr_b16 v[116:117], v147 offset:4096\n\t" \
  "ds_read_b64_tr_b16 v[118:119], v147 offset:4608\n\t" \
  "ds_read_b64_tr_b16 v[120:121], v70 offset:0\n\t" \
  "ds_read_b64_tr_b16 v[122:123], v70 offset:512\n\t" \
  "ds_read_b64_tr_b16 v[124:125], v70 offset:4096\n\t" \
  "ds_read_b64_tr_b16 v[126:127], v70 offset:4608\n\t" \
  "ds_read_b64_tr_b16 v[128:129], v71 offset:0\n\t" \
  "ds_read_b64_tr_b16 v[130:131], v71 offset:512\n\t" \
  "ds_read_b64_tr_b16 v[132:133], v71 offset:4096\n\t" \
  "ds_read_b64_tr_b16 v[134:135], v71 offset:4608\n\t" \
  "s_barrier\n\t" \
  "s_waitcnt lgkmcnt(12)\n\t" \
  "v_mfma_f32_16x16x32_bf16 a[0:3], v[104:107], v[160:163], a[0:3]\n\t" \
  "s_lshl_b32 s71, s75, 12\n\t" \
  "v_mfma_f32_16x16x32_bf16 a[0:3], v[108:111], v[164:167], a[0:3]\n\t" \
  "s_add_u32 s71, s71, s74\n\t" \
  "s_waitcnt lgkmcnt(8)\n\t" \
  "v_mfma_f32_16x16x32_bf16 a[8:11], v[112:115], v[160:163], a[8:11]\n\t" \
  "s_add_u32 m0, s71, 110592\n\t" \
  "v_mfma_f32_16x16x32_bf16 a[8:11], v[116:119], v[164:167], a[8:11]\n\t" \
  "s_mov_b32 s70, 1310720\n\t" \
  "s_waitcnt lgkmcnt(4)\n\t" \
  "v_mfma_f32_16x16x32_bf16 a[16:19], v[120:123], v[160:163], a[16:19]\n\t" \
  "buffer_load_dwordx4 v140, s[92:95], s70 offen offset:0 lds\n\t" \
  "v_mfma_f32_16x16x32_bf16 a[16:19], v[124:127], v[164:167], a[16:19]\n\t" \
  "buffer_load_dwordx4 v141, s[92:95], s70 offen offset:1024 lds\n\t" \
  "s_waitcnt lgkmcnt(0)\n\t" \
  "v_mfma_f32_16x16x32_bf16 a[24:27], v[128:131], v[160:163], a[24:27]\n\t" \
  "buffer_load_dwordx4 v142, s[92:95], s70 offen offset:2048 lds\n\t" \
  "v_mfma_f32_16x16x32_bf16 a[24:27], v[132:135], v[164:167], a[24:27]\n\t" \
  "buffer_load_dwordx4 v143, s[92:95], s70 offen offset:3072 lds\n\t" \
  "v_mfma_f32_16x16x32_bf16 a[4:7], v[104:107], v[168:171], a[4:7]\n\t" \
  "v_and_b32 v255, 0xffff0000, v239\n\t" \
  "v_lshlrev_b32 v254, 16, v239\n\t" \
  "v_mfma_f32_16x16x32_bf16 a[4:7], v[108:111], v[172:175], a[4:7]\n\t" \
  "v_and_b32 v253, 0xffff0000, v238\n\t" \
  "v_lshlrev_b32 v252, 16, v238\n\t" \
  "v_mfma_f32_16x16x32_bf16 a[12:15], v[112:115], v[168:171], a[12:15]\n\t" \
  "v_and_b32 v251, 0xffff0000, v237\n\t" \
  "v_lshlrev_b32 v250, 16, v237\n\t" \
  "v_mfma_f32_16x16x32_bf16 a[12:15], v[116:119], v[172:175], a[12:15]\n\t" \
  "v_and_b32 v249, 0xffff0000, v236\n\t" \
  "v_lshlrev_b32 v248, 16, v236\n\t" \
  "v_mfma_f32_16x16x32_bf16 a[20:23], v[120:123], v[168:171], a[20:23]\n\t" \
  "v_and_b32 v247, 0xffff0000, v235\n\t" \
  "v_lshlrev_b32 v246, 16, v235\n\t" \
  "v_mfma_f32_16x16x32_bf16 a[20:23], v[124:127], v[172:175], a[20:23]\n\t" \
  "v_and_b32 v245, 0xffff0000, v234\n\t" \
  "v_lshlrev_b32 v244, 16, v234\n\t" \
  "v_mfma_f32_16x16x32_bf16 a[28:31], v[128:131], v[168:171], a[28:31]\n\t" \
  "v_and_b32 v243, 0xffff0000, v233\n\t" \
  "v_lshlrev_b32 v242, 16, v233\n\t" \
  "v_mfma_f32_16x16x32_bf16 a[28:31], v[132:135], v[172:175], a[28:31]\n\t" \
  "v_and_b32 v241, 0xffff0000, v232\n\t" \
  "v_lshlrev_b32 v240, 16, v232\n\t" \
  "v_mfma_f32_16x16x32_bf16 a[32:35], v[104:107], v[176:179], a[32:35]\n\t" \
  "v_and_b32 v239, 0xffff0000, v231\n\t" \
  "v_lshlrev_b32 v238, 16, v231\n\t" \
  "v_mfma_f32_16x16x32_bf16 a[32:35], v[108:111], v[180:183], a[32:35]\n\t" \
  "v_and_b32 v237, 0xffff0000, v230\n\t" \
  "v_lshlrev_b32 v236, 16, v230\n\t" \
  "v_mfma_f32_16x16x32_bf16 a[40:43], v[112:115], v[176:179], a[40:43]\n\t" \
  "v_and_b32 v235, 0xffff0000, v229\n\t" \
  "v_lshlrev_b32 v234, 16, v229\n\t" \
  "v_mfma_f32_16x16x32_bf16 a[40:43], v[116:119], v[180:183], a[40:43]\n\t" \
  "v_and_b32 v233, 0xffff0000, v228\n\t" \
  "v_lshlrev_b32 v232, 16, v228\n\t" \
  "v_mfma_f32_16x16x32_bf16 a[48:51], v[120:123], v[176:179], a[48:51]\n\t" \
  "v_and_b32 v231, 0xffff0000, v227\n\t" \
  "v_lshlrev_b32 v230, 16, v227\n\t" \
  "v_mfma_f32_16x16x32_bf16 a[48:51], v[124:127], v[180:183], a[48:51]\n\t" \
  "v_and_b32 v229, 0xffff0000, v226\n\t" \
  "v_lshlrev_b32 v228, 16, v226\n\t" \
  "v_mfma_f32_16x16x32_bf16 a[56:59], v[128:131], v[176:179], a[56:59]\n\t" \
  "v_and_b32 v227, 0xffff0000, v225\n\t" \
  "v_lshlrev_b32 v226, 16, v225\n\t" \
  "v_mfma_f32_16x16x32_bf16 a[56:59], v[132:135], v[180:183], a[56:59]\n\t" \
  "v_and_b32 v225, 0xffff0000, v224\n\t" \
  "v_lshlrev_b32 v224, 16, v224\n\t" \
  "v_mfma_f32_16x16x32_bf16 a[36:39], v[104:107], v[184:187], a[36:39]\n\t" \
  "v_mfma_f32_16x16x32_bf16 a[36:39], v[108:111], v[188:191], a[36:39]\n\t" \
  "v_mfma_f32_16x16x32_bf16 a[44:47], v[112:115], v[184:187], a[44:47]\n\t" \
  "v_mfma_f32_16x16x32_bf16 a[44:47], v[116:119], v[188:191], a[44:47]\n\t" \
  "v_mfma_f32_16x16x32_bf16 a[52:55], v[120:123], v[184:187], a[52:55]\n\t" \
  "v_mfma_f32_16x16x32_bf16 a[52:55], v[124:127], v[188:191], a[52:55]\n\t" \
  "v_mfma_f32_16x16x32_bf16 a[60:63], v[128:131], v[184:187], a[60:63]\n\t" \
  "v_mfma_f32_16x16x32_bf16 a[60:63], v[132:135], v[188:191], a[60:63]\n\t" \
  "s_waitcnt vmcnt(8)\n\t" \
  "v_mfma_f32_16x16x32_bf16 a[64:67], v[104:107], v[192:195], a[64:67]\n\t" \
  "ds_read_b64_tr_b16 v[72:73], v146 offset:8192\n\t" \
  "v_mfma_f32_16x16x32_bf16 a[64:67], v[108:111], v[196:199], a[64:67]\n\t" \
  "ds_read_b64_tr_b16 v[74:75], v146 offset:8704\n\t" \
  "v_mfma_f32_16x16x32_bf16 a[72:75], v[112:115], v[192:195], a[72:75]\n\t" \
  "ds_read_b64_tr_b16 v[76:77], v146 offset:12288\n\t" \
  "v_mfma_f32_16x16x32_bf16 a[72:75], v[116:119], v[196:199], a[72:75]\n\t" \
  "ds_read_b64_tr_b16 v[78:79], v146 offset:12800\n\t" \
  "v_mfma_f32_16x16x32_bf16 a[80:83], v[120:123], v[192:195], a[80:83]\n\t" \
  "ds_read_b64_tr_b16 v[80:81], v147 offset:8192\n\t" \
  "v_mfma_f32_16x16x32_bf16 a[80:83], v[124:127], v[196:199], a[80:83]\n\t" \
  "ds_read_b64_tr_b16 v[82:83], v147 offset:8704\n\t" \
  "v_mfma_f32_16x16x32_bf16 a[88:91], v[128:131], v[192:195], a[88:91]\n\t" \
  "ds_read_b64_tr_b16 v[84:85], v147 offset:12288\n\t" \
  "v_mfma_f32_16x16x32_bf16 a[88:91], v[132:135], v[196:199], a[88:91]\n\t" \
  "ds_read_b64_tr_b16 v[86:87], v147 offset:12800\n\t" \
  "v_mfma_f32_16x16x32_bf16 a[68:71], v[104:107], v[200:203], a[68:71]\n\t" \
  "ds_read_b64_tr_b16 v[88:89], v70 offset:8192\n\t" \
  "v_mfma_f32_16x16x32_bf16 a[68:71], v[108:111], v[204:207], a[68:71]\n\t" \
  "ds_read_b64_tr_b16 v[90:91], v70 offset:8704\n\t" \
  "v_mfma_f32_16x16x32_bf16 a[76:79], v[112:115], v[200:203], a[76:79]\n\t" \
  "ds_read_b64_tr_b16 v[92:93], v70 offset:12288\n\t" \
  "v_mfma_f32_16x16x32_bf16 a[76:79], v[116:119], v[204:207], a[76:79]\n\t" \
  "ds_read_b64_tr_b16 v[94:95], v70 offset:12800\n\t" \
  "v_mfma_f32_16x16x32_bf16 a[84:87], v[120:123], v[200:203], a[84:87]\n\t" \
  "ds_read_b64_tr_b16 v[96:97], v71 offset:8192\n\t" \
  "v_mfma_f32_16x16x32_bf16 a[84:87], v[124:127], v[204:207], a[84:87]\n\t" \
  "ds_read_b64_tr_b16 v[98:99], v71 offset:8704\n\t" \
  "v_mfma_f32_16x16x32_bf16 a[92:95], v[128:131], v[200:203], a[92:95]\n\t" \
  "ds_read_b64_tr_b16 v[100:101], v71 offset:12288\n\t" \
  "v_mfma_f32_16x16x32_bf16 a[92:95], v[132:135], v[204:207], a[92:95]\n\t" \
  "ds_read_b64_tr_b16 v[102:103], v71 offset:12800\n\t" \
  "v_mfma_f32_16x16x32_bf16 a[96:99], v[104:107], v[208:211], a[96:99]\n\t" \
  "v_mfma_f32_16x16x32_bf16 a[96:99], v[108:111], v[212:215], a[96:99]\n\t" \
  "v_mfma_f32_16x16x32_bf16 a[104:107], v[112:115], v[208:211], a[104:107]\n\t" \
  "v_mfma_f32_16x16x32_bf16 a[104:107], v[116:119], v[212:215], a[104:107]\n\t" \
  "v_mfma_f32_16x16x32_bf16 a[112:115], v[120:123], v[208:211], a[112:115]\n\t" \
  "v_mfma_f32_16x16x32_bf16 a[112:115], v[124:127], v[212:215], a[112:115]\n\t" \
  "v_mfma_f32_16x16x32_bf16 a[120:123], v[128:131], v[208:211], a[120:123]\n\t" \
  "v_mfma_f32_16x16x32_bf16 a[120:123], v[132:135], v[212:215], a[120:123]\n\t" \
  "v_mfma_f32_16x16x32_bf16 a[100:103], v[104:107], v[216:219], a[100:103]\n\t" \
  "v_mfma_f32_16x16x32_bf16 a[100:103], v[108:111], v[220:223], a[100:103]\n\t" \
  "v_mfma_f32_16x16x32_bf16 a[108:111], v[112:115], v[216:219], a[108:111]\n\t" \
  "v_mfma_f32_16x16x32_bf16 a[108:111], v[116:119], v[220:223], a[108:111]\n\t" \
  "v_mfma_f32_16x16x32_bf16 a[116:119], v[120:123], v[216:219], a[116:119]\n\t" \
  "v_mfma_f32_16x16x32_bf16 a[116:119], v[124:127], v[220:223], a[116:119]\n\t" \
  "v_mfma_f32_16x16x32_bf16 a[124:127], v[128:131], v[216:219], a[124:127]\n\t" \
  "v_mfma_f32_16x16x32_bf16 a[124:127], v[132:135], v[220:223], a[124:127]\n\t" \
  "s_waitcnt vmcnt(0)\n\t" \
  "s_barrier\n\t" \
  "s_cmp_lg_u32 s75, 0\n\t" \
  "s_cbranch_scc1 LPUB%=\n\t" \
  "s_mov_b64 exec, 1\n\t" \
  "v_mov_b32 v136, 0\n\t" \
  "v_mov_b32 v137, 1\n\t" \
  "global_atomic_add v136, v137, s[76:77]\n\t" \
  "s_mov_b64 exec, -1\n\t" \
  "LPUB%=:\n\t" \
  "v_mov_b32 v137, 0\n\t" \
  "s_waitcnt lgkmcnt(12)\n\t" \
  "v_mfma_f32_16x16x32_bf16 a[32:35], v[72:75], a[192:195], a[32:35]\n\t" \
  "buffer_load_dwordx4 v[160:163], v155, s[52:55], 0 offen offset:0\n\t" \
  "v_mfma_f32_16x16x32_bf16 a[32:35], v[76:79], a[196:199], a[32:35]\n\t" \
  "buffer_load_dwordx4 v[164:167], v155, s[52:55], 0 offen offset:32\n\t" \
  "s_waitcnt lgkmcnt(8)\n\t" \
  "v_mfma_f32_16x16x32_bf16 a[40:43], v[80:83], a[192:195], a[40:43]\n\t" \
  "buffer_load_dwordx4 v[168:171], v155, s[52:55], 0 offen offset:64\n\t" \
  "v_mfma_f32_16x16x32_bf16 a[40:43], v[84:87], a[196:199], a[40:43]\n\t" \
  "buffer_load_dwordx4 v[172:175], v155, s[52:55], 0 offen offset:96\n\t" \
  "s_waitcnt lgkmcnt(4)\n\t" \
  "v_mfma_f32_16x16x32_bf16 a[48:51], v[88:91], a[192:195], a[48:51]\n\t" \
  "v_mfma_f32_16x16x32_bf16 a[48:51], v[92:95], a[196:199], a[48:51]\n\t" \
  "s_waitcnt lgkmcnt(0)\n\t" \
  "v_mfma_f32_16x16x32_bf16 a[56:59], v[96:99], a[192:195], a[56:59]\n\t" \
  "v_mfma_f32_16x16x32_bf16 a[56:59], v[100:103], a[196:199], a[56:59]\n\t" \
  "v_mfma_f32_16x16x32_bf16 a[36:39], v[72:75], a[200:203], a[36:39]\n\t" \
  "v_mfma_f32_16x16x32_bf16 a[36:39], v[76:79], a[204:207], a[36:39]\n\t" \
  "v_mfma_f32_16x16x32_bf16 a[44:47], v[80:83], a[200:203], a[44:47]\n\t" \
  "v_mfma_f32_16x16x32_bf16 a[44:47], v[84:87], a[204:207], a[44:47]\n\t" \
  "v_mfma_f32_16x16x32_bf16 a[52:55], v[88:91], a[200:203], a[52:55]\n\t" \
  "v_mfma_f32_16x16x32_bf16 a[52:55], v[92:95], a[204:207], a[52:55]\n\t" \
  "v_mfma_f32_16x16x32_bf16 a[60:63], v[96:99], a[200:203], a[60:63]\n\t" \
  "v_mfma_f32_16x16x32_bf16 a[60:63], v[100:103], a[204:207], a[60:63]\n\t" \
  "v_mfma_f32_16x16x32_bf16 a[96:99], v[72:75], a[240:243], a[96:99]\n\t" \
  "global_load_dword v136, v137, s[78:79] sc1\n\t" \
  "v_mfma_f32_16x16x32_bf16 a[96:99], v[76:79], a[244:247], a[96:99]\n\t" \
  "v_mfma_f32_16x16x32_bf16 a[104:107], v[80:83], a[240:243], a[104:107]\n\t" \
  "v_mfma_f32_16x16x32_bf16 a[104:107], v[84:87], a[244:247], a[104:107]\n\t" \
  "v_mfma_f32_16x16x32_bf16 a[112:115], v[88:91], a[240:243], a[112:115]\n\t" \
  "v_mfma_f32_16x16x32_bf16 a[112:115], v[92:95], a[244:247], a[112:115]\n\t" \
  "v_mfma_f32_16x16x32_bf16 a[120:123], v[96:99], a[240:243], a[120:123]\n\t" \
  "v_mfma_f32_16x16x32_bf16 a[120:123], v[100:103], a[244:247], a[120:123]\n\t" \
  "v_mfma_f32_16x16x32_bf16 a[100:103], v[72:75], a[248:251], a[100:103]\n\t" \
  "v_mfma_f32_16x16x32_bf16 a[100:103], v[76:79], a[252:255], a[100:103]\n\t" \
  "v_mfma_f32_16x16x32_bf16 a[108:111], v[80:83], a[248:251], a[108:111]\n\t" \
  "v_mfma_f32_16x16x32_bf16 a[108:111], v[84:87], a[252:255], a[108:111]\n\t" \
  "v_mfma_f32_16x16x32_bf16 a[116:119], v[88:91], a[248:251], a[116:119]\n\t" \
  "v_mfma_f32_16x16x32_bf16 a[116:119], v[92:95], a[252:255], a[116:119]\n\t" \
  "v_mfma_f32_16x16x32_bf16 a[124:127], v[96:99], a[248:251], a[124:127]\n\t" \
  "v_mfma_f32_16x16x32_bf16 a[124:127], v[100:103], a[252:255], a[124:127]\n\t" \
  "s_nop 7\n\t" \
  "v_mfma_f32_16x16x32_bf16 a[64:67], v[72:75], a[208:211], a[64:67]\n\t" \
  "ds_write_b128 v152, a[32:35] offset:0\n\t" \
  "v_mfma_f32_16x16x32_bf16 a[64:67], v[76:79], a[212:215], a[64:67]\n\t" \
  "ds_write_b128 v152, a[36:39] offset:1024\n\t" \
  "v_mfma_f32_16x16x32_bf16 a[72:75], v[80:83], a[208:211], a[72:75]\n\t" \
  "ds_write_b128 v152, a[40:43] offset:2048\n\t" \
  "v_mfma_f32_16x16x32_bf16 a[72:75], v[84:87], a[212:215], a[72:75]\n\t" \
  "ds_write_b128 v152, a[44:47] offset:3072\n\t" \
  "v_mfma_f32_16x16x32_bf16 a[80:83], v[88:91], a[208:211], a[80:83]\n\t" \
  "ds_write_b128 v152, a[48:51] offset:4096\n\t" \
  "v_mfma_f32_16x16x32_bf16 a[80:83], v[92:95], a[212:215], a[80:83]\n\t" \
  "ds_write_b128 v152, a[52:55] offset:5120\n\t" \
  "v_mfma_f32_16x16x32_bf16 a[88:91], v[96:99], a[208:211], a[88:91]\n\t" \
  "ds_write_b128 v152, a[56:59] offset:6144\n\t" \
  "v_mfma_f32_16x16x32_bf16 a[88:91], v[100:103], a[212:215], a[88:91]\n\t" \
  "ds_write_b128 v152, a[60:63] offset:7168\n\t" \
  "v_mfma_f32_16x16x32_bf16 a[68:71], v[72:75], a[216:219], a[68:71]\n\t" \
  "ds_write_b128 v152, a[96:99] offset:8192\n\t" \
  "v_mfma_f32_16x16x32_bf16 a[68:71], v[76:79], a[220:223], a[68:71]\n\t" \
  "ds_write_b128 v152, a[100:103] offset:9216\n\t" \
  "v_mfma_f32_16x16x32_bf16 a[76:79], v[80:83], a[216:219], a[76:79]\n\t" \
  "ds_write_b128 v152, a[104:107] offset:10240\n\t" \
  "v_mfma_f32_16x16x32_bf16 a[76:79], v[84:87], a[220:223], a[76:79]\n\t" \
  "ds_write_b128 v152, a[108:111] offset:11264\n\t" \
  "v_mfma_f32_16x16x32_bf16 a[84:87], v[88:91], a[216:219], a[84:87]\n\t" \
  "ds_write_b128 v152, a[112:115] offset:12288\n\t" \
  "v_mfma_f32_16x16x32_bf16 a[84:87], v[92:95], a[220:223], a[84:87]\n\t" \
  "ds_write_b128 v152, a[116:119] offset:13312\n\t" \
  "v_mfma_f32_16x16x32_bf16 a[92:95], v[96:99], a[216:219], a[92:95]\n\t" \
  "ds_write_b128 v152, a[120:123] offset:14336\n\t" \
  "v_mfma_f32_16x16x32_bf16 a[92:95], v[100:103], a[220:223], a[92:95]\n\t" \
  "ds_write_b128 v152, a[124:127] offset:15360\n\t" \
  "s_waitcnt vmcnt(0)\n\t" \
  "v_readfirstlane_b32 s71, v136\n\t" \
  "s_mov_b32 s63, 0x40000\n\t" \
  "LPOLL%=:\n\t" \
  "s_cmp_ge_u32 s71, 32\n\t" \
  "s_cbranch_scc1 LPOK%=\n\t" \
  "global_load_dword v136, v137, s[78:79] sc1\n\t" \
  "s_waitcnt vmcnt(0)\n\t" \
  "v_readfirstlane_b32 s71, v136\n\t" \
  "s_sub_u32 s63, s63, 1\n\t" \
  "s_cbranch_scc0 LPOLL%=\n\t" \
  "LPOK%=:\n\t" \
  "v_mfma_f32_16x16x32_bf16 a[0:3], v[72:75], a[176:179], a[0:3]\n\t" \
  "s_mov_b32 s69, 0\n\t" \
  "buffer_load_dwordx4 a[128:131], v144, s[88:91], s69 offen offset:0 sc1\n\t" \
  "v_mfma_f32_16x16x32_bf16 a[0:3], v[76:79], a[180:183], a[0:3]\n\t" \
  "buffer_load_dwordx4 a[132:135], v144, s[88:91], s69 offen offset:1024 sc1\n\t" \
  "v_mfma_f32_16x16x32_bf16 a[8:11], v[80:83], a[176:179], a[8:11]\n\t" \
  "buffer_load_dwordx4 a[136:139], v144, s[88:91], s69 offen offset:2048 sc1\n\t" \
  "v_mfma_f32_16x16x32_bf16 a[8:11], v[84:87], a[180:183], a[8:11]\n\t" \
  "buffer_load_dwordx4 a[140:143], v144, s[88:91], s69 offen offset:3072 sc1\n\t" \
  "v_mfma_f32_16x16x32_bf16 a[16:19], v[88:91], a[176:179], a[16:19]\n\t" \
  "s_mov_b32 s69, 4096\n\t" \
  "buffer_load_dwordx4 a[144:147], v144, s[88:91], s69 offen offset:0 sc1\n\t" \
  "v_mfma_f32_16x16x32_bf16 a[16:19], v[92:95], a[180:183], a[16:19]\n\t" \
  "buffer_load_dwordx4 a[148:151], v144, s[88:91], s69 offen offset:1024 sc1\n\t" \
  "v_mfma_f32_16x16x32_bf16 a[24:27], v[96:99], a[176:179], a[24:27]\n\t" \
  "buffer_load_dwordx4 a[152:155], v144, s[88:91], s69 offen offset:2048 sc1\n\t" \
  "v_mfma_f32_16x16x32_bf16 a[24:27], v[100:103], a[180:183], a[24:27]\n\t" \
  "buffer_load_dwordx4 a[156:159], v144, s[88:91], s69 offen offset:3072 sc1\n\t" \
  "v_mfma_f32_16x16x32_bf16 a[4:7], v[72:75], a[184:187], a[4:7]\n\t" \
  "s_mov_b32 s69, 8192\n\t" \
  "buffer_load_dwordx4 a[160:163], v144, s[88:91], s69 offen offset:0 sc1\n\t" \
  "v_mfma_f32_16x16x32_bf16 a[4:7], v[76:79], a[188:191], a[4:7]\n\t" \
  "buffer_load_dwordx4 a[164:167], v144, s[88:91], s69 offen offset:1024 sc1\n\t" \
  "v_mfma_f32_16x16x32_bf16 a[12:15], v[80:83], a[184:187], a[12:15]\n\t" \
  "buffer_load_dwordx4 a[168:171], v144, s[88:91], s69 offen offset:2048 sc1\n\t" \
  "v_mfma_f32_16x16x32_bf16 a[12:15], v[84:87], a[188:191], a[12:15]\n\t" \
  "buffer_load_dwordx4 a[172:175], v144, s[88:91], s69 offen offset:3072 sc1\n\t" \
  "v_mfma_f32_16x16x32_bf16 a[20:23], v[88:91], a[184:187], a[20:23]\n\t" \
  "v_mfma_f32_16x16x32_bf16 a[20:23], v[92:95], a[188:191], a[20:23]\n\t" \
  "v_mfma_f32_16x16x32_bf16 a[28:31], v[96:99], a[184:187], a[28:31]\n\t" \
  "v_mfma_f32_16x16x32_bf16 a[28:31], v[100:103], a[188:191], a[28:31]\n\t" \
  "ds_read_b64_tr_b16 v[32:33], v64 offset:32768\n\t" \
  "ds_read_b64_tr_b16 v[34:35], v64 offset:33792\n\t" \
  "ds_read_b64_tr_b16 v[36:37], v67 offset:32768\n\t" \
  "ds_read_b64_tr_b16 v[38:39], v67 offset:33792\n\t" \
  "ds_read_b64_tr_b16 v[40:41], v64 offset:34816\n\t" \
  "ds_read_b64_tr_b16 v[42:43], v64 offset:35840\n\t" \
  "ds_read_b64_tr_b16 v[44:45], v67 offset:34816\n\t" \
  "ds_read_b64_tr_b16 v[46:47], v67 offset:35840\n\t" \
  "ds_read_b64_tr_b16 v[48:49], v64 offset:36864\n\t" \
  "ds_read_b64_tr_b16 v[50:51], v64 offset:37888\n\t" \
  "ds_read_b64_tr_b16 v[52:53], v67 offset:36864\n\t" \
  "ds_read_b64_tr_b16 v[54:55], v67 offset:37888\n\t" \
  "s_waitcnt vmcnt(11) lgkmcnt(8)\n\t" \
  "v_mfma_f32_32x32x16_bf16 v[0:15], v[32:35], a[128:131], 0\n\t" \
  "ds_read_b64_tr_b16 v[56:57], v64 offset:38912\n\t" \
  "ds_read_b64_tr_b16 v[58:59], v64 offset:39936\n\t" \
  "s_mov_b32 s69, 12288\n\t" \
  "v_mfma_f32_32x32x16_bf16 v[16:31], v[36:39], a[128:131], 0\n\t" \
  "buffer_load_dwordx4 a[176:179], v144, s[88:91], s69 offen offset:0 sc1\n\t" \
  "ds_read_b64_tr_b16 v[60:61], v67 offset:38912\n\t" \
  "ds_read_b64_tr_b16 v[62:63], v67 offset:39936\n\t" \
  "s_barrier\n\t" \
  "s_waitcnt vmcnt(11) lgkmcnt(8)\n\t" \
  "v_mfma_f32_32x32x16_bf16 v[0:15], v[40:43], a[132:135], v[0:15]\n\t" \
  "ds_read_b64_tr_b16 v[32:33], v64 offset:40960\n\t" \
  "ds_read_b64_tr_b16 v[34:35], v64 offset:41984\n\t" \
  "ds_read_b128 v[104:107], v153 offset:8192\n\t" \
  "v_accvgpr_read_b32 v192, a64\n\t" \
  "v_accvgpr_read_b32 v193, a65\n\t" \
  "v_accvgpr_read_b32 v194, a66\n\t" \
  "v_mfma_f32_32x32x16_bf16 v[16:31], v[44:47], a[132:135], v[16:31]\n\t" \
  "buffer_load_dwordx4 a[180:183], v144, s[88:91], s69 offen offset:1024 sc1\n\t" \
  "ds_read_b64_tr_b16 v[36:37], v67 offset:40960\n\t" \
  "ds_read_b64_tr_b16 v[38:39], v67 offset:41984\n\t" \
  "ds_read_b128 v[108:111], v153 offset:9216\n\t" \
  "v_accvgpr_read_b32 v195, a67\n\t" \
  "v_accvgpr_read_b32 v196, a68\n\t" \
  "v_accvgpr_read_b32 v197, a69\n\t" \
  "s_waitcnt vmcnt(11) lgkmcnt(10)\n\t" \
  "v_mfma_f32_32x32x16_bf16 v[0:15], v[48:51], a[136:139], v[0:15]\n\t" \
  "ds_read_b64_tr_b16 v[40:41], v64 offset:43008\n\t" \
  "ds_read_b64_tr_b16 v[42:43], v64 offset:44032\n\t" \
  "ds_read_b128 v[112:115], v153 offset:10240\n\t" \
  "v_accvgpr_read_b32 v198, a70\n\t" \
  "v_accvgpr_read_b32 v199, a71\n\t" \
  "v_accvgpr_read_b32 v200, a72\n\t" \
  "v_mfma_f32_32x32x16_bf16 v[16:31], v[52:55], a[136:139], v[16:31]\n\t" \
  "buffer_load_dwordx4 a[184:187], v144, s[88:91], s69 offen offset:2048 sc1\n\t" \
  "ds_read_b64_tr_b16 v[44:45], v67 offset:43008\n\t" \
  "ds_read_b64_tr_b16 v[46:47], v67 offset:44032\n\t" \
  "ds_read_b128 v[116:119], v153 offset:11264\n\t" \
  "v_accvgpr_read_b32 v201, a73\n\t" \
  "v_accvgpr_read_b32 v202, a74\n\t" \
  "v_accvgpr_read_b32 v203, a75\n\t" \
  "s_waitcnt vmcnt(11) lgkmcnt(12)\n\t" \
  "v_mfma_f32_32x32x16_bf16 v[0:15], v[56:59], a[140:143], v[0:15]\n\t" \
  "ds_read_b64_tr_b16 v[48:49], v64 offset:45056\n\t" \
  "ds_read_b64_tr_b16 v[50:51], v64 offset:46080\n\t" \
  "ds_read_b128 v[120:123], v153 offset:12288\n\t" \
  "v_accvgpr_read_b32 v204, a76\n\t" \
  "v_accvgpr_read_b32 v205, a77\n\t" \
  "v_accvgpr_read_b32 v206, a78\n\t" \
  "v_mfma_f32_32x32x16_bf16 v[16:31], v[60:63], a[140:143], v[16:31]\n\t" \
  "buffer_load_dwordx4 a[188:191], v144, s[88:91], s69 offen offset:3072 sc1\n\t" \
  "ds_read_b64_tr_b16 v[52:53], v67 offset:45056\n\t" \
  "ds_read_b64_tr_b16 v[54:55], v67 offset:46080\n\t" \
  "ds_read_b128 v[124:127], v153 offset:13312\n\t" \
  "v_accvgpr_read_b32 v207, a79\n\t" \
  "v_accvgpr_read_b32 v208, a80\n\t" \
  "v_accvgpr_read_b32 v209, a81\n\t" \
  "s_waitcnt vmcnt(11) lgkmcnt(13)\n\t" \
  "v_mfma_f32_32x32x16_bf16 v[0:15], v[32:35], a[144:147], v[0:15]\n\t" \
  "ds_read_b64_tr_b16 v[56:57], v64 offset:47104\n\t" \
  "ds_read_b64_tr_b16 v[58:59], v64 offset:48128\n\t" \
  "ds_read_b128 v[128:131], v153 offset:14336\n\t" \
  "s_mov_b32 s69, 16384\n\t" \
  "v_accvgpr_read_b32 v210, a82\n\t" \
  "v_accvgpr_read_b32 v211, a83\n\t" \
  "v_accvgpr_read_b32 v212, a84\n\t" \
  "v_mfma_f32_32x32x16_bf16 v[16:31], v[36:39], a[144:147], v[16:31]\n\t" \
  "buffer_load_dwordx4 a[192:195], v144, s[88:91], s69 offen offset:0 sc1\n\t" \
  "ds_read_b64_tr_b16 v[60:61], v67 offset:47104\n\t" \
  "ds_read_b64_tr_b16 v[62:63], v67 offset:48128\n\t" \
  "ds_read_b128 v[132:135], v153 offset:15360\n\t" \
  "v_accvgpr_read_b32 v213, a85\n\t" \
  "v_accvgpr_read_b32 v214, a86\n\t" \
  "v_accvgpr_read_b32 v215, a87\n\t" \
  "s_waitcnt vmcnt(11) lgkmcnt(13)\n\t" \
  "v_mfma_f32_32x32x16_bf16 v[0:15], v[40:43], a[148:151], v[0:15]\n\t" \
  "ds_read_b64_tr_b16 v[32:33], v64 offset:49152\n\t" \
  "ds_read_b64_tr_b16 v[34:35], v64 offset:50176\n\t" \
  "ds_read_b128 v[72:75], v153 offset:0\n\t" \
  "v_accvgpr_read_b32 v216, a88\n\t" \
  "v_accvgpr_read_b32 v217, a89\n\t" \
  "v_accvgpr_read_b32 v218, a90\n\t" \
  "v_mfma_f32_32x32x16_bf16 v[16:31], v[44:47], a[148:151], v[16:31]\n\t" \
  "buffer_load_dwordx4 a[196:199], v144, s[88:91], s69 offen offset:1024 sc1\n\t" \
  "ds_read_b64_tr_b16 v[36:37], v67 offset:49152\n\t" \
  "ds_read_b64_tr_b16 v[38:39], v67 offset:50176\n\t" \
  "ds_read_b128 v[76:79], v153 offset:1024\n\t" \
  "v_accvgpr_read_b32 v219, a91\n\t" \
  "v_accvgpr_read_b32 v220, a92\n\t" \
  "v_accvgpr_read_b32 v221, a93\n\t" \
  "s_waitcnt vmcnt(11) lgkmcnt(13)\n\t" \
  "v_mfma_f32_32x32x16_bf16 v[0:15], v[48:51], a[152:155], v[0:15]\n\t" \
  "ds_read_b64_tr_b16 v[40:41], v64 offset:51200\n\t" \
  "ds_read_b64_tr_b16 v[42:43], v64 offset:52224\n\t" \
  "ds_read_b128 v[80:83], v153 offset:2048\n\t" \
  "v_accvgpr_read_b32 v222, a94\n\t" \
  "v_accvgpr_read_b32 v223, a95\n\t" \
  "v_mfma_f32_32x32x16_bf16 v[16:31], v[52:55], a[152:155], v[16:31]\n\t" \
  "buffer_load_dwordx4 a[200:203], v144, s[88:91], s69 offen offset:2048 sc1\n\t" \
  "ds_read_b64_tr_b16 v[44:45], v67 offset:51200\n\t" \
  "ds_read_b64_tr_b16 v[46:47], v67 offset:52224\n\t" \
  "ds_read_b128 v[84:87], v153 offset:3072\n\t" \
  "s_waitcnt vmcnt(11) lgkmcnt(13)\n\t" \
  "v_mfma_f32_32x32x16_bf16 v[0:15], v[56:59], a[156:159], v[0:15]\n\t" \
  "ds_read_b64_tr_b16 v[48:49], v64 offset:53248\n\t" \
  "ds_read_b64_tr_b16 v[50:51], v64 offset:54272\n\t" \
  "ds_read_b128 v[88:91], v153 offset:4096\n\t" \
  "v_mfma_f32_32x32x16_bf16 v[16:31], v[60:63], a[156:159], v[16:31]\n\t" \
  "buffer_load_dwordx4 a[204:207], v144, s[88:91], s69 offen offset:3072 sc1\n\t" \
  "ds_read_b64_tr_b16 v[52:53], v67 offset:53248\n\t" \
  "ds_read_b64_tr_b16 v[54:55], v67 offset:54272\n\t" \
  "ds_read_b128 v[92:95], v153 offset:5120\n\t" \
  "s_waitcnt vmcnt(11) lgkmcnt(13)\n\t" \
  "v_mfma_f32_32x32x16_bf16 v[0:15], v[32:35], a[160:163], v[0:15]\n\t" \
  "ds_read_b64_tr_b16 v[56:57], v64 offset:55296\n\t" \
  "ds_read_b64_tr_b16 v[58:59], v64 offset:56320\n\t" \
  "ds_read_b128 v[96:99], v153 offset:6144\n\t" \
  "s_mov_b32 s69, 20480\n\t" \
  "v_mfma_f32_32x32x16_bf16 v[16:31], v[36:39], a[160:163], v[16:31]\n\t" \
  "buffer_load_dwordx4 a[208:211], v144, s[88:91], s69 offen offset:0 sc1\n\t" \
  "ds_read_b64_tr_b16 v[60:61], v67 offset:55296\n\t" \
  "ds_read_b64_tr_b16 v[62:63], v67 offset:56320\n\t" \
  "ds_read_b128 v[100:103], v153 offset:7168\n\t" \
  "s_waitcnt vmcnt(11) lgkmcnt(13)\n\t" \
  "v_mfma_f32_32x32x16_bf16 v[0:15], v[40:43], a[164:167], v[0:15]\n\t" \
  "ds_read_b64_tr_b16 v[32:33], v64 offset:57344\n\t" \
  "ds_read_b64_tr_b16 v[34:35], v64 offset:58368\n\t" \
  "s_xor_b32 s71, s75, 1\n\t" \
  "s_waitcnt lgkmcnt(2)\n\t" \
  "v_add_f32 v104, v104, v192\n\t" \
  "v_add_f32 v105, v105, v193\n\t" \
  "v_mfma_f32_32x32x16_bf16 v[16:31], v[44:47], a[164:167], v[16:31]\n\t" \
  "buffer_load_dwordx4 a[212:215], v144, s[88:91], s69 offen offset:1024 sc1\n\t" \
  "ds_read_b64_tr_b16 v[36:37], v67 offset:57344\n\t" \
  "ds_read_b64_tr_b16 v[38:39], v67 offset:58368\n\t" \
  "s_lshl_b32 s71, s71, 1\n\t" \
  "v_add_f32 v106, v106, v194\n\t" \
  "v_add_f32 v107, v107, v195\n\t" \
  "s_waitcnt vmcnt(11)\n\t" \
  "v_mfma_f32_32x32x16_bf16 v[0:15], v[48:51], a[168:171], v[0:15]\n\t" \
  "ds_read_b64_tr_b16 v[40:41], v64 offset:59392\n\t" \
  "ds_read_b64_tr_b16 v[42:43], v64 offset:60416\n\t" \
  "ds_write_b128 v153, v[104:107] offset:0\n\t" \
  "v_add_f32 v108, v108, v196\n\t" \
  "v_add_f32 v109, v109, v197\n\t" \
  "v_add_f32 v110, v110, v198\n\t" \
  "v_mfma_f32_32x32x16_bf16 v[16:31], v[52:55], a[168:171], v[16:31]\n\t" \
  "buffer_load_dwordx4 a[216:219], v144, s[88:91], s69 offen offset:2048 sc1\n\t" \
  "ds_read_b64_tr_b16 v[44:45], v67 offset:59392\n\t" \
  "ds_read_b64_tr_b16 v[46:47], v67 offset:60416\n\t" \
  "s_sub_u32 s71, s71, s75\n\t" \
  "v_add_f32 v111, v111, v199\n\t" \
  "s_waitcnt vmcnt(11)\n\t" \
  "v_mfma_f32_32x32x16_bf16 v[0:15], v[56:59], a[172:175], v[0:15]\n\t" \
  "ds_read_b64_tr_b16 v[48:49], v64 offset:61440\n\t" \
  "ds_read_b64_tr_b16 v[50:51], v64 offset:62464\n\t" \
  "ds_write_b128 v153, v[108:111] offset:1024\n\t" \
  "v_add_f32 v112, v112, v200\n\t" \
  "v_add_f32 v113, v113, v201\n\t" \
  "v_add_f32 v114, v114, v202\n\t" \
  "v_mfma_f32_32x32x16_bf16 v[16:31], v[60:63], a[172:175], v[16:31]\n\t" \
  "buffer_load_dwordx4 a[220:223], v144, s[88:91], s69 offen offset:3072 sc1\n\t" \
  "ds_read_b64_tr_b16 v[52:53], v67 offset:61440\n\t" \
  "ds_read_b64_tr_b16 v[54:55], v67 offset:62464\n\t" \
  "s_add_u32 s71, s71, 24\n\t" \
  "v_add_f32 v115, v115, v203\n\t" \
  "s_waitcnt vmcnt(11) lgkmcnt(10)\n\t" \
  "v_mfma_f32_32x32x16_bf16 v[0:15], v[32:35], a[176:179], v[0:15]\n\t" \
  "ds_read_b64_tr_b16 v[56:57], v64 offset:63488\n\t" \
  "ds_read_b64_tr_b16 v[58:59], v64 offset:64512\n\t" \
  "ds_write_b128 v153, v[112:115] offset:2048\n\t" \
  "s_mov_b32 s69, 24576\n\t" \
  "v_add_f32 v116, v116, v204\n\t" \
  "v_add_f32 v117, v117, v205\n\t" \
  "v_add_f32 v118, v118, v206\n\t" \
  "v_mfma_f32_32x32x16_bf16 v[16:31], v[36:39], a[176:179], v[16:31]\n\t" \
  "buffer_load_dwordx4 a[224:227], v144, s[88:91], s69 offen offset:0 sc1\n\t" \
  "ds_read_b64_tr_b16 v[60:61], v67 offset:63488\n\t" \
  "ds_read_b64_tr_b16 v[62:63], v67 offset:64512\n\t" \
  "s_lshl_b32 s70, s71, 16\n\t" \
  "v_add_f32 v119, v119, v207\n\t" \
  "s_waitcnt vmcnt(11) lgkmcnt(10)\n\t" \
  "v_mfma_f32_32x32x16_bf16 v[0:15], v[40:43], a[180:183], v[0:15]\n\t" \
  "ds_read_b64_tr_b16 v[32:33], v65 offset:0\n\t" \
  "ds_read_b64_tr_b16 v[34:35], v65 offset:1024\n\t" \
  "ds_write_b128 v153, v[116:119] offset:3072\n\t" \
  "v_add_f32 v120, v120, v208\n\t" \
  "v_add_f32 v121, v121, v209\n\t" \
  "v_add_f32 v122, v122, v210\n\t" \
  "v_mfma_f32_32x32x16_bf16 v[16:31], v[44:47], a[180:183], v[16:31]\n\t" \
  "buffer_load_dwordx4 a[228:231], v144, s[88:91], s69 offen offset:1024 sc1\n\t" \
  "ds_read_b64_tr_b16 v[36:37], v68 offset:0\n\t" \
  "ds_read_b64_tr_b16 v[38:39], v68 offset:1024\n\t" \
  "s_xor_b32 s62, s72, 16384\n\t" \
  "v_add_f32 v123, v123, v211\n\t" \
  "s_waitcnt vmcnt(11) lgkmcnt(10)\n\t" \
  "v_mfma_f32_32x32x16_bf16 v[0:15], v[48:51], a[184:187], v[0:15]\n\t" \
  "ds_read_b64_tr_b16 v[40:41], v65 offset:2048\n\t" \
  "ds_read_b64_tr_b16 v[42:43], v65 offset:3072\n\t" \
  "ds_write_b128 v153, v[120:123] offset:4096\n\t" \
  "v_add_f32 v124, v124, v212\n\t" \
  "v_add_f32 v125, v125, v213\n\t" \
  "v_add_f32 v126, v126, v214\n\t" \
  "v_mfma_f32_32x32x16_bf16 v[16:31], v[52:55], a[184:187], v[16:31]\n\t" \
  "buffer_load_dwordx4 a[232:235], v144, s[88:91], s69 offen offset:2048 sc1\n\t" \
  "ds_read_b64_tr_b16 v[44:45], v68 offset:2048\n\t" \
  "ds_read_b64_tr_b16 v[46:47], v68 offset:3072\n\t" \
  "v_add_f32 v127, v127, v215\n\t" \
  "s_waitcnt vmcnt(11) lgkmcnt(10)\n\t" \
  "v_mfma_f32_32x32x16_bf16 v[0:15], v[56:59], a[188:191], v[0:15]\n\t" \
  "ds_read_b64_tr_b16 v[48:49], v65 offset:4096\n\t" \
  "ds_read_b64_tr_b16 v[50:51], v65 offset:5120\n\t" \
  "ds_write_b128 v153, v[124:127] offset:5120\n\t" \
  "v_add_f32 v128, v128, v216\n\t" \
  "v_add_f32 v129, v129, v217\n\t" \
  "v_add_f32 v130, v130, v218\n\t" \
  "v_mfma_f32_32x32x16_bf16 v[16:31], v[60:63], a[188:191], v[16:31]\n\t" \
  "buffer_load_dwordx4 a[236:239], v144, s[88:91], s69 offen offset:3072 sc1\n\t" \
  "ds_read_b64_tr_b16 v[52:53], v68 offset:4096\n\t" \
  "ds_read_b64_tr_b16 v[54:55], v68 offset:5120\n\t" \
  "v_add_f32 v131, v131, v219\n\t" \
  "s_waitcnt vmcnt(11) lgkmcnt(10)\n\t" \
  "v_mfma_f32_32x32x16_bf16 v[0:15], v[32:35], a[192:195], v[0:15]\n\t" \
  "ds_read_b64_tr_b16 v[56:57], v65 offset:6144\n\t" \
  "ds_read_b64_tr_b16 v[58:59], v65 offset:7168\n\t" \
  "ds_write_b128 v153, v[128:131] offset:6144\n\t" \
  "s_mov_b32 s69, 28672\n\t" \
  "v_add_f32 v132, v132, v220\n\t" \
  "v_add_f32 v133, v133, v221\n\t" \
  "v_add_f32 v134, v134, v222\n\t" \
  "v_mfma_f32_32x32x16_bf16 v[16:31], v[36:39], a[192:195], v[16:31]\n\t" \
  "buffer_load_dwordx4 a[240:243], v144, s[88:91], s69 offen offset:0 sc1\n\t" \
  "ds_read_b64_tr_b16 v[60:61], v68 offset:6144\n\t" \
  "ds_read_b64_tr_b16 v[62:63], v68 offset:7168\n\t" \
  "v_add_f32 v135, v135, v223\n\t" \
  "s_waitcnt vmcnt(11) lgkmcnt(10)\n\t" \
  "v_mfma_f32_32x32x16_bf16 v[0:15], v[40:43], a[196:199], v[0:15]\n\t" \
  "ds_read_b64_tr_b16 v[32:33], v65 offset:40960\n\t" \
  "ds_read_b64_tr_b16 v[34:35], v65 offset:41984\n\t" \
  "ds_write_b128 v153, v[132:135] offset:7168\n\t" \
  "v_mfma_f32_32x32x16_bf16 v[16:31], v[44:47], a[196:199], v[16:31]\n\t" \
  "buffer_load_dwordx4 a[244:247], v144, s[88:91], s69 offen offset:1024 sc1\n\t" \
  "ds_read_b64_tr_b16 v[36:37], v68 offset:40960\n\t" \
  "ds_read_b64_tr_b16 v[38:39], v68 offset:41984\n\t" \
  "s_add_u32 m0, s62, 8192\n\t" \
  "v_accvgpr_read_b32 v156, a0\n\t" \
  "v_accvgpr_read_b32 v157, a1\n\t" \
  "v_add_f32 v72, v72, v156\n\t" \
  "s_waitcnt vmcnt(11) lgkmcnt(10)\n\t" \
  "v_mfma_f32_32x32x16_bf16 v[0:15], v[48:51], a[200:203], v[0:15]\n\t" \
  "ds_read_b64_tr_b16 v[40:41], v65 offset:43008\n\t" \
  "ds_read_b64_tr_b16 v[42:43], v65 offset:44032\n\t" \
  "buffer_load_dwordx4 v140, s[92:95], s70 offen offset:0 lds\n\t" \
  "v_add_f32 v73, v73, v157\n\t" \
  "v_accvgpr_read_b32 v156, a2\n\t" \
  "v_accvgpr_read_b32 v157, a3\n\t" \
  "v_mfma_f32_32x32x16_bf16 v[16:31], v[52:55], a[200:203], v[16:31]\n\t" \
  "buffer_load_dwordx4 a[248:251], v144, s[88:91], s69 offen offset:2048 sc1\n\t" \
  "ds_read_b64_tr_b16 v[44:45], v68 offset:43008\n\t" \
  "ds_read_b64_tr_b16 v[46:47], v68 offset:44032\n\t" \
  "buffer_load_dwordx4 v141, s[92:95], s70 offen offset:1024 lds\n\t" \
  "v_add_f32 v74, v74, v156\n\t" \
  "v_add_f32 v75, v75, v157\n\t" \
  "v_accvgpr_read_b32 v156, a4\n\t" \
  "s_waitcnt vmcnt(13) lgkmcnt(9)\n\t" \
  "v_mfma_f32_32x32x16_bf16 v[0:15], v[56:59], a[204:207], v[0:15]\n\t" \
  "ds_read_b64_tr_b16 v[48:49], v66 offset:28672\n\t" \
  "ds_read_b64_tr_b16 v[50:51], v66 offset:29696\n\t" \
  "buffer_load_dwordx4 v142, s[92:95], s70 offen offset:2048 lds\n\t" \
  "v_accvgpr_read_b32 v157, a5\n\t" \
  "v_add_f32 v76, v76, v156\n\t" \
  "v_add_f32 v77, v77, v157\n\t" \
  "v_mfma_f32_32x32x16_bf16 v[16:31], v[60:63], a[204:207], v[16:31]\n\t" \
  "buffer_load_dwordx4 a[252:255], v144, s[88:91], s69 offen offset:3072 sc1\n\t" \
  "ds_read_b64_tr_b16 v[52:53], v69 offset:28672\n\t" \
  "ds_read_b64_tr_b16 v[54:55], v69 offset:29696\n\t" \
  "buffer_load_dwordx4 v143, s[92:95], s70 offen offset:3072 lds\n\t" \
  "v_accvgpr_read_b32 v156, a6\n\t" \
  "v_accvgpr_read_b32 v157, a7\n\t" \
  "v_add_f32 v78, v78, v156\n\t" \
  "s_waitcnt vmcnt(15) lgkmcnt(8)\n\t" \
  "v_mfma_f32_32x32x16_bf16 v[0:15], v[32:35], a[208:211], v[0:15]\n\t" \
  "ds_read_b64_tr_b16 v[56:57], v66 offset:30720\n\t" \
  "ds_read_b64_tr_b16 v[58:59], v66 offset:31744\n\t" \
  "s_add_u32 m0, s62, 12288\n\t" \
  "s_mov_b32 s69, 32768\n\t" \
  "v_add_f32 v79, v79, v157\n\t" \
  "v_accvgpr_read_b32 v156, a8\n\t" \
  "v_accvgpr_read_b32 v157, a9\n\t" \
  "v_mfma_f32_32x32x16_bf16 v[16:31], v[36:39], a[208:211], v[16:31]\n\t" \
  "buffer_load_dwordx4 a[128:131], v144, s[88:91], s69 offen offset:0 sc1\n\t" \
  "ds_read_b64_tr_b16 v[60:61], v69 offset:30720\n\t" \
  "ds_read_b64_tr_b16 v[62:63], v69 offset:31744\n\t" \
  "s_add_u32 s70, s70, 65536\n\t" \
  "v_add_f32 v80, v80, v156\n\t" \
  "v_add_f32 v81, v81, v157\n\t" \
  "v_accvgpr_read_b32 v156, a10\n\t" \
  "s_waitcnt vmcnt(15) lgkmcnt(8)\n\t" \
  "v_mfma_f32_32x32x16_bf16 v[0:15], v[40:43], a[212:215], v[0:15]\n\t" \
  "ds_read_b64_tr_b16 v[32:33], v65 offset:61440\n\t" \
  "ds_read_b64_tr_b16 v[34:35], v65 offset:62464\n\t" \
  "buffer_load_dwordx4 v140, s[92:95], s70 offen offset:0 lds\n\t" \
  "v_accvgpr_read_b32 v157, a11\n\t" \
  "v_add_f32 v82, v82, v156\n\t" \
  "v_add_f32 v83, v83, v157\n\t" \
  "v_mfma_f32_32x32x16_bf16 v[16:31], v[44:47], a[212:215], v[16:31]\n\t" \
  "buffer_load_dwordx4 a[132:135], v144, s[88:91], s69 offen offset:1024 sc1\n\t" \
  "ds_read_b64_tr_b16 v[36:37], v68 offset:61440\n\t" \
  "ds_read_b64_tr_b16 v[38:39], v68 offset:62464\n\t" \
  "buffer_load_dwordx4 v141, s[92:95], s70 offen offset:1024 lds\n\t" \
  "v_accvgpr_read_b32 v156, a12\n\t" \
  "v_accvgpr_read_b32 v157, a13\n\t" \
  "v_add_f32 v84, v84, v156\n\t" \
  "s_waitcnt vmcnt(17) lgkmcnt(8)\n\t" \
  "v_mfma_f32_32x32x16_bf16 v[0:15], v[48:51], a[216:219], v[0:15]\n\t" \
  "ds_read_b64_tr_b16 v[40:41], v65 offset:63488\n\t" \
  "ds_read_b64_tr_b16 v[42:43], v65 offset:64512\n\t" \
  "buffer_load_dwordx4 v142, s[92:95], s70 offen offset:2048 lds\n\t" \
  "v_add_f32 v85, v85, v157\n\t" \
  "v_accvgpr_read_b32 v156, a14\n\t" \
  "v_accvgpr_read_b32 v157, a15\n\t" \
  "v_mfma_f32_32x32x16_bf16 v[16:31], v[52:55], a[216:219], v[16:31]\n\t" \
  "buffer_load_dwordx4 a[136:139], v144, s[88:91], s69 offen offset:2048 sc1\n\t" \
  "ds_read_b64_tr_b16 v[44:45], v68 offset:63488\n\t" \
  "ds_read_b64_tr_b16 v[46:47], v68 offset:64512\n\t" \
  "buffer_load_dwordx4 v143, s[92:95], s70 offen offset:3072 lds\n\t" \
  "v_add_f32 v86, v86, v156\n\t" \
  "v_add_f32 v87, v87, v157\n\t" \
  "v_accvgpr_read_b32 v156, a16\n\t" \
  "s_waitcnt vmcnt(19) lgkmcnt(8)\n\t" \
  "v_mfma_f32_32x32x16_bf16 v[0:15], v[56:59], a[220:223], v[0:15]\n\t" \
  "ds_read_b64_tr_b16 v[48:49], v66 offset:0\n\t" \
  "ds_read_b64_tr_b16 v[50:51], v66 offset:1024\n\t" \
  "s_barrier\n\t" \
  "v_accvgpr_read_b32 v157, a17\n\t" \
  "v_add_f32 v88, v88, v156\n\t" \
  "v_add_f32 v89, v89, v157\n\t" \
  "v_mfma_f32_32x32x16_bf16 v[16:31], v[60:63], a[220:223], v[16:31]\n\t" \
  "buffer_load_dwordx4 a[140:143], v144, s[88:91], s69 offen offset:3072 sc1\n\t" \
  "ds_read_b64_tr_b16 v[52:53], v69 offset:0\n\t" \
  "ds_read_b64_tr_b16 v[54:55], v69 offset:1024\n\t" \
  "ds_read_b128 v[104:107], v154 offset:0\n\t" \
  "v_accvgpr_read_b32 v156, a18\n\t" \
  "v_accvgpr_read_b32 v157, a19\n\t" \
  "v_add_f32 v90, v90, v156\n\t" \
  "s_waitcnt vmcnt(19) lgkmcnt(9)\n\t" \
  "v_mfma_f32_32x32x16_bf16 v[0:15], v[32:35], a[224:227], v[0:15]\n\t" \
  "ds_read_b64_tr_b16 v[56:57], v66 offset:2048\n\t" \
  "ds_read_b64_tr_b16 v[58:59], v66 offset:3072\n\t" \
  "ds_read_b128 v[108:111], v154 offset:1024\n\t" \
  "s_mov_b32 s69, 36864\n\t" \
  "v_add_f32 v91, v91, v157\n\t" \
  "v_accvgpr_read_b32 v156, a20\n\t" \
  "v_accvgpr_read_b32 v157, a21\n\t" \
  "v_mfma_f32_32x32x16_bf16 v[16:31], v[36:39], a[224:227], v[16:31]\n\t" \
  "buffer_load_dwordx4 a[144:147], v144, s[88:91], s69 offen offset:0 sc1\n\t" \
  "ds_read_b64_tr_b16 v[60:61], v69 offset:2048\n\t" \
  "ds_read_b64_tr_b16 v[62:63], v69 offset:3072\n\t" \
  "ds_read_b128 v[112:115], v154 offset:2048\n\t" \
  "v_add_f32 v92, v92, v156\n\t" \
  "v_add_f32 v93, v93, v157\n\t" \
  "v_accvgpr_read_b32 v156, a22\n\t" \
  "s_waitcnt vmcnt(19) lgkmcnt(11)\n\t" \
  "v_mfma_f32_32x32x16_bf16 v[0:15], v[40:43], a[228:231], v[0:15]\n\t" \
  "ds_read_b64_tr_b16 v[32:33], v66 offset:4096\n\t" \
  "ds_read_b64_tr_b16 v[34:35], v66 offset:5120\n\t" \
  "ds_read_b128 v[116:119], v154 offset:3072\n\t" \
  "v_accvgpr_read_b32 v157, a23\n\t" \
  "v_add_f32 v94, v94, v156\n\t" \
  "v_add_f32 v95, v95, v157\n\t" \
  "v_mfma_f32_32x32x16_bf16 v[16:31], v[44:47], a[228:231], v[16:31]\n\t" \
  "buffer_load_dwordx4 a[148:151], v144, s[88:91], s69 offen offset:1024 sc1\n\t" \
  "ds_read_b64_tr_b16 v[36:37], v69 offset:4096\n\t" \
  "ds_read_b64_tr_b16 v[38:39], v69 offset:5120\n\t" \
  "ds_read_b128 v[120:123], v154 offset:4096\n\t" \
  "v_accvgpr_read_b32 v156, a24\n\t" \
  "v_accvgpr_read_b32 v157, a25\n\t" \
  "v_add_f32 v96, v96, v156\n\t" \
  "s_waitcnt vmcnt(19) lgkmcnt(13)\n\t" \
  "v_mfma_f32_32x32x16_bf16 v[0:15], v[48:51], a[232:235], v[0:15]\n\t" \
  "ds_read_b64_tr_b16 v[40:41], v66 offset:6144\n\t" \
  "ds_read_b64_tr_b16 v[42:43], v66 offset:7168\n\t" \
  "ds_read_b128 v[124:127], v154 offset:5120\n\t" \
  "v_add_f32 v97, v97, v157\n\t" \
  "v_accvgpr_read_b32 v156, a26\n\t" \
  "v_accvgpr_read_b32 v157, a27\n\t" \
  "v_mfma_f32_32x32x16_bf16 v[16:31], v[52:55], a[232:235], v[16:31]\n\t" \
  "buffer_load_dwordx4 a[152:155], v144, s[88:91], s69 offen offset:2048 sc1\n\t" \
  "ds_read_b64_tr_b16 v[44:45], v69 offset:6144\n\t" \
  "ds_read_b64_tr_b16 v[46:47], v69 offset:7168\n\t" \
  "ds_read_b128 v[128:131], v154 offset:6144\n\t" \
  "v_add_f32 v98, v98, v156\n\t" \
  "v_add_f32 v99, v99, v157\n\t" \
  "v_accvgpr_read_b32 v156, a28\n\t" \
  "s_waitcnt vmcnt(19) lgkmcnt(13)\n\t" \
  "v_mfma_f32_32x32x16_bf16 v[0:15], v[56:59], a[236:239], v[0:15]\n\t" \
  "ds_read_b64_tr_b16 v[48:49], v66 offset:8192\n\t" \
  "ds_read_b64_tr_b16 v[50:51], v66 offset:9216\n\t" \
  "ds_read_b128 v[132:135], v154 offset:7168\n\t" \
  "v_accvgpr_read_b32 v157, a29\n\t" \
  "v_add_f32 v100, v100, v156\n\t" \
  "v_add_f32 v101, v101, v157\n\t" \
  "v_mfma_f32_32x32x16_bf16 v[16:31], v[60:63], a[236:239], v[16:31]\n\t" \
  "buffer_load_dwordx4 a[156:159], v144, s[88:91], s69 offen offset:3072 sc1\n\t" \
  "ds_read_b64_tr_b16 v[52:53], v69 offset:8192\n\t" \
  "ds_read_b64_tr_b16 v[54:55], v69 offset:9216\n\t" \
  "v_accvgpr_read_b32 v156, a30\n\t" \
  "v_accvgpr_read_b32 v157, a31\n\t" \
  "v_add_f32 v102, v102, v156\n\t" \
  "s_waitcnt vmcnt(19) lgkmcnt(12)\n\t" \
  "v_mfma_f32_32x32x16_bf16 v[0:15], v[32:35], a[240:243], v[0:15]\n\t" \
  "ds_read_b64_tr_b16 v[56:57], v66 offset:10240\n\t" \
  "ds_read_b64_tr_b16 v[58:59], v66 offset:11264\n\t" \
  "s_mov_b32 s69, 40960\n\t" \
  "v_add_f32 v103, v103, v157\n\t" \
  "v_mfma_f32_32x32x16_bf16 v[16:31], v[36:39], a[240:243], v[16:31]\n\t" \
  "buffer_load_dwordx4 a[160:163], v144, s[88:91], s69 offen offset:0 sc1\n\t" \
  "ds_read_b64_tr_b16 v[60:61], v69 offset:10240\n\t" \
  "ds_read_b64_tr_b16 v[62:63], v69 offset:11264\n\t" \
  "s_waitcnt lgkmcnt(6)\n\t" \
  "v_add_f32 v72, v72, v104\n\t" \
  "v_add_f32 v73, v73, v105\n\t" \
  "s_waitcnt vmcnt(19)\n\t" \
  "v_mfma_f32_32x32x16_bf16 v[0:15], v[40:43], a[244:247], v[0:15]\n\t" \
  "ds_read_b64_tr_b16 v[32:33], v66 offset:12288\n\t" \
  "ds_read_b64_tr_b16 v[34:35], v66 offset:13312\n\t" \
  "v_add_f32 v74, v74, v106\n\t" \
  "v_add_f32 v75, v75, v107\n\t" \
  "v_add_f32 v76, v76, v108\n\t" \
  "v_mfma_f32_32x32x16_bf16 v[16:31], v[44:47], a[244:247], v[16:31]\n\t" \
  "buffer_load_dwordx4 a[164:167], v144, s[88:91], s69 offen offset:1024 sc1\n\t" \
  "ds_read_b64_tr_b16 v[36:37], v69 offset:12288\n\t" \
  "ds_read_b64_tr_b16 v[38:39], v69 offset:13312\n\t" \
  "v_add_f32 v77, v77, v109\n\t" \
  "v_add_f32 v78, v78, v110\n\t" \
  "v_add_f32 v79, v79, v111\n\t" \
  "s_waitcnt vmcnt(18) lgkmcnt(8)\n\t" \
  "v_mfma_f32_32x32x16_bf16 v[0:15], v[48:51], a[248:251], v[0:15]\n\t" \
  "ds_read_b64_tr_b16 v[40:41], v66 offset:14336\n\t" \
  "ds_read_b64_tr_b16 v[42:43], v66 offset:15360\n\t" \
  "v_add_f32 v80, v80, v112\n\t" \
  "v_add_f32 v81, v81, v113\n\t" \
  "v_add_f32 v82, v82, v114\n\t" \
  "v_mfma_f32_32x32x16_bf16 v[16:31], v[52:55], a[248:251], v[16:31]\n\t" \
  "buffer_load_dwordx4 a[168:171], v144, s[88:91], s69 offen offset:2048 sc1\n\t" \
  "ds_read_b64_tr_b16 v[44:45], v69 offset:14336\n\t" \
  "ds_read_b64_tr_b16 v[46:47], v69 offset:15360\n\t" \
  "v_add_f32 v83, v83, v115\n\t" \
  "v_add_f32 v84, v84, v116\n\t" \
  "v_add_f32 v85, v85, v117\n\t" \
  "s_waitcnt vmcnt(16) lgkmcnt(8)\n\t" \
  "v_mfma_f32_32x32x16_bf16 v[0:15], v[56:59], a[252:255], v[0:15]\n\t" \
  "ds_read_b64_tr_b16 v[48:49], v66 offset:16384\n\t" \
  "ds_read_b64_tr_b16 v[50:51], v66 offset:17408\n\t" \
  "v_add_f32 v86, v86, v118\n\t" \
  "v_add_f32 v87, v87, v119\n\t" \
  "v_add_f32 v88, v88, v120\n\t" \
  "v_mfma_f32_32x32x16_bf16 v[16:31], v[60:63], a[252:255], v[16:31]\n\t" \
  "buffer_load_dwordx4 a[172:175], v144, s[88:91], s69 offen offset:3072 sc1\n\t" \
  "ds_read_b64_tr_b16 v[52:53], v69 offset:16384\n\t" \
  "ds_read_b64_tr_b16 v[54:55], v69 offset:17408\n\t" \
  "v_add_f32 v89, v89, v121\n\t" \
  "v_add_f32 v90, v90, v122\n\t" \
  "v_add_f32 v91, v91, v123\n\t" \
  "s_waitcnt vmcnt(15) lgkmcnt(8)\n\t" \
  "v_mfma_f32_32x32x16_bf16 v[0:15], v[32:35], a[128:131], v[0:15]\n\t" \
  "ds_read_b64_tr_b16 v[56:57], v66 offset:18432\n\t" \
  "ds_read_b64_tr_b16 v[58:59], v66 offset:19456\n\t" \
  "s_mov_b32 s69, 45056\n\t" \
  "v_add_f32 v92, v92, v124\n\t" \
  "v_add_f32 v93, v93, v125\n\t" \
  "v_add_f32 v94, v94, v126\n\t" \
  "v_mfma_f32_32x32x16_bf16 v[16:31], v[36:39], a[128:131], v[16:31]\n\t" \
  "buffer_load_dwordx4 a[176:179], v144, s[88:91], s69 offen offset:0 sc1\n\t" \
  "ds_read_b64_tr_b16 v[60:61], v69 offset:18432\n\t" \
  "ds_read_b64_tr_b16 v[62:63], v69 offset:19456\n\t" \
  "v_add_f32 v95, v95, v127\n\t" \
  "v_add_f32 v96, v96, v128\n\t" \
  "v_add_f32 v97, v97, v129\n\t" \
  "s_waitcnt vmcnt(14) lgkmcnt(8)\n\t" \
  "v_mfma_f32_32x32x16_bf16 v[0:15], v[40:43], a[132:135], v[0:15]\n\t" \
  "ds_read_b64_tr_b16 v[32:33], v66 offset:20480\n\t" \
  "ds_read_b64_tr_b16 v[34:35], v66 offset:21504\n\t" \
  "v_add_f32 v98, v98, v130\n\t" \
  "v_add_f32 v99, v99, v131\n\t" \
  "v_add_f32 v100, v100, v132\n\t" \
  "v_mfma_f32_32x32x16_bf16 v[16:31], v[44:47], a[132:135], v[16:31]\n\t" \
  "buffer_load_dwordx4 a[180:183], v144, s[88:91], s69 offen offset:1024 sc1\n\t" \
  "ds_read_b64_tr_b16 v[36:37], v69 offset:20480\n\t" \
  "ds_read_b64_tr_b16 v[38:39], v69 offset:21504\n\t" \
  "v_add_f32 v101, v101, v133\n\t" \
  "v_add_f32 v102, v102, v134\n\t" \
  "v_add_f32 v103, v103, v135\n\t" \
  "s_waitcnt vmcnt(12) lgkmcnt(8)\n\t" \
  "v_mfma_f32_32x32x16_bf16 v[0:15], v[48:51], a[136:139], v[0:15]\n\t" \
  "ds_read_b64_tr_b16 v[40:41], v66 offset:22528\n\t" \
  "ds_read_b64_tr_b16 v[42:43], v66 offset:23552\n\t" \
  "v_permlane16_swap_b32 v72, v76\n\t" \
  "v_permlane16_swap_b32 v73, v77\n\t" \
  "v_permlane16_swap_b32 v74, v78\n\t" \
  "v_mfma_f32_32x32x16_bf16 v[16:31], v[52:55], a[136:139], v[16:31]\n\t" \
  "buffer_load_dwordx4 a[184:187], v144, s[88:91], s69 offen offset:2048 sc1\n\t" \
  "ds_read_b64_tr_b16 v[44:45], v69 offset:22528\n\t" \
  "ds_read_b64_tr_b16 v[46:47], v69 offset:23552\n\t" \
  "v_permlane16_swap_b32 v75, v79\n\t" \
  "v_permlane16_swap_b32 v80, v84\n\t" \
  "v_permlane16_swap_b32 v81, v85\n\t" \
  "s_waitcnt vmcnt(11) lgkmcnt(8)\n\t" \
  "v_mfma_f32_32x32x16_bf16 v[0:15], v[56:59], a[140:143], v[0:15]\n\t" \
  "ds_read_b64_tr_b16 v[48:49], v66 offset:24576\n\t" \
  "ds_read_b64_tr_b16 v[50:51], v66 offset:25600\n\t" \
  "v_permlane16_swap_b32 v82, v86\n\t" \
  "v_permlane16_swap_b32 v83, v87\n\t" \
  "v_permlane16_swap_b32 v88, v92\n\t" \
  "v_mfma_f32_32x32x16_bf16 v[16:31], v[60:63], a[140:143], v[16:31]\n\t" \
  "buffer_load_dwordx4 a[188:191], v144, s[88:91], s69 offen offset:3072 sc1\n\t" \
  "ds_read_b64_tr_b16 v[52:53], v69 offset:24576\n\t" \
  "ds_read_b64_tr_b16 v[54:55], v69 offset:25600\n\t" \
  "v_permlane16_swap_b32 v89, v93\n\t" \
  "v_permlane16_swap_b32 v90, v94\n\t" \
  "v_permlane16_swap_b32 v91, v95\n\t" \
  "s_waitcnt vmcnt(11) lgkmcnt(8)\n\t" \
  "v_mfma_f32_32x32x16_bf16 v[0:15], v[32:35], a[144:147], v[0:15]\n\t" \
  "ds_read_b64_tr_b16 v[56:57], v66 offset:26624\n\t" \
  "ds_read_b64_tr_b16 v[58:59], v66 offset:27648\n\t" \
  "s_mov_b32 s69, 49152\n\t" \
  "v_permlane16_swap_b32 v96, v100\n\t" \
  "v_permlane16_swap_b32 v97, v101\n\t" \
  "v_permlane16_swap_b32 v98, v102\n\t" \
  "v_mfma_f32_32x32x16_bf16 v[16:31], v[36:39], a[144:147], v[16:31]\n\t" \
  "buffer_load_dwordx4 a[192:195], v144, s[88:91], s69 offen offset:0 sc1\n\t" \
  "ds_read_b64_tr_b16 v[60:61], v69 offset:26624\n\t" \
  "ds_read_b64_tr_b16 v[62:63], v69 offset:27648\n\t" \
  "v_permlane16_swap_b32 v99, v103\n\t" \
  "v_add_f32 v72, v72, v224\n\t" \
  "v_add_f32 v73, v73, v225\n\t" \
  "s_waitcnt vmcnt(11) lgkmcnt(8)\n\t" \
  "v_mfma_f32_32x32x16_bf16 v[0:15], v[40:43], a[148:151], v[0:15]\n\t" \
  "ds_read_b64_tr_b16 v[32:33], v65 offset:45056\n\t" \
  "ds_read_b64_tr_b16 v[34:35], v65 offset:46080\n\t" \
  "v_mul_f32 v156, 0xbfb8aa3b, v72\n\t" \
  "v_mul_f32 v157, 0xbfb8aa3b, v73\n\t" \
  "v_exp_f32 v156, v156\n\t" \
  "v_mfma_f32_32x32x16_bf16 v[16:31], v[44:47], a[148:151], v[16:31]\n\t" \
  "buffer_load_dwordx4 a[196:199], v144, s[88:91], s69 offen offset:1024 sc1\n\t" \
  "ds_read_b64_tr_b16 v[36:37], v68 offset:45056\n\t" \
  "ds_read_b64_tr_b16 v[38:39], v68 offset:46080\n\t" \
  "v_exp_f32 v157, v157\n\t" \
  "v_add_f32 v156, 1.0, v156\n\t" \
  "v_add_f32 v157, 1.0, v157\n\t" \
  "s_waitcnt vmcnt(11) lgkmcnt(8)\n\t" \
  "v_mfma_f32_32x32x16_bf16 v[0:15], v[48:51], a[152:155], v[0:15]\n\t" \
  "ds_read_b64_tr_b16 v[40:41], v65 offset:47104\n\t" \
  "ds_read_b64_tr_b16 v[42:43], v65 offset:48128\n\t" \
  "v_rcp_f32 v156, v156\n\t" \
  "v_rcp_f32 v157, v157\n\t" \
  "v_mul_f32 v72, v72, v156\n\t" \
  "v_mfma_f32_32x32x16_bf16 v[16:31], v[52:55], a[152:155], v[16:31]\n\t" \
  "buffer_load_dwordx4 a[200:203], v144, s[88:91], s69 offen offset:2048 sc1\n\t" \
  "ds_read_b64_tr_b16 v[44:45], v68 offset:47104\n\t" \
  "ds_read_b64_tr_b16 v[46:47], v68 offset:48128\n\t" \
  "v_mul_f32 v73, v73, v157\n\t" \
  "v_add_f32 v74, v74, v226\n\t" \
  "v_add_f32 v75, v75, v227\n\t" \
  "s_waitcnt vmcnt(11) lgkmcnt(8)\n\t" \
  "v_mfma_f32_32x32x16_bf16 v[0:15], v[56:59], a[156:159], v[0:15]\n\t" \
  "ds_read_b64_tr_b16 v[48:49], v65 offset:49152\n\t" \
  "ds_read_b64_tr_b16 v[50:51], v65 offset:50176\n\t" \
  "v_mul_f32 v156, 0xbfb8aa3b, v74\n\t" \
  "v_mul_f32 v157, 0xbfb8aa3b, v75\n\t" \
  "v_exp_f32 v156, v156\n\t" \
  "v_mfma_f32_32x32x16_bf16 v[16:31], v[60:63], a[156:159], v[16:31]\n\t" \
  "buffer_load_dwordx4 a[204:207], v144, s[88:91], s69 offen offset:3072 sc1\n\t" \
  "ds_read_b64_tr_b16 v[52:53], v68 offset:49152\n\t" \
  "ds_read_b64_tr_b16 v[54:55], v68 offset:50176\n\t" \
  "v_exp_f32 v157, v157\n\t" \
  "v_add_f32 v156, 1.0, v156\n\t" \
  "v_add_f32 v157, 1.0, v157\n\t" \
  "s_waitcnt vmcnt(11) lgkmcnt(8)\n\t" \
  "v_mfma_f32_32x32x16_bf16 v[0:15], v[32:35], a[160:163], v[0:15]\n\t" \
  "ds_read_b64_tr_b16 v[56:57], v65 offset:51200\n\t" \
  "ds_read_b64_tr_b16 v[58:59], v65 offset:52224\n\t" \
  "s_mov_b32 s69, 53248\n\t" \
  "v_rcp_f32 v156, v156\n\t" \
  "v_rcp_f32 v157, v157\n\t" \
  "v_mul_f32 v74, v74, v156\n\t" \
  "v_mfma_f32_32x32x16_bf16 v[16:31], v[36:39], a[160:163], v[16:31]\n\t" \
  "buffer_load_dwordx4 a[208:211], v144, s[88:91], s69 offen offset:0 sc1\n\t" \
  "ds_read_b64_tr_b16 v[60:61], v68 offset:51200\n\t" \
  "ds_read_b64_tr_b16 v[62:63], v68 offset:52224\n\t" \
  "v_mul_f32 v75, v75, v157\n\t" \
  "v_add_f32 v76, v76, v228\n\t" \
  "v_add_f32 v77, v77, v229\n\t" \
  "s_waitcnt vmcnt(11) lgkmcnt(8)\n\t" \
  "v_mfma_f32_32x32x16_bf16 v[0:15], v[40:43], a[164:167], v[0:15]\n\t" \
  "ds_read_b64_tr_b16 v[32:33], v65 offset:53248\n\t" \
  "ds_read_b64_tr_b16 v[34:35], v65 offset:54272\n\t" \
  "v_mul_f32 v156, 0xbfb8aa3b, v76\n\t" \
  "v_mul_f32 v157, 0xbfb8aa3b, v77\n\t" \
  "v_exp_f32 v156, v156\n\t" \
  "v_mfma_f32_32x32x16_bf16 v[16:31], v[44:47], a[164:167], v[16:31]\n\t" \
  "buffer_load_dwordx4 a[212:215], v144, s[88:91], s69 offen offset:1024 sc1\n\t" \
  "ds_read_b64_tr_b16 v[36:37], v68 offset:53248\n\t" \
  "ds_read_b64_tr_b16 v[38:39], v68 offset:54272\n\t" \
  "v_exp_f32 v157, v157\n\t" \
  "v_add_f32 v156, 1.0, v156\n\t" \
  "v_add_f32 v157, 1.0, v157\n\t" \
  "s_waitcnt vmcnt(11) lgkmcnt(8)\n\t" \
  "v_mfma_f32_32x32x16_bf16 v[0:15], v[48:51], a[168:171], v[0:15]\n\t" \
  "ds_read_b64_tr_b16 v[40:41], v65 offset:55296\n\t" \
  "ds_read_b64_tr_b16 v[42:43], v65 offset:56320\n\t" \
  "v_rcp_f32 v156, v156\n\t" \
  "v_rcp_f32 v157, v157\n\t" \
  "v_mul_f32 v76, v76, v156\n\t" \
  "v_mfma_f32_32x32x16_bf16 v[16:31], v[52:55], a[168:171], v[16:31]\n\t" \
  "buffer_load_dwordx4 a[216:219], v144, s[88:91], s69 offen offset:2048 sc1\n\t" \
  "ds_read_b64_tr_b16 v[44:45], v68 offset:55296\n\t" \
  "ds_read_b64_tr_b16 v[46:47], v68 offset:56320\n\t" \
  "v_mul_f32 v77, v77, v157\n\t" \
  "v_add_f32 v78, v78, v230\n\t" \
  "v_add_f32 v79, v79, v231\n\t" \
  "s_waitcnt vmcnt(11) lgkmcnt(8)\n\t" \
  "v_mfma_f32_32x32x16_bf16 v[0:15], v[56:59], a[172:175], v[0:15]\n\t" \
  "ds_read_b64_tr_b16 v[48:49], v65 offset:57344\n\t" \
  "ds_read_b64_tr_b16 v[50:51], v65 offset:58368\n\t" \
  "v_mul_f32 v156, 0xbfb8aa3b, v78\n\t" \
  "v_mul_f32 v157, 0xbfb8aa3b, v79\n\t" \
  "v_exp_f32 v156, v156\n\t" \
  "v_mfma_f32_32x32x16_bf16 v[16:31], v[60:63], a[172:175], v[16:31]\n\t" \
  "buffer_load_dwordx4 a[220:223], v144, s[88:91], s69 offen offset:3072 sc1\n\t" \
  "ds_read_b64_tr_b16 v[52:53], v68 offset:57344\n\t" \
  "ds_read_b64_tr_b16 v[54:55], v68 offset:58368\n\t" \
  "v_exp_f32 v157, v157\n\t" \
  "v_add_f32 v156, 1.0, v156\n\t" \
  "v_add_f32 v157, 1.0, v157\n\t" \
  "s_barrier\n\t" \
  "s_waitcnt vmcnt(11) lgkmcnt(8)\n\t" \
  "v_mfma_f32_32x32x16_bf16 v[0:15], v[32:35], a[176:179], v[0:15]\n\t" \
  "ds_read_b64_tr_b16 v[56:57], v65 offset:59392\n\t" \
  "ds_read_b64_tr_b16 v[58:59], v65 offset:60416\n\t" \
  "s_mov_b32 s69, 57344\n\t" \
  "v_rcp_f32 v156, v156\n\t" \
  "v_rcp_f32 v157, v157\n\t" \
  "v_mul_f32 v78, v78, v156\n\t" \
  "v_mfma_f32_32x32x16_bf16 v[16:31], v[36:39], a[176:179], v[16:31]\n\t" \
  "buffer_load_dwordx4 a[224:227], v144, s[88:91], s69 offen offset:0 sc1\n\t" \
  "ds_read_b64_tr_b16 v[60:61], v68 offset:59392\n\t" \
  "ds_read_b64_tr_b16 v[62:63], v68 offset:60416\n\t" \
  "v_mul_f32 v79, v79, v157\n\t" \
  "v_add_f32 v80, v80, v232\n\t" \
  "v_add_f32 v81, v81, v233\n\t" \
  "s_waitcnt vmcnt(11) lgkmcnt(8)\n\t" \
  "v_mfma_f32_32x32x16_bf16 v[0:15], v[40:43], a[180:183], v[0:15]\n\t" \
  "ds_read_b64_tr_b16 v[32:33], v64 offset:8192\n\t" \
  "ds_read_b64_tr_b16 v[34:35], v64 offset:9216\n\t" \
  "v_mul_f32 v156, 0xbfb8aa3b, v80\n\t" \
  "v_mul_f32 v157, 0xbfb8aa3b, v81\n\t" \
  "v_exp_f32 v156, v156\n\t" \
  "v_mfma_f32_32x32x16_bf16 v[16:31], v[44:47], a[180:183], v[16:31]\n\t" \
  "buffer_load_dwordx4 a[228:231], v144, s[88:91], s69 offen offset:1024 sc1\n\t" \
  "ds_read_b64_tr_b16 v[36:37], v67 offset:8192\n\t" \
  "ds_read_b64_tr_b16 v[38:39], v67 offset:9216\n\t" \
  "v_exp_f32 v157, v157\n\t" \
  "v_add_f32 v156, 1.0, v156\n\t" \
  "v_add_f32 v157, 1.0, v157\n\t" \
  "s_waitcnt vmcnt(11) lgkmcnt(8)\n\t" \
  "v_mfma_f32_32x32x16_bf16 v[0:15], v[48:51], a[184:187], v[0:15]\n\t" \
  "ds_read_b64_tr_b16 v[40:41], v64 offset:10240\n\t" \
  "ds_read_b64_tr_b16 v[42:43], v64 offset:11264\n\t" \
  "v_rcp_f32 v156, v156\n\t" \
  "v_rcp_f32 v157, v157\n\t" \
  "v_mul_f32 v80, v80, v156\n\t" \
  "v_mfma_f32_32x32x16_bf16 v[16:31], v[52:55], a[184:187], v[16:31]\n\t" \
  "buffer_load_dwordx4 a[232:235], v144, s[88:91], s69 offen offset:2048 sc1\n\t" \
  "ds_read_b64_tr_b16 v[44:45], v67 offset:10240\n\t" \
  "ds_read_b64_tr_b16 v[46:47], v67 offset:11264\n\t" \
  "v_mul_f32 v81, v81, v157\n\t" \
  "v_add_f32 v82, v82, v234\n\t" \
  "v_add_f32 v83, v83, v235\n\t" \
  "s_waitcnt vmcnt(11) lgkmcnt(8)\n\t" \
  "v_mfma_f32_32x32x16_bf16 v[0:15], v[56:59], a[188:191], v[0:15]\n\t" \
  "ds_read_b64_tr_b16 v[48:49], v64 offset:12288\n\t" \
  "ds_read_b64_tr_b16 v[50:51], v64 offset:13312\n\t" \
  "v_mul_f32 v156, 0xbfb8aa3b, v82\n\t" \
  "v_mul_f32 v157, 0xbfb8aa3b, v83\n\t" \
  "v_exp_f32 v156, v156\n\t" \
  "v_mfma_f32_32x32x16_bf16 v[16:31], v[60:63], a[188:191], v[16:31]\n\t" \
  "buffer_load_dwordx4 a[236:239], v144, s[88:91], s69 offen offset:3072 sc1\n\t" \
  "ds_read_b64_tr_b16 v[52:53], v67 offset:12288\n\t" \
  "ds_read_b64_tr_b16 v[54:55], v67 offset:13312\n\t" \
  "v_exp_f32 v157, v157\n\t" \
  "v_add_f32 v156, 1.0, v156\n\t" \
  "v_add_f32 v157, 1.0, v157\n\t" \
  "s_waitcnt vmcnt(11) lgkmcnt(8)\n\t" \
  "v_mfma_f32_32x32x16_bf16 v[0:15], v[32:35], a[192:195], v[0:15]\n\t" \
  "ds_read_b64_tr_b16 v[56:57], v64 offset:14336\n\t" \
  "ds_read_b64_tr_b16 v[58:59], v64 offset:15360\n\t" \
  "s_mov_b32 s69, 61440\n\t" \
  "v_rcp_f32 v156, v156\n\t" \
  "v_rcp_f32 v157, v157\n\t" \
  "v_mul_f32 v82, v82, v156\n\t" \
  "v_mfma_f32_32x32x16_bf16 v[16:31], v[36:39], a[192:195], v[16:31]\n\t" \
  "buffer_load_dwordx4 a[240:243], v144, s[88:91], s69 offen offset:0 sc1\n\t" \
  "ds_read_b64_tr_b16 v[60:61], v67 offset:14336\n\t" \
  "ds_read_b64_tr_b16 v[62:63], v67 offset:15360\n\t" \
  "v_mul_f32 v83, v83, v157\n\t" \
  "v_add_f32 v84, v84, v236\n\t" \
  "v_add_f32 v85, v85, v237\n\t" \
  "s_waitcnt vmcnt(11) lgkmcnt(8)\n\t" \
  "v_mfma_f32_32x32x16_bf16 v[0:15], v[40:43], a[196:199], v[0:15]\n\t" \
  "ds_read_b64_tr_b16 v[32:33], v64 offset:24576\n\t" \
  "ds_read_b64_tr_b16 v[34:35], v64 offset:25600\n\t" \
  "v_mul_f32 v156, 0xbfb8aa3b, v84\n\t" \
  "v_mul_f32 v157, 0xbfb8aa3b, v85\n\t" \
  "v_exp_f32 v156, v156\n\t" \
  "v_mfma_f32_32x32x16_bf16 v[16:31], v[44:47], a[196:199], v[16:31]\n\t" \
  "buffer_load_dwordx4 a[244:247], v144, s[88:91], s69 offen offset:1024 sc1\n\t" \
  "ds_read_b64_tr_b16 v[36:37], v67 offset:24576\n\t" \
  "ds_read_b64_tr_b16 v[38:39], v67 offset:25600\n\t" \
  "v_exp_f32 v157, v157\n\t" \
  "v_add_f32 v156, 1.0, v156\n\t" \
  "v_add_f32 v157, 1.0, v157\n\t" \
  "s_waitcnt vmcnt(11) lgkmcnt(8)\n\t" \
  "v_mfma_f32_32x32x16_bf16 v[0:15], v[48:51], a[200:203], v[0:15]\n\t" \
  "ds_read_b64_tr_b16 v[40:41], v64 offset:26624\n\t" \
  "ds_read_b64_tr_b16 v[42:43], v64 offset:27648\n\t" \
  "v_rcp_f32 v156, v156\n\t" \
  "v_rcp_f32 v157, v157\n\t" \
  "v_mul_f32 v84, v84, v156\n\t" \
  "v_mfma_f32_32x32x16_bf16 v[16:31], v[52:55], a[200:203], v[16:31]\n\t" \
  "buffer_load_dwordx4 a[248:251], v144, s[88:91], s69 offen offset:2048 sc1\n\t" \
  "ds_read_b64_tr_b16 v[44:45], v67 offset:26624\n\t" \
  "ds_read_b64_tr_b16 v[46:47], v67 offset:27648\n\t" \
  "v_mul_f32 v85, v85, v157\n\t" \
  "v_add_f32 v86, v86, v238\n\t" \
  "v_add_f32 v87, v87, v239\n\t" \
  "s_waitcnt vmcnt(11) lgkmcnt(8)\n\t" \
  "v_mfma_f32_32x32x16_bf16 v[0:15], v[56:59], a[204:207], v[0:15]\n\t" \
  "ds_read_b64_tr_b16 v[48:49], v64 offset:28672\n\t" \
  "ds_read_b64_tr_b16 v[50:51], v64 offset:29696\n\t" \
  "v_mul_f32 v156, 0xbfb8aa3b, v86\n\t" \
  "v_mul_f32 v157, 0xbfb8aa3b, v87\n\t" \
  "v_exp_f32 v156, v156\n\t" \
  "v_mfma_f32_32x32x16_bf16 v[16:31], v[60:63], a[204:207], v[16:31]\n\t" \
  "buffer_load_dwordx4 a[252:255], v144, s[88:91], s69 offen offset:3072 sc1\n\t" \
  "ds_read_b64_tr_b16 v[52:53], v67 offset:28672\n\t" \
  "ds_read_b64_tr_b16 v[54:55], v67 offset:29696\n\t" \
  "v_exp_f32 v157, v157\n\t" \
  "v_add_f32 v156, 1.0, v156\n\t" \
  "v_add_f32 v157, 1.0, v157\n\t" \
  "s_waitcnt vmcnt(11) lgkmcnt(8)\n\t" \
  "v_mfma_f32_32x32x16_bf16 v[0:15], v[32:35], a[208:211], v[0:15]\n\t" \
  "ds_read_b64_tr_b16 v[56:57], v64 offset:30720\n\t" \
  "ds_read_b64_tr_b16 v[58:59], v64 offset:31744\n\t" \
  "v_rcp_f32 v156, v156\n\t" \
  "v_rcp_f32 v157, v157\n\t" \
  "v_mul_f32 v86, v86, v156\n\t" \
  "v_mfma_f32_32x32x16_bf16 v[16:31], v[36:39], a[208:211], v[16:31]\n\t" \
  "ds_read_b64_tr_b16 v[60:61], v67 offset:30720\n\t" \
  "ds_read_b64_tr_b16 v[62:63], v67 offset:31744\n\t" \
  "v_mul_f32 v87, v87, v157\n\t" \
  "v_add_f32 v88, v88, v240\n\t" \
  "v_add_f32 v89, v89, v241\n\t" \
  "s_waitcnt vmcnt(10) lgkmcnt(8)\n\t" \
  "v_mfma_f32_32x32x16_bf16 v[0:15], v[40:43], a[212:215], v[0:15]\n\t" \
  "ds_read_b64_tr_b16 v[32:33], v65 offset:16384\n\t" \
  "ds_read_b64_tr_b16 v[34:35], v65 offset:17408\n\t" \
  "v_mul_f32 v156, 0xbfb8aa3b, v88\n\t" \
  "v_mul_f32 v157, 0xbfb8aa3b, v89\n\t" \
  "v_exp_f32 v156, v156\n\t" \
  "v_mfma_f32_32x32x16_bf16 v[16:31], v[44:47], a[212:215], v[16:31]\n\t" \
  "ds_read_b64_tr_b16 v[36:37], v68 offset:16384\n\t" \
  "ds_read_b64_tr_b16 v[38:39], v68 offset:17408\n\t" \
  "v_exp_f32 v157, v157\n\t" \
  "v_add_f32 v156, 1.0, v156\n\t" \
  "v_add_f32 v157, 1.0, v157\n\t" \
  "s_waitcnt vmcnt(9) lgkmcnt(8)\n\t" \
  "v_mfma_f32_32x32x16_bf16 v[0:15], v[48:51], a[216:219], v[0:15]\n\t" \
  "ds_read_b64_tr_b16 v[40:41], v65 offset:18432\n\t" \
  "ds_read_b64_tr_b16 v[42:43], v65 offset:19456\n\t" \
  "v_rcp_f32 v156, v156\n\t" \
  "v_rcp_f32 v157, v157\n\t" \
  "v_mul_f32 v88, v88, v156\n\t" \
  "v_mfma_f32_32x32x16_bf16 v[16:31], v[52:55], a[216:219], v[16:31]\n\t" \
  "ds_read_b64_tr_b16 v[44:45], v68 offset:18432\n\t" \
  "ds_read_b64_tr_b16 v[46:47], v68 offset:19456\n\t" \
  "v_mul_f32 v89, v89, v157\n\t" \
  "v_add_f32 v90, v90, v242\n\t" \
  "v_add_f32 v91, v91, v243\n\t" \
  "s_waitcnt vmcnt(8) lgkmcnt(8)\n\t" \
  "v_mfma_f32_32x32x16_bf16 v[0:15], v[56:59], a[220:223], v[0:15]\n\t" \
  "ds_read_b64_tr_b16 v[48:49], v65 offset:20480\n\t" \
  "ds_read_b64_tr_b16 v[50:51], v65 offset:21504\n\t" \
  "v_mul_f32 v156, 0xbfb8aa3b, v90\n\t" \
  "v_mul_f32 v157, 0xbfb8aa3b, v91\n\t" \
  "v_exp_f32 v156, v156\n\t" \
  "v_mfma_f32_32x32x16_bf16 v[16:31], v[60:63], a[220:223], v[16:31]\n\t" \
  "ds_read_b64_tr_b16 v[52:53], v68 offset:20480\n\t" \
  "ds_read_b64_tr_b16 v[54:55], v68 offset:21504\n\t" \
  "v_exp_f32 v157, v157\n\t" \
  "v_add_f32 v156, 1.0, v156\n\t" \
  "v_add_f32 v157, 1.0, v157\n\t" \
  "s_waitcnt vmcnt(7) lgkmcnt(8)\n\t" \
  "v_mfma_f32_32x32x16_bf16 v[0:15], v[32:35], a[224:227], v[0:15]\n\t" \
  "ds_read_b64_tr_b16 v[56:57], v65 offset:22528\n\t" \
  "ds_read_b64_tr_b16 v[58:59], v65 offset:23552\n\t" \
  "v_rcp_f32 v156, v156\n\t" \
  "v_rcp_f32 v157, v157\n\t" \
  "v_mul_f32 v90, v90, v156\n\t" \
  "v_mfma_f32_32x32x16_bf16 v[16:31], v[36:39], a[224:227], v[16:31]\n\t" \
  "ds_read_b64_tr_b16 v[60:61], v68 offset:22528\n\t" \
  "ds_read_b64_tr_b16 v[62:63], v68 offset:23552\n\t" \
  "v_mul_f32 v91, v91, v157\n\t" \
  "v_add_f32 v92, v92, v244\n\t" \
  "v_add_f32 v93, v93, v245\n\t" \
  "s_waitcnt vmcnt(6) lgkmcnt(8)\n\t" \
  "v_mfma_f32_32x32x16_bf16 v[0:15], v[40:43], a[228:231], v[0:15]\n\t" \
  "ds_read_b64_tr_b16 v[32:33], v65 offset:32768\n\t" \
  "ds_read_b64_tr_b16 v[34:35], v65 offset:33792\n\t" \
  "v_mul_f32 v156, 0xbfb8aa3b, v92\n\t" \
  "v_mul_f32 v157, 0xbfb8aa3b, v93\n\t" \
  "v_exp_f32 v156, v156\n\t" \
  "v_mfma_f32_32x32x16_bf16 v[16:31], v[44:47], a[228:231], v[16:31]\n\t" \
  "ds_read_b64_tr_b16 v[36:37], v68 offset:32768\n\t" \
  "ds_read_b64_tr_b16 v[38:39], v68 offset:33792\n\t" \
  "v_exp_f32 v157, v157\n\t" \
  "v_add_f32 v156, 1.0, v156\n\t" \
  "v_add_f32 v157, 1.0, v157\n\t" \
  "s_waitcnt vmcnt(5) lgkmcnt(8)\n\t" \
  "v_mfma_f32_32x32x16_bf16 v[0:15], v[48:51], a[232:235], v[0:15]\n\t" \
  "ds_read_b64_tr_b16 v[40:41], v65 offset:34816\n\t" \
  "ds_read_b64_tr_b16 v[42:43], v65 offset:35840\n\t" \
  "v_rcp_f32 v156, v156\n\t" \
  "v_rcp_f32 v157, v157\n\t" \
  "v_mul_f32 v92, v92, v156\n\t" \
  "v_mfma_f32_32x32x16_bf16 v[16:31], v[52:55], a[232:235], v[16:31]\n\t" \
  "ds_read_b64_tr_b16 v[44:45], v68 offset:34816\n\t" \
  "ds_read_b64_tr_b16 v[46:47], v68 offset:35840\n\t" \
  "v_mul_f32 v93, v93, v157\n\t" \
  "v_add_f32 v94, v94, v246\n\t" \
  "v_add_f32 v95, v95, v247\n\t" \
  "s_waitcnt vmcnt(4) lgkmcnt(8)\n\t" \
  "v_mfma_f32_32x32x16_bf16 v[0:15], v[56:59], a[236:239], v[0:15]\n\t" \
  "ds_read_b64_tr_b16 v[48:49], v65 offset:36864\n\t" \
  "ds_read_b64_tr_b16 v[50:51], v65 offset:37888\n\t" \
  "v_mul_f32 v156, 0xbfb8aa3b, v94\n\t" \
  "v_mul_f32 v157, 0xbfb8aa3b, v95\n\t" \
  "v_exp_f32 v156, v156\n\t" \
  "v_mfma_f32_32x32x16_bf16 v[16:31], v[60:63], a[236:239], v[16:31]\n\t" \
  "ds_read_b64_tr_b16 v[52:53], v68 offset:36864\n\t" \
  "ds_read_b64_tr_b16 v[54:55], v68 offset:37888\n\t" \
  "v_exp_f32 v157, v157\n\t" \
  "v_add_f32 v156, 1.0, v156\n\t" \
  "v_add_f32 v157, 1.0, v157\n\t" \
  "s_waitcnt vmcnt(3) lgkmcnt(8)\n\t" \
  "v_mfma_f32_32x32x16_bf16 v[0:15], v[32:35], a[240:243], v[0:15]\n\t" \
  "ds_read_b64_tr_b16 v[56:57], v65 offset:38912\n\t" \
  "ds_read_b64_tr_b16 v[58:59], v65 offset:39936\n\t" \
  "v_rcp_f32 v156, v156\n\t" \
  "v_rcp_f32 v157, v157\n\t" \
  "v_mul_f32 v94, v94, v156\n\t" \
  "v_mfma_f32_32x32x16_bf16 v[16:31], v[36:39], a[240:243], v[16:31]\n\t" \
  "ds_read_b64_tr_b16 v[60:61], v68 offset:38912\n\t" \
  "ds_read_b64_tr_b16 v[62:63], v68 offset:39936\n\t" \
  "v_mul_f32 v95, v95, v157\n\t" \
  "v_add_f32 v96, v96, v248\n\t" \
  "v_add_f32 v97, v97, v249\n\t" \
  "s_waitcnt vmcnt(2) lgkmcnt(8)\n\t" \
  "v_mfma_f32_32x32x16_bf16 v[0:15], v[40:43], a[244:247], v[0:15]\n\t" \
  "v_mul_f32 v156, 0xbfb8aa3b, v96\n\t" \
  "v_mul_f32 v157, 0xbfb8aa3b, v97\n\t" \
  "v_exp_f32 v156, v156\n\t" \
  "v_mfma_f32_32x32x16_bf16 v[16:31], v[44:47], a[244:247], v[16:31]\n\t" \
  "v_exp_f32 v157, v157\n\t" \
  "v_add_f32 v156, 1.0, v156\n\t" \
  "v_add_f32 v157, 1.0, v157\n\t" \
  "s_waitcnt vmcnt(1) lgkmcnt(4)\n\t" \
  "v_mfma_f32_32x32x16_bf16 v[0:15], v[48:51], a[248:251], v[0:15]\n\t" \
  "v_rcp_f32 v156, v156\n\t" \
  "v_rcp_f32 v157, v157\n\t" \
  "v_mul_f32 v96, v96, v156\n\t" \
  "v_mfma_f32_32x32x16_bf16 v[16:31], v[52:55], a[248:251], v[16:31]\n\t" \
  "v_mul_f32 v97, v97, v157\n\t" \
  "v_add_f32 v98, v98, v250\n\t" \
  "v_add_f32 v99, v99, v251\n\t" \
  "s_waitcnt vmcnt(0) lgkmcnt(0)\n\t" \
  "v_mfma_f32_32x32x16_bf16 v[0:15], v[56:59], a[252:255], v[0:15]\n\t" \
  "v_mul_f32 v156, 0xbfb8aa3b, v98\n\t" \
  "v_mul_f32 v157, 0xbfb8aa3b, v99\n\t" \
  "v_exp_f32 v156, v156\n\t" \
  "v_mfma_f32_32x32x16_bf16 v[16:31], v[60:63], a[252:255], v[16:31]\n\t" \
  "v_exp_f32 v157, v157\n\t" \
  "v_add_f32 v156, 1.0, v156\n\t" \
  "v_add_f32 v157, 1.0, v157\n\t" \
  "v_rcp_f32 v156, v156\n\t" \
  "v_rcp_f32 v157, v157\n\t" \
  "v_mul_f32 v98, v98, v156\n\t" \
  "v_mul_f32 v99, v99, v157\n\t" \
  "v_add_f32 v100, v100, v252\n\t" \
  "v_add_f32 v101, v101, v253\n\t" \
  "v_mul_f32 v156, 0xbfb8aa3b, v100\n\t" \
  "v_mul_f32 v157, 0xbfb8aa3b, v101\n\t" \
  "v_exp_f32 v156, v156\n\t" \
  "v_exp_f32 v157, v157\n\t" \
  "v_add_f32 v156, 1.0, v156\n\t" \
  "v_add_f32 v157, 1.0, v157\n\t" \
  "v_rcp_f32 v156, v156\n\t" \
  "v_rcp_f32 v157, v157\n\t" \
  "v_mul_f32 v100, v100, v156\n\t" \
  "v_mul_f32 v101, v101, v157\n\t" \
  "v_add_f32 v102, v102, v254\n\t" \
  "v_add_f32 v103, v103, v255\n\t" \
  "v_mul_f32 v156, 0xbfb8aa3b, v102\n\t" \
  "v_mul_f32 v157, 0xbfb8aa3b, v103\n\t" \
  "v_exp_f32 v156, v156\n\t" \
  "v_exp_f32 v157, v157\n\t" \
  "v_add_f32 v156, 1.0, v156\n\t" \
  "v_add_f32 v157, 1.0, v157\n\t" \
  "v_rcp_f32 v156, v156\n\t" \
  "v_rcp_f32 v157, v157\n\t" \
  "v_mul_f32 v102, v102, v156\n\t" \
  "v_mul_f32 v103, v103, v157\n\t" \
  "v_permlane32_swap_b32 v160, v162\n\t" \
  "v_permlane32_swap_b32 v161, v163\n\t" \
  "v_permlane32_swap_b32 v164, v166\n\t" \
  "v_permlane32_swap_b32 v165, v167\n\t" \
  "v_permlane32_swap_b32 v168, v170\n\t" \
  "v_permlane32_swap_b32 v169, v171\n\t" \
  "v_permlane32_swap_b32 v172, v174\n\t" \
  "v_permlane32_swap_b32 v173, v175\n\t" \
  "v_and_b32 v191, 0xffff0000, v175\n\t" \
  "v_lshlrev_b32 v190, 16, v175\n\t" \
  "v_and_b32 v189, 0xffff0000, v174\n\t" \
  "v_lshlrev_b32 v188, 16, v174\n\t" \
  "v_and_b32 v187, 0xffff0000, v173\n\t" \
  "v_lshlrev_b32 v186, 16, v173\n\t" \
  "v_and_b32 v185, 0xffff0000, v172\n\t" \
  "v_lshlrev_b32 v184, 16, v172\n\t" \
  "v_and_b32 v183, 0xffff0000, v171\n\t" \
  "v_lshlrev_b32 v182, 16, v171\n\t" \
  "v_and_b32 v181, 0xffff0000, v170\n\t" \
  "v_lshlrev_b32 v180, 16, v170\n\t" \
  "v_and_b32 v179, 0xffff0000, v169\n\t" \
  "v_lshlrev_b32 v178, 16, v169\n\t" \
  "v_and_b32 v177, 0xffff0000, v168\n\t" \
  "v_lshlrev_b32 v176, 16, v168\n\t" \
  "v_and_b32 v175, 0xffff0000, v167\n\t" \
  "v_lshlrev_b32 v174, 16, v167\n\t" \
  "v_and_b32 v173, 0xffff0000, v166\n\t" \
  "v_lshlrev_b32 v172, 16, v166\n\t" \
  "v_and_b32 v171, 0xffff0000, v165\n\t" \
  "v_lshlrev_b32 v170, 16, v165\n\t" \
  "v_and_b32 v169, 0xffff0000, v164\n\t" \
  "v_lshlrev_b32 v168, 16, v164\n\t" \
  "v_and_b32 v167, 0xffff0000, v163\n\t" \
  "v_lshlrev_b32 v166, 16, v163\n\t" \
  "v_and_b32 v165, 0xffff0000, v162\n\t" \
  "v_lshlrev_b32 v164, 16, v162\n\t" \
  "v_and_b32 v163, 0xffff0000, v161\n\t" \
  "v_lshlrev_b32 v162, 16, v161\n\t" \
  "v_and_b32 v161, 0xffff0000, v160\n\t" \
  "v_lshlrev_b32 v160, 16, v160\n\t" \
  "s_nop 15\n\t" \
  "s_nop 3\n\t"
#define P1_ASM \
  "s_waitcnt lgkmcnt(0)\n\t" \
  "s_mov_b32 s64, 0\n\t" \
  "buffer_load_dwordx4 a[0:3], v178, s[80:83], s64 offen offset:0 sc1\n\t" \
  "buffer_load_dwordx4 a[16:19], v178, s[80:83], s64 offen offset:1024 sc1\n\t" \
  "buffer_load_dwordx4 a[32:35], v178, s[80:83], s64 offen offset:2048 sc1\n\t" \
  "buffer_load_dwordx4 a[48:51], v178, s[80:83], s64 offen offset:3072 sc1\n\t" \
  "buffer_load_dwordx4 a[4:7], v179, s[80:83], s64 offen offset:0 sc1\n\t" \
  "buffer_load_dwordx4 a[20:23], v179, s[80:83], s64 offen offset:1024 sc1\n\t" \
  "buffer_load_dwordx4 a[36:39], v179, s[80:83], s64 offen offset:2048 sc1\n\t" \
  "buffer_load_dwordx4 a[52:55], v179, s[80:83], s64 offen offset:3072 sc1\n\t" \
  "buffer_load_dwordx4 a[8:11], v180, s[80:83], s64 offen offset:0 sc1\n\t" \
  "buffer_load_dwordx4 a[24:27], v180, s[80:83], s64 offen offset:1024 sc1\n\t" \
  "buffer_load_dwordx4 a[40:43], v180, s[80:83], s64 offen offset:2048 sc1\n\t" \
  "buffer_load_dwordx4 a[56:59], v180, s[80:83], s64 offen offset:3072 sc1\n\t" \
  "buffer_load_dwordx4 a[12:15], v181, s[80:83], s64 offen offset:0 sc1\n\t" \
  "buffer_load_dwordx4 a[28:31], v181, s[80:83], s64 offen offset:1024 sc1\n\t" \
  "buffer_load_dwordx4 a[44:47], v181, s[80:83], s64 offen offset:2048 sc1\n\t" \
  "buffer_load_dwordx4 a[60:63], v181, s[80:83], s64 offen offset:3072 sc1\n\t" \
  "s_barrier\n\t" \
  "ds_read_b64_tr_b16 v[144:145], v171 offset:0\n\t" \
  "ds_read_b64_tr_b16 v[146:147], v171 offset:1792\n\t" \
  "ds_read_b64_tr_b16 v[148:149], v171 offset:7168\n\t" \
  "ds_read_b64_tr_b16 v[150:151], v171 offset:8960\n\t" \
  "ds_read_b64_tr_b16 v[152:153], v171 offset:14336\n\t" \
  "ds_read_b64_tr_b16 v[154:155], v171 offset:16128\n\t" \
  "ds_read_b64_tr_b16 v[156:157], v171 offset:21504\n\t" \
  "ds_read_b64_tr_b16 v[158:159], v171 offset:23296\n\t" \
  "s_waitcnt lgkmcnt(0)\n\t" \
  "s_waitcnt vmcnt(12)\n\t" \
  "v_mfma_f32_16x16x32_bf16 v[32:35], v[144:147], a[0:3], v[224:227]\n\t" \
  "ds_read_b64_tr_b16 v[192:193], v171 offset:32\n\t" \
  "v_mfma_f32_16x16x32_bf16 v[32:35], v[148:151], a[16:19], v[32:35]\n\t" \
  "s_mov_b32 s64, 4096\n\t" \
  "buffer_load_dwordx4 a[64:67], v178, s[80:83], s64 offen offset:0 sc1\n\t" \
  "v_mfma_f32_16x16x32_bf16 v[32:35], v[152:155], a[32:35], v[32:35]\n\t" \
  "ds_read_b64_tr_b16 v[194:195], v171 offset:1824\n\t" \
  "v_mfma_f32_16x16x32_bf16 v[32:35], v[156:159], a[48:51], v[32:35]\n\t" \
  "ds_read_b64_tr_b16 v[196:197], v171 offset:7200\n\t" \
  "s_waitcnt vmcnt(9)\n\t" \
  "v_mfma_f32_16x16x32_bf16 v[36:39], v[144:147], a[4:7], v[224:227]\n\t" \
  "ds_read_b64_tr_b16 v[198:199], v171 offset:8992\n\t" \
  "v_mfma_f32_16x16x32_bf16 v[36:39], v[148:151], a[20:23], v[36:39]\n\t" \
  "buffer_load_dwordx4 a[80:83], v178, s[80:83], s64 offen offset:1024 sc1\n\t" \
  "v_mfma_f32_16x16x32_bf16 v[36:39], v[152:155], a[36:39], v[36:39]\n\t" \
  "ds_read_b64_tr_b16 v[200:201], v171 offset:14368\n\t" \
  "v_mfma_f32_16x16x32_bf16 v[36:39], v[156:159], a[52:55], v[36:39]\n\t" \
  "ds_read_b64_tr_b16 v[202:203], v171 offset:16160\n\t" \
  "s_waitcnt vmcnt(6)\n\t" \
  "v_mfma_f32_16x16x32_bf16 v[40:43], v[144:147], a[8:11], v[224:227]\n\t" \
  "ds_read_b64_tr_b16 v[204:205], v171 offset:21536\n\t" \
  "v_mfma_f32_16x16x32_bf16 v[40:43], v[148:151], a[24:27], v[40:43]\n\t" \
  "buffer_load_dwordx4 a[96:99], v178, s[80:83], s64 offen offset:2048 sc1\n\t" \
  "v_mfma_f32_16x16x32_bf16 v[40:43], v[152:155], a[40:43], v[40:43]\n\t" \
  "ds_read_b64_tr_b16 v[206:207], v171 offset:23328\n\t" \
  "v_mfma_f32_16x16x32_bf16 v[40:43], v[156:159], a[56:59], v[40:43]\n\t" \
  "s_waitcnt vmcnt(3)\n\t" \
  "v_mfma_f32_16x16x32_bf16 v[44:47], v[144:147], a[12:15], v[224:227]\n\t" \
  "v_mfma_f32_16x16x32_bf16 v[44:47], v[148:151], a[28:31], v[44:47]\n\t" \
  "buffer_load_dwordx4 a[112:115], v178, s[80:83], s64 offen offset:3072 sc1\n\t" \
  "v_mfma_f32_16x16x32_bf16 v[44:47], v[152:155], a[44:47], v[44:47]\n\t" \
  "v_mfma_f32_16x16x32_bf16 v[44:47], v[156:159], a[60:63], v[44:47]\n\t" \
  "s_waitcnt lgkmcnt(0)\n\t" \
  "v_mfma_f32_16x16x32_bf16 v[48:51], v[192:195], a[0:3], v[228:231]\n\t" \
  "ds_read_b64_tr_b16 v[144:145], v171 offset:64\n\t" \
  "v_mfma_f32_16x16x32_bf16 v[48:51], v[196:199], a[16:19], v[48:51]\n\t" \
  "buffer_load_dwordx4 a[68:71], v179, s[80:83], s64 offen offset:0 sc1\n\t" \
  "v_mfma_f32_16x16x32_bf16 v[48:51], v[200:203], a[32:35], v[48:51]\n\t" \
  "ds_read_b64_tr_b16 v[146:147], v171 offset:1856\n\t" \
  "v_mfma_f32_16x16x32_bf16 v[48:51], v[204:207], a[48:51], v[48:51]\n\t" \
  "ds_read_b64_tr_b16 v[148:149], v171 offset:7232\n\t" \
  "v_mfma_f32_16x16x32_bf16 v[52:55], v[192:195], a[4:7], v[228:231]\n\t" \
  "ds_read_b64_tr_b16 v[150:151], v171 offset:9024\n\t" \
  "v_mfma_f32_16x16x32_bf16 v[52:55], v[196:199], a[20:23], v[52:55]\n\t" \
  "buffer_load_dwordx4 a[84:87], v179, s[80:83], s64 offen offset:1024 sc1\n\t" \
  "v_mfma_f32_16x16x32_bf16 v[52:55], v[200:203], a[36:39], v[52:55]\n\t" \
  "ds_read_b64_tr_b16 v[152:153], v171 offset:14400\n\t" \
  "v_mfma_f32_16x16x32_bf16 v[52:55], v[204:207], a[52:55], v[52:55]\n\t" \
  "ds_read_b64_tr_b16 v[154:155], v171 offset:16192\n\t" \
  "v_mfma_f32_16x16x32_bf16 v[56:59], v[192:195], a[8:11], v[228:231]\n\t" \
  "ds_read_b64_tr_b16 v[156:157], v171 offset:21568\n\t" \
  "v_mfma_f32_16x16x32_bf16 v[56:59], v[196:199], a[24:27], v[56:59]\n\t" \
  "buffer_load_dwordx4 a[100:103], v179, s[80:83], s64 offen offset:2048 sc1\n\t" \
  "v_mfma_f32_16x16x32_bf16 v[56:59], v[200:203], a[40:43], v[56:59]\n\t" \
  "ds_read_b64_tr_b16 v[158:159], v171 offset:23360\n\t" \
  "v_mfma_f32_16x16x32_bf16 v[56:59], v[204:207], a[56:59], v[56:59]\n\t" \
  "v_mfma_f32_16x16x32_bf16 v[60:63], v[192:195], a[12:15], v[228:231]\n\t" \
  "v_mfma_f32_16x16x32_bf16 v[60:63], v[196:199], a[28:31], v[60:63]\n\t" \
  "buffer_load_dwordx4 a[116:119], v179, s[80:83], s64 offen offset:3072 sc1\n\t" \
  "v_mfma_f32_16x16x32_bf16 v[60:63], v[200:203], a[44:47], v[60:63]\n\t" \
  "v_mfma_f32_16x16x32_bf16 v[60:63], v[204:207], a[60:63], v[60:63]\n\t" \
  "s_waitcnt lgkmcnt(0)\n\t" \
  "v_mfma_f32_16x16x32_bf16 v[64:67], v[144:147], a[0:3], v[232:235]\n\t" \
  "ds_read_b64_tr_b16 v[192:193], v171 offset:96\n\t" \
  "v_mfma_f32_16x16x32_bf16 v[64:67], v[148:151], a[16:19], v[64:67]\n\t" \
  "buffer_load_dwordx4 a[72:75], v180, s[80:83], s64 offen offset:0 sc1\n\t" \
  "v_mfma_f32_16x16x32_bf16 v[64:67], v[152:155], a[32:35], v[64:67]\n\t" \
  "ds_read_b64_tr_b16 v[194:195], v171 offset:1888\n\t" \
  "v_mfma_f32_16x16x32_bf16 v[64:67], v[156:159], a[48:51], v[64:67]\n\t" \
  "ds_read_b64_tr_b16 v[196:197], v171 offset:7264\n\t" \
  "v_mfma_f32_16x16x32_bf16 v[68:71], v[144:147], a[4:7], v[232:235]\n\t" \
  "ds_read_b64_tr_b16 v[198:199], v171 offset:9056\n\t" \
  "v_mfma_f32_16x16x32_bf16 v[68:71], v[148:151], a[20:23], v[68:71]\n\t" \
  "buffer_load_dwordx4 a[88:91], v180, s[80:83], s64 offen offset:1024 sc1\n\t" \
  "v_mfma_f32_16x16x32_bf16 v[68:71], v[152:155], a[36:39], v[68:71]\n\t" \
  "ds_read_b64_tr_b16 v[200:201], v171 offset:14432\n\t" \
  "v_mfma_f32_16x16x32_bf16 v[68:71], v[156:159], a[52:55], v[68:71]\n\t" \
  "ds_read_b64_tr_b16 v[202:203], v171 offset:16224\n\t" \
  "v_mfma_f32_16x16x32_bf16 v[72:75], v[144:147], a[8:11], v[232:235]\n\t" \
  "ds_read_b64_tr_b16 v[204:205], v171 offset:21600\n\t" \
  "v_mfma_f32_16x16x32_bf16 v[72:75], v[148:151], a[24:27], v[72:75]\n\t" \
  "buffer_load_dwordx4 a[104:107], v180, s[80:83], s64 offen offset:2048 sc1\n\t" \
  "v_mfma_f32_16x16x32_bf16 v[72:75], v[152:155], a[40:43], v[72:75]\n\t" \
  "ds_read_b64_tr_b16 v[206:207], v171 offset:23392\n\t" \
  "v_mfma_f32_16x16x32_bf16 v[72:75], v[156:159], a[56:59], v[72:75]\n\t" \
  "v_mfma_f32_16x16x32_bf16 v[76:79], v[144:147], a[12:15], v[232:235]\n\t" \
  "v_mfma_f32_16x16x32_bf16 v[76:79], v[148:151], a[28:31], v[76:79]\n\t" \
  "buffer_load_dwordx4 a[120:123], v180, s[80:83], s64 offen offset:3072 sc1\n\t" \
  "v_mfma_f32_16x16x32_bf16 v[76:79], v[152:155], a[44:47], v[76:79]\n\t" \
  "v_mfma_f32_16x16x32_bf16 v[76:79], v[156:159], a[60:63], v[76:79]\n\t" \
  "s_waitcnt lgkmcnt(0)\n\t" \
  "v_mfma_f32_16x16x32_bf16 v[80:83], v[192:195], a[0:3], v[236:239]\n\t" \
  "ds_read_b64_tr_b16 v[144:145], v171 offset:128\n\t" \
  "v_mfma_f32_16x16x32_bf16 v[80:83], v[196:199], a[16:19], v[80:83]\n\t" \
  "buffer_load_dwordx4 a[76:79], v181, s[80:83], s64 offen offset:0 sc1\n\t" \
  "v_mfma_f32_16x16x32_bf16 v[80:83], v[200:203], a[32:35], v[80:83]\n\t" \
  "ds_read_b64_tr_b16 v[146:147], v171 offset:1920\n\t" \
  "v_mfma_f32_16x16x32_bf16 v[80:83], v[204:207], a[48:51], v[80:83]\n\t" \
  "ds_read_b64_tr_b16 v[148:149], v171 offset:7296\n\t" \
  "v_mfma_f32_16x16x32_bf16 v[84:87], v[192:195], a[4:7], v[236:239]\n\t" \
  "ds_read_b64_tr_b16 v[150:151], v171 offset:9088\n\t" \
  "v_mfma_f32_16x16x32_bf16 v[84:87], v[196:199], a[20:23], v[84:87]\n\t" \
  "buffer_load_dwordx4 a[92:95], v181, s[80:83], s64 offen offset:1024 sc1\n\t" \
  "v_mfma_f32_16x16x32_bf16 v[84:87], v[200:203], a[36:39], v[84:87]\n\t" \
  "ds_read_b64_tr_b16 v[152:153], v171 offset:14464\n\t" \
  "v_mfma_f32_16x16x32_bf16 v[84:87], v[204:207], a[52:55], v[84:87]\n\t" \
  "ds_read_b64_tr_b16 v[154:155], v171 offset:16256\n\t" \
  "v_mfma_f32_16x16x32_bf16 v[88:91], v[192:195], a[8:11], v[236:239]\n\t" \
  "ds_read_b64_tr_b16 v[156:157], v171 offset:21632\n\t" \
  "v_mfma_f32_16x16x32_bf16 v[88:91], v[196:199], a[24:27], v[88:91]\n\t" \
  "buffer_load_dwordx4 a[108:111], v181, s[80:83], s64 offen offset:2048 sc1\n\t" \
  "v_mfma_f32_16x16x32_bf16 v[88:91], v[200:203], a[40:43], v[88:91]\n\t" \
  "ds_read_b64_tr_b16 v[158:159], v171 offset:23424\n\t" \
  "v_mfma_f32_16x16x32_bf16 v[88:91], v[204:207], a[56:59], v[88:91]\n\t" \
  "v_mfma_f32_16x16x32_bf16 v[92:95], v[192:195], a[12:15], v[236:239]\n\t" \
  "v_mfma_f32_16x16x32_bf16 v[92:95], v[196:199], a[28:31], v[92:95]\n\t" \
  "buffer_load_dwordx4 a[124:127], v181, s[80:83], s64 offen offset:3072 sc1\n\t" \
  "v_mfma_f32_16x16x32_bf16 v[92:95], v[200:203], a[44:47], v[92:95]\n\t" \
  "s_add_u32 m0, s73, 73728\n\t" \
  "s_mov_b32 s65, 524288\n\t" \
  "v_mfma_f32_16x16x32_bf16 v[92:95], v[204:207], a[60:63], v[92:95]\n\t" \
  "s_waitcnt lgkmcnt(0)\n\t" \
  "v_mfma_f32_16x16x32_bf16 v[96:99], v[144:147], a[0:3], v[240:243]\n\t" \
  "ds_read_b64_tr_b16 v[192:193], v171 offset:160\n\t" \
  "v_mfma_f32_16x16x32_bf16 v[96:99], v[148:151], a[16:19], v[96:99]\n\t" \
  "buffer_load_dwordx4 v160, s[88:91], s65 offen offset:0 lds\n\t" \
  "v_mfma_f32_16x16x32_bf16 v[96:99], v[152:155], a[32:35], v[96:99]\n\t" \
  "ds_read_b64_tr_b16 v[194:195], v171 offset:1952\n\t" \
  "v_mfma_f32_16x16x32_bf16 v[96:99], v[156:159], a[48:51], v[96:99]\n\t" \
  "ds_read_b64_tr_b16 v[196:197], v171 offset:7328\n\t" \
  "v_mfma_f32_16x16x32_bf16 v[100:103], v[144:147], a[4:7], v[240:243]\n\t" \
  "ds_read_b64_tr_b16 v[198:199], v171 offset:9120\n\t" \
  "v_mfma_f32_16x16x32_bf16 v[100:103], v[148:151], a[20:23], v[100:103]\n\t" \
  "buffer_load_dwordx4 v161, s[88:91], s65 offen offset:1024 lds\n\t" \
  "v_mfma_f32_16x16x32_bf16 v[100:103], v[152:155], a[36:39], v[100:103]\n\t" \
  "ds_read_b64_tr_b16 v[200:201], v171 offset:14496\n\t" \
  "v_mfma_f32_16x16x32_bf16 v[100:103], v[156:159], a[52:55], v[100:103]\n\t" \
  "ds_read_b64_tr_b16 v[202:203], v171 offset:16288\n\t" \
  "v_mfma_f32_16x16x32_bf16 v[104:107], v[144:147], a[8:11], v[240:243]\n\t" \
  "ds_read_b64_tr_b16 v[204:205], v171 offset:21664\n\t" \
  "v_mfma_f32_16x16x32_bf16 v[104:107], v[148:151], a[24:27], v[104:107]\n\t" \
  "s_add_u32 m0, s74, 73728\n\t" \
  "s_mov_b32 s66, 1835008\n\t" \
  "v_mfma_f32_16x16x32_bf16 v[104:107], v[152:155], a[40:43], v[104:107]\n\t" \
  "ds_read_b64_tr_b16 v[206:207], v171 offset:23456\n\t" \
  "v_mfma_f32_16x16x32_bf16 v[104:107], v[156:159], a[56:59], v[104:107]\n\t" \
  "v_mfma_f32_16x16x32_bf16 v[108:111], v[144:147], a[12:15], v[240:243]\n\t" \
  "v_mfma_f32_16x16x32_bf16 v[108:111], v[148:151], a[28:31], v[108:111]\n\t" \
  "buffer_load_dwordx4 v162, s[92:95], s66 offen offset:0 lds\n\t" \
  "v_mfma_f32_16x16x32_bf16 v[108:111], v[152:155], a[44:47], v[108:111]\n\t" \
  "v_mfma_f32_16x16x32_bf16 v[108:111], v[156:159], a[60:63], v[108:111]\n\t" \
  "s_waitcnt lgkmcnt(0)\n\t" \
  "v_mfma_f32_16x16x32_bf16 v[112:115], v[192:195], a[0:3], v[244:247]\n\t" \
  "ds_read_b64_tr_b16 v[144:145], v171 offset:192\n\t" \
  "v_mfma_f32_16x16x32_bf16 v[112:115], v[196:199], a[16:19], v[112:115]\n\t" \
  "buffer_load_dwordx4 v163, s[92:95], s66 offen offset:1024 lds\n\t" \
  "v_mfma_f32_16x16x32_bf16 v[112:115], v[200:203], a[32:35], v[112:115]\n\t" \
  "ds_read_b64_tr_b16 v[146:147], v171 offset:1984\n\t" \
  "v_mfma_f32_16x16x32_bf16 v[112:115], v[204:207], a[48:51], v[112:115]\n\t" \
  "ds_read_b64_tr_b16 v[148:149], v171 offset:7360\n\t" \
  "v_mfma_f32_16x16x32_bf16 v[116:119], v[192:195], a[4:7], v[244:247]\n\t" \
  "ds_read_b64_tr_b16 v[150:151], v171 offset:9152\n\t" \
  "v_mfma_f32_16x16x32_bf16 v[116:119], v[196:199], a[20:23], v[116:119]\n\t" \
  "buffer_load_dwordx4 v164, s[92:95], s66 offen offset:2048 lds\n\t" \
  "v_mfma_f32_16x16x32_bf16 v[116:119], v[200:203], a[36:39], v[116:119]\n\t" \
  "ds_read_b64_tr_b16 v[152:153], v171 offset:14528\n\t" \
  "v_mfma_f32_16x16x32_bf16 v[116:119], v[204:207], a[52:55], v[116:119]\n\t" \
  "ds_read_b64_tr_b16 v[154:155], v171 offset:16320\n\t" \
  "v_mfma_f32_16x16x32_bf16 v[120:123], v[192:195], a[8:11], v[244:247]\n\t" \
  "ds_read_b64_tr_b16 v[156:157], v171 offset:21696\n\t" \
  "v_mfma_f32_16x16x32_bf16 v[120:123], v[196:199], a[24:27], v[120:123]\n\t" \
  "buffer_load_dwordx4 v165, s[92:95], s66 offen offset:3072 lds\n\t" \
  "v_mfma_f32_16x16x32_bf16 v[120:123], v[200:203], a[40:43], v[120:123]\n\t" \
  "ds_read_b64_tr_b16 v[158:159], v171 offset:23488\n\t" \
  "v_mfma_f32_16x16x32_bf16 v[120:123], v[204:207], a[56:59], v[120:123]\n\t" \
  "s_add_u32 m0, s74, 77824\n\t" \
  "v_mfma_f32_16x16x32_bf16 v[124:127], v[192:195], a[12:15], v[244:247]\n\t" \
  "v_mfma_f32_16x16x32_bf16 v[124:127], v[196:199], a[28:31], v[124:127]\n\t" \
  "buffer_load_dwordx4 v166, s[92:95], s66 offen offset:0 lds\n\t" \
  "v_mfma_f32_16x16x32_bf16 v[124:127], v[200:203], a[44:47], v[124:127]\n\t" \
  "v_mfma_f32_16x16x32_bf16 v[124:127], v[204:207], a[60:63], v[124:127]\n\t" \
  "s_waitcnt lgkmcnt(0)\n\t" \
  "v_mfma_f32_16x16x32_bf16 v[208:211], v[144:147], a[0:3], v[248:251]\n\t" \
  "ds_read_b64_tr_b16 v[192:193], v169 offset:0\n\t" \
  "v_mfma_f32_16x16x32_bf16 v[208:211], v[148:151], a[16:19], v[208:211]\n\t" \
  "buffer_load_dwordx4 v167, s[92:95], s66 offen offset:1024 lds\n\t" \
  "v_mfma_f32_16x16x32_bf16 v[208:211], v[152:155], a[32:35], v[208:211]\n\t" \
  "ds_read_b64_tr_b16 v[194:195], v169 offset:512\n\t" \
  "v_mfma_f32_16x16x32_bf16 v[208:211], v[156:159], a[48:51], v[208:211]\n\t" \
  "ds_read_b64_tr_b16 v[196:197], v169 offset:2048\n\t" \
  "v_mfma_f32_16x16x32_bf16 v[212:215], v[144:147], a[4:7], v[248:251]\n\t" \
  "ds_read_b64_tr_b16 v[198:199], v169 offset:2560\n\t" \
  "v_mfma_f32_16x16x32_bf16 v[212:215], v[148:151], a[20:23], v[212:215]\n\t" \
  "buffer_load_dwordx4 v168, s[92:95], s66 offen offset:2048 lds\n\t" \
  "v_mfma_f32_16x16x32_bf16 v[212:215], v[152:155], a[36:39], v[212:215]\n\t" \
  "ds_read_b64_tr_b16 v[200:201], v169 offset:4096\n\t" \
  "v_mfma_f32_16x16x32_bf16 v[212:215], v[156:159], a[52:55], v[212:215]\n\t" \
  "ds_read_b64_tr_b16 v[202:203], v169 offset:4608\n\t" \
  "v_mfma_f32_16x16x32_bf16 v[216:219], v[144:147], a[8:11], v[248:251]\n\t" \
  "ds_read_b64_tr_b16 v[204:205], v169 offset:6144\n\t" \
  "v_mfma_f32_16x16x32_bf16 v[216:219], v[148:151], a[24:27], v[216:219]\n\t" \
  "s_mov_b32 m0, s75\n\t" \
  "v_mfma_f32_16x16x32_bf16 v[216:219], v[152:155], a[40:43], v[216:219]\n\t" \
  "ds_read_b64_tr_b16 v[206:207], v169 offset:6656\n\t" \
  "v_mfma_f32_16x16x32_bf16 v[216:219], v[156:159], a[56:59], v[216:219]\n\t" \
  "v_mfma_f32_16x16x32_bf16 v[220:223], v[144:147], a[12:15], v[248:251]\n\t" \
  "v_mfma_f32_16x16x32_bf16 v[220:223], v[148:151], a[28:31], v[220:223]\n\t" \
  "global_load_lds_dword v186, s[48:49]\n\t" \
  "v_mfma_f32_16x16x32_bf16 v[220:223], v[152:155], a[44:47], v[220:223]\n\t" \
  "v_mfma_f32_16x16x32_bf16 v[220:223], v[156:159], a[60:63], v[220:223]\n\t" \
  "s_waitcnt lgkmcnt(0)\n\t" \
  "v_mfma_f32_16x16x32_bf16 v[0:3], v[192:195], a[0:3], 0\n\t" \
  "ds_read_b64_tr_b16 v[144:145], v170 offset:0\n\t" \
  "v_mfma_f32_16x16x32_bf16 v[0:3], v[196:199], a[16:19], v[0:3]\n\t" \
  "v_mfma_f32_16x16x32_bf16 v[0:3], v[200:203], a[32:35], v[0:3]\n\t" \
  "ds_read_b64_tr_b16 v[146:147], v170 offset:512\n\t" \
  "v_mfma_f32_16x16x32_bf16 v[0:3], v[204:207], a[48:51], v[0:3]\n\t" \
  "ds_read_b64_tr_b16 v[148:149], v170 offset:2048\n\t" \
  "v_mfma_f32_16x16x32_bf16 v[4:7], v[192:195], a[4:7], 0\n\t" \
  "ds_read_b64_tr_b16 v[150:151], v170 offset:2560\n\t" \
  "v_mfma_f32_16x16x32_bf16 v[4:7], v[196:199], a[20:23], v[4:7]\n\t" \
  "v_mfma_f32_16x16x32_bf16 v[4:7], v[200:203], a[36:39], v[4:7]\n\t" \
  "ds_read_b64_tr_b16 v[152:153], v170 offset:4096\n\t" \
  "v_mfma_f32_16x16x32_bf16 v[4:7], v[204:207], a[52:55], v[4:7]\n\t" \
  "ds_read_b64_tr_b16 v[154:155], v170 offset:4608\n\t" \
  "v_mfma_f32_16x16x32_bf16 v[8:11], v[192:195], a[8:11], 0\n\t" \
  "ds_read_b64_tr_b16 v[156:157], v170 offset:6144\n\t" \
  "v_mfma_f32_16x16x32_bf16 v[8:11], v[196:199], a[24:27], v[8:11]\n\t" \
  "v_mfma_f32_16x16x32_bf16 v[8:11], v[200:203], a[40:43], v[8:11]\n\t" \
  "ds_read_b64_tr_b16 v[158:159], v170 offset:6656\n\t" \
  "v_mfma_f32_16x16x32_bf16 v[8:11], v[204:207], a[56:59], v[8:11]\n\t" \
  "v_mfma_f32_16x16x32_bf16 v[12:15], v[192:195], a[12:15], 0\n\t" \
  "v_mfma_f32_16x16x32_bf16 v[12:15], v[196:199], a[28:31], v[12:15]\n\t" \
  "v_mfma_f32_16x16x32_bf16 v[12:15], v[200:203], a[44:47], v[12:15]\n\t" \
  "v_mfma_f32_16x16x32_bf16 v[12:15], v[204:207], a[60:63], v[12:15]\n\t" \
  "s_waitcnt lgkmcnt(0)\n\t" \
  "s_barrier\n\t" \
  "v_mfma_f32_16x16x32_bf16 v[16:19], v[144:147], a[0:3], 0\n\t" \
  "ds_read_b64_tr_b16 v[192:193], v174 offset:0\n\t" \
  "v_mfma_f32_16x16x32_bf16 v[16:19], v[148:151], a[16:19], v[16:19]\n\t" \
  "v_mfma_f32_16x16x32_bf16 v[16:19], v[152:155], a[32:35], v[16:19]\n\t" \
  "ds_read_b64_tr_b16 v[194:195], v174 offset:1792\n\t" \
  "v_mfma_f32_16x16x32_bf16 v[16:19], v[156:159], a[48:51], v[16:19]\n\t" \
  "ds_read_b64_tr_b16 v[196:197], v174 offset:7168\n\t" \
  "v_mfma_f32_16x16x32_bf16 v[20:23], v[144:147], a[4:7], 0\n\t" \
  "ds_read_b64_tr_b16 v[198:199], v174 offset:8960\n\t" \
  "v_mfma_f32_16x16x32_bf16 v[20:23], v[148:151], a[20:23], v[20:23]\n\t" \
  "v_mfma_f32_16x16x32_bf16 v[20:23], v[152:155], a[36:39], v[20:23]\n\t" \
  "ds_read_b64_tr_b16 v[200:201], v174 offset:14336\n\t" \
  "v_mfma_f32_16x16x32_bf16 v[20:23], v[156:159], a[52:55], v[20:23]\n\t" \
  "ds_read_b64_tr_b16 v[202:203], v174 offset:16128\n\t" \
  "v_mfma_f32_16x16x32_bf16 v[24:27], v[144:147], a[8:11], 0\n\t" \
  "ds_read_b64_tr_b16 v[204:205], v174 offset:21504\n\t" \
  "v_mfma_f32_16x16x32_bf16 v[24:27], v[148:151], a[24:27], v[24:27]\n\t" \
  "v_mfma_f32_16x16x32_bf16 v[24:27], v[152:155], a[40:43], v[24:27]\n\t" \
  "ds_read_b64_tr_b16 v[206:207], v174 offset:23296\n\t" \
  "v_mfma_f32_16x16x32_bf16 v[24:27], v[156:159], a[56:59], v[24:27]\n\t" \
  "v_mfma_f32_16x16x32_bf16 v[28:31], v[144:147], a[12:15], 0\n\t" \
  "v_mfma_f32_16x16x32_bf16 v[28:31], v[148:151], a[28:31], v[28:31]\n\t" \
  "v_mfma_f32_16x16x32_bf16 v[28:31], v[152:155], a[44:47], v[28:31]\n\t" \
  "v_mfma_f32_16x16x32_bf16 v[28:31], v[156:159], a[60:63], v[28:31]\n\t" \
  "s_waitcnt lgkmcnt(0)\n\t" \
  "s_waitcnt vmcnt(22)\n\t" \
  "v_mfma_f32_16x16x32_bf16 v[32:35], v[192:195], a[64:67], v[32:35]\n\t" \
  "ds_read_b64_tr_b16 v[144:145], v174 offset:32\n\t" \
  "v_mfma_f32_16x16x32_bf16 v[32:35], v[196:199], a[80:83], v[32:35]\n\t" \
  "s_mov_b32 s64, 8192\n\t" \
  "buffer_load_dwordx4 a[0:3], v178, s[80:83], s64 offen offset:0 sc1\n\t" \
  "v_mfma_f32_16x16x32_bf16 v[32:35], v[200:203], a[96:99], v[32:35]\n\t" \
  "ds_read_b64_tr_b16 v[146:147], v174 offset:1824\n\t" \
  "v_mfma_f32_16x16x32_bf16 v[32:35], v[204:207], a[112:115], v[32:35]\n\t" \
  "ds_read_b64_tr_b16 v[148:149], v174 offset:7200\n\t" \
  "s_waitcnt vmcnt(19)\n\t" \
  "v_mfma_f32_16x16x32_bf16 v[36:39], v[192:195], a[68:71], v[36:39]\n\t" \
  "ds_read_b64_tr_b16 v[150:151], v174 offset:8992\n\t" \
  "v_mfma_f32_16x16x32_bf16 v[36:39], v[196:199], a[84:87], v[36:39]\n\t" \
  "buffer_load_dwordx4 a[16:19], v178, s[80:83], s64 offen offset:1024 sc1\n\t" \
  "v_mfma_f32_16x16x32_bf16 v[36:39], v[200:203], a[100:103], v[36:39]\n\t" \
  "ds_read_b64_tr_b16 v[152:153], v174 offset:14368\n\t" \
  "v_mfma_f32_16x16x32_bf16 v[36:39], v[204:207], a[116:119], v[36:39]\n\t" \
  "ds_read_b64_tr_b16 v[154:155], v174 offset:16160\n\t" \
  "s_waitcnt vmcnt(16)\n\t" \
  "v_mfma_f32_16x16x32_bf16 v[40:43], v[192:195], a[72:75], v[40:43]\n\t" \
  "ds_read_b64_tr_b16 v[156:157], v174 offset:21536\n\t" \
  "v_mfma_f32_16x16x32_bf16 v[40:43], v[196:199], a[88:91], v[40:43]\n\t" \
  "buffer_load_dwordx4 a[32:35], v178, s[80:83], s64 offen offset:2048 sc1\n\t" \
  "v_mfma_f32_16x16x32_bf16 v[40:43], v[200:203], a[104:107], v[40:43]\n\t" \
  "ds_read_b64_tr_b16 v[158:159], v174 offset:23328\n\t" \
  "v_mfma_f32_16x16x32_bf16 v[40:43], v[204:207], a[120:123], v[40:43]\n\t" \
  "s_waitcnt vmcnt(13)\n\t" \
  "v_mfma_f32_16x16x32_bf16 v[44:47], v[192:195], a[76:79], v[44:47]\n\t" \
  "v_mfma_f32_16x16x32_bf16 v[44:47], v[196:199], a[92:95], v[44:47]\n\t" \
  "buffer_load_dwordx4 a[48:51], v178, s[80:83], s64 offen offset:3072 sc1\n\t" \
  "v_mfma_f32_16x16x32_bf16 v[44:47], v[200:203], a[108:111], v[44:47]\n\t" \
  "v_mfma_f32_16x16x32_bf16 v[44:47], v[204:207], a[124:127], v[44:47]\n\t" \
  "s_waitcnt lgkmcnt(0)\n\t" \
  "v_mfma_f32_16x16x32_bf16 v[48:51], v[144:147], a[64:67], v[48:51]\n\t" \
  "ds_read_b64_tr_b16 v[192:193], v174 offset:64\n\t" \
  "v_mfma_f32_16x16x32_bf16 v[48:51], v[148:151], a[80:83], v[48:51]\n\t" \
  "buffer_load_dwordx4 a[4:7], v179, s[80:83], s64 offen offset:0 sc1\n\t" \
  "v_mfma_f32_16x16x32_bf16 v[48:51], v[152:155], a[96:99], v[48:51]\n\t" \
  "ds_read_b64_tr_b16 v[194:195], v174 offset:1856\n\t" \
  "v_mfma_f32_16x16x32_bf16 v[48:51], v[156:159], a[112:115], v[48:51]\n\t" \
  "ds_read_b64_tr_b16 v[196:197], v174 offset:7232\n\t" \
  "v_mfma_f32_16x16x32_bf16 v[52:55], v[144:147], a[68:71], v[52:55]\n\t" \
  "ds_read_b64_tr_b16 v[198:199], v174 offset:9024\n\t" \
  "v_mfma_f32_16x16x32_bf16 v[52:55], v[148:151], a[84:87], v[52:55]\n\t" \
  "buffer_load_dwordx4 a[20:23], v179, s[80:83], s64 offen offset:1024 sc1\n\t" \
  "v_mfma_f32_16x16x32_bf16 v[52:55], v[152:155], a[100:103], v[52:55]\n\t" \
  "ds_read_b64_tr_b16 v[200:201], v174 offset:14400\n\t" \
  "v_mfma_f32_16x16x32_bf16 v[52:55], v[156:159], a[116:119], v[52:55]\n\t" \
  "ds_read_b64_tr_b16 v[202:203], v174 offset:16192\n\t" \
  "v_mfma_f32_16x16x32_bf16 v[56:59], v[144:147], a[72:75], v[56:59]\n\t" \
  "ds_read_b64_tr_b16 v[204:205], v174 offset:21568\n\t" \
  "v_mfma_f32_16x16x32_bf16 v[56:59], v[148:151], a[88:91], v[56:59]\n\t" \
  "buffer_load_dwordx4 a[36:39], v179, s[80:83], s64 offen offset:2048 sc1\n\t" \
  "v_mfma_f32_16x16x32_bf16 v[56:59], v[152:155], a[104:107], v[56:59]\n\t" \
  "ds_read_b64_tr_b16 v[206:207], v174 offset:23360\n\t" \
  "v_mfma_f32_16x16x32_bf16 v[56:59], v[156:159], a[120:123], v[56:59]\n\t" \
  "v_mfma_f32_16x16x32_bf16 v[60:63], v[144:147], a[76:79], v[60:63]\n\t" \
  "v_mfma_f32_16x16x32_bf16 v[60:63], v[148:151], a[92:95], v[60:63]\n\t" \
  "buffer_load_dwordx4 a[52:55], v179, s[80:83], s64 offen offset:3072 sc1\n\t" \
  "v_mfma_f32_16x16x32_bf16 v[60:63], v[152:155], a[108:111], v[60:63]\n\t" \
  "v_mfma_f32_16x16x32_bf16 v[60:63], v[156:159], a[124:127], v[60:63]\n\t" \
  "s_waitcnt lgkmcnt(0)\n\t" \
  "v_mfma_f32_16x16x32_bf16 v[64:67], v[192:195], a[64:67], v[64:67]\n\t" \
  "ds_read_b64_tr_b16 v[144:145], v174 offset:96\n\t" \
  "v_mfma_f32_16x16x32_bf16 v[64:67], v[196:199], a[80:83], v[64:67]\n\t" \
  "buffer_load_dwordx4 a[8:11], v180, s[80:83], s64 offen offset:0 sc1\n\t" \
  "v_mfma_f32_16x16x32_bf16 v[64:67], v[200:203], a[96:99], v[64:67]\n\t" \
  "ds_read_b64_tr_b16 v[146:147], v174 offset:1888\n\t" \
  "v_mfma_f32_16x16x32_bf16 v[64:67], v[204:207], a[112:115], v[64:67]\n\t" \
  "ds_read_b64_tr_b16 v[148:149], v174 offset:7264\n\t" \
  "v_mfma_f32_16x16x32_bf16 v[68:71], v[192:195], a[68:71], v[68:71]\n\t" \
  "ds_read_b64_tr_b16 v[150:151], v174 offset:9056\n\t" \
  "v_mfma_f32_16x16x32_bf16 v[68:71], v[196:199], a[84:87], v[68:71]\n\t" \
  "buffer_load_dwordx4 a[24:27], v180, s[80:83], s64 offen offset:1024 sc1\n\t" \
  "v_mfma_f32_16x16x32_bf16 v[68:71], v[200:203], a[100:103], v[68:71]\n\t" \
  "ds_read_b64_tr_b16 v[152:153], v174 offset:14432\n\t" \
  "v_mfma_f32_16x16x32_bf16 v[68:71], v[204:207], a[116:119], v[68:71]\n\t" \
  "ds_read_b64_tr_b16 v[154:155], v174 offset:16224\n\t" \
  "v_mfma_f32_16x16x32_bf16 v[72:75], v[192:195], a[72:75], v[72:75]\n\t" \
  "ds_read_b64_tr_b16 v[156:157], v174 offset:21600\n\t" \
  "v_mfma_f32_16x16x32_bf16 v[72:75], v[196:199], a[88:91], v[72:75]\n\t" \
  "buffer_load_dwordx4 a[40:43], v180, s[80:83], s64 offen offset:2048 sc1\n\t" \
  "v_mfma_f32_16x16x32_bf16 v[72:75], v[200:203], a[104:107], v[72:75]\n\t" \
  "ds_read_b64_tr_b16 v[158:159], v174 offset:23392\n\t" \
  "v_mfma_f32_16x16x32_bf16 v[72:75], v[204:207], a[120:123], v[72:75]\n\t" \
  "v_mfma_f32_16x16x32_bf16 v[76:79], v[192:195], a[76:79], v[76:79]\n\t" \
  "v_mfma_f32_16x16x32_bf16 v[76:79], v[196:199], a[92:95], v[76:79]\n\t" \
  "buffer_load_dwordx4 a[56:59], v180, s[80:83], s64 offen offset:3072 sc1\n\t" \
  "v_mfma_f32_16x16x32_bf16 v[76:79], v[200:203], a[108:111], v[76:79]\n\t" \
  "v_mfma_f32_16x16x32_bf16 v[76:79], v[204:207], a[124:127], v[76:79]\n\t" \
  "s_waitcnt lgkmcnt(0)\n\t" \
  "v_mfma_f32_16x16x32_bf16 v[80:83], v[144:147], a[64:67], v[80:83]\n\t" \
  "ds_read_b64_tr_b16 v[192:193], v174 offset:128\n\t" \
  "v_mfma_f32_16x16x32_bf16 v[80:83], v[148:151], a[80:83], v[80:83]\n\t" \
  "buffer_load_dwordx4 a[12:15], v181, s[80:83], s64 offen offset:0 sc1\n\t" \
  "v_mfma_f32_16x16x32_bf16 v[80:83], v[152:155], a[96:99], v[80:83]\n\t" \
  "ds_read_b64_tr_b16 v[194:195], v174 offset:1920\n\t" \
  "v_mfma_f32_16x16x32_bf16 v[80:83], v[156:159], a[112:115], v[80:83]\n\t" \
  "ds_read_b64_tr_b16 v[196:197], v174 offset:7296\n\t" \
  "v_mfma_f32_16x16x32_bf16 v[84:87], v[144:147], a[68:71], v[84:87]\n\t" \
  "ds_read_b64_tr_b16 v[198:199], v174 offset:9088\n\t" \
  "v_mfma_f32_16x16x32_bf16 v[84:87], v[148:151], a[84:87], v[84:87]\n\t" \
  "buffer_load_dwordx4 a[28:31], v181, s[80:83], s64 offen offset:1024 sc1\n\t" \
  "v_mfma_f32_16x16x32_bf16 v[84:87], v[152:155], a[100:103], v[84:87]\n\t" \
  "ds_read_b64_tr_b16 v[200:201], v174 offset:14464\n\t" \
  "v_mfma_f32_16x16x32_bf16 v[84:87], v[156:159], a[116:119], v[84:87]\n\t" \
  "ds_read_b64_tr_b16 v[202:203], v174 offset:16256\n\t" \
  "v_mfma_f32_16x16x32_bf16 v[88:91], v[144:147], a[72:75], v[88:91]\n\t" \
  "ds_read_b64_tr_b16 v[204:205], v174 offset:21632\n\t" \
  "v_mfma_f32_16x16x32_bf16 v[88:91], v[148:151], a[88:91], v[88:91]\n\t" \
  "buffer_load_dwordx4 a[44:47], v181, s[80:83], s64 offen offset:2048 sc1\n\t" \
  "v_mfma_f32_16x16x32_bf16 v[88:91], v[152:155], a[104:107], v[88:91]\n\t" \
  "ds_read_b64_tr_b16 v[206:207], v174 offset:23424\n\t" \
  "v_mfma_f32_16x16x32_bf16 v[88:91], v[156:159], a[120:123], v[88:91]\n\t" \
  "v_mfma_f32_16x16x32_bf16 v[92:95], v[144:147], a[76:79], v[92:95]\n\t" \
  "v_mfma_f32_16x16x32_bf16 v[92:95], v[148:151], a[92:95], v[92:95]\n\t" \
  "buffer_load_dwordx4 a[60:63], v181, s[80:83], s64 offen offset:3072 sc1\n\t" \
  "v_mfma_f32_16x16x32_bf16 v[92:95], v[152:155], a[108:111], v[92:95]\n\t" \
  "s_add_u32 m0, s73, 0\n\t" \
  "s_mov_b32 s65, 786432\n\t" \
  "v_mfma_f32_16x16x32_bf16 v[92:95], v[156:159], a[124:127], v[92:95]\n\t" \
  "s_waitcnt lgkmcnt(0)\n\t" \
  "v_mfma_f32_16x16x32_bf16 v[96:99], v[192:195], a[64:67], v[96:99]\n\t" \
  "ds_read_b64_tr_b16 v[144:145], v174 offset:160\n\t" \
  "v_mfma_f32_16x16x32_bf16 v[96:99], v[196:199], a[80:83], v[96:99]\n\t" \
  "buffer_load_dwordx4 v160, s[88:91], s65 offen offset:0 lds\n\t" \
  "v_mfma_f32_16x16x32_bf16 v[96:99], v[200:203], a[96:99], v[96:99]\n\t" \
  "ds_read_b64_tr_b16 v[146:147], v174 offset:1952\n\t" \
  "v_mfma_f32_16x16x32_bf16 v[96:99], v[204:207], a[112:115], v[96:99]\n\t" \
  "ds_read_b64_tr_b16 v[148:149], v174 offset:7328\n\t" \
  "v_mfma_f32_16x16x32_bf16 v[100:103], v[192:195], a[68:71], v[100:103]\n\t" \
  "ds_read_b64_tr_b16 v[150:151], v174 offset:9120\n\t" \
  "v_mfma_f32_16x16x32_bf16 v[100:103], v[196:199], a[84:87], v[100:103]\n\t" \
  "buffer_load_dwordx4 v161, s[88:91], s65 offen offset:1024 lds\n\t" \
  "v_mfma_f32_16x16x32_bf16 v[100:103], v[200:203], a[100:103], v[100:103]\n\t" \
  "ds_read_b64_tr_b16 v[152:153], v174 offset:14496\n\t" \
  "v_mfma_f32_16x16x32_bf16 v[100:103], v[204:207], a[116:119], v[100:103]\n\t" \
  "ds_read_b64_tr_b16 v[154:155], v174 offset:16288\n\t" \
  "v_mfma_f32_16x16x32_bf16 v[104:107], v[192:195], a[72:75], v[104:107]\n\t" \
  "ds_read_b64_tr_b16 v[156:157], v174 offset:21664\n\t" \
  "v_mfma_f32_16x16x32_bf16 v[104:107], v[196:199], a[88:91], v[104:107]\n\t" \
  "s_add_u32 m0, s74, 0\n\t" \
  "s_mov_b32 s66, 2752512\n\t" \
  "v_mfma_f32_16x16x32_bf16 v[104:107], v[200:203], a[104:107], v[104:107]\n\t" \
  "ds_read_b64_tr_b16 v[158:159], v174 offset:23456\n\t" \
  "v_mfma_f32_16x16x32_bf16 v[104:107], v[204:207], a[120:123], v[104:107]\n\t" \
  "v_mfma_f32_16x16x32_bf16 v[108:111], v[192:195], a[76:79], v[108:111]\n\t" \
  "v_mfma_f32_16x16x32_bf16 v[108:111], v[196:199], a[92:95], v[108:111]\n\t" \
  "buffer_load_dwordx4 v162, s[92:95], s66 offen offset:0 lds\n\t" \
  "v_mfma_f32_16x16x32_bf16 v[108:111], v[200:203], a[108:111], v[108:111]\n\t" \
  "v_mfma_f32_16x16x32_bf16 v[108:111], v[204:207], a[124:127], v[108:111]\n\t" \
  "s_waitcnt lgkmcnt(0)\n\t" \
  "v_mfma_f32_16x16x32_bf16 v[112:115], v[144:147], a[64:67], v[112:115]\n\t" \
  "ds_read_b64_tr_b16 v[192:193], v174 offset:192\n\t" \
  "v_mfma_f32_16x16x32_bf16 v[112:115], v[148:151], a[80:83], v[112:115]\n\t" \
  "buffer_load_dwordx4 v163, s[92:95], s66 offen offset:1024 lds\n\t" \
  "v_mfma_f32_16x16x32_bf16 v[112:115], v[152:155], a[96:99], v[112:115]\n\t" \
  "ds_read_b64_tr_b16 v[194:195], v174 offset:1984\n\t" \
  "v_mfma_f32_16x16x32_bf16 v[112:115], v[156:159], a[112:115], v[112:115]\n\t" \
  "ds_read_b64_tr_b16 v[196:197], v174 offset:7360\n\t" \
  "v_mfma_f32_16x16x32_bf16 v[116:119], v[144:147], a[68:71], v[116:119]\n\t" \
  "ds_read_b64_tr_b16 v[198:199], v174 offset:9152\n\t" \
  "v_mfma_f32_16x16x32_bf16 v[116:119], v[148:151], a[84:87], v[116:119]\n\t" \
  "buffer_load_dwordx4 v164, s[92:95], s66 offen offset:2048 lds\n\t" \
  "v_mfma_f32_16x16x32_bf16 v[116:119], v[152:155], a[100:103], v[116:119]\n\t" \
  "ds_read_b64_tr_b16 v[200:201], v174 offset:14528\n\t" \
  "v_mfma_f32_16x16x32_bf16 v[116:119], v[156:159], a[116:119], v[116:119]\n\t" \
  "ds_read_b64_tr_b16 v[202:203], v174 offset:16320\n\t" \
  "v_mfma_f32_16x16x32_bf16 v[120:123], v[144:147], a[72:75], v[120:123]\n\t" \
  "ds_read_b64_tr_b16 v[204:205], v174 offset:21696\n\t" \
  "v_mfma_f32_16x16x32_bf16 v[120:123], v[148:151], a[88:91], v[120:123]\n\t" \
  "buffer_load_dwordx4 v165, s[92:95], s66 offen offset:3072 lds\n\t" \
  "v_mfma_f32_16x16x32_bf16 v[120:123], v[152:155], a[104:107], v[120:123]\n\t" \
  "ds_read_b64_tr_b16 v[206:207], v174 offset:23488\n\t" \
  "v_mfma_f32_16x16x32_bf16 v[120:123], v[156:159], a[120:123], v[120:123]\n\t" \
  "s_add_u32 m0, s74, 4096\n\t" \
  "v_mfma_f32_16x16x32_bf16 v[124:127], v[144:147], a[76:79], v[124:127]\n\t" \
  "v_mfma_f32_16x16x32_bf16 v[124:127], v[148:151], a[92:95], v[124:127]\n\t" \
  "buffer_load_dwordx4 v166, s[92:95], s66 offen offset:0 lds\n\t" \
  "v_mfma_f32_16x16x32_bf16 v[124:127], v[152:155], a[108:111], v[124:127]\n\t" \
  "v_mfma_f32_16x16x32_bf16 v[124:127], v[156:159], a[124:127], v[124:127]\n\t" \
  "s_waitcnt lgkmcnt(0)\n\t" \
  "v_mfma_f32_16x16x32_bf16 v[208:211], v[192:195], a[64:67], v[208:211]\n\t" \
  "ds_read_b64_tr_b16 v[144:145], v172 offset:0\n\t" \
  "v_mfma_f32_16x16x32_bf16 v[208:211], v[196:199], a[80:83], v[208:211]\n\t" \
  "buffer_load_dwordx4 v167, s[92:95], s66 offen offset:1024 lds\n\t" \
  "v_mfma_f32_16x16x32_bf16 v[208:211], v[200:203], a[96:99], v[208:211]\n\t" \
  "ds_read_b64_tr_b16 v[146:147], v172 offset:512\n\t" \
  "v_mfma_f32_16x16x32_bf16 v[208:211], v[204:207], a[112:115], v[208:211]\n\t" \
  "ds_read_b64_tr_b16 v[148:149], v172 offset:2048\n\t" \
  "v_mfma_f32_16x16x32_bf16 v[212:215], v[192:195], a[68:71], v[212:215]\n\t" \
  "ds_read_b64_tr_b16 v[150:151], v172 offset:2560\n\t" \
  "v_mfma_f32_16x16x32_bf16 v[212:215], v[196:199], a[84:87], v[212:215]\n\t" \
  "buffer_load_dwordx4 v168, s[92:95], s66 offen offset:2048 lds\n\t" \
  "v_mfma_f32_16x16x32_bf16 v[212:215], v[200:203], a[100:103], v[212:215]\n\t" \
  "ds_read_b64_tr_b16 v[152:153], v172 offset:4096\n\t" \
  "v_mfma_f32_16x16x32_bf16 v[212:215], v[204:207], a[116:119], v[212:215]\n\t" \
  "ds_read_b64_tr_b16 v[154:155], v172 offset:4608\n\t" \
  "v_mfma_f32_16x16x32_bf16 v[216:219], v[192:195], a[72:75], v[216:219]\n\t" \
  "ds_read_b64_tr_b16 v[156:157], v172 offset:6144\n\t" \
  "v_mfma_f32_16x16x32_bf16 v[216:219], v[196:199], a[88:91], v[216:219]\n\t" \
  "s_mov_b32 m0, s75\n\t" \
  "s_add_u32 s48, s48, s50\n\t" \
  "s_addc_u32 s49, s49, 0\n\t" \
  "v_mfma_f32_16x16x32_bf16 v[216:219], v[200:203], a[104:107], v[216:219]\n\t" \
  "ds_read_b64_tr_b16 v[158:159], v172 offset:6656\n\t" \
  "v_mfma_f32_16x16x32_bf16 v[216:219], v[204:207], a[120:123], v[216:219]\n\t" \
  "v_mfma_f32_16x16x32_bf16 v[220:223], v[192:195], a[76:79], v[220:223]\n\t" \
  "v_mfma_f32_16x16x32_bf16 v[220:223], v[196:199], a[92:95], v[220:223]\n\t" \
  "global_load_lds_dword v186, s[48:49]\n\t" \
  "v_mfma_f32_16x16x32_bf16 v[220:223], v[200:203], a[108:111], v[220:223]\n\t" \
  "v_mfma_f32_16x16x32_bf16 v[220:223], v[204:207], a[124:127], v[220:223]\n\t" \
  "s_waitcnt lgkmcnt(0)\n\t" \
  "v_mfma_f32_16x16x32_bf16 v[0:3], v[144:147], a[64:67], v[0:3]\n\t" \
  "ds_read_b64_tr_b16 v[192:193], v173 offset:0\n\t" \
  "v_mfma_f32_16x16x32_bf16 v[0:3], v[148:151], a[80:83], v[0:3]\n\t" \
  "v_mfma_f32_16x16x32_bf16 v[0:3], v[152:155], a[96:99], v[0:3]\n\t" \
  "ds_read_b64_tr_b16 v[194:195], v173 offset:512\n\t" \
  "v_mfma_f32_16x16x32_bf16 v[0:3], v[156:159], a[112:115], v[0:3]\n\t" \
  "ds_read_b64_tr_b16 v[196:197], v173 offset:2048\n\t" \
  "v_mfma_f32_16x16x32_bf16 v[4:7], v[144:147], a[68:71], v[4:7]\n\t" \
  "ds_read_b64_tr_b16 v[198:199], v173 offset:2560\n\t" \
  "v_mfma_f32_16x16x32_bf16 v[4:7], v[148:151], a[84:87], v[4:7]\n\t" \
  "v_mfma_f32_16x16x32_bf16 v[4:7], v[152:155], a[100:103], v[4:7]\n\t" \
  "ds_read_b64_tr_b16 v[200:201], v173 offset:4096\n\t" \
  "v_mfma_f32_16x16x32_bf16 v[4:7], v[156:159], a[116:119], v[4:7]\n\t" \
  "ds_read_b64_tr_b16 v[202:203], v173 offset:4608\n\t" \
  "v_mfma_f32_16x16x32_bf16 v[8:11], v[144:147], a[72:75], v[8:11]\n\t" \
  "ds_read_b64_tr_b16 v[204:205], v173 offset:6144\n\t" \
  "v_mfma_f32_16x16x32_bf16 v[8:11], v[148:151], a[88:91], v[8:11]\n\t" \
  "v_mfma_f32_16x16x32_bf16 v[8:11], v[152:155], a[104:107], v[8:11]\n\t" \
  "ds_read_b64_tr_b16 v[206:207], v173 offset:6656\n\t" \
  "v_mfma_f32_16x16x32_bf16 v[8:11], v[156:159], a[120:123], v[8:11]\n\t" \
  "v_mfma_f32_16x16x32_bf16 v[12:15], v[144:147], a[76:79], v[12:15]\n\t" \
  "v_mfma_f32_16x16x32_bf16 v[12:15], v[148:151], a[92:95], v[12:15]\n\t" \
  "v_mfma_f32_16x16x32_bf16 v[12:15], v[152:155], a[108:111], v[12:15]\n\t" \
  "v_mfma_f32_16x16x32_bf16 v[12:15], v[156:159], a[124:127], v[12:15]\n\t" \
  "s_waitcnt lgkmcnt(0)\n\t" \
  "s_waitcnt vmcnt(27)\n\t" \
  "s_barrier\n\t" \
  "v_mfma_f32_16x16x32_bf16 v[16:19], v[192:195], a[64:67], v[16:19]\n\t" \
  "ds_read_b64_tr_b16 v[144:145], v177 offset:0\n\t" \
  "v_mfma_f32_16x16x32_bf16 v[16:19], v[196:199], a[80:83], v[16:19]\n\t" \
  "v_mfma_f32_16x16x32_bf16 v[16:19], v[200:203], a[96:99], v[16:19]\n\t" \
  "ds_read_b64_tr_b16 v[146:147], v177 offset:1792\n\t" \
  "v_mfma_f32_16x16x32_bf16 v[16:19], v[204:207], a[112:115], v[16:19]\n\t" \
  "ds_read_b64_tr_b16 v[148:149], v177 offset:7168\n\t" \
  "v_mfma_f32_16x16x32_bf16 v[20:23], v[192:195], a[68:71], v[20:23]\n\t" \
  "ds_read_b64_tr_b16 v[150:151], v177 offset:8960\n\t" \
  "v_mfma_f32_16x16x32_bf16 v[20:23], v[196:199], a[84:87], v[20:23]\n\t" \
  "v_mfma_f32_16x16x32_bf16 v[20:23], v[200:203], a[100:103], v[20:23]\n\t" \
  "ds_read_b64_tr_b16 v[152:153], v177 offset:14336\n\t" \
  "v_mfma_f32_16x16x32_bf16 v[20:23], v[204:207], a[116:119], v[20:23]\n\t" \
  "ds_read_b64_tr_b16 v[154:155], v177 offset:16128\n\t" \
  "v_mfma_f32_16x16x32_bf16 v[24:27], v[192:195], a[72:75], v[24:27]\n\t" \
  "ds_read_b64_tr_b16 v[156:157], v177 offset:21504\n\t" \
  "v_mfma_f32_16x16x32_bf16 v[24:27], v[196:199], a[88:91], v[24:27]\n\t" \
  "v_mfma_f32_16x16x32_bf16 v[24:27], v[200:203], a[104:107], v[24:27]\n\t" \
  "ds_read_b64_tr_b16 v[158:159], v177 offset:23296\n\t" \
  "v_mfma_f32_16x16x32_bf16 v[24:27], v[204:207], a[120:123], v[24:27]\n\t" \
  "v_mfma_f32_16x16x32_bf16 v[28:31], v[192:195], a[76:79], v[28:31]\n\t" \
  "v_mfma_f32_16x16x32_bf16 v[28:31], v[196:199], a[92:95], v[28:31]\n\t" \
  "v_mfma_f32_16x16x32_bf16 v[28:31], v[200:203], a[108:111], v[28:31]\n\t" \
  "v_mfma_f32_16x16x32_bf16 v[28:31], v[204:207], a[124:127], v[28:31]\n\t" \
  "s_waitcnt lgkmcnt(0)\n\t" \
  "s_waitcnt vmcnt(22)\n\t" \
  "v_mfma_f32_16x16x32_bf16 v[32:35], v[144:147], a[0:3], v[32:35]\n\t" \
  "ds_read_b64_tr_b16 v[192:193], v177 offset:32\n\t" \
  "v_mfma_f32_16x16x32_bf16 v[32:35], v[148:151], a[16:19], v[32:35]\n\t" \
  "s_mov_b32 s64, 12288\n\t" \
  "buffer_load_dwordx4 a[64:67], v178, s[80:83], s64 offen offset:0 sc1\n\t" \
  "v_mfma_f32_16x16x32_bf16 v[32:35], v[152:155], a[32:35], v[32:35]\n\t" \
  "ds_read_b64_tr_b16 v[194:195], v177 offset:1824\n\t" \
  "v_mfma_f32_16x16x32_bf16 v[32:35], v[156:159], a[48:51], v[32:35]\n\t" \
  "ds_read_b64_tr_b16 v[196:197], v177 offset:7200\n\t" \
  "s_waitcnt vmcnt(19)\n\t" \
  "v_mfma_f32_16x16x32_bf16 v[36:39], v[144:147], a[4:7], v[36:39]\n\t" \
  "ds_read_b64_tr_b16 v[198:199], v177 offset:8992\n\t" \
  "v_mfma_f32_16x16x32_bf16 v[36:39], v[148:151], a[20:23], v[36:39]\n\t" \
  "buffer_load_dwordx4 a[80:83], v178, s[80:83], s64 offen offset:1024 sc1\n\t" \
  "v_mfma_f32_16x16x32_bf16 v[36:39], v[152:155], a[36:39], v[36:39]\n\t" \
  "ds_read_b64_tr_b16 v[200:201], v177 offset:14368\n\t" \
  "v_mfma_f32_16x16x32_bf16 v[36:39], v[156:159], a[52:55], v[36:39]\n\t" \
  "ds_read_b64_tr_b16 v[202:203], v177 offset:16160\n\t" \
  "s_waitcnt vmcnt(16)\n\t" \
  "v_mfma_f32_16x16x32_bf16 v[40:43], v[144:147], a[8:11], v[40:43]\n\t" \
  "ds_read_b64_tr_b16 v[204:205], v177 offset:21536\n\t" \
  "v_mfma_f32_16x16x32_bf16 v[40:43], v[148:151], a[24:27], v[40:43]\n\t" \
  "buffer_load_dwordx4 a[96:99], v178, s[80:83], s64 offen offset:2048 sc1\n\t" \
  "v_mfma_f32_16x16x32_bf16 v[40:43], v[152:155], a[40:43], v[40:43]\n\t" \
  "ds_read_b64_tr_b16 v[206:207], v177 offset:23328\n\t" \
  "v_mfma_f32_16x16x32_bf16 v[40:43], v[156:159], a[56:59], v[40:43]\n\t" \
  "s_waitcnt vmcnt(13)\n\t" \
  "v_mfma_f32_16x16x32_bf16 v[44:47], v[144:147], a[12:15], v[44:47]\n\t" \
  "v_mfma_f32_16x16x32_bf16 v[44:47], v[148:151], a[28:31], v[44:47]\n\t" \
  "buffer_load_dwordx4 a[112:115], v178, s[80:83], s64 offen offset:3072 sc1\n\t" \
  "v_mfma_f32_16x16x32_bf16 v[44:47], v[152:155], a[44:47], v[44:47]\n\t" \
  "v_mfma_f32_16x16x32_bf16 v[44:47], v[156:159], a[60:63], v[44:47]\n\t" \
  "s_waitcnt lgkmcnt(0)\n\t" \
  "v_mfma_f32_16x16x32_bf16 v[48:51], v[192:195], a[0:3], v[48:51]\n\t" \
  "ds_read_b64_tr_b16 v[144:145], v177 offset:64\n\t" \
  "v_mfma_f32_16x16x32_bf16 v[48:51], v[196:199], a[16:19], v[48:51]\n\t" \
  "buffer_load_dwordx4 a[68:71], v179, s[80:83], s64 offen offset:0 sc1\n\t" \
  "v_mfma_f32_16x16x32_bf16 v[48:51], v[200:203], a[32:35], v[48:51]\n\t" \
  "ds_read_b64_tr_b16 v[146:147], v177 offset:1856\n\t" \
  "v_mfma_f32_16x16x32_bf16 v[48:51], v[204:207], a[48:51], v[48:51]\n\t" \
  "ds_read_b64_tr_b16 v[148:149], v177 offset:7232\n\t" \
  "v_mfma_f32_16x16x32_bf16 v[52:55], v[192:195], a[4:7], v[52:55]\n\t" \
  "ds_read_b64_tr_b16 v[150:151], v177 offset:9024\n\t" \
  "v_mfma_f32_16x16x32_bf16 v[52:55], v[196:199], a[20:23], v[52:55]\n\t" \
  "buffer_load_dwordx4 a[84:87], v179, s[80:83], s64 offen offset:1024 sc1\n\t" \
  "v_mfma_f32_16x16x32_bf16 v[52:55], v[200:203], a[36:39], v[52:55]\n\t" \
  "ds_read_b64_tr_b16 v[152:153], v177 offset:14400\n\t" \
  "v_mfma_f32_16x16x32_bf16 v[52:55], v[204:207], a[52:55], v[52:55]\n\t" \
  "ds_read_b64_tr_b16 v[154:155], v177 offset:16192\n\t" \
  "v_mfma_f32_16x16x32_bf16 v[56:59], v[192:195], a[8:11], v[56:59]\n\t" \
  "ds_read_b64_tr_b16 v[156:157], v177 offset:21568\n\t" \
  "v_mfma_f32_16x16x32_bf16 v[56:59], v[196:199], a[24:27], v[56:59]\n\t" \
  "buffer_load_dwordx4 a[100:103], v179, s[80:83], s64 offen offset:2048 sc1\n\t" \
  "v_mfma_f32_16x16x32_bf16 v[56:59], v[200:203], a[40:43], v[56:59]\n\t" \
  "ds_read_b64_tr_b16 v[158:159], v177 offset:23360\n\t" \
  "v_mfma_f32_16x16x32_bf16 v[56:59], v[204:207], a[56:59], v[56:59]\n\t" \
  "v_mfma_f32_16x16x32_bf16 v[60:63], v[192:195], a[12:15], v[60:63]\n\t" \
  "v_mfma_f32_16x16x32_bf16 v[60:63], v[196:199], a[28:31], v[60:63]\n\t" \
  "buffer_load_dwordx4 a[116:119], v179, s[80:83], s64 offen offset:3072 sc1\n\t" \
  "v_mfma_f32_16x16x32_bf16 v[60:63], v[200:203], a[44:47], v[60:63]\n\t" \
  "v_mfma_f32_16x16x32_bf16 v[60:63], v[204:207], a[60:63], v[60:63]\n\t" \
  "s_waitcnt lgkmcnt(0)\n\t" \
  "v_mfma_f32_16x16x32_bf16 v[64:67], v[144:147], a[0:3], v[64:67]\n\t" \
  "ds_read_b64_tr_b16 v[192:193], v177 offset:96\n\t" \
  "v_mfma_f32_16x16x32_bf16 v[64:67], v[148:151], a[16:19], v[64:67]\n\t" \
  "buffer_load_dwordx4 a[72:75], v180, s[80:83], s64 offen offset:0 sc1\n\t" \
  "v_mfma_f32_16x16x32_bf16 v[64:67], v[152:155], a[32:35], v[64:67]\n\t" \
  "ds_read_b64_tr_b16 v[194:195], v177 offset:1888\n\t" \
  "v_mfma_f32_16x16x32_bf16 v[64:67], v[156:159], a[48:51], v[64:67]\n\t" \
  "ds_read_b64_tr_b16 v[196:197], v177 offset:7264\n\t" \
  "v_mfma_f32_16x16x32_bf16 v[68:71], v[144:147], a[4:7], v[68:71]\n\t" \
  "ds_read_b64_tr_b16 v[198:199], v177 offset:9056\n\t" \
  "v_mfma_f32_16x16x32_bf16 v[68:71], v[148:151], a[20:23], v[68:71]\n\t" \
  "buffer_load_dwordx4 a[88:91], v180, s[80:83], s64 offen offset:1024 sc1\n\t" \
  "v_mfma_f32_16x16x32_bf16 v[68:71], v[152:155], a[36:39], v[68:71]\n\t" \
  "ds_read_b64_tr_b16 v[200:201], v177 offset:14432\n\t" \
  "v_mfma_f32_16x16x32_bf16 v[68:71], v[156:159], a[52:55], v[68:71]\n\t" \
  "ds_read_b64_tr_b16 v[202:203], v177 offset:16224\n\t" \
  "v_mfma_f32_16x16x32_bf16 v[72:75], v[144:147], a[8:11], v[72:75]\n\t" \
  "ds_read_b64_tr_b16 v[204:205], v177 offset:21600\n\t" \
  "v_mfma_f32_16x16x32_bf16 v[72:75], v[148:151], a[24:27], v[72:75]\n\t" \
  "buffer_load_dwordx4 a[104:107], v180, s[80:83], s64 offen offset:2048 sc1\n\t" \
  "v_mfma_f32_16x16x32_bf16 v[72:75], v[152:155], a[40:43], v[72:75]\n\t" \
  "ds_read_b64_tr_b16 v[206:207], v177 offset:23392\n\t" \
  "v_mfma_f32_16x16x32_bf16 v[72:75], v[156:159], a[56:59], v[72:75]\n\t" \
  "v_mfma_f32_16x16x32_bf16 v[76:79], v[144:147], a[12:15], v[76:79]\n\t" \
  "v_mfma_f32_16x16x32_bf16 v[76:79], v[148:151], a[28:31], v[76:79]\n\t" \
  "buffer_load_dwordx4 a[120:123], v180, s[80:83], s64 offen offset:3072 sc1\n\t" \
  "v_mfma_f32_16x16x32_bf16 v[76:79], v[152:155], a[44:47], v[76:79]\n\t" \
  "v_mfma_f32_16x16x32_bf16 v[76:79], v[156:159], a[60:63], v[76:79]\n\t" \
  "s_waitcnt lgkmcnt(0)\n\t" \
  "v_mfma_f32_16x16x32_bf16 v[80:83], v[192:195], a[0:3], v[80:83]\n\t" \
  "ds_read_b64_tr_b16 v[144:145], v177 offset:128\n\t" \
  "v_mfma_f32_16x16x32_bf16 v[80:83], v[196:199], a[16:19], v[80:83]\n\t" \
  "buffer_load_dwordx4 a[76:79], v181, s[80:83], s64 offen offset:0 sc1\n\t" \
  "v_mfma_f32_16x16x32_bf16 v[80:83], v[200:203], a[32:35], v[80:83]\n\t" \
  "ds_read_b64_tr_b16 v[146:147], v177 offset:1920\n\t" \
  "v_mfma_f32_16x16x32_bf16 v[80:83], v[204:207], a[48:51], v[80:83]\n\t" \
  "ds_read_b64_tr_b16 v[148:149], v177 offset:7296\n\t" \
  "v_mfma_f32_16x16x32_bf16 v[84:87], v[192:195], a[4:7], v[84:87]\n\t" \
  "ds_read_b64_tr_b16 v[150:151], v177 offset:9088\n\t" \
  "v_mfma_f32_16x16x32_bf16 v[84:87], v[196:199], a[20:23], v[84:87]\n\t" \
  "buffer_load_dwordx4 a[92:95], v181, s[80:83], s64 offen offset:1024 sc1\n\t" \
  "v_mfma_f32_16x16x32_bf16 v[84:87], v[200:203], a[36:39], v[84:87]\n\t" \
  "ds_read_b64_tr_b16 v[152:153], v177 offset:14464\n\t" \
  "v_mfma_f32_16x16x32_bf16 v[84:87], v[204:207], a[52:55], v[84:87]\n\t" \
  "ds_read_b64_tr_b16 v[154:155], v177 offset:16256\n\t" \
  "v_mfma_f32_16x16x32_bf16 v[88:91], v[192:195], a[8:11], v[88:91]\n\t" \
  "ds_read_b64_tr_b16 v[156:157], v177 offset:21632\n\t" \
  "v_mfma_f32_16x16x32_bf16 v[88:91], v[196:199], a[24:27], v[88:91]\n\t" \
  "buffer_load_dwordx4 a[108:111], v181, s[80:83], s64 offen offset:2048 sc1\n\t" \
  "v_mfma_f32_16x16x32_bf16 v[88:91], v[200:203], a[40:43], v[88:91]\n\t" \
  "ds_read_b64_tr_b16 v[158:159], v177 offset:23424\n\t" \
  "v_mfma_f32_16x16x32_bf16 v[88:91], v[204:207], a[56:59], v[88:91]\n\t" \
  "v_mfma_f32_16x16x32_bf16 v[92:95], v[192:195], a[12:15], v[92:95]\n\t" \
  "v_mfma_f32_16x16x32_bf16 v[92:95], v[196:199], a[28:31], v[92:95]\n\t" \
  "buffer_load_dwordx4 a[124:127], v181, s[80:83], s64 offen offset:3072 sc1\n\t" \
  "v_mfma_f32_16x16x32_bf16 v[92:95], v[200:203], a[44:47], v[92:95]\n\t" \
  "s_add_u32 m0, s73, 36864\n\t" \
  "s_mov_b32 s65, 1048576\n\t" \
  "v_mfma_f32_16x16x32_bf16 v[92:95], v[204:207], a[60:63], v[92:95]\n\t" \
  "s_waitcnt lgkmcnt(0)\n\t" \
  "v_mfma_f32_16x16x32_bf16 v[96:99], v[144:147], a[0:3], v[96:99]\n\t" \
  "ds_read_b64_tr_b16 v[192:193], v177 offset:160\n\t" \
  "v_mfma_f32_16x16x32_bf16 v[96:99], v[148:151], a[16:19], v[96:99]\n\t" \
  "buffer_load_dwordx4 v160, s[88:91], s65 offen offset:0 lds\n\t" \
  "v_mfma_f32_16x16x32_bf16 v[96:99], v[152:155], a[32:35], v[96:99]\n\t" \
  "ds_read_b64_tr_b16 v[194:195], v177 offset:1952\n\t" \
  "v_mfma_f32_16x16x32_bf16 v[96:99], v[156:159], a[48:51], v[96:99]\n\t" \
  "ds_read_b64_tr_b16 v[196:197], v177 offset:7328\n\t" \
  "v_mfma_f32_16x16x32_bf16 v[100:103], v[144:147], a[4:7], v[100:103]\n\t" \
  "ds_read_b64_tr_b16 v[198:199], v177 offset:9120\n\t" \
  "v_mfma_f32_16x16x32_bf16 v[100:103], v[148:151], a[20:23], v[100:103]\n\t" \
  "buffer_load_dwordx4 v161, s[88:91], s65 offen offset:1024 lds\n\t" \
  "v_mfma_f32_16x16x32_bf16 v[100:103], v[152:155], a[36:39], v[100:103]\n\t" \
  "ds_read_b64_tr_b16 v[200:201], v177 offset:14496\n\t" \
  "v_mfma_f32_16x16x32_bf16 v[100:103], v[156:159], a[52:55], v[100:103]\n\t" \
  "ds_read_b64_tr_b16 v[202:203], v177 offset:16288\n\t" \
  "v_mfma_f32_16x16x32_bf16 v[104:107], v[144:147], a[8:11], v[104:107]\n\t" \
  "ds_read_b64_tr_b16 v[204:205], v177 offset:21664\n\t" \
  "v_mfma_f32_16x16x32_bf16 v[104:107], v[148:151], a[24:27], v[104:107]\n\t" \
  "s_add_u32 m0, s74, 36864\n\t" \
  "s_mov_b32 s66, 3670016\n\t" \
  "v_mfma_f32_16x16x32_bf16 v[104:107], v[152:155], a[40:43], v[104:107]\n\t" \
  "ds_read_b64_tr_b16 v[206:207], v177 offset:23456\n\t" \
  "v_mfma_f32_16x16x32_bf16 v[104:107], v[156:159], a[56:59], v[104:107]\n\t" \
  "v_mfma_f32_16x16x32_bf16 v[108:111], v[144:147], a[12:15], v[108:111]\n\t" \
  "v_mfma_f32_16x16x32_bf16 v[108:111], v[148:151], a[28:31], v[108:111]\n\t" \
  "buffer_load_dwordx4 v162, s[92:95], s66 offen offset:0 lds\n\t" \
  "v_mfma_f32_16x16x32_bf16 v[108:111], v[152:155], a[44:47], v[108:111]\n\t" \
  "v_mfma_f32_16x16x32_bf16 v[108:111], v[156:159], a[60:63], v[108:111]\n\t" \
  "s_waitcnt lgkmcnt(0)\n\t" \
  "v_mfma_f32_16x16x32_bf16 v[112:115], v[192:195], a[0:3], v[112:115]\n\t" \
  "ds_read_b64_tr_b16 v[144:145], v177 offset:192\n\t" \
  "v_mfma_f32_16x16x32_bf16 v[112:115], v[196:199], a[16:19], v[112:115]\n\t" \
  "buffer_load_dwordx4 v163, s[92:95], s66 offen offset:1024 lds\n\t" \
  "v_mfma_f32_16x16x32_bf16 v[112:115], v[200:203], a[32:35], v[112:115]\n\t" \
  "ds_read_b64_tr_b16 v[146:147], v177 offset:1984\n\t" \
  "v_mfma_f32_16x16x32_bf16 v[112:115], v[204:207], a[48:51], v[112:115]\n\t" \
  "ds_read_b64_tr_b16 v[148:149], v177 offset:7360\n\t" \
  "v_mfma_f32_16x16x32_bf16 v[116:119], v[192:195], a[4:7], v[116:119]\n\t" \
  "ds_read_b64_tr_b16 v[150:151], v177 offset:9152\n\t" \
  "v_mfma_f32_16x16x32_bf16 v[116:119], v[196:199], a[20:23], v[116:119]\n\t" \
  "buffer_load_dwordx4 v164, s[92:95], s66 offen offset:2048 lds\n\t" \
  "v_mfma_f32_16x16x32_bf16 v[116:119], v[200:203], a[36:39], v[116:119]\n\t" \
  "ds_read_b64_tr_b16 v[152:153], v177 offset:14528\n\t" \
  "v_mfma_f32_16x16x32_bf16 v[116:119], v[204:207], a[52:55], v[116:119]\n\t" \
  "ds_read_b64_tr_b16 v[154:155], v177 offset:16320\n\t" \
  "v_mfma_f32_16x16x32_bf16 v[120:123], v[192:195], a[8:11], v[120:123]\n\t" \
  "ds_read_b64_tr_b16 v[156:157], v177 offset:21696\n\t" \
  "v_mfma_f32_16x16x32_bf16 v[120:123], v[196:199], a[24:27], v[120:123]\n\t" \
  "buffer_load_dwordx4 v165, s[92:95], s66 offen offset:3072 lds\n\t" \
  "v_mfma_f32_16x16x32_bf16 v[120:123], v[200:203], a[40:43], v[120:123]\n\t" \
  "ds_read_b64_tr_b16 v[158:159], v177 offset:23488\n\t" \
  "v_mfma_f32_16x16x32_bf16 v[120:123], v[204:207], a[56:59], v[120:123]\n\t" \
  "s_add_u32 m0, s74, 40960\n\t" \
  "v_mfma_f32_16x16x32_bf16 v[124:127], v[192:195], a[12:15], v[124:127]\n\t" \
  "v_mfma_f32_16x16x32_bf16 v[124:127], v[196:199], a[28:31], v[124:127]\n\t" \
  "buffer_load_dwordx4 v166, s[92:95], s66 offen offset:0 lds\n\t" \
  "v_mfma_f32_16x16x32_bf16 v[124:127], v[200:203], a[44:47], v[124:127]\n\t" \
  "v_mfma_f32_16x16x32_bf16 v[124:127], v[204:207], a[60:63], v[124:127]\n\t" \
  "s_waitcnt lgkmcnt(0)\n\t" \
  "v_mfma_f32_16x16x32_bf16 v[208:211], v[144:147], a[0:3], v[208:211]\n\t" \
  "ds_read_b64_tr_b16 v[192:193], v175 offset:0\n\t" \
  "v_mfma_f32_16x16x32_bf16 v[208:211], v[148:151], a[16:19], v[208:211]\n\t" \
  "buffer_load_dwordx4 v167, s[92:95], s66 offen offset:1024 lds\n\t" \
  "v_mfma_f32_16x16x32_bf16 v[208:211], v[152:155], a[32:35], v[208:211]\n\t" \
  "ds_read_b64_tr_b16 v[194:195], v175 offset:512\n\t" \
  "v_mfma_f32_16x16x32_bf16 v[208:211], v[156:159], a[48:51], v[208:211]\n\t" \
  "ds_read_b64_tr_b16 v[196:197], v175 offset:2048\n\t" \
  "v_mfma_f32_16x16x32_bf16 v[212:215], v[144:147], a[4:7], v[212:215]\n\t" \
  "ds_read_b64_tr_b16 v[198:199], v175 offset:2560\n\t" \
  "v_mfma_f32_16x16x32_bf16 v[212:215], v[148:151], a[20:23], v[212:215]\n\t" \
  "buffer_load_dwordx4 v168, s[92:95], s66 offen offset:2048 lds\n\t" \
  "v_mfma_f32_16x16x32_bf16 v[212:215], v[152:155], a[36:39], v[212:215]\n\t" \
  "ds_read_b64_tr_b16 v[200:201], v175 offset:4096\n\t" \
  "v_mfma_f32_16x16x32_bf16 v[212:215], v[156:159], a[52:55], v[212:215]\n\t" \
  "ds_read_b64_tr_b16 v[202:203], v175 offset:4608\n\t" \
  "v_mfma_f32_16x16x32_bf16 v[216:219], v[144:147], a[8:11], v[216:219]\n\t" \
  "ds_read_b64_tr_b16 v[204:205], v175 offset:6144\n\t" \
  "v_mfma_f32_16x16x32_bf16 v[216:219], v[148:151], a[24:27], v[216:219]\n\t" \
  "s_mov_b32 m0, s75\n\t" \
  "s_add_u32 s48, s48, s50\n\t" \
  "s_addc_u32 s49, s49, 0\n\t" \
  "v_mfma_f32_16x16x32_bf16 v[216:219], v[152:155], a[40:43], v[216:219]\n\t" \
  "ds_read_b64_tr_b16 v[206:207], v175 offset:6656\n\t" \
  "v_mfma_f32_16x16x32_bf16 v[216:219], v[156:159], a[56:59], v[216:219]\n\t" \
  "v_mfma_f32_16x16x32_bf16 v[220:223], v[144:147], a[12:15], v[220:223]\n\t" \
  "v_mfma_f32_16x16x32_bf16 v[220:223], v[148:151], a[28:31], v[220:223]\n\t" \
  "global_load_lds_dword v186, s[48:49]\n\t" \
  "v_mfma_f32_16x16x32_bf16 v[220:223], v[152:155], a[44:47], v[220:223]\n\t" \
  "v_mfma_f32_16x16x32_bf16 v[220:223], v[156:159], a[60:63], v[220:223]\n\t" \
  "s_waitcnt lgkmcnt(0)\n\t" \
  "v_mfma_f32_16x16x32_bf16 v[0:3], v[192:195], a[0:3], v[0:3]\n\t" \
  "ds_read_b64_tr_b16 v[144:145], v176 offset:0\n\t" \
  "v_mfma_f32_16x16x32_bf16 v[0:3], v[196:199], a[16:19], v[0:3]\n\t" \
  "v_mfma_f32_16x16x32_bf16 v[0:3], v[200:203], a[32:35], v[0:3]\n\t" \
  "ds_read_b64_tr_b16 v[146:147], v176 offset:512\n\t" \
  "v_mfma_f32_16x16x32_bf16 v[0:3], v[204:207], a[48:51], v[0:3]\n\t" \
  "ds_read_b64_tr_b16 v[148:149], v176 offset:2048\n\t" \
  "v_mfma_f32_16x16x32_bf16 v[4:7], v[192:195], a[4:7], v[4:7]\n\t" \
  "ds_read_b64_tr_b16 v[150:151], v176 offset:2560\n\t" \
  "v_mfma_f32_16x16x32_bf16 v[4:7], v[196:199], a[20:23], v[4:7]\n\t" \
  "v_mfma_f32_16x16x32_bf16 v[4:7], v[200:203], a[36:39], v[4:7]\n\t" \
  "ds_read_b64_tr_b16 v[152:153], v176 offset:4096\n\t" \
  "v_mfma_f32_16x16x32_bf16 v[4:7], v[204:207], a[52:55], v[4:7]\n\t" \
  "ds_read_b64_tr_b16 v[154:155], v176 offset:4608\n\t" \
  "v_mfma_f32_16x16x32_bf16 v[8:11], v[192:195], a[8:11], v[8:11]\n\t" \
  "ds_read_b64_tr_b16 v[156:157], v176 offset:6144\n\t" \
  "v_mfma_f32_16x16x32_bf16 v[8:11], v[196:199], a[24:27], v[8:11]\n\t" \
  "v_mfma_f32_16x16x32_bf16 v[8:11], v[200:203], a[40:43], v[8:11]\n\t" \
  "ds_read_b64_tr_b16 v[158:159], v176 offset:6656\n\t" \
  "v_mfma_f32_16x16x32_bf16 v[8:11], v[204:207], a[56:59], v[8:11]\n\t" \
  "v_mfma_f32_16x16x32_bf16 v[12:15], v[192:195], a[12:15], v[12:15]\n\t" \
  "v_mfma_f32_16x16x32_bf16 v[12:15], v[196:199], a[28:31], v[12:15]\n\t" \
  "v_mfma_f32_16x16x32_bf16 v[12:15], v[200:203], a[44:47], v[12:15]\n\t" \
  "v_mfma_f32_16x16x32_bf16 v[12:15], v[204:207], a[60:63], v[12:15]\n\t" \
  "s_waitcnt lgkmcnt(0)\n\t" \
  "s_waitcnt vmcnt(27)\n\t" \
  "s_barrier\n\t" \
  "v_mfma_f32_16x16x32_bf16 v[16:19], v[144:147], a[0:3], v[16:19]\n\t" \
  "ds_read_b64_tr_b16 v[192:193], v171 offset:0\n\t" \
  "v_mfma_f32_16x16x32_bf16 v[16:19], v[148:151], a[16:19], v[16:19]\n\t" \
  "v_mfma_f32_16x16x32_bf16 v[16:19], v[152:155], a[32:35], v[16:19]\n\t" \
  "ds_read_b64_tr_b16 v[194:195], v171 offset:1792\n\t" \
  "v_mfma_f32_16x16x32_bf16 v[16:19], v[156:159], a[48:51], v[16:19]\n\t" \
  "ds_read_b64_tr_b16 v[196:197], v171 offset:7168\n\t" \
  "v_mfma_f32_16x16x32_bf16 v[20:23], v[144:147], a[4:7], v[20:23]\n\t" \
  "ds_read_b64_tr_b16 v[198:199], v171 offset:8960\n\t" \
  "v_mfma_f32_16x16x32_bf16 v[20:23], v[148:151], a[20:23], v[20:23]\n\t" \
  "v_mfma_f32_16x16x32_bf16 v[20:23], v[152:155], a[36:39], v[20:23]\n\t" \
  "ds_read_b64_tr_b16 v[200:201], v171 offset:14336\n\t" \
  "v_mfma_f32_16x16x32_bf16 v[20:23], v[156:159], a[52:55], v[20:23]\n\t" \
  "ds_read_b64_tr_b16 v[202:203], v171 offset:16128\n\t" \
  "v_mfma_f32_16x16x32_bf16 v[24:27], v[144:147], a[8:11], v[24:27]\n\t" \
  "ds_read_b64_tr_b16 v[204:205], v171 offset:21504\n\t" \
  "v_mfma_f32_16x16x32_bf16 v[24:27], v[148:151], a[24:27], v[24:27]\n\t" \
  "v_mfma_f32_16x16x32_bf16 v[24:27], v[152:155], a[40:43], v[24:27]\n\t" \
  "ds_read_b64_tr_b16 v[206:207], v171 offset:23296\n\t" \
  "v_mfma_f32_16x16x32_bf16 v[24:27], v[156:159], a[56:59], v[24:27]\n\t" \
  "v_mfma_f32_16x16x32_bf16 v[28:31], v[144:147], a[12:15], v[28:31]\n\t" \
  "v_mfma_f32_16x16x32_bf16 v[28:31], v[148:151], a[28:31], v[28:31]\n\t" \
  "v_mfma_f32_16x16x32_bf16 v[28:31], v[152:155], a[44:47], v[28:31]\n\t" \
  "v_mfma_f32_16x16x32_bf16 v[28:31], v[156:159], a[60:63], v[28:31]\n\t" \
  "s_waitcnt lgkmcnt(0)\n\t" \
  "s_waitcnt vmcnt(22)\n\t" \
  "v_mfma_f32_16x16x32_bf16 v[32:35], v[192:195], a[64:67], v[32:35]\n\t" \
  "ds_read_b64_tr_b16 v[144:145], v171 offset:32\n\t" \
  "v_mfma_f32_16x16x32_bf16 v[32:35], v[196:199], a[80:83], v[32:35]\n\t" \
  "s_mov_b32 s64, 16384\n\t" \
  "buffer_load_dwordx4 a[0:3], v178, s[80:83], s64 offen offset:0 sc1\n\t" \
  "v_mfma_f32_16x16x32_bf16 v[32:35], v[200:203], a[96:99], v[32:35]\n\t" \
  "ds_read_b64_tr_b16 v[146:147], v171 offset:1824\n\t" \
  "v_mfma_f32_16x16x32_bf16 v[32:35], v[204:207], a[112:115], v[32:35]\n\t" \
  "ds_read_b64_tr_b16 v[148:149], v171 offset:7200\n\t" \
  "s_waitcnt vmcnt(19)\n\t" \
  "v_mfma_f32_16x16x32_bf16 v[36:39], v[192:195], a[68:71], v[36:39]\n\t" \
  "ds_read_b64_tr_b16 v[150:151], v171 offset:8992\n\t" \
  "v_mfma_f32_16x16x32_bf16 v[36:39], v[196:199], a[84:87], v[36:39]\n\t" \
  "buffer_load_dwordx4 a[16:19], v178, s[80:83], s64 offen offset:1024 sc1\n\t" \
  "v_mfma_f32_16x16x32_bf16 v[36:39], v[200:203], a[100:103], v[36:39]\n\t" \
  "ds_read_b64_tr_b16 v[152:153], v171 offset:14368\n\t" \
  "v_mfma_f32_16x16x32_bf16 v[36:39], v[204:207], a[116:119], v[36:39]\n\t" \
  "ds_read_b64_tr_b16 v[154:155], v171 offset:16160\n\t" \
  "s_waitcnt vmcnt(16)\n\t" \
  "v_mfma_f32_16x16x32_bf16 v[40:43], v[192:195], a[72:75], v[40:43]\n\t" \
  "ds_read_b64_tr_b16 v[156:157], v171 offset:21536\n\t" \
  "v_mfma_f32_16x16x32_bf16 v[40:43], v[196:199], a[88:91], v[40:43]\n\t" \
  "buffer_load_dwordx4 a[32:35], v178, s[80:83], s64 offen offset:2048 sc1\n\t" \
  "v_mfma_f32_16x16x32_bf16 v[40:43], v[200:203], a[104:107], v[40:43]\n\t" \
  "ds_read_b64_tr_b16 v[158:159], v171 offset:23328\n\t" \
  "v_mfma_f32_16x16x32_bf16 v[40:43], v[204:207], a[120:123], v[40:43]\n\t" \
  "s_waitcnt vmcnt(13)\n\t" \
  "v_mfma_f32_16x16x32_bf16 v[44:47], v[192:195], a[76:79], v[44:47]\n\t" \
  "v_mfma_f32_16x16x32_bf16 v[44:47], v[196:199], a[92:95], v[44:47]\n\t" \
  "buffer_load_dwordx4 a[48:51], v178, s[80:83], s64 offen offset:3072 sc1\n\t" \
  "v_mfma_f32_16x16x32_bf16 v[44:47], v[200:203], a[108:111], v[44:47]\n\t" \
  "v_mfma_f32_16x16x32_bf16 v[44:47], v[204:207], a[124:127], v[44:47]\n\t" \
  "s_waitcnt lgkmcnt(0)\n\t" \
  "v_mfma_f32_16x16x32_bf16 v[48:51], v[144:147], a[64:67], v[48:51]\n\t" \
  "ds_read_b64_tr_b16 v[192:193], v171 offset:64\n\t" \
  "v_mfma_f32_16x16x32_bf16 v[48:51], v[148:151], a[80:83], v[48:51]\n\t" \
  "buffer_load_dwordx4 a[4:7], v179, s[80:83], s64 offen offset:0 sc1\n\t" \
  "v_mfma_f32_16x16x32_bf16 v[48:51], v[152:155], a[96:99], v[48:51]\n\t" \
  "ds_read_b64_tr_b16 v[194:195], v171 offset:1856\n\t" \
  "v_mfma_f32_16x16x32_bf16 v[48:51], v[156:159], a[112:115], v[48:51]\n\t" \
  "ds_read_b64_tr_b16 v[196:197], v171 offset:7232\n\t" \
  "v_mfma_f32_16x16x32_bf16 v[52:55], v[144:147], a[68:71], v[52:55]\n\t" \
  "ds_read_b64_tr_b16 v[198:199], v171 offset:9024\n\t" \
  "v_mfma_f32_16x16x32_bf16 v[52:55], v[148:151], a[84:87], v[52:55]\n\t" \
  "buffer_load_dwordx4 a[20:23], v179, s[80:83], s64 offen offset:1024 sc1\n\t" \
  "v_mfma_f32_16x16x32_bf16 v[52:55], v[152:155], a[100:103], v[52:55]\n\t" \
  "ds_read_b64_tr_b16 v[200:201], v171 offset:14400\n\t" \
  "v_mfma_f32_16x16x32_bf16 v[52:55], v[156:159], a[116:119], v[52:55]\n\t" \
  "ds_read_b64_tr_b16 v[202:203], v171 offset:16192\n\t" \
  "v_mfma_f32_16x16x32_bf16 v[56:59], v[144:147], a[72:75], v[56:59]\n\t" \
  "ds_read_b64_tr_b16 v[204:205], v171 offset:21568\n\t" \
  "v_mfma_f32_16x16x32_bf16 v[56:59], v[148:151], a[88:91], v[56:59]\n\t" \
  "buffer_load_dwordx4 a[36:39], v179, s[80:83], s64 offen offset:2048 sc1\n\t" \
  "v_mfma_f32_16x16x32_bf16 v[56:59], v[152:155], a[104:107], v[56:59]\n\t" \
  "ds_read_b64_tr_b16 v[206:207], v171 offset:23360\n\t" \
  "v_mfma_f32_16x16x32_bf16 v[56:59], v[156:159], a[120:123], v[56:59]\n\t" \
  "v_mfma_f32_16x16x32_bf16 v[60:63], v[144:147], a[76:79], v[60:63]\n\t" \
  "v_mfma_f32_16x16x32_bf16 v[60:63], v[148:151], a[92:95], v[60:63]\n\t" \
  "buffer_load_dwordx4 a[52:55], v179, s[80:83], s64 offen offset:3072 sc1\n\t" \
  "v_mfma_f32_16x16x32_bf16 v[60:63], v[152:155], a[108:111], v[60:63]\n\t" \
  "v_mfma_f32_16x16x32_bf16 v[60:63], v[156:159], a[124:127], v[60:63]\n\t" \
  "s_waitcnt lgkmcnt(0)\n\t" \
  "v_mfma_f32_16x16x32_bf16 v[64:67], v[192:195], a[64:67], v[64:67]\n\t" \
  "ds_read_b64_tr_b16 v[144:145], v171 offset:96\n\t" \
  "v_mfma_f32_16x16x32_bf16 v[64:67], v[196:199], a[80:83], v[64:67]\n\t" \
  "buffer_load_dwordx4 a[8:11], v180, s[80:83], s64 offen offset:0 sc1\n\t" \
  "v_mfma_f32_16x16x32_bf16 v[64:67], v[200:203], a[96:99], v[64:67]\n\t" \
  "ds_read_b64_tr_b16 v[146:147], v171 offset:1888\n\t" \
  "v_mfma_f32_16x16x32_bf16 v[64:67], v[204:207], a[112:115], v[64:67]\n\t" \
  "ds_read_b64_tr_b16 v[148:149], v171 offset:7264\n\t" \
  "v_mfma_f32_16x16x32_bf16 v[68:71], v[192:195], a[68:71], v[68:71]\n\t" \
  "ds_read_b64_tr_b16 v[150:151], v171 offset:9056\n\t" \
  "v_mfma_f32_16x16x32_bf16 v[68:71], v[196:199], a[84:87], v[68:71]\n\t" \
  "buffer_load_dwordx4 a[24:27], v180, s[80:83], s64 offen offset:1024 sc1\n\t" \
  "v_mfma_f32_16x16x32_bf16 v[68:71], v[200:203], a[100:103], v[68:71]\n\t" \
  "ds_read_b64_tr_b16 v[152:153], v171 offset:14432\n\t" \
  "v_mfma_f32_16x16x32_bf16 v[68:71], v[204:207], a[116:119], v[68:71]\n\t" \
  "ds_read_b64_tr_b16 v[154:155], v171 offset:16224\n\t" \
  "v_mfma_f32_16x16x32_bf16 v[72:75], v[192:195], a[72:75], v[72:75]\n\t" \
  "ds_read_b64_tr_b16 v[156:157], v171 offset:21600\n\t" \
  "v_mfma_f32_16x16x32_bf16 v[72:75], v[196:199], a[88:91], v[72:75]\n\t" \
  "buffer_load_dwordx4 a[40:43], v180, s[80:83], s64 offen offset:2048 sc1\n\t" \
  "v_mfma_f32_16x16x32_bf16 v[72:75], v[200:203], a[104:107], v[72:75]\n\t" \
  "ds_read_b64_tr_b16 v[158:159], v171 offset:23392\n\t" \
  "v_mfma_f32_16x16x32_bf16 v[72:75], v[204:207], a[120:123], v[72:75]\n\t" \
  "v_mfma_f32_16x16x32_bf16 v[76:79], v[192:195], a[76:79], v[76:79]\n\t" \
  "v_mfma_f32_16x16x32_bf16 v[76:79], v[196:199], a[92:95], v[76:79]\n\t" \
  "buffer_load_dwordx4 a[56:59], v180, s[80:83], s64 offen offset:3072 sc1\n\t" \
  "v_mfma_f32_16x16x32_bf16 v[76:79], v[200:203], a[108:111], v[76:79]\n\t" \
  "v_mfma_f32_16x16x32_bf16 v[76:79], v[204:207], a[124:127], v[76:79]\n\t" \
  "s_waitcnt lgkmcnt(0)\n\t" \
  "v_mfma_f32_16x16x32_bf16 v[80:83], v[144:147], a[64:67], v[80:83]\n\t" \
  "ds_read_b64_tr_b16 v[192:193], v171 offset:128\n\t" \
  "v_mfma_f32_16x16x32_bf16 v[80:83], v[148:151], a[80:83], v[80:83]\n\t" \
  "buffer_load_dwordx4 a[12:15], v181, s[80:83], s64 offen offset:0 sc1\n\t" \
  "v_mfma_f32_16x16x32_bf16 v[80:83], v[152:155], a[96:99], v[80:83]\n\t" \
  "ds_read_b64_tr_b16 v[194:195], v171 offset:1920\n\t" \
  "v_mfma_f32_16x16x32_bf16 v[80:83], v[156:159], a[112:115], v[80:83]\n\t" \
  "ds_read_b64_tr_b16 v[196:197], v171 offset:7296\n\t" \
  "v_mfma_f32_16x16x32_bf16 v[84:87], v[144:147], a[68:71], v[84:87]\n\t" \
  "ds_read_b64_tr_b16 v[198:199], v171 offset:9088\n\t" \
  "v_mfma_f32_16x16x32_bf16 v[84:87], v[148:151], a[84:87], v[84:87]\n\t" \
  "buffer_load_dwordx4 a[28:31], v181, s[80:83], s64 offen offset:1024 sc1\n\t" \
  "v_mfma_f32_16x16x32_bf16 v[84:87], v[152:155], a[100:103], v[84:87]\n\t" \
  "ds_read_b64_tr_b16 v[200:201], v171 offset:14464\n\t" \
  "v_mfma_f32_16x16x32_bf16 v[84:87], v[156:159], a[116:119], v[84:87]\n\t" \
  "ds_read_b64_tr_b16 v[202:203], v171 offset:16256\n\t" \
  "v_mfma_f32_16x16x32_bf16 v[88:91], v[144:147], a[72:75], v[88:91]\n\t" \
  "ds_read_b64_tr_b16 v[204:205], v171 offset:21632\n\t" \
  "v_mfma_f32_16x16x32_bf16 v[88:91], v[148:151], a[88:91], v[88:91]\n\t" \
  "buffer_load_dwordx4 a[44:47], v181, s[80:83], s64 offen offset:2048 sc1\n\t" \
  "v_mfma_f32_16x16x32_bf16 v[88:91], v[152:155], a[104:107], v[88:91]\n\t" \
  "ds_read_b64_tr_b16 v[206:207], v171 offset:23424\n\t" \
  "v_mfma_f32_16x16x32_bf16 v[88:91], v[156:159], a[120:123], v[88:91]\n\t" \
  "v_mfma_f32_16x16x32_bf16 v[92:95], v[144:147], a[76:79], v[92:95]\n\t" \
  "v_mfma_f32_16x16x32_bf16 v[92:95], v[148:151], a[92:95], v[92:95]\n\t" \
  "buffer_load_dwordx4 a[60:63], v181, s[80:83], s64 offen offset:3072 sc1\n\t" \
  "v_mfma_f32_16x16x32_bf16 v[92:95], v[152:155], a[108:111], v[92:95]\n\t" \
  "s_add_u32 m0, s73, 73728\n\t" \
  "s_mov_b32 s65, 1310720\n\t" \
  "v_mfma_f32_16x16x32_bf16 v[92:95], v[156:159], a[124:127], v[92:95]\n\t" \
  "s_waitcnt lgkmcnt(0)\n\t" \
  "v_mfma_f32_16x16x32_bf16 v[96:99], v[192:195], a[64:67], v[96:99]\n\t" \
  "ds_read_b64_tr_b16 v[144:145], v171 offset:160\n\t" \
  "v_mfma_f32_16x16x32_bf16 v[96:99], v[196:199], a[80:83], v[96:99]\n\t" \
  "buffer_load_dwordx4 v160, s[88:91], s65 offen offset:0 lds\n\t" \
  "v_mfma_f32_16x16x32_bf16 v[96:99], v[200:203], a[96:99], v[96:99]\n\t" \
  "ds_read_b64_tr_b16 v[146:147], v171 offset:1952\n\t" \
  "v_mfma_f32_16x16x32_bf16 v[96:99], v[204:207], a[112:115], v[96:99]\n\t" \
  "ds_read_b64_tr_b16 v[148:149], v171 offset:7328\n\t" \
  "v_mfma_f32_16x16x32_bf16 v[100:103], v[192:195], a[68:71], v[100:103]\n\t" \
  "ds_read_b64_tr_b16 v[150:151], v171 offset:9120\n\t" \
  "v_mfma_f32_16x16x32_bf16 v[100:103], v[196:199], a[84:87], v[100:103]\n\t" \
  "buffer_load_dwordx4 v161, s[88:91], s65 offen offset:1024 lds\n\t" \
  "v_mfma_f32_16x16x32_bf16 v[100:103], v[200:203], a[100:103], v[100:103]\n\t" \
  "ds_read_b64_tr_b16 v[152:153], v171 offset:14496\n\t" \
  "v_mfma_f32_16x16x32_bf16 v[100:103], v[204:207], a[116:119], v[100:103]\n\t" \
  "ds_read_b64_tr_b16 v[154:155], v171 offset:16288\n\t" \
  "v_mfma_f32_16x16x32_bf16 v[104:107], v[192:195], a[72:75], v[104:107]\n\t" \
  "ds_read_b64_tr_b16 v[156:157], v171 offset:21664\n\t" \
  "v_mfma_f32_16x16x32_bf16 v[104:107], v[196:199], a[88:91], v[104:107]\n\t" \
  "s_add_u32 m0, s74, 73728\n\t" \
  "s_mov_b32 s66, 4587520\n\t" \
  "v_mfma_f32_16x16x32_bf16 v[104:107], v[200:203], a[104:107], v[104:107]\n\t" \
  "ds_read_b64_tr_b16 v[158:159], v171 offset:23456\n\t" \
  "v_mfma_f32_16x16x32_bf16 v[104:107], v[204:207], a[120:123], v[104:107]\n\t" \
  "v_mfma_f32_16x16x32_bf16 v[108:111], v[192:195], a[76:79], v[108:111]\n\t" \
  "v_mfma_f32_16x16x32_bf16 v[108:111], v[196:199], a[92:95], v[108:111]\n\t" \
  "buffer_load_dwordx4 v162, s[92:95], s66 offen offset:0 lds\n\t" \
  "v_mfma_f32_16x16x32_bf16 v[108:111], v[200:203], a[108:111], v[108:111]\n\t" \
  "v_mfma_f32_16x16x32_bf16 v[108:111], v[204:207], a[124:127], v[108:111]\n\t" \
  "s_waitcnt lgkmcnt(0)\n\t" \
  "v_mfma_f32_16x16x32_bf16 v[112:115], v[144:147], a[64:67], v[112:115]\n\t" \
  "ds_read_b64_tr_b16 v[192:193], v171 offset:192\n\t" \
  "v_mfma_f32_16x16x32_bf16 v[112:115], v[148:151], a[80:83], v[112:115]\n\t" \
  "buffer_load_dwordx4 v163, s[92:95], s66 offen offset:1024 lds\n\t" \
  "v_mfma_f32_16x16x32_bf16 v[112:115], v[152:155], a[96:99], v[112:115]\n\t" \
  "ds_read_b64_tr_b16 v[194:195], v171 offset:1984\n\t" \
  "v_mfma_f32_16x16x32_bf16 v[112:115], v[156:159], a[112:115], v[112:115]\n\t" \
  "ds_read_b64_tr_b16 v[196:197], v171 offset:7360\n\t" \
  "v_mfma_f32_16x16x32_bf16 v[116:119], v[144:147], a[68:71], v[116:119]\n\t" \
  "ds_read_b64_tr_b16 v[198:199], v171 offset:9152\n\t" \
  "v_mfma_f32_16x16x32_bf16 v[116:119], v[148:151], a[84:87], v[116:119]\n\t" \
  "buffer_load_dwordx4 v164, s[92:95], s66 offen offset:2048 lds\n\t" \
  "v_mfma_f32_16x16x32_bf16 v[116:119], v[152:155], a[100:103], v[116:119]\n\t" \
  "ds_read_b64_tr_b16 v[200:201], v171 offset:14528\n\t" \
  "v_mfma_f32_16x16x32_bf16 v[116:119], v[156:159], a[116:119], v[116:119]\n\t" \
  "ds_read_b64_tr_b16 v[202:203], v171 offset:16320\n\t" \
  "v_mfma_f32_16x16x32_bf16 v[120:123], v[144:147], a[72:75], v[120:123]\n\t" \
  "ds_read_b64_tr_b16 v[204:205], v171 offset:21696\n\t" \
  "v_mfma_f32_16x16x32_bf16 v[120:123], v[148:151], a[88:91], v[120:123]\n\t" \
  "buffer_load_dwordx4 v165, s[92:95], s66 offen offset:3072 lds\n\t" \
  "v_mfma_f32_16x16x32_bf16 v[120:123], v[152:155], a[104:107], v[120:123]\n\t" \
  "ds_read_b64_tr_b16 v[206:207], v171 offset:23488\n\t" \
  "v_mfma_f32_16x16x32_bf16 v[120:123], v[156:159], a[120:123], v[120:123]\n\t" \
  "s_add_u32 m0, s74, 77824\n\t" \
  "v_mfma_f32_16x16x32_bf16 v[124:127], v[144:147], a[76:79], v[124:127]\n\t" \
  "v_mfma_f32_16x16x32_bf16 v[124:127], v[148:151], a[92:95], v[124:127]\n\t" \
  "buffer_load_dwordx4 v166, s[92:95], s66 offen offset:0 lds\n\t" \
  "v_mfma_f32_16x16x32_bf16 v[124:127], v[152:155], a[108:111], v[124:127]\n\t" \
  "v_mfma_f32_16x16x32_bf16 v[124:127], v[156:159], a[124:127], v[124:127]\n\t" \
  "s_waitcnt lgkmcnt(0)\n\t" \
  "v_mfma_f32_16x16x32_bf16 v[208:211], v[192:195], a[64:67], v[208:211]\n\t" \
  "ds_read_b64_tr_b16 v[144:145], v169 offset:0\n\t" \
  "v_mfma_f32_16x16x32_bf16 v[208:211], v[196:199], a[80:83], v[208:211]\n\t" \
  "buffer_load_dwordx4 v167, s[92:95], s66 offen offset:1024 lds\n\t" \
  "v_mfma_f32_16x16x32_bf16 v[208:211], v[200:203], a[96:99], v[208:211]\n\t" \
  "ds_read_b64_tr_b16 v[146:147], v169 offset:512\n\t" \
  "v_mfma_f32_16x16x32_bf16 v[208:211], v[204:207], a[112:115], v[208:211]\n\t" \
  "ds_read_b64_tr_b16 v[148:149], v169 offset:2048\n\t" \
  "v_mfma_f32_16x16x32_bf16 v[212:215], v[192:195], a[68:71], v[212:215]\n\t" \
  "ds_read_b64_tr_b16 v[150:151], v169 offset:2560\n\t" \
  "v_mfma_f32_16x16x32_bf16 v[212:215], v[196:199], a[84:87], v[212:215]\n\t" \
  "buffer_load_dwordx4 v168, s[92:95], s66 offen offset:2048 lds\n\t" \
  "v_mfma_f32_16x16x32_bf16 v[212:215], v[200:203], a[100:103], v[212:215]\n\t" \
  "ds_read_b64_tr_b16 v[152:153], v169 offset:4096\n\t" \
  "v_mfma_f32_16x16x32_bf16 v[212:215], v[204:207], a[116:119], v[212:215]\n\t" \
  "ds_read_b64_tr_b16 v[154:155], v169 offset:4608\n\t" \
  "v_mfma_f32_16x16x32_bf16 v[216:219], v[192:195], a[72:75], v[216:219]\n\t" \
  "ds_read_b64_tr_b16 v[156:157], v169 offset:6144\n\t" \
  "v_mfma_f32_16x16x32_bf16 v[216:219], v[196:199], a[88:91], v[216:219]\n\t" \
  "s_mov_b32 m0, s75\n\t" \
  "s_add_u32 s48, s48, s50\n\t" \
  "s_addc_u32 s49, s49, 0\n\t" \
  "v_mfma_f32_16x16x32_bf16 v[216:219], v[200:203], a[104:107], v[216:219]\n\t" \
  "ds_read_b64_tr_b16 v[158:159], v169 offset:6656\n\t" \
  "v_mfma_f32_16x16x32_bf16 v[216:219], v[204:207], a[120:123], v[216:219]\n\t" \
  "v_mfma_f32_16x16x32_bf16 v[220:223], v[192:195], a[76:79], v[220:223]\n\t" \
  "v_mfma_f32_16x16x32_bf16 v[220:223], v[196:199], a[92:95], v[220:223]\n\t" \
  "global_load_lds_dword v186, s[48:49]\n\t" \
  "v_mfma_f32_16x16x32_bf16 v[220:223], v[200:203], a[108:111], v[220:223]\n\t" \
  "v_mfma_f32_16x16x32_bf16 v[220:223], v[204:207], a[124:127], v[220:223]\n\t" \
  "s_waitcnt lgkmcnt(0)\n\t" \
  "v_mfma_f32_16x16x32_bf16 v[0:3], v[144:147], a[64:67], v[0:3]\n\t" \
  "ds_read_b64_tr_b16 v[192:193], v170 offset:0\n\t" \
  "v_mfma_f32_16x16x32_bf16 v[0:3], v[148:151], a[80:83], v[0:3]\n\t" \
  "v_mfma_f32_16x16x32_bf16 v[0:3], v[152:155], a[96:99], v[0:3]\n\t" \
  "ds_read_b64_tr_b16 v[194:195], v170 offset:512\n\t" \
  "v_mfma_f32_16x16x32_bf16 v[0:3], v[156:159], a[112:115], v[0:3]\n\t" \
  "ds_read_b64_tr_b16 v[196:197], v170 offset:2048\n\t" \
  "v_mfma_f32_16x16x32_bf16 v[4:7], v[144:147], a[68:71], v[4:7]\n\t" \
  "ds_read_b64_tr_b16 v[198:199], v170 offset:2560\n\t" \
  "v_mfma_f32_16x16x32_bf16 v[4:7], v[148:151], a[84:87], v[4:7]\n\t" \
  "v_mfma_f32_16x16x32_bf16 v[4:7], v[152:155], a[100:103], v[4:7]\n\t" \
  "ds_read_b64_tr_b16 v[200:201], v170 offset:4096\n\t" \
  "v_mfma_f32_16x16x32_bf16 v[4:7], v[156:159], a[116:119], v[4:7]\n\t" \
  "ds_read_b64_tr_b16 v[202:203], v170 offset:4608\n\t" \
  "v_mfma_f32_16x16x32_bf16 v[8:11], v[144:147], a[72:75], v[8:11]\n\t" \
  "ds_read_b64_tr_b16 v[204:205], v170 offset:6144\n\t" \
  "v_mfma_f32_16x16x32_bf16 v[8:11], v[148:151], a[88:91], v[8:11]\n\t" \
  "v_mfma_f32_16x16x32_bf16 v[8:11], v[152:155], a[104:107], v[8:11]\n\t" \
  "ds_read_b64_tr_b16 v[206:207], v170 offset:6656\n\t" \
  "v_mfma_f32_16x16x32_bf16 v[8:11], v[156:159], a[120:123], v[8:11]\n\t" \
  "v_mfma_f32_16x16x32_bf16 v[12:15], v[144:147], a[76:79], v[12:15]\n\t" \
  "v_mfma_f32_16x16x32_bf16 v[12:15], v[148:151], a[92:95], v[12:15]\n\t" \
  "v_mfma_f32_16x16x32_bf16 v[12:15], v[152:155], a[108:111], v[12:15]\n\t" \
  "v_mfma_f32_16x16x32_bf16 v[12:15], v[156:159], a[124:127], v[12:15]\n\t" \
  "s_waitcnt lgkmcnt(0)\n\t" \
  "s_waitcnt vmcnt(27)\n\t" \
  "s_barrier\n\t" \
  "v_mfma_f32_16x16x32_bf16 v[16:19], v[192:195], a[64:67], v[16:19]\n\t" \
  "ds_read_b64_tr_b16 v[144:145], v174 offset:0\n\t" \
  "v_mfma_f32_16x16x32_bf16 v[16:19], v[196:199], a[80:83], v[16:19]\n\t" \
  "v_mfma_f32_16x16x32_bf16 v[16:19], v[200:203], a[96:99], v[16:19]\n\t" \
  "ds_read_b64_tr_b16 v[146:147], v174 offset:1792\n\t" \
  "v_mfma_f32_16x16x32_bf16 v[16:19], v[204:207], a[112:115], v[16:19]\n\t" \
  "ds_read_b64_tr_b16 v[148:149], v174 offset:7168\n\t" \
  "v_mfma_f32_16x16x32_bf16 v[20:23], v[192:195], a[68:71], v[20:23]\n\t" \
  "ds_read_b64_tr_b16 v[150:151], v174 offset:8960\n\t" \
  "v_mfma_f32_16x16x32_bf16 v[20:23], v[196:199], a[84:87], v[20:23]\n\t" \
  "v_mfma_f32_16x16x32_bf16 v[20:23], v[200:203], a[100:103], v[20:23]\n\t" \
  "ds_read_b64_tr_b16 v[152:153], v174 offset:14336\n\t" \
  "v_mfma_f32_16x16x32_bf16 v[20:23], v[204:207], a[116:119], v[20:23]\n\t" \
  "ds_read_b64_tr_b16 v[154:155], v174 offset:16128\n\t" \
  "v_mfma_f32_16x16x32_bf16 v[24:27], v[192:195], a[72:75], v[24:27]\n\t" \
  "ds_read_b64_tr_b16 v[156:157], v174 offset:21504\n\t" \
  "v_mfma_f32_16x16x32_bf16 v[24:27], v[196:199], a[88:91], v[24:27]\n\t" \
  "v_mfma_f32_16x16x32_bf16 v[24:27], v[200:203], a[104:107], v[24:27]\n\t" \
  "ds_read_b64_tr_b16 v[158:159], v174 offset:23296\n\t" \
  "v_mfma_f32_16x16x32_bf16 v[24:27], v[204:207], a[120:123], v[24:27]\n\t" \
  "v_mfma_f32_16x16x32_bf16 v[28:31], v[192:195], a[76:79], v[28:31]\n\t" \
  "v_mfma_f32_16x16x32_bf16 v[28:31], v[196:199], a[92:95], v[28:31]\n\t" \
  "v_mfma_f32_16x16x32_bf16 v[28:31], v[200:203], a[108:111], v[28:31]\n\t" \
  "v_mfma_f32_16x16x32_bf16 v[28:31], v[204:207], a[124:127], v[28:31]\n\t" \
  "s_waitcnt lgkmcnt(0)\n\t" \
  "s_waitcnt vmcnt(22)\n\t" \
  "v_mfma_f32_16x16x32_bf16 v[32:35], v[144:147], a[0:3], v[32:35]\n\t" \
  "ds_read_b64_tr_b16 v[192:193], v174 offset:32\n\t" \
  "v_mfma_f32_16x16x32_bf16 v[32:35], v[148:151], a[16:19], v[32:35]\n\t" \
  "s_mov_b32 s64, 20480\n\t" \
  "buffer_load_dwordx4 a[64:67], v178, s[80:83], s64 offen offset:0 sc1\n\t" \
  "v_mfma_f32_16x16x32_bf16 v[32:35], v[152:155], a[32:35], v[32:35]\n\t" \
  "ds_read_b64_tr_b16 v[194:195], v174 offset:1824\n\t" \
  "v_mfma_f32_16x16x32_bf16 v[32:35], v[156:159], a[48:51], v[32:35]\n\t" \
  "ds_read_b64_tr_b16 v[196:197], v174 offset:7200\n\t" \
  "s_waitcnt vmcnt(19)\n\t" \
  "v_mfma_f32_16x16x32_bf16 v[36:39], v[144:147], a[4:7], v[36:39]\n\t" \
  "ds_read_b64_tr_b16 v[198:199], v174 offset:8992\n\t" \
  "v_mfma_f32_16x16x32_bf16 v[36:39], v[148:151], a[20:23], v[36:39]\n\t" \
  "buffer_load_dwordx4 a[80:83], v178, s[80:83], s64 offen offset:1024 sc1\n\t" \
  "v_mfma_f32_16x16x32_bf16 v[36:39], v[152:155], a[36:39], v[36:39]\n\t" \
  "ds_read_b64_tr_b16 v[200:201], v174 offset:14368\n\t" \
  "v_mfma_f32_16x16x32_bf16 v[36:39], v[156:159], a[52:55], v[36:39]\n\t" \
  "ds_read_b64_tr_b16 v[202:203], v174 offset:16160\n\t" \
  "s_waitcnt vmcnt(16)\n\t" \
  "v_mfma_f32_16x16x32_bf16 v[40:43], v[144:147], a[8:11], v[40:43]\n\t" \
  "ds_read_b64_tr_b16 v[204:205], v174 offset:21536\n\t" \
  "v_mfma_f32_16x16x32_bf16 v[40:43], v[148:151], a[24:27], v[40:43]\n\t" \
  "buffer_load_dwordx4 a[96:99], v178, s[80:83], s64 offen offset:2048 sc1\n\t" \
  "v_mfma_f32_16x16x32_bf16 v[40:43], v[152:155], a[40:43], v[40:43]\n\t" \
  "ds_read_b64_tr_b16 v[206:207], v174 offset:23328\n\t" \
  "v_mfma_f32_16x16x32_bf16 v[40:43], v[156:159], a[56:59], v[40:43]\n\t" \
  "s_waitcnt vmcnt(13)\n\t" \
  "v_mfma_f32_16x16x32_bf16 v[44:47], v[144:147], a[12:15], v[44:47]\n\t" \
  "v_mfma_f32_16x16x32_bf16 v[44:47], v[148:151], a[28:31], v[44:47]\n\t" \
  "buffer_load_dwordx4 a[112:115], v178, s[80:83], s64 offen offset:3072 sc1\n\t" \
  "v_mfma_f32_16x16x32_bf16 v[44:47], v[152:155], a[44:47], v[44:47]\n\t" \
  "v_mfma_f32_16x16x32_bf16 v[44:47], v[156:159], a[60:63], v[44:47]\n\t" \
  "s_waitcnt lgkmcnt(0)\n\t" \
  "v_mfma_f32_16x16x32_bf16 v[48:51], v[192:195], a[0:3], v[48:51]\n\t" \
  "ds_read_b64_tr_b16 v[144:145], v174 offset:64\n\t" \
  "v_mfma_f32_16x16x32_bf16 v[48:51], v[196:199], a[16:19], v[48:51]\n\t" \
  "buffer_load_dwordx4 a[68:71], v179, s[80:83], s64 offen offset:0 sc1\n\t" \
  "v_mfma_f32_16x16x32_bf16 v[48:51], v[200:203], a[32:35], v[48:51]\n\t" \
  "ds_read_b64_tr_b16 v[146:147], v174 offset:1856\n\t" \
  "v_mfma_f32_16x16x32_bf16 v[48:51], v[204:207], a[48:51], v[48:51]\n\t" \
  "ds_read_b64_tr_b16 v[148:149], v174 offset:7232\n\t" \
  "v_mfma_f32_16x16x32_bf16 v[52:55], v[192:195], a[4:7], v[52:55]\n\t" \
  "ds_read_b64_tr_b16 v[150:151], v174 offset:9024\n\t" \
  "v_mfma_f32_16x16x32_bf16 v[52:55], v[196:199], a[20:23], v[52:55]\n\t" \
  "buffer_load_dwordx4 a[84:87], v179, s[80:83], s64 offen offset:1024 sc1\n\t" \
  "v_mfma_f32_16x16x32_bf16 v[52:55], v[200:203], a[36:39], v[52:55]\n\t" \
  "ds_read_b64_tr_b16 v[152:153], v174 offset:14400\n\t" \
  "v_mfma_f32_16x16x32_bf16 v[52:55], v[204:207], a[52:55], v[52:55]\n\t" \
  "ds_read_b64_tr_b16 v[154:155], v174 offset:16192\n\t" \
  "v_mfma_f32_16x16x32_bf16 v[56:59], v[192:195], a[8:11], v[56:59]\n\t" \
  "ds_read_b64_tr_b16 v[156:157], v174 offset:21568\n\t" \
  "v_mfma_f32_16x16x32_bf16 v[56:59], v[196:199], a[24:27], v[56:59]\n\t" \
  "buffer_load_dwordx4 a[100:103], v179, s[80:83], s64 offen offset:2048 sc1\n\t" \
  "v_mfma_f32_16x16x32_bf16 v[56:59], v[200:203], a[40:43], v[56:59]\n\t" \
  "ds_read_b64_tr_b16 v[158:159], v174 offset:23360\n\t" \
  "v_mfma_f32_16x16x32_bf16 v[56:59], v[204:207], a[56:59], v[56:59]\n\t" \
  "v_mfma_f32_16x16x32_bf16 v[60:63], v[192:195], a[12:15], v[60:63]\n\t" \
  "v_mfma_f32_16x16x32_bf16 v[60:63], v[196:199], a[28:31], v[60:63]\n\t" \
  "buffer_load_dwordx4 a[116:119], v179, s[80:83], s64 offen offset:3072 sc1\n\t" \
  "v_mfma_f32_16x16x32_bf16 v[60:63], v[200:203], a[44:47], v[60:63]\n\t" \
  "v_mfma_f32_16x16x32_bf16 v[60:63], v[204:207], a[60:63], v[60:63]\n\t" \
  "s_waitcnt lgkmcnt(0)\n\t" \
  "v_mfma_f32_16x16x32_bf16 v[64:67], v[144:147], a[0:3], v[64:67]\n\t" \
  "ds_read_b64_tr_b16 v[192:193], v174 offset:96\n\t" \
  "v_mfma_f32_16x16x32_bf16 v[64:67], v[148:151], a[16:19], v[64:67]\n\t" \
  "buffer_load_dwordx4 a[72:75], v180, s[80:83], s64 offen offset:0 sc1\n\t" \
  "v_mfma_f32_16x16x32_bf16 v[64:67], v[152:155], a[32:35], v[64:67]\n\t" \
  "ds_read_b64_tr_b16 v[194:195], v174 offset:1888\n\t" \
  "v_mfma_f32_16x16x32_bf16 v[64:67], v[156:159], a[48:51], v[64:67]\n\t" \
  "ds_read_b64_tr_b16 v[196:197], v174 offset:7264\n\t" \
  "v_mfma_f32_16x16x32_bf16 v[68:71], v[144:147], a[4:7], v[68:71]\n\t" \
  "ds_read_b64_tr_b16 v[198:199], v174 offset:9056\n\t" \
  "v_mfma_f32_16x16x32_bf16 v[68:71], v[148:151], a[20:23], v[68:71]\n\t" \
  "buffer_load_dwordx4 a[88:91], v180, s[80:83], s64 offen offset:1024 sc1\n\t" \
  "v_mfma_f32_16x16x32_bf16 v[68:71], v[152:155], a[36:39], v[68:71]\n\t" \
  "ds_read_b64_tr_b16 v[200:201], v174 offset:14432\n\t" \
  "v_mfma_f32_16x16x32_bf16 v[68:71], v[156:159], a[52:55], v[68:71]\n\t" \
  "ds_read_b64_tr_b16 v[202:203], v174 offset:16224\n\t" \
  "v_mfma_f32_16x16x32_bf16 v[72:75], v[144:147], a[8:11], v[72:75]\n\t" \
  "ds_read_b64_tr_b16 v[204:205], v174 offset:21600\n\t" \
  "v_mfma_f32_16x16x32_bf16 v[72:75], v[148:151], a[24:27], v[72:75]\n\t" \
  "buffer_load_dwordx4 a[104:107], v180, s[80:83], s64 offen offset:2048 sc1\n\t" \
  "v_mfma_f32_16x16x32_bf16 v[72:75], v[152:155], a[40:43], v[72:75]\n\t" \
  "ds_read_b64_tr_b16 v[206:207], v174 offset:23392\n\t" \
  "v_mfma_f32_16x16x32_bf16 v[72:75], v[156:159], a[56:59], v[72:75]\n\t" \
  "v_mfma_f32_16x16x32_bf16 v[76:79], v[144:147], a[12:15], v[76:79]\n\t" \
  "v_mfma_f32_16x16x32_bf16 v[76:79], v[148:151], a[28:31], v[76:79]\n\t" \
  "buffer_load_dwordx4 a[120:123], v180, s[80:83], s64 offen offset:3072 sc1\n\t" \
  "v_mfma_f32_16x16x32_bf16 v[76:79], v[152:155], a[44:47], v[76:79]\n\t" \
  "v_mfma_f32_16x16x32_bf16 v[76:79], v[156:159], a[60:63], v[76:79]\n\t" \
  "s_waitcnt lgkmcnt(0)\n\t" \
  "v_mfma_f32_16x16x32_bf16 v[80:83], v[192:195], a[0:3], v[80:83]\n\t" \
  "ds_read_b64_tr_b16 v[144:145], v174 offset:128\n\t" \
  "v_mfma_f32_16x16x32_bf16 v[80:83], v[196:199], a[16:19], v[80:83]\n\t" \
  "buffer_load_dwordx4 a[76:79], v181, s[80:83], s64 offen offset:0 sc1\n\t" \
  "v_mfma_f32_16x16x32_bf16 v[80:83], v[200:203], a[32:35], v[80:83]\n\t" \
  "ds_read_b64_tr_b16 v[146:147], v174 offset:1920\n\t" \
  "v_mfma_f32_16x16x32_bf16 v[80:83], v[204:207], a[48:51], v[80:83]\n\t" \
  "ds_read_b64_tr_b16 v[148:149], v174 offset:7296\n\t" \
  "v_mfma_f32_16x16x32_bf16 v[84:87], v[192:195], a[4:7], v[84:87]\n\t" \
  "ds_read_b64_tr_b16 v[150:151], v174 offset:9088\n\t" \
  "v_mfma_f32_16x16x32_bf16 v[84:87], v[196:199], a[20:23], v[84:87]\n\t" \
  "buffer_load_dwordx4 a[92:95], v181, s[80:83], s64 offen offset:1024 sc1\n\t" \
  "v_mfma_f32_16x16x32_bf16 v[84:87], v[200:203], a[36:39], v[84:87]\n\t" \
  "ds_read_b64_tr_b16 v[152:153], v174 offset:14464\n\t" \
  "v_mfma_f32_16x16x32_bf16 v[84:87], v[204:207], a[52:55], v[84:87]\n\t" \
  "ds_read_b64_tr_b16 v[154:155], v174 offset:16256\n\t" \
  "v_mfma_f32_16x16x32_bf16 v[88:91], v[192:195], a[8:11], v[88:91]\n\t" \
  "ds_read_b64_tr_b16 v[156:157], v174 offset:21632\n\t" \
  "v_mfma_f32_16x16x32_bf16 v[88:91], v[196:199], a[24:27], v[88:91]\n\t" \
  "buffer_load_dwordx4 a[108:111], v181, s[80:83], s64 offen offset:2048 sc1\n\t" \
  "v_mfma_f32_16x16x32_bf16 v[88:91], v[200:203], a[40:43], v[88:91]\n\t" \
  "ds_read_b64_tr_b16 v[158:159], v174 offset:23424\n\t" \
  "v_mfma_f32_16x16x32_bf16 v[88:91], v[204:207], a[56:59], v[88:91]\n\t" \
  "v_mfma_f32_16x16x32_bf16 v[92:95], v[192:195], a[12:15], v[92:95]\n\t" \
  "v_mfma_f32_16x16x32_bf16 v[92:95], v[196:199], a[28:31], v[92:95]\n\t" \
  "buffer_load_dwordx4 a[124:127], v181, s[80:83], s64 offen offset:3072 sc1\n\t" \
  "v_mfma_f32_16x16x32_bf16 v[92:95], v[200:203], a[44:47], v[92:95]\n\t" \
  "s_add_u32 m0, s73, 0\n\t" \
  "s_mov_b32 s65, 1572864\n\t" \
  "v_mfma_f32_16x16x32_bf16 v[92:95], v[204:207], a[60:63], v[92:95]\n\t" \
  "s_waitcnt lgkmcnt(0)\n\t" \
  "v_mfma_f32_16x16x32_bf16 v[96:99], v[144:147], a[0:3], v[96:99]\n\t" \
  "ds_read_b64_tr_b16 v[192:193], v174 offset:160\n\t" \
  "v_mfma_f32_16x16x32_bf16 v[96:99], v[148:151], a[16:19], v[96:99]\n\t" \
  "buffer_load_dwordx4 v160, s[88:91], s65 offen offset:0 lds\n\t" \
  "v_mfma_f32_16x16x32_bf16 v[96:99], v[152:155], a[32:35], v[96:99]\n\t" \
  "ds_read_b64_tr_b16 v[194:195], v174 offset:1952\n\t" \
  "v_mfma_f32_16x16x32_bf16 v[96:99], v[156:159], a[48:51], v[96:99]\n\t" \
  "ds_read_b64_tr_b16 v[196:197], v174 offset:7328\n\t" \
  "v_mfma_f32_16x16x32_bf16 v[100:103], v[144:147], a[4:7], v[100:103]\n\t" \
  "ds_read_b64_tr_b16 v[198:199], v174 offset:9120\n\t" \
  "v_mfma_f32_16x16x32_bf16 v[100:103], v[148:151], a[20:23], v[100:103]\n\t" \
  "buffer_load_dwordx4 v161, s[88:91], s65 offen offset:1024 lds\n\t" \
  "v_mfma_f32_16x16x32_bf16 v[100:103], v[152:155], a[36:39], v[100:103]\n\t" \
  "ds_read_b64_tr_b16 v[200:201], v174 offset:14496\n\t" \
  "v_mfma_f32_16x16x32_bf16 v[100:103], v[156:159], a[52:55], v[100:103]\n\t" \
  "ds_read_b64_tr_b16 v[202:203], v174 offset:16288\n\t" \
  "v_mfma_f32_16x16x32_bf16 v[104:107], v[144:147], a[8:11], v[104:107]\n\t" \
  "ds_read_b64_tr_b16 v[204:205], v174 offset:21664\n\t" \
  "v_mfma_f32_16x16x32_bf16 v[104:107], v[148:151], a[24:27], v[104:107]\n\t" \
  "s_add_u32 m0, s74, 0\n\t" \
  "s_mov_b32 s66, 5505024\n\t" \
  "v_mfma_f32_16x16x32_bf16 v[104:107], v[152:155], a[40:43], v[104:107]\n\t" \
  "ds_read_b64_tr_b16 v[206:207], v174 offset:23456\n\t" \
  "v_mfma_f32_16x16x32_bf16 v[104:107], v[156:159], a[56:59], v[104:107]\n\t" \
  "v_mfma_f32_16x16x32_bf16 v[108:111], v[144:147], a[12:15], v[108:111]\n\t" \
  "v_mfma_f32_16x16x32_bf16 v[108:111], v[148:151], a[28:31], v[108:111]\n\t" \
  "buffer_load_dwordx4 v162, s[92:95], s66 offen offset:0 lds\n\t" \
  "v_mfma_f32_16x16x32_bf16 v[108:111], v[152:155], a[44:47], v[108:111]\n\t" \
  "v_mfma_f32_16x16x32_bf16 v[108:111], v[156:159], a[60:63], v[108:111]\n\t" \
  "s_waitcnt lgkmcnt(0)\n\t" \
  "v_mfma_f32_16x16x32_bf16 v[112:115], v[192:195], a[0:3], v[112:115]\n\t" \
  "ds_read_b64_tr_b16 v[144:145], v174 offset:192\n\t" \
  "v_mfma_f32_16x16x32_bf16 v[112:115], v[196:199], a[16:19], v[112:115]\n\t" \
  "buffer_load_dwordx4 v163, s[92:95], s66 offen offset:1024 lds\n\t" \
  "v_mfma_f32_16x16x32_bf16 v[112:115], v[200:203], a[32:35], v[112:115]\n\t" \
  "ds_read_b64_tr_b16 v[146:147], v174 offset:1984\n\t" \
  "v_mfma_f32_16x16x32_bf16 v[112:115], v[204:207], a[48:51], v[112:115]\n\t" \
  "ds_read_b64_tr_b16 v[148:149], v174 offset:7360\n\t" \
  "v_mfma_f32_16x16x32_bf16 v[116:119], v[192:195], a[4:7], v[116:119]\n\t" \
  "ds_read_b64_tr_b16 v[150:151], v174 offset:9152\n\t" \
  "v_mfma_f32_16x16x32_bf16 v[116:119], v[196:199], a[20:23], v[116:119]\n\t" \
  "buffer_load_dwordx4 v164, s[92:95], s66 offen offset:2048 lds\n\t" \
  "v_mfma_f32_16x16x32_bf16 v[116:119], v[200:203], a[36:39], v[116:119]\n\t" \
  "ds_read_b64_tr_b16 v[152:153], v174 offset:14528\n\t" \
  "v_mfma_f32_16x16x32_bf16 v[116:119], v[204:207], a[52:55], v[116:119]\n\t" \
  "ds_read_b64_tr_b16 v[154:155], v174 offset:16320\n\t" \
  "v_mfma_f32_16x16x32_bf16 v[120:123], v[192:195], a[8:11], v[120:123]\n\t" \
  "ds_read_b64_tr_b16 v[156:157], v174 offset:21696\n\t" \
  "v_mfma_f32_16x16x32_bf16 v[120:123], v[196:199], a[24:27], v[120:123]\n\t" \
  "buffer_load_dwordx4 v165, s[92:95], s66 offen offset:3072 lds\n\t" \
  "v_mfma_f32_16x16x32_bf16 v[120:123], v[200:203], a[40:43], v[120:123]\n\t" \
  "ds_read_b64_tr_b16 v[158:159], v174 offset:23488\n\t" \
  "v_mfma_f32_16x16x32_bf16 v[120:123], v[204:207], a[56:59], v[120:123]\n\t" \
  "s_add_u32 m0, s74, 4096\n\t" \
  "v_mfma_f32_16x16x32_bf16 v[124:127], v[192:195], a[12:15], v[124:127]\n\t" \
  "v_mfma_f32_16x16x32_bf16 v[124:127], v[196:199], a[28:31], v[124:127]\n\t" \
  "buffer_load_dwordx4 v166, s[92:95], s66 offen offset:0 lds\n\t" \
  "v_mfma_f32_16x16x32_bf16 v[124:127], v[200:203], a[44:47], v[124:127]\n\t" \
  "v_mfma_f32_16x16x32_bf16 v[124:127], v[204:207], a[60:63], v[124:127]\n\t" \
  "s_waitcnt lgkmcnt(0)\n\t" \
  "v_mfma_f32_16x16x32_bf16 v[208:211], v[144:147], a[0:3], v[208:211]\n\t" \
  "ds_read_b64_tr_b16 v[192:193], v172 offset:0\n\t" \
  "v_mfma_f32_16x16x32_bf16 v[208:211], v[148:151], a[16:19], v[208:211]\n\t" \
  "buffer_load_dwordx4 v167, s[92:95], s66 offen offset:1024 lds\n\t" \
  "v_mfma_f32_16x16x32_bf16 v[208:211], v[152:155], a[32:35], v[208:211]\n\t" \
  "ds_read_b64_tr_b16 v[194:195], v172 offset:512\n\t" \
  "v_mfma_f32_16x16x32_bf16 v[208:211], v[156:159], a[48:51], v[208:211]\n\t" \
  "ds_read_b64_tr_b16 v[196:197], v172 offset:2048\n\t" \
  "v_mfma_f32_16x16x32_bf16 v[212:215], v[144:147], a[4:7], v[212:215]\n\t" \
  "ds_read_b64_tr_b16 v[198:199], v172 offset:2560\n\t" \
  "v_mfma_f32_16x16x32_bf16 v[212:215], v[148:151], a[20:23], v[212:215]\n\t" \
  "buffer_load_dwordx4 v168, s[92:95], s66 offen offset:2048 lds\n\t" \
  "v_mfma_f32_16x16x32_bf16 v[212:215], v[152:155], a[36:39], v[212:215]\n\t" \
  "ds_read_b64_tr_b16 v[200:201], v172 offset:4096\n\t" \
  "v_mfma_f32_16x16x32_bf16 v[212:215], v[156:159], a[52:55], v[212:215]\n\t" \
  "ds_read_b64_tr_b16 v[202:203], v172 offset:4608\n\t" \
  "v_mfma_f32_16x16x32_bf16 v[216:219], v[144:147], a[8:11], v[216:219]\n\t" \
  "ds_read_b64_tr_b16 v[204:205], v172 offset:6144\n\t" \
  "v_mfma_f32_16x16x32_bf16 v[216:219], v[148:151], a[24:27], v[216:219]\n\t" \
  "v_mfma_f32_16x16x32_bf16 v[216:219], v[152:155], a[40:43], v[216:219]\n\t" \
  "ds_read_b64_tr_b16 v[206:207], v172 offset:6656\n\t" \
  "v_mfma_f32_16x16x32_bf16 v[216:219], v[156:159], a[56:59], v[216:219]\n\t" \
  "v_mfma_f32_16x16x32_bf16 v[220:223], v[144:147], a[12:15], v[220:223]\n\t" \
  "v_mfma_f32_16x16x32_bf16 v[220:223], v[148:151], a[28:31], v[220:223]\n\t" \
  "v_mfma_f32_16x16x32_bf16 v[220:223], v[152:155], a[44:47], v[220:223]\n\t" \
  "v_mfma_f32_16x16x32_bf16 v[220:223], v[156:159], a[60:63], v[220:223]\n\t" \
  "s_waitcnt lgkmcnt(0)\n\t" \
  "v_mfma_f32_16x16x32_bf16 v[0:3], v[192:195], a[0:3], v[0:3]\n\t" \
  "ds_read_b64_tr_b16 v[144:145], v173 offset:0\n\t" \
  "v_mfma_f32_16x16x32_bf16 v[0:3], v[196:199], a[16:19], v[0:3]\n\t" \
  "v_mfma_f32_16x16x32_bf16 v[0:3], v[200:203], a[32:35], v[0:3]\n\t" \
  "ds_read_b64_tr_b16 v[146:147], v173 offset:512\n\t" \
  "v_mfma_f32_16x16x32_bf16 v[0:3], v[204:207], a[48:51], v[0:3]\n\t" \
  "ds_read_b64_tr_b16 v[148:149], v173 offset:2048\n\t" \
  "v_mfma_f32_16x16x32_bf16 v[4:7], v[192:195], a[4:7], v[4:7]\n\t" \
  "ds_read_b64_tr_b16 v[150:151], v173 offset:2560\n\t" \
  "v_mfma_f32_16x16x32_bf16 v[4:7], v[196:199], a[20:23], v[4:7]\n\t" \
  "v_mfma_f32_16x16x32_bf16 v[4:7], v[200:203], a[36:39], v[4:7]\n\t" \
  "ds_read_b64_tr_b16 v[152:153], v173 offset:4096\n\t" \
  "v_mfma_f32_16x16x32_bf16 v[4:7], v[204:207], a[52:55], v[4:7]\n\t" \
  "ds_read_b64_tr_b16 v[154:155], v173 offset:4608\n\t" \
  "v_mfma_f32_16x16x32_bf16 v[8:11], v[192:195], a[8:11], v[8:11]\n\t" \
  "ds_read_b64_tr_b16 v[156:157], v173 offset:6144\n\t" \
  "v_mfma_f32_16x16x32_bf16 v[8:11], v[196:199], a[24:27], v[8:11]\n\t" \
  "v_mfma_f32_16x16x32_bf16 v[8:11], v[200:203], a[40:43], v[8:11]\n\t" \
  "ds_read_b64_tr_b16 v[158:159], v173 offset:6656\n\t" \
  "v_mfma_f32_16x16x32_bf16 v[8:11], v[204:207], a[56:59], v[8:11]\n\t" \
  "v_mfma_f32_16x16x32_bf16 v[12:15], v[192:195], a[12:15], v[12:15]\n\t" \
  "v_mfma_f32_16x16x32_bf16 v[12:15], v[196:199], a[28:31], v[12:15]\n\t" \
  "v_mfma_f32_16x16x32_bf16 v[12:15], v[200:203], a[44:47], v[12:15]\n\t" \
  "v_mfma_f32_16x16x32_bf16 v[12:15], v[204:207], a[60:63], v[12:15]\n\t" \
  "s_waitcnt lgkmcnt(0)\n\t" \
  "s_waitcnt vmcnt(26)\n\t" \
  "s_barrier\n\t" \
  "v_mfma_f32_16x16x32_bf16 v[16:19], v[144:147], a[0:3], v[16:19]\n\t" \
  "ds_read_b64_tr_b16 v[192:193], v177 offset:0\n\t" \
  "v_mfma_f32_16x16x32_bf16 v[16:19], v[148:151], a[16:19], v[16:19]\n\t" \
  "v_mfma_f32_16x16x32_bf16 v[16:19], v[152:155], a[32:35], v[16:19]\n\t" \
  "ds_read_b64_tr_b16 v[194:195], v177 offset:1792\n\t" \
  "v_mfma_f32_16x16x32_bf16 v[16:19], v[156:159], a[48:51], v[16:19]\n\t" \
  "ds_read_b64_tr_b16 v[196:197], v177 offset:7168\n\t" \
  "v_mfma_f32_16x16x32_bf16 v[20:23], v[144:147], a[4:7], v[20:23]\n\t" \
  "ds_read_b64_tr_b16 v[198:199], v177 offset:8960\n\t" \
  "v_mfma_f32_16x16x32_bf16 v[20:23], v[148:151], a[20:23], v[20:23]\n\t" \
  "v_mfma_f32_16x16x32_bf16 v[20:23], v[152:155], a[36:39], v[20:23]\n\t" \
  "ds_read_b64_tr_b16 v[200:201], v177 offset:14336\n\t" \
  "v_mfma_f32_16x16x32_bf16 v[20:23], v[156:159], a[52:55], v[20:23]\n\t" \
  "ds_read_b64_tr_b16 v[202:203], v177 offset:16128\n\t" \
  "v_mfma_f32_16x16x32_bf16 v[24:27], v[144:147], a[8:11], v[24:27]\n\t" \
  "ds_read_b64_tr_b16 v[204:205], v177 offset:21504\n\t" \
  "v_mfma_f32_16x16x32_bf16 v[24:27], v[148:151], a[24:27], v[24:27]\n\t" \
  "v_mfma_f32_16x16x32_bf16 v[24:27], v[152:155], a[40:43], v[24:27]\n\t" \
  "ds_read_b64_tr_b16 v[206:207], v177 offset:23296\n\t" \
  "v_mfma_f32_16x16x32_bf16 v[24:27], v[156:159], a[56:59], v[24:27]\n\t" \
  "v_mfma_f32_16x16x32_bf16 v[28:31], v[144:147], a[12:15], v[28:31]\n\t" \
  "v_mfma_f32_16x16x32_bf16 v[28:31], v[148:151], a[28:31], v[28:31]\n\t" \
  "v_mfma_f32_16x16x32_bf16 v[28:31], v[152:155], a[44:47], v[28:31]\n\t" \
  "v_mfma_f32_16x16x32_bf16 v[28:31], v[156:159], a[60:63], v[28:31]\n\t" \
  "s_waitcnt lgkmcnt(0)\n\t" \
  "s_waitcnt vmcnt(21)\n\t" \
  "v_mfma_f32_16x16x32_bf16 v[32:35], v[192:195], a[64:67], v[32:35]\n\t" \
  "ds_read_b64_tr_b16 v[144:145], v177 offset:32\n\t" \
  "v_mfma_f32_16x16x32_bf16 v[32:35], v[196:199], a[80:83], v[32:35]\n\t" \
  "s_mov_b32 s64, 24576\n\t" \
  "buffer_load_dwordx4 a[0:3], v178, s[80:83], s64 offen offset:0 sc1\n\t" \
  "v_mfma_f32_16x16x32_bf16 v[32:35], v[200:203], a[96:99], v[32:35]\n\t" \
  "ds_read_b64_tr_b16 v[146:147], v177 offset:1824\n\t" \
  "v_mfma_f32_16x16x32_bf16 v[32:35], v[204:207], a[112:115], v[32:35]\n\t" \
  "ds_read_b64_tr_b16 v[148:149], v177 offset:7200\n\t" \
  "s_waitcnt vmcnt(18)\n\t" \
  "v_mfma_f32_16x16x32_bf16 v[36:39], v[192:195], a[68:71], v[36:39]\n\t" \
  "ds_read_b64_tr_b16 v[150:151], v177 offset:8992\n\t" \
  "v_mfma_f32_16x16x32_bf16 v[36:39], v[196:199], a[84:87], v[36:39]\n\t" \
  "buffer_load_dwordx4 a[16:19], v178, s[80:83], s64 offen offset:1024 sc1\n\t" \
  "v_mfma_f32_16x16x32_bf16 v[36:39], v[200:203], a[100:103], v[36:39]\n\t" \
  "ds_read_b64_tr_b16 v[152:153], v177 offset:14368\n\t" \
  "v_mfma_f32_16x16x32_bf16 v[36:39], v[204:207], a[116:119], v[36:39]\n\t" \
  "ds_read_b64_tr_b16 v[154:155], v177 offset:16160\n\t" \
  "s_waitcnt vmcnt(15)\n\t" \
  "v_mfma_f32_16x16x32_bf16 v[40:43], v[192:195], a[72:75], v[40:43]\n\t" \
  "ds_read_b64_tr_b16 v[156:157], v177 offset:21536\n\t" \
  "v_mfma_f32_16x16x32_bf16 v[40:43], v[196:199], a[88:91], v[40:43]\n\t" \
  "buffer_load_dwordx4 a[32:35], v178, s[80:83], s64 offen offset:2048 sc1\n\t" \
  "v_mfma_f32_16x16x32_bf16 v[40:43], v[200:203], a[104:107], v[40:43]\n\t" \
  "ds_read_b64_tr_b16 v[158:159], v177 offset:23328\n\t" \
  "v_mfma_f32_16x16x32_bf16 v[40:43], v[204:207], a[120:123], v[40:43]\n\t" \
  "s_waitcnt vmcnt(12)\n\t" \
  "v_mfma_f32_16x16x32_bf16 v[44:47], v[192:195], a[76:79], v[44:47]\n\t" \
  "v_mfma_f32_16x16x32_bf16 v[44:47], v[196:199], a[92:95], v[44:47]\n\t" \
  "buffer_load_dwordx4 a[48:51], v178, s[80:83], s64 offen offset:3072 sc1\n\t" \
  "v_mfma_f32_16x16x32_bf16 v[44:47], v[200:203], a[108:111], v[44:47]\n\t" \
  "v_mfma_f32_16x16x32_bf16 v[44:47], v[204:207], a[124:127], v[44:47]\n\t" \
  "s_waitcnt lgkmcnt(0)\n\t" \
  "v_mfma_f32_16x16x32_bf16 v[48:51], v[144:147], a[64:67], v[48:51]\n\t" \
  "ds_read_b64_tr_b16 v[192:193], v177 offset:64\n\t" \
  "v_mfma_f32_16x16x32_bf16 v[48:51], v[148:151], a[80:83], v[48:51]\n\t" \
  "buffer_load_dwordx4 a[4:7], v179, s[80:83], s64 offen offset:0 sc1\n\t" \
  "v_mfma_f32_16x16x32_bf16 v[48:51], v[152:155], a[96:99], v[48:51]\n\t" \
  "ds_read_b64_tr_b16 v[194:195], v177 offset:1856\n\t" \
  "v_mfma_f32_16x16x32_bf16 v[48:51], v[156:159], a[112:115], v[48:51]\n\t" \
  "ds_read_b64_tr_b16 v[196:197], v177 offset:7232\n\t" \
  "v_mfma_f32_16x16x32_bf16 v[52:55], v[144:147], a[68:71], v[52:55]\n\t" \
  "ds_read_b64_tr_b16 v[198:199], v177 offset:9024\n\t" \
  "v_mfma_f32_16x16x32_bf16 v[52:55], v[148:151], a[84:87], v[52:55]\n\t" \
  "buffer_load_dwordx4 a[20:23], v179, s[80:83], s64 offen offset:1024 sc1\n\t" \
  "v_mfma_f32_16x16x32_bf16 v[52:55], v[152:155], a[100:103], v[52:55]\n\t" \
  "ds_read_b64_tr_b16 v[200:201], v177 offset:14400\n\t" \
  "v_mfma_f32_16x16x32_bf16 v[52:55], v[156:159], a[116:119], v[52:55]\n\t" \
  "ds_read_b64_tr_b16 v[202:203], v177 offset:16192\n\t" \
  "v_mfma_f32_16x16x32_bf16 v[56:59], v[144:147], a[72:75], v[56:59]\n\t" \
  "ds_read_b64_tr_b16 v[204:205], v177 offset:21568\n\t" \
  "v_mfma_f32_16x16x32_bf16 v[56:59], v[148:151], a[88:91], v[56:59]\n\t" \
  "buffer_load_dwordx4 a[36:39], v179, s[80:83], s64 offen offset:2048 sc1\n\t" \
  "v_mfma_f32_16x16x32_bf16 v[56:59], v[152:155], a[104:107], v[56:59]\n\t" \
  "ds_read_b64_tr_b16 v[206:207], v177 offset:23360\n\t" \
  "v_mfma_f32_16x16x32_bf16 v[56:59], v[156:159], a[120:123], v[56:59]\n\t" \
  "v_mfma_f32_16x16x32_bf16 v[60:63], v[144:147], a[76:79], v[60:63]\n\t" \
  "v_mfma_f32_16x16x32_bf16 v[60:63], v[148:151], a[92:95], v[60:63]\n\t" \
  "buffer_load_dwordx4 a[52:55], v179, s[80:83], s64 offen offset:3072 sc1\n\t" \
  "v_mfma_f32_16x16x32_bf16 v[60:63], v[152:155], a[108:111], v[60:63]\n\t" \
  "v_mfma_f32_16x16x32_bf16 v[60:63], v[156:159], a[124:127], v[60:63]\n\t" \
  "s_waitcnt lgkmcnt(0)\n\t" \
  "v_mfma_f32_16x16x32_bf16 v[64:67], v[192:195], a[64:67], v[64:67]\n\t" \
  "ds_read_b64_tr_b16 v[144:145], v177 offset:96\n\t" \
  "v_mfma_f32_16x16x32_bf16 v[64:67], v[196:199], a[80:83], v[64:67]\n\t" \
  "buffer_load_dwordx4 a[8:11], v180, s[80:83], s64 offen offset:0 sc1\n\t" \
  "v_mfma_f32_16x16x32_bf16 v[64:67], v[200:203], a[96:99], v[64:67]\n\t" \
  "ds_read_b64_tr_b16 v[146:147], v177 offset:1888\n\t" \
  "v_mfma_f32_16x16x32_bf16 v[64:67], v[204:207], a[112:115], v[64:67]\n\t" \
  "ds_read_b64_tr_b16 v[148:149], v177 offset:7264\n\t" \
  "v_mfma_f32_16x16x32_bf16 v[68:71], v[192:195], a[68:71], v[68:71]\n\t" \
  "ds_read_b64_tr_b16 v[150:151], v177 offset:9056\n\t" \
  "v_mfma_f32_16x16x32_bf16 v[68:71], v[196:199], a[84:87], v[68:71]\n\t" \
  "buffer_load_dwordx4 a[24:27], v180, s[80:83], s64 offen offset:1024 sc1\n\t" \
  "v_mfma_f32_16x16x32_bf16 v[68:71], v[200:203], a[100:103], v[68:71]\n\t" \
  "ds_read_b64_tr_b16 v[152:153], v177 offset:14432\n\t" \
  "v_mfma_f32_16x16x32_bf16 v[68:71], v[204:207], a[116:119], v[68:71]\n\t" \
  "ds_read_b64_tr_b16 v[154:155], v177 offset:16224\n\t" \
  "v_mfma_f32_16x16x32_bf16 v[72:75], v[192:195], a[72:75], v[72:75]\n\t" \
  "ds_read_b64_tr_b16 v[156:157], v177 offset:21600\n\t" \
  "v_mfma_f32_16x16x32_bf16 v[72:75], v[196:199], a[88:91], v[72:75]\n\t" \
  "buffer_load_dwordx4 a[40:43], v180, s[80:83], s64 offen offset:2048 sc1\n\t" \
  "v_mfma_f32_16x16x32_bf16 v[72:75], v[200:203], a[104:107], v[72:75]\n\t" \
  "ds_read_b64_tr_b16 v[158:159], v177 offset:23392\n\t" \
  "v_mfma_f32_16x16x32_bf16 v[72:75], v[204:207], a[120:123], v[72:75]\n\t" \
  "v_mfma_f32_16x16x32_bf16 v[76:79], v[192:195], a[76:79], v[76:79]\n\t" \
  "v_mfma_f32_16x16x32_bf16 v[76:79], v[196:199], a[92:95], v[76:79]\n\t" \
  "buffer_load_dwordx4 a[56:59], v180, s[80:83], s64 offen offset:3072 sc1\n\t" \
  "v_mfma_f32_16x16x32_bf16 v[76:79], v[200:203], a[108:111], v[76:79]\n\t" \
  "v_mfma_f32_16x16x32_bf16 v[76:79], v[204:207], a[124:127], v[76:79]\n\t" \
  "s_waitcnt lgkmcnt(0)\n\t" \
  "v_mfma_f32_16x16x32_bf16 v[80:83], v[144:147], a[64:67], v[80:83]\n\t" \
  "ds_read_b64_tr_b16 v[192:193], v177 offset:128\n\t" \
  "v_mfma_f32_16x16x32_bf16 v[80:83], v[148:151], a[80:83], v[80:83]\n\t" \
  "buffer_load_dwordx4 a[12:15], v181, s[80:83], s64 offen offset:0 sc1\n\t" \
  "v_mfma_f32_16x16x32_bf16 v[80:83], v[152:155], a[96:99], v[80:83]\n\t" \
  "ds_read_b64_tr_b16 v[194:195], v177 offset:1920\n\t" \
  "v_mfma_f32_16x16x32_bf16 v[80:83], v[156:159], a[112:115], v[80:83]\n\t" \
  "ds_read_b64_tr_b16 v[196:197], v177 offset:7296\n\t" \
  "v_mfma_f32_16x16x32_bf16 v[84:87], v[144:147], a[68:71], v[84:87]\n\t" \
  "ds_read_b64_tr_b16 v[198:199], v177 offset:9088\n\t" \
  "v_mfma_f32_16x16x32_bf16 v[84:87], v[148:151], a[84:87], v[84:87]\n\t" \
  "buffer_load_dwordx4 a[28:31], v181, s[80:83], s64 offen offset:1024 sc1\n\t" \
  "v_mfma_f32_16x16x32_bf16 v[84:87], v[152:155], a[100:103], v[84:87]\n\t" \
  "ds_read_b64_tr_b16 v[200:201], v177 offset:14464\n\t" \
  "v_mfma_f32_16x16x32_bf16 v[84:87], v[156:159], a[116:119], v[84:87]\n\t" \
  "ds_read_b64_tr_b16 v[202:203], v177 offset:16256\n\t" \
  "v_mfma_f32_16x16x32_bf16 v[88:91], v[144:147], a[72:75], v[88:91]\n\t" \
  "ds_read_b64_tr_b16 v[204:205], v177 offset:21632\n\t" \
  "v_mfma_f32_16x16x32_bf16 v[88:91], v[148:151], a[88:91], v[88:91]\n\t" \
  "buffer_load_dwordx4 a[44:47], v181, s[80:83], s64 offen offset:2048 sc1\n\t" \
  "v_mfma_f32_16x16x32_bf16 v[88:91], v[152:155], a[104:107], v[88:91]\n\t" \
  "ds_read_b64_tr_b16 v[206:207], v177 offset:23424\n\t" \
  "v_mfma_f32_16x16x32_bf16 v[88:91], v[156:159], a[120:123], v[88:91]\n\t" \
  "v_mfma_f32_16x16x32_bf16 v[92:95], v[144:147], a[76:79], v[92:95]\n\t" \
  "v_mfma_f32_16x16x32_bf16 v[92:95], v[148:151], a[92:95], v[92:95]\n\t" \
  "buffer_load_dwordx4 a[60:63], v181, s[80:83], s64 offen offset:3072 sc1\n\t" \
  "v_mfma_f32_16x16x32_bf16 v[92:95], v[152:155], a[108:111], v[92:95]\n\t" \
  "s_add_u32 m0, s73, 36864\n\t" \
  "s_mov_b32 s65, 1835008\n\t" \
  "v_mfma_f32_16x16x32_bf16 v[92:95], v[156:159], a[124:127], v[92:95]\n\t" \
  "s_waitcnt lgkmcnt(0)\n\t" \
  "v_mfma_f32_16x16x32_bf16 v[96:99], v[192:195], a[64:67], v[96:99]\n\t" \
  "ds_read_b64_tr_b16 v[144:145], v177 offset:160\n\t" \
  "v_mfma_f32_16x16x32_bf16 v[96:99], v[196:199], a[80:83], v[96:99]\n\t" \
  "buffer_load_dwordx4 v160, s[88:91], s65 offen offset:0 lds\n\t" \
  "v_mfma_f32_16x16x32_bf16 v[96:99], v[200:203], a[96:99], v[96:99]\n\t" \
  "ds_read_b64_tr_b16 v[146:147], v177 offset:1952\n\t" \
  "v_mfma_f32_16x16x32_bf16 v[96:99], v[204:207], a[112:115], v[96:99]\n\t" \
  "ds_read_b64_tr_b16 v[148:149], v177 offset:7328\n\t" \
  "v_mfma_f32_16x16x32_bf16 v[100:103], v[192:195], a[68:71], v[100:103]\n\t" \
  "ds_read_b64_tr_b16 v[150:151], v177 offset:9120\n\t" \
  "v_mfma_f32_16x16x32_bf16 v[100:103], v[196:199], a[84:87], v[100:103]\n\t" \
  "buffer_load_dwordx4 v161, s[88:91], s65 offen offset:1024 lds\n\t" \
  "v_mfma_f32_16x16x32_bf16 v[100:103], v[200:203], a[100:103], v[100:103]\n\t" \
  "ds_read_b64_tr_b16 v[152:153], v177 offset:14496\n\t" \
  "v_mfma_f32_16x16x32_bf16 v[100:103], v[204:207], a[116:119], v[100:103]\n\t" \
  "ds_read_b64_tr_b16 v[154:155], v177 offset:16288\n\t" \
  "v_mfma_f32_16x16x32_bf16 v[104:107], v[192:195], a[72:75], v[104:107]\n\t" \
  "ds_read_b64_tr_b16 v[156:157], v177 offset:21664\n\t" \
  "v_mfma_f32_16x16x32_bf16 v[104:107], v[196:199], a[88:91], v[104:107]\n\t" \
  "s_add_u32 m0, s74, 36864\n\t" \
  "s_mov_b32 s66, 6422528\n\t" \
  "v_mfma_f32_16x16x32_bf16 v[104:107], v[200:203], a[104:107], v[104:107]\n\t" \
  "ds_read_b64_tr_b16 v[158:159], v177 offset:23456\n\t" \
  "v_mfma_f32_16x16x32_bf16 v[104:107], v[204:207], a[120:123], v[104:107]\n\t" \
  "v_mfma_f32_16x16x32_bf16 v[108:111], v[192:195], a[76:79], v[108:111]\n\t" \
  "v_mfma_f32_16x16x32_bf16 v[108:111], v[196:199], a[92:95], v[108:111]\n\t" \
  "buffer_load_dwordx4 v162, s[92:95], s66 offen offset:0 lds\n\t" \
  "v_mfma_f32_16x16x32_bf16 v[108:111], v[200:203], a[108:111], v[108:111]\n\t" \
  "v_mfma_f32_16x16x32_bf16 v[108:111], v[204:207], a[124:127], v[108:111]\n\t" \
  "s_waitcnt lgkmcnt(0)\n\t" \
  "v_mfma_f32_16x16x32_bf16 v[112:115], v[144:147], a[64:67], v[112:115]\n\t" \
  "ds_read_b64_tr_b16 v[192:193], v177 offset:192\n\t" \
  "v_mfma_f32_16x16x32_bf16 v[112:115], v[148:151], a[80:83], v[112:115]\n\t" \
  "buffer_load_dwordx4 v163, s[92:95], s66 offen offset:1024 lds\n\t" \
  "v_mfma_f32_16x16x32_bf16 v[112:115], v[152:155], a[96:99], v[112:115]\n\t" \
  "ds_read_b64_tr_b16 v[194:195], v177 offset:1984\n\t" \
  "v_mfma_f32_16x16x32_bf16 v[112:115], v[156:159], a[112:115], v[112:115]\n\t" \
  "ds_read_b64_tr_b16 v[196:197], v177 offset:7360\n\t" \
  "v_mfma_f32_16x16x32_bf16 v[116:119], v[144:147], a[68:71], v[116:119]\n\t" \
  "ds_read_b64_tr_b16 v[198:199], v177 offset:9152\n\t" \
  "v_mfma_f32_16x16x32_bf16 v[116:119], v[148:151], a[84:87], v[116:119]\n\t" \
  "buffer_load_dwordx4 v164, s[92:95], s66 offen offset:2048 lds\n\t" \
  "v_mfma_f32_16x16x32_bf16 v[116:119], v[152:155], a[100:103], v[116:119]\n\t" \
  "ds_read_b64_tr_b16 v[200:201], v177 offset:14528\n\t" \
  "v_mfma_f32_16x16x32_bf16 v[116:119], v[156:159], a[116:119], v[116:119]\n\t" \
  "ds_read_b64_tr_b16 v[202:203], v177 offset:16320\n\t" \
  "v_mfma_f32_16x16x32_bf16 v[120:123], v[144:147], a[72:75], v[120:123]\n\t" \
  "ds_read_b64_tr_b16 v[204:205], v177 offset:21696\n\t" \
  "v_mfma_f32_16x16x32_bf16 v[120:123], v[148:151], a[88:91], v[120:123]\n\t" \
  "buffer_load_dwordx4 v165, s[92:95], s66 offen offset:3072 lds\n\t" \
  "v_mfma_f32_16x16x32_bf16 v[120:123], v[152:155], a[104:107], v[120:123]\n\t" \
  "ds_read_b64_tr_b16 v[206:207], v177 offset:23488\n\t" \
  "v_mfma_f32_16x16x32_bf16 v[120:123], v[156:159], a[120:123], v[120:123]\n\t" \
  "s_add_u32 m0, s74, 40960\n\t" \
  "v_mfma_f32_16x16x32_bf16 v[124:127], v[144:147], a[76:79], v[124:127]\n\t" \
  "v_mfma_f32_16x16x32_bf16 v[124:127], v[148:151], a[92:95], v[124:127]\n\t" \
  "buffer_load_dwordx4 v166, s[92:95], s66 offen offset:0 lds\n\t" \
  "v_mfma_f32_16x16x32_bf16 v[124:127], v[152:155], a[108:111], v[124:127]\n\t" \
  "v_mfma_f32_16x16x32_bf16 v[124:127], v[156:159], a[124:127], v[124:127]\n\t" \
  "s_waitcnt lgkmcnt(0)\n\t" \
  "v_mfma_f32_16x16x32_bf16 v[208:211], v[192:195], a[64:67], v[208:211]\n\t" \
  "ds_read_b64_tr_b16 v[144:145], v175 offset:0\n\t" \
  "v_mfma_f32_16x16x32_bf16 v[208:211], v[196:199], a[80:83], v[208:211]\n\t" \
  "buffer_load_dwordx4 v167, s[92:95], s66 offen offset:1024 lds\n\t" \
  "v_mfma_f32_16x16x32_bf16 v[208:211], v[200:203], a[96:99], v[208:211]\n\t" \
  "ds_read_b64_tr_b16 v[146:147], v175 offset:512\n\t" \
  "v_mfma_f32_16x16x32_bf16 v[208:211], v[204:207], a[112:115], v[208:211]\n\t" \
  "ds_read_b64_tr_b16 v[148:149], v175 offset:2048\n\t" \
  "v_mfma_f32_16x16x32_bf16 v[212:215], v[192:195], a[68:71], v[212:215]\n\t" \
  "ds_read_b64_tr_b16 v[150:151], v175 offset:2560\n\t" \
  "v_mfma_f32_16x16x32_bf16 v[212:215], v[196:199], a[84:87], v[212:215]\n\t" \
  "buffer_load_dwordx4 v168, s[92:95], s66 offen offset:2048 lds\n\t" \
  "v_mfma_f32_16x16x32_bf16 v[212:215], v[200:203], a[100:103], v[212:215]\n\t" \
  "ds_read_b64_tr_b16 v[152:153], v175 offset:4096\n\t" \
  "v_mfma_f32_16x16x32_bf16 v[212:215], v[204:207], a[116:119], v[212:215]\n\t" \
  "ds_read_b64_tr_b16 v[154:155], v175 offset:4608\n\t" \
  "v_mfma_f32_16x16x32_bf16 v[216:219], v[192:195], a[72:75], v[216:219]\n\t" \
  "ds_read_b64_tr_b16 v[156:157], v175 offset:6144\n\t" \
  "v_mfma_f32_16x16x32_bf16 v[216:219], v[196:199], a[88:91], v[216:219]\n\t" \
  "v_mfma_f32_16x16x32_bf16 v[216:219], v[200:203], a[104:107], v[216:219]\n\t" \
  "ds_read_b64_tr_b16 v[158:159], v175 offset:6656\n\t" \
  "v_mfma_f32_16x16x32_bf16 v[216:219], v[204:207], a[120:123], v[216:219]\n\t" \
  "v_mfma_f32_16x16x32_bf16 v[220:223], v[192:195], a[76:79], v[220:223]\n\t" \
  "v_mfma_f32_16x16x32_bf16 v[220:223], v[196:199], a[92:95], v[220:223]\n\t" \
  "v_mfma_f32_16x16x32_bf16 v[220:223], v[200:203], a[108:111], v[220:223]\n\t" \
  "v_mfma_f32_16x16x32_bf16 v[220:223], v[204:207], a[124:127], v[220:223]\n\t" \
  "s_waitcnt lgkmcnt(0)\n\t" \
  "v_mfma_f32_16x16x32_bf16 v[0:3], v[144:147], a[64:67], v[0:3]\n\t" \
  "ds_read_b64_tr_b16 v[192:193], v176 offset:0\n\t" \
  "v_mfma_f32_16x16x32_bf16 v[0:3], v[148:151], a[80:83], v[0:3]\n\t" \
  "v_mfma_f32_16x16x32_bf16 v[0:3], v[152:155], a[96:99], v[0:3]\n\t" \
  "ds_read_b64_tr_b16 v[194:195], v176 offset:512\n\t" \
  "v_mfma_f32_16x16x32_bf16 v[0:3], v[156:159], a[112:115], v[0:3]\n\t" \
  "ds_read_b64_tr_b16 v[196:197], v176 offset:2048\n\t" \
  "v_mfma_f32_16x16x32_bf16 v[4:7], v[144:147], a[68:71], v[4:7]\n\t" \
  "ds_read_b64_tr_b16 v[198:199], v176 offset:2560\n\t" \
  "v_mfma_f32_16x16x32_bf16 v[4:7], v[148:151], a[84:87], v[4:7]\n\t" \
  "v_mfma_f32_16x16x32_bf16 v[4:7], v[152:155], a[100:103], v[4:7]\n\t" \
  "ds_read_b64_tr_b16 v[200:201], v176 offset:4096\n\t" \
  "v_mfma_f32_16x16x32_bf16 v[4:7], v[156:159], a[116:119], v[4:7]\n\t" \
  "ds_read_b64_tr_b16 v[202:203], v176 offset:4608\n\t" \
  "v_mfma_f32_16x16x32_bf16 v[8:11], v[144:147], a[72:75], v[8:11]\n\t" \
  "ds_read_b64_tr_b16 v[204:205], v176 offset:6144\n\t" \
  "v_mfma_f32_16x16x32_bf16 v[8:11], v[148:151], a[88:91], v[8:11]\n\t" \
  "v_mfma_f32_16x16x32_bf16 v[8:11], v[152:155], a[104:107], v[8:11]\n\t" \
  "ds_read_b64_tr_b16 v[206:207], v176 offset:6656\n\t" \
  "v_mfma_f32_16x16x32_bf16 v[8:11], v[156:159], a[120:123], v[8:11]\n\t" \
  "v_mfma_f32_16x16x32_bf16 v[12:15], v[144:147], a[76:79], v[12:15]\n\t" \
  "v_mfma_f32_16x16x32_bf16 v[12:15], v[148:151], a[92:95], v[12:15]\n\t" \
  "v_mfma_f32_16x16x32_bf16 v[12:15], v[152:155], a[108:111], v[12:15]\n\t" \
  "v_mfma_f32_16x16x32_bf16 v[12:15], v[156:159], a[124:127], v[12:15]\n\t" \
  "s_waitcnt lgkmcnt(0)\n\t" \
  "s_waitcnt vmcnt(25)\n\t" \
  "s_barrier\n\t" \
  "v_mfma_f32_16x16x32_bf16 v[16:19], v[192:195], a[64:67], v[16:19]\n\t" \
  "ds_read_b64_tr_b16 v[144:145], v171 offset:0\n\t" \
  "v_mfma_f32_16x16x32_bf16 v[16:19], v[196:199], a[80:83], v[16:19]\n\t" \
  "v_mfma_f32_16x16x32_bf16 v[16:19], v[200:203], a[96:99], v[16:19]\n\t" \
  "ds_read_b64_tr_b16 v[146:147], v171 offset:1792\n\t" \
  "v_mfma_f32_16x16x32_bf16 v[16:19], v[204:207], a[112:115], v[16:19]\n\t" \
  "ds_read_b64_tr_b16 v[148:149], v171 offset:7168\n\t" \
  "v_mfma_f32_16x16x32_bf16 v[20:23], v[192:195], a[68:71], v[20:23]\n\t" \
  "ds_read_b64_tr_b16 v[150:151], v171 offset:8960\n\t" \
  "v_mfma_f32_16x16x32_bf16 v[20:23], v[196:199], a[84:87], v[20:23]\n\t" \
  "v_mfma_f32_16x16x32_bf16 v[20:23], v[200:203], a[100:103], v[20:23]\n\t" \
  "ds_read_b64_tr_b16 v[152:153], v171 offset:14336\n\t" \
  "v_mfma_f32_16x16x32_bf16 v[20:23], v[204:207], a[116:119], v[20:23]\n\t" \
  "ds_read_b64_tr_b16 v[154:155], v171 offset:16128\n\t" \
  "v_mfma_f32_16x16x32_bf16 v[24:27], v[192:195], a[72:75], v[24:27]\n\t" \
  "ds_read_b64_tr_b16 v[156:157], v171 offset:21504\n\t" \
  "v_mfma_f32_16x16x32_bf16 v[24:27], v[196:199], a[88:91], v[24:27]\n\t" \
  "v_mfma_f32_16x16x32_bf16 v[24:27], v[200:203], a[104:107], v[24:27]\n\t" \
  "ds_read_b64_tr_b16 v[158:159], v171 offset:23296\n\t" \
  "v_mfma_f32_16x16x32_bf16 v[24:27], v[204:207], a[120:123], v[24:27]\n\t" \
  "v_mfma_f32_16x16x32_bf16 v[28:31], v[192:195], a[76:79], v[28:31]\n\t" \
  "v_mfma_f32_16x16x32_bf16 v[28:31], v[196:199], a[92:95], v[28:31]\n\t" \
  "v_mfma_f32_16x16x32_bf16 v[28:31], v[200:203], a[108:111], v[28:31]\n\t" \
  "v_mfma_f32_16x16x32_bf16 v[28:31], v[204:207], a[124:127], v[28:31]\n\t" \
  "s_waitcnt lgkmcnt(0)\n\t" \
  "s_waitcnt vmcnt(21)\n\t" \
  "v_mfma_f32_16x16x32_bf16 v[32:35], v[144:147], a[0:3], v[32:35]\n\t" \
  "ds_read_b64_tr_b16 v[192:193], v171 offset:32\n\t" \
  "v_mfma_f32_16x16x32_bf16 v[32:35], v[148:151], a[16:19], v[32:35]\n\t" \
  "s_mov_b32 s64, 28672\n\t" \
  "buffer_load_dwordx4 a[64:67], v178, s[80:83], s64 offen offset:0 sc1\n\t" \
  "v_mfma_f32_16x16x32_bf16 v[32:35], v[152:155], a[32:35], v[32:35]\n\t" \
  "ds_read_b64_tr_b16 v[194:195], v171 offset:1824\n\t" \
  "v_mfma_f32_16x16x32_bf16 v[32:35], v[156:159], a[48:51], v[32:35]\n\t" \
  "ds_read_b64_tr_b16 v[196:197], v171 offset:7200\n\t" \
  "s_waitcnt vmcnt(18)\n\t" \
  "v_mfma_f32_16x16x32_bf16 v[36:39], v[144:147], a[4:7], v[36:39]\n\t" \
  "ds_read_b64_tr_b16 v[198:199], v171 offset:8992\n\t" \
  "v_mfma_f32_16x16x32_bf16 v[36:39], v[148:151], a[20:23], v[36:39]\n\t" \
  "buffer_load_dwordx4 a[80:83], v178, s[80:83], s64 offen offset:1024 sc1\n\t" \
  "v_mfma_f32_16x16x32_bf16 v[36:39], v[152:155], a[36:39], v[36:39]\n\t" \
  "ds_read_b64_tr_b16 v[200:201], v171 offset:14368\n\t" \
  "v_mfma_f32_16x16x32_bf16 v[36:39], v[156:159], a[52:55], v[36:39]\n\t" \
  "ds_read_b64_tr_b16 v[202:203], v171 offset:16160\n\t" \
  "s_waitcnt vmcnt(15)\n\t" \
  "v_mfma_f32_16x16x32_bf16 v[40:43], v[144:147], a[8:11], v[40:43]\n\t" \
  "ds_read_b64_tr_b16 v[204:205], v171 offset:21536\n\t" \
  "v_mfma_f32_16x16x32_bf16 v[40:43], v[148:151], a[24:27], v[40:43]\n\t" \
  "buffer_load_dwordx4 a[96:99], v178, s[80:83], s64 offen offset:2048 sc1\n\t" \
  "v_mfma_f32_16x16x32_bf16 v[40:43], v[152:155], a[40:43], v[40:43]\n\t" \
  "ds_read_b64_tr_b16 v[206:207], v171 offset:23328\n\t" \
  "v_mfma_f32_16x16x32_bf16 v[40:43], v[156:159], a[56:59], v[40:43]\n\t" \
  "s_waitcnt vmcnt(12)\n\t" \
  "v_mfma_f32_16x16x32_bf16 v[44:47], v[144:147], a[12:15], v[44:47]\n\t" \
  "v_mfma_f32_16x16x32_bf16 v[44:47], v[148:151], a[28:31], v[44:47]\n\t" \
  "buffer_load_dwordx4 a[112:115], v178, s[80:83], s64 offen offset:3072 sc1\n\t" \
  "v_mfma_f32_16x16x32_bf16 v[44:47], v[152:155], a[44:47], v[44:47]\n\t" \
  "v_mfma_f32_16x16x32_bf16 v[44:47], v[156:159], a[60:63], v[44:47]\n\t" \
  "s_waitcnt lgkmcnt(0)\n\t" \
  "v_mfma_f32_16x16x32_bf16 v[48:51], v[192:195], a[0:3], v[48:51]\n\t" \
  "ds_read_b64_tr_b16 v[144:145], v171 offset:64\n\t" \
  "v_mfma_f32_16x16x32_bf16 v[48:51], v[196:199], a[16:19], v[48:51]\n\t" \
  "buffer_load_dwordx4 a[68:71], v179, s[80:83], s64 offen offset:0 sc1\n\t" \
  "v_mfma_f32_16x16x32_bf16 v[48:51], v[200:203], a[32:35], v[48:51]\n\t" \
  "ds_read_b64_tr_b16 v[146:147], v171 offset:1856\n\t" \
  "v_mfma_f32_16x16x32_bf16 v[48:51], v[204:207], a[48:51], v[48:51]\n\t" \
  "ds_read_b64_tr_b16 v[148:149], v171 offset:7232\n\t" \
  "v_mfma_f32_16x16x32_bf16 v[52:55], v[192:195], a[4:7], v[52:55]\n\t" \
  "ds_read_b64_tr_b16 v[150:151], v171 offset:9024\n\t" \
  "v_mfma_f32_16x16x32_bf16 v[52:55], v[196:199], a[20:23], v[52:55]\n\t" \
  "buffer_load_dwordx4 a[84:87], v179, s[80:83], s64 offen offset:1024 sc1\n\t" \
  "v_mfma_f32_16x16x32_bf16 v[52:55], v[200:203], a[36:39], v[52:55]\n\t" \
  "ds_read_b64_tr_b16 v[152:153], v171 offset:14400\n\t" \
  "v_mfma_f32_16x16x32_bf16 v[52:55], v[204:207], a[52:55], v[52:55]\n\t" \
  "ds_read_b64_tr_b16 v[154:155], v171 offset:16192\n\t" \
  "v_mfma_f32_16x16x32_bf16 v[56:59], v[192:195], a[8:11], v[56:59]\n\t" \
  "ds_read_b64_tr_b16 v[156:157], v171 offset:21568\n\t" \
  "v_mfma_f32_16x16x32_bf16 v[56:59], v[196:199], a[24:27], v[56:59]\n\t" \
  "buffer_load_dwordx4 a[100:103], v179, s[80:83], s64 offen offset:2048 sc1\n\t" \
  "v_mfma_f32_16x16x32_bf16 v[56:59], v[200:203], a[40:43], v[56:59]\n\t" \
  "ds_read_b64_tr_b16 v[158:159], v171 offset:23360\n\t" \
  "v_mfma_f32_16x16x32_bf16 v[56:59], v[204:207], a[56:59], v[56:59]\n\t" \
  "v_mfma_f32_16x16x32_bf16 v[60:63], v[192:195], a[12:15], v[60:63]\n\t" \
  "v_mfma_f32_16x16x32_bf16 v[60:63], v[196:199], a[28:31], v[60:63]\n\t" \
  "buffer_load_dwordx4 a[116:119], v179, s[80:83], s64 offen offset:3072 sc1\n\t" \
  "v_mfma_f32_16x16x32_bf16 v[60:63], v[200:203], a[44:47], v[60:63]\n\t" \
  "v_mfma_f32_16x16x32_bf16 v[60:63], v[204:207], a[60:63], v[60:63]\n\t" \
  "s_waitcnt lgkmcnt(0)\n\t" \
  "v_mfma_f32_16x16x32_bf16 v[64:67], v[144:147], a[0:3], v[64:67]\n\t" \
  "ds_read_b64_tr_b16 v[192:193], v171 offset:96\n\t" \
  "v_mfma_f32_16x16x32_bf16 v[64:67], v[148:151], a[16:19], v[64:67]\n\t" \
  "buffer_load_dwordx4 a[72:75], v180, s[80:83], s64 offen offset:0 sc1\n\t" \
  "v_mfma_f32_16x16x32_bf16 v[64:67], v[152:155], a[32:35], v[64:67]\n\t" \
  "ds_read_b64_tr_b16 v[194:195], v171 offset:1888\n\t" \
  "v_mfma_f32_16x16x32_bf16 v[64:67], v[156:159], a[48:51], v[64:67]\n\t" \
  "ds_read_b64_tr_b16 v[196:197], v171 offset:7264\n\t" \
  "v_mfma_f32_16x16x32_bf16 v[68:71], v[144:147], a[4:7], v[68:71]\n\t" \
  "ds_read_b64_tr_b16 v[198:199], v171 offset:9056\n\t" \
  "v_mfma_f32_16x16x32_bf16 v[68:71], v[148:151], a[20:23], v[68:71]\n\t" \
  "buffer_load_dwordx4 a[88:91], v180, s[80:83], s64 offen offset:1024 sc1\n\t" \
  "v_mfma_f32_16x16x32_bf16 v[68:71], v[152:155], a[36:39], v[68:71]\n\t" \
  "ds_read_b64_tr_b16 v[200:201], v171 offset:14432\n\t" \
  "v_mfma_f32_16x16x32_bf16 v[68:71], v[156:159], a[52:55], v[68:71]\n\t" \
  "ds_read_b64_tr_b16 v[202:203], v171 offset:16224\n\t" \
  "v_mfma_f32_16x16x32_bf16 v[72:75], v[144:147], a[8:11], v[72:75]\n\t" \
  "ds_read_b64_tr_b16 v[204:205], v171 offset:21600\n\t" \
  "v_mfma_f32_16x16x32_bf16 v[72:75], v[148:151], a[24:27], v[72:75]\n\t" \
  "buffer_load_dwordx4 a[104:107], v180, s[80:83], s64 offen offset:2048 sc1\n\t" \
  "v_mfma_f32_16x16x32_bf16 v[72:75], v[152:155], a[40:43], v[72:75]\n\t" \
  "ds_read_b64_tr_b16 v[206:207], v171 offset:23392\n\t" \
  "v_mfma_f32_16x16x32_bf16 v[72:75], v[156:159], a[56:59], v[72:75]\n\t" \
  "v_mfma_f32_16x16x32_bf16 v[76:79], v[144:147], a[12:15], v[76:79]\n\t" \
  "v_mfma_f32_16x16x32_bf16 v[76:79], v[148:151], a[28:31], v[76:79]\n\t" \
  "buffer_load_dwordx4 a[120:123], v180, s[80:83], s64 offen offset:3072 sc1\n\t" \
  "v_mfma_f32_16x16x32_bf16 v[76:79], v[152:155], a[44:47], v[76:79]\n\t" \
  "v_mfma_f32_16x16x32_bf16 v[76:79], v[156:159], a[60:63], v[76:79]\n\t" \
  "s_waitcnt lgkmcnt(0)\n\t" \
  "v_mfma_f32_16x16x32_bf16 v[80:83], v[192:195], a[0:3], v[80:83]\n\t" \
  "ds_read_b64_tr_b16 v[144:145], v171 offset:128\n\t" \
  "v_mfma_f32_16x16x32_bf16 v[80:83], v[196:199], a[16:19], v[80:83]\n\t" \
  "buffer_load_dwordx4 a[76:79], v181, s[80:83], s64 offen offset:0 sc1\n\t" \
  "v_mfma_f32_16x16x32_bf16 v[80:83], v[200:203], a[32:35], v[80:83]\n\t" \
  "ds_read_b64_tr_b16 v[146:147], v171 offset:1920\n\t" \
  "v_mfma_f32_16x16x32_bf16 v[80:83], v[204:207], a[48:51], v[80:83]\n\t" \
  "ds_read_b64_tr_b16 v[148:149], v171 offset:7296\n\t" \
  "v_mfma_f32_16x16x32_bf16 v[84:87], v[192:195], a[4:7], v[84:87]\n\t" \
  "ds_read_b64_tr_b16 v[150:151], v171 offset:9088\n\t" \
  "v_mfma_f32_16x16x32_bf16 v[84:87], v[196:199], a[20:23], v[84:87]\n\t" \
  "buffer_load_dwordx4 a[92:95], v181, s[80:83], s64 offen offset:1024 sc1\n\t" \
  "v_mfma_f32_16x16x32_bf16 v[84:87], v[200:203], a[36:39], v[84:87]\n\t" \
  "ds_read_b64_tr_b16 v[152:153], v171 offset:14464\n\t" \
  "v_mfma_f32_16x16x32_bf16 v[84:87], v[204:207], a[52:55], v[84:87]\n\t" \
  "ds_read_b64_tr_b16 v[154:155], v171 offset:16256\n\t" \
  "v_mfma_f32_16x16x32_bf16 v[88:91], v[192:195], a[8:11], v[88:91]\n\t" \
  "ds_read_b64_tr_b16 v[156:157], v171 offset:21632\n\t" \
  "v_mfma_f32_16x16x32_bf16 v[88:91], v[196:199], a[24:27], v[88:91]\n\t" \
  "buffer_load_dwordx4 a[108:111], v181, s[80:83], s64 offen offset:2048 sc1\n\t" \
  "v_mfma_f32_16x16x32_bf16 v[88:91], v[200:203], a[40:43], v[88:91]\n\t" \
  "ds_read_b64_tr_b16 v[158:159], v171 offset:23424\n\t" \
  "v_mfma_f32_16x16x32_bf16 v[88:91], v[204:207], a[56:59], v[88:91]\n\t" \
  "v_mfma_f32_16x16x32_bf16 v[92:95], v[192:195], a[12:15], v[92:95]\n\t" \
  "v_mfma_f32_16x16x32_bf16 v[92:95], v[196:199], a[28:31], v[92:95]\n\t" \
  "buffer_load_dwordx4 a[124:127], v181, s[80:83], s64 offen offset:3072 sc1\n\t" \
  "v_mfma_f32_16x16x32_bf16 v[92:95], v[200:203], a[44:47], v[92:95]\n\t" \
  "v_mfma_f32_16x16x32_bf16 v[92:95], v[204:207], a[60:63], v[92:95]\n\t" \
  "s_waitcnt lgkmcnt(0)\n\t" \
  "v_mfma_f32_16x16x32_bf16 v[96:99], v[144:147], a[0:3], v[96:99]\n\t" \
  "ds_read_b64_tr_b16 v[192:193], v171 offset:160\n\t" \
  "v_mfma_f32_16x16x32_bf16 v[96:99], v[148:151], a[16:19], v[96:99]\n\t" \
  "global_load_dword v191, v187, s[78:79] sc1\n\t" \
  "v_mfma_f32_16x16x32_bf16 v[96:99], v[152:155], a[32:35], v[96:99]\n\t" \
  "ds_read_b64_tr_b16 v[194:195], v171 offset:1952\n\t" \
  "v_mfma_f32_16x16x32_bf16 v[96:99], v[156:159], a[48:51], v[96:99]\n\t" \
  "ds_read_b64_tr_b16 v[196:197], v171 offset:7328\n\t" \
  "v_mfma_f32_16x16x32_bf16 v[100:103], v[144:147], a[4:7], v[100:103]\n\t" \
  "ds_read_b64_tr_b16 v[198:199], v171 offset:9120\n\t" \
  "v_mfma_f32_16x16x32_bf16 v[100:103], v[148:151], a[20:23], v[100:103]\n\t" \
  "s_mov_b32 m0, s75\n\t" \
  "v_mfma_f32_16x16x32_bf16 v[100:103], v[152:155], a[36:39], v[100:103]\n\t" \
  "ds_read_b64_tr_b16 v[200:201], v171 offset:14496\n\t" \
  "v_mfma_f32_16x16x32_bf16 v[100:103], v[156:159], a[52:55], v[100:103]\n\t" \
  "ds_read_b64_tr_b16 v[202:203], v171 offset:16288\n\t" \
  "v_mfma_f32_16x16x32_bf16 v[104:107], v[144:147], a[8:11], v[104:107]\n\t" \
  "ds_read_b64_tr_b16 v[204:205], v171 offset:21664\n\t" \
  "v_mfma_f32_16x16x32_bf16 v[104:107], v[148:151], a[24:27], v[104:107]\n\t" \
  "global_load_lds_dword v186, s[46:47]\n\t" \
  "v_mfma_f32_16x16x32_bf16 v[104:107], v[152:155], a[40:43], v[104:107]\n\t" \
  "ds_read_b64_tr_b16 v[206:207], v171 offset:23456\n\t" \
  "v_mfma_f32_16x16x32_bf16 v[104:107], v[156:159], a[56:59], v[104:107]\n\t" \
  "s_mov_b32 s67, 4096\n\t" \
  "s_mov_b32 s68, 230400\n\t" \
  "s_mov_b32 s69, 234496\n\t" \
  "v_mfma_f32_16x16x32_bf16 v[108:111], v[144:147], a[12:15], v[108:111]\n\t" \
  "v_mfma_f32_16x16x32_bf16 v[108:111], v[148:151], a[28:31], v[108:111]\n\t" \
  "v_mfma_f32_16x16x32_bf16 v[108:111], v[152:155], a[44:47], v[108:111]\n\t" \
  "v_mfma_f32_16x16x32_bf16 v[108:111], v[156:159], a[60:63], v[108:111]\n\t" \
  "s_waitcnt lgkmcnt(0)\n\t" \
  "v_mfma_f32_16x16x32_bf16 v[112:115], v[192:195], a[0:3], v[112:115]\n\t" \
  "ds_read_b64_tr_b16 v[144:145], v171 offset:192\n\t" \
  "v_mfma_f32_16x16x32_bf16 v[112:115], v[196:199], a[16:19], v[112:115]\n\t" \
  "v_mfma_f32_16x16x32_bf16 v[112:115], v[200:203], a[32:35], v[112:115]\n\t" \
  "ds_read_b64_tr_b16 v[146:147], v171 offset:1984\n\t" \
  "v_mfma_f32_16x16x32_bf16 v[112:115], v[204:207], a[48:51], v[112:115]\n\t" \
  "ds_read_b64_tr_b16 v[148:149], v171 offset:7360\n\t" \
  "v_mfma_f32_16x16x32_bf16 v[116:119], v[192:195], a[4:7], v[116:119]\n\t" \
  "ds_read_b64_tr_b16 v[150:151], v171 offset:9152\n\t" \
  "v_mfma_f32_16x16x32_bf16 v[116:119], v[196:199], a[20:23], v[116:119]\n\t" \
  "v_mfma_f32_16x16x32_bf16 v[116:119], v[200:203], a[36:39], v[116:119]\n\t" \
  "ds_read_b64_tr_b16 v[152:153], v171 offset:14528\n\t" \
  "v_mfma_f32_16x16x32_bf16 v[116:119], v[204:207], a[52:55], v[116:119]\n\t" \
  "ds_read_b64_tr_b16 v[154:155], v171 offset:16320\n\t" \
  "v_mfma_f32_16x16x32_bf16 v[120:123], v[192:195], a[8:11], v[120:123]\n\t" \
  "ds_read_b64_tr_b16 v[156:157], v171 offset:21696\n\t" \
  "v_mfma_f32_16x16x32_bf16 v[120:123], v[196:199], a[24:27], v[120:123]\n\t" \
  "v_mfma_f32_16x16x32_bf16 v[120:123], v[200:203], a[40:43], v[120:123]\n\t" \
  "ds_read_b64_tr_b16 v[158:159], v171 offset:23488\n\t" \
  "v_mfma_f32_16x16x32_bf16 v[120:123], v[204:207], a[56:59], v[120:123]\n\t" \
  "v_mfma_f32_16x16x32_bf16 v[124:127], v[192:195], a[12:15], v[124:127]\n\t" \
  "v_mfma_f32_16x16x32_bf16 v[124:127], v[196:199], a[28:31], v[124:127]\n\t" \
  "v_mfma_f32_16x16x32_bf16 v[124:127], v[200:203], a[44:47], v[124:127]\n\t" \
  "v_mfma_f32_16x16x32_bf16 v[124:127], v[204:207], a[60:63], v[124:127]\n\t" \
  "s_waitcnt lgkmcnt(0)\n\t" \
  "v_mfma_f32_16x16x32_bf16 v[208:211], v[144:147], a[0:3], v[208:211]\n\t" \
  "ds_read_b64_tr_b16 v[192:193], v169 offset:0\n\t" \
  "v_mfma_f32_16x16x32_bf16 v[208:211], v[148:151], a[16:19], v[208:211]\n\t" \
  "v_mfma_f32_16x16x32_bf16 v[208:211], v[152:155], a[32:35], v[208:211]\n\t" \
  "ds_read_b64_tr_b16 v[194:195], v169 offset:512\n\t" \
  "v_mfma_f32_16x16x32_bf16 v[208:211], v[156:159], a[48:51], v[208:211]\n\t" \
  "ds_read_b64_tr_b16 v[196:197], v169 offset:2048\n\t" \
  "v_mfma_f32_16x16x32_bf16 v[212:215], v[144:147], a[4:7], v[212:215]\n\t" \
  "ds_read_b64_tr_b16 v[198:199], v169 offset:2560\n\t" \
  "v_mfma_f32_16x16x32_bf16 v[212:215], v[148:151], a[20:23], v[212:215]\n\t" \
  "v_mfma_f32_16x16x32_bf16 v[212:215], v[152:155], a[36:39], v[212:215]\n\t" \
  "ds_read_b64_tr_b16 v[200:201], v169 offset:4096\n\t" \
  "v_mfma_f32_16x16x32_bf16 v[212:215], v[156:159], a[52:55], v[212:215]\n\t" \
  "ds_read_b64_tr_b16 v[202:203], v169 offset:4608\n\t" \
  "v_mfma_f32_16x16x32_bf16 v[216:219], v[144:147], a[8:11], v[216:219]\n\t" \
  "ds_read_b64_tr_b16 v[204:205], v169 offset:6144\n\t" \
  "v_mfma_f32_16x16x32_bf16 v[216:219], v[148:151], a[24:27], v[216:219]\n\t" \
  "v_mfma_f32_16x16x32_bf16 v[216:219], v[152:155], a[40:43], v[216:219]\n\t" \
  "ds_read_b64_tr_b16 v[206:207], v169 offset:6656\n\t" \
  "v_mfma_f32_16x16x32_bf16 v[216:219], v[156:159], a[56:59], v[216:219]\n\t" \
  "v_mfma_f32_16x16x32_bf16 v[220:223], v[144:147], a[12:15], v[220:223]\n\t" \
  "v_mfma_f32_16x16x32_bf16 v[220:223], v[148:151], a[28:31], v[220:223]\n\t" \
  "v_mfma_f32_16x16x32_bf16 v[220:223], v[152:155], a[44:47], v[220:223]\n\t" \
  "v_mfma_f32_16x16x32_bf16 v[220:223], v[156:159], a[60:63], v[220:223]\n\t" \
  "s_waitcnt lgkmcnt(0)\n\t" \
  "v_mfma_f32_16x16x32_bf16 v[0:3], v[192:195], a[0:3], v[0:3]\n\t" \
  "ds_read_b64_tr_b16 v[144:145], v170 offset:0\n\t" \
  "v_mfma_f32_16x16x32_bf16 v[0:3], v[196:199], a[16:19], v[0:3]\n\t" \
  "v_mfma_f32_16x16x32_bf16 v[0:3], v[200:203], a[32:35], v[0:3]\n\t" \
  "ds_read_b64_tr_b16 v[146:147], v170 offset:512\n\t" \
  "v_mfma_f32_16x16x32_bf16 v[0:3], v[204:207], a[48:51], v[0:3]\n\t" \
  "ds_read_b64_tr_b16 v[148:149], v170 offset:2048\n\t" \
  "v_mfma_f32_16x16x32_bf16 v[4:7], v[192:195], a[4:7], v[4:7]\n\t" \
  "ds_read_b64_tr_b16 v[150:151], v170 offset:2560\n\t" \
  "v_mfma_f32_16x16x32_bf16 v[4:7], v[196:199], a[20:23], v[4:7]\n\t" \
  "v_mfma_f32_16x16x32_bf16 v[4:7], v[200:203], a[36:39], v[4:7]\n\t" \
  "ds_read_b64_tr_b16 v[152:153], v170 offset:4096\n\t" \
  "v_mfma_f32_16x16x32_bf16 v[4:7], v[204:207], a[52:55], v[4:7]\n\t" \
  "ds_read_b64_tr_b16 v[154:155], v170 offset:4608\n\t" \
  "v_mfma_f32_16x16x32_bf16 v[8:11], v[192:195], a[8:11], v[8:11]\n\t" \
  "ds_read_b64_tr_b16 v[156:157], v170 offset:6144\n\t" \
  "v_mfma_f32_16x16x32_bf16 v[8:11], v[196:199], a[24:27], v[8:11]\n\t" \
  "v_mfma_f32_16x16x32_bf16 v[8:11], v[200:203], a[40:43], v[8:11]\n\t" \
  "ds_read_b64_tr_b16 v[158:159], v170 offset:6656\n\t" \
  "v_mfma_f32_16x16x32_bf16 v[8:11], v[204:207], a[56:59], v[8:11]\n\t" \
  "v_mfma_f32_16x16x32_bf16 v[12:15], v[192:195], a[12:15], v[12:15]\n\t" \
  "v_mfma_f32_16x16x32_bf16 v[12:15], v[196:199], a[28:31], v[12:15]\n\t" \
  "v_mfma_f32_16x16x32_bf16 v[12:15], v[200:203], a[44:47], v[12:15]\n\t" \
  "v_mfma_f32_16x16x32_bf16 v[12:15], v[204:207], a[60:63], v[12:15]\n\t" \
  "s_waitcnt lgkmcnt(0)\n\t" \
  "s_waitcnt vmcnt(18)\n\t" \
  "s_barrier\n\t" \
  "v_mfma_f32_16x16x32_bf16 v[16:19], v[144:147], a[0:3], v[16:19]\n\t" \
  "ds_read_b64_tr_b16 v[192:193], v174 offset:0\n\t" \
  "v_mfma_f32_16x16x32_bf16 v[16:19], v[148:151], a[16:19], v[16:19]\n\t" \
  "v_mfma_f32_16x16x32_bf16 v[16:19], v[152:155], a[32:35], v[16:19]\n\t" \
  "ds_read_b64_tr_b16 v[194:195], v174 offset:1792\n\t" \
  "v_mfma_f32_16x16x32_bf16 v[16:19], v[156:159], a[48:51], v[16:19]\n\t" \
  "ds_read_b64_tr_b16 v[196:197], v174 offset:7168\n\t" \
  "v_mfma_f32_16x16x32_bf16 v[20:23], v[144:147], a[4:7], v[20:23]\n\t" \
  "ds_read_b64_tr_b16 v[198:199], v174 offset:8960\n\t" \
  "v_mfma_f32_16x16x32_bf16 v[20:23], v[148:151], a[20:23], v[20:23]\n\t" \
  "v_mfma_f32_16x16x32_bf16 v[20:23], v[152:155], a[36:39], v[20:23]\n\t" \
  "ds_read_b64_tr_b16 v[200:201], v174 offset:14336\n\t" \
  "v_mfma_f32_16x16x32_bf16 v[20:23], v[156:159], a[52:55], v[20:23]\n\t" \
  "ds_read_b64_tr_b16 v[202:203], v174 offset:16128\n\t" \
  "v_mfma_f32_16x16x32_bf16 v[24:27], v[144:147], a[8:11], v[24:27]\n\t" \
  "ds_read_b64_tr_b16 v[204:205], v174 offset:21504\n\t" \
  "v_mfma_f32_16x16x32_bf16 v[24:27], v[148:151], a[24:27], v[24:27]\n\t" \
  "v_mfma_f32_16x16x32_bf16 v[24:27], v[152:155], a[40:43], v[24:27]\n\t" \
  "ds_read_b64_tr_b16 v[206:207], v174 offset:23296\n\t" \
  "v_mfma_f32_16x16x32_bf16 v[24:27], v[156:159], a[56:59], v[24:27]\n\t" \
  "v_mfma_f32_16x16x32_bf16 v[28:31], v[144:147], a[12:15], v[28:31]\n\t" \
  "v_mfma_f32_16x16x32_bf16 v[28:31], v[148:151], a[28:31], v[28:31]\n\t" \
  "v_mfma_f32_16x16x32_bf16 v[28:31], v[152:155], a[44:47], v[28:31]\n\t" \
  "v_mfma_f32_16x16x32_bf16 v[28:31], v[156:159], a[60:63], v[28:31]\n\t" \
  "s_waitcnt vmcnt(1)\n\t" \
  "s_mov_b32 s63, 0x40000\n\t" \
  "LKV%=:\n\t" \
  "v_cmp_gt_u32 vcc, 32, v191\n\t" \
  "s_cbranch_vccz LKVOK%=\n\t" \
  "s_cmp_lg_u32 s76, 3\n\t" \
  "s_cbranch_scc1 LKVN%=\n\t" \
  "global_load_dwordx4 v[128:131], v178, s[78:79] offset:2048 sc1\n\t" \
  "s_waitcnt vmcnt(0)\n\t" \
  "global_store_dwordx4 v178, v[128:131], s[78:79] offset:3072 sc1\n\t" \
  "LKVN%=:\n\t" \
  "s_sleep 1\n\t" \
  "global_load_dword v191, v187, s[78:79] sc1\n\t" \
  "s_waitcnt vmcnt(0)\n\t" \
  "s_sub_u32 s63, s63, 1\n\t" \
  "s_cbranch_scc0 LKV%=\n\t" \
  "LKVOK%=:\n\t" \
  "s_waitcnt lgkmcnt(0)\n\t" \
  "v_mfma_f32_16x16x32_bf16 v[32:35], v[192:195], a[64:67], v[32:35]\n\t" \
  "ds_read_b64_tr_b16 v[144:145], v174 offset:32\n\t" \
  "v_mfma_f32_16x16x32_bf16 v[32:35], v[196:199], a[80:83], v[32:35]\n\t" \
  "s_add_u32 m0, s77, 0\n\t" \
  "s_mov_b32 s65, 0\n\t" \
  "v_mfma_f32_16x16x32_bf16 v[32:35], v[200:203], a[96:99], v[32:35]\n\t" \
  "ds_read_b64_tr_b16 v[146:147], v174 offset:1824\n\t" \
  "v_mfma_f32_16x16x32_bf16 v[32:35], v[204:207], a[112:115], v[32:35]\n\t" \
  "ds_read_b64_tr_b16 v[148:149], v174 offset:7200\n\t" \
  "v_mfma_f32_16x16x32_bf16 v[36:39], v[192:195], a[68:71], v[36:39]\n\t" \
  "ds_read_b64_tr_b16 v[150:151], v174 offset:8992\n\t" \
  "v_mfma_f32_16x16x32_bf16 v[36:39], v[196:199], a[84:87], v[36:39]\n\t" \
  "buffer_load_dwordx4 v178, s[56:59], s65 offen offset:0 sc1 lds\n\t" \
  "v_mfma_f32_16x16x32_bf16 v[36:39], v[200:203], a[100:103], v[36:39]\n\t" \
  "ds_read_b64_tr_b16 v[152:153], v174 offset:14368\n\t" \
  "v_mfma_f32_16x16x32_bf16 v[36:39], v[204:207], a[116:119], v[36:39]\n\t" \
  "ds_read_b64_tr_b16 v[154:155], v174 offset:16160\n\t" \
  "v_mfma_f32_16x16x32_bf16 v[40:43], v[192:195], a[72:75], v[40:43]\n\t" \
  "ds_read_b64_tr_b16 v[156:157], v174 offset:21536\n\t" \
  "v_mfma_f32_16x16x32_bf16 v[40:43], v[196:199], a[88:91], v[40:43]\n\t" \
  "buffer_load_dwordx4 v178, s[56:59], s65 offen offset:1024 sc1 lds\n\t" \
  "v_mfma_f32_16x16x32_bf16 v[40:43], v[200:203], a[104:107], v[40:43]\n\t" \
  "ds_read_b64_tr_b16 v[158:159], v174 offset:23328\n\t" \
  "v_mfma_f32_16x16x32_bf16 v[40:43], v[204:207], a[120:123], v[40:43]\n\t" \
  "buffer_load_dwordx4 v178, s[56:59], s65 offen offset:2048 sc1 lds\n\t" \
  "v_mfma_f32_16x16x32_bf16 v[44:47], v[192:195], a[76:79], v[44:47]\n\t" \
  "buffer_load_dwordx4 v178, s[56:59], s65 offen offset:3072 sc1 lds\n\t" \
  "v_mfma_f32_16x16x32_bf16 v[44:47], v[196:199], a[92:95], v[44:47]\n\t" \
  "s_add_u32 m0, s77, 16384\n\t" \
  "s_mov_b32 s65, 16384\n\t" \
  "v_mfma_f32_16x16x32_bf16 v[44:47], v[200:203], a[108:111], v[44:47]\n\t" \
  "buffer_load_dwordx4 v178, s[56:59], s65 offen offset:0 sc1 lds\n\t" \
  "v_mfma_f32_16x16x32_bf16 v[44:47], v[204:207], a[124:127], v[44:47]\n\t" \
  "buffer_load_dwordx4 v178, s[56:59], s65 offen offset:1024 sc1 lds\n\t" \
  "s_waitcnt lgkmcnt(0)\n\t" \
  "v_mfma_f32_16x16x32_bf16 v[48:51], v[144:147], a[64:67], v[48:51]\n\t" \
  "ds_read_b64_tr_b16 v[192:193], v174 offset:64\n\t" \
  "v_mfma_f32_16x16x32_bf16 v[48:51], v[148:151], a[80:83], v[48:51]\n\t" \
  "buffer_load_dwordx4 v178, s[56:59], s65 offen offset:2048 sc1 lds\n\t" \
  "v_mfma_f32_16x16x32_bf16 v[48:51], v[152:155], a[96:99], v[48:51]\n\t" \
  "ds_read_b64_tr_b16 v[194:195], v174 offset:1856\n\t" \
  "v_mfma_f32_16x16x32_bf16 v[48:51], v[156:159], a[112:115], v[48:51]\n\t" \
  "ds_read_b64_tr_b16 v[196:197], v174 offset:7232\n\t" \
  "v_mul_f32 v32, v32, |v32|\n\t" \
  "v_mul_f32 v33, v33, |v33|\n\t" \
  "v_mul_f32 v34, v34, |v34|\n\t" \
  "v_mfma_f32_16x16x32_bf16 v[52:55], v[144:147], a[68:71], v[52:55]\n\t" \
  "ds_read_b64_tr_b16 v[198:199], v174 offset:9024\n\t" \
  "v_mul_f32 v35, v35, |v35|\n\t" \
  "v_mul_f32 v36, v36, |v36|\n\t" \
  "v_mfma_f32_16x16x32_bf16 v[52:55], v[148:151], a[84:87], v[52:55]\n\t" \
  "buffer_load_dwordx4 v178, s[56:59], s65 offen offset:3072 sc1 lds\n\t" \
  "v_mul_f32 v37, v37, |v37|\n\t" \
  "v_mul_f32 v38, v38, |v38|\n\t" \
  "v_mfma_f32_16x16x32_bf16 v[52:55], v[152:155], a[100:103], v[52:55]\n\t" \
  "ds_read_b64_tr_b16 v[200:201], v174 offset:14400\n\t" \
  "v_mul_f32 v39, v39, |v39|\n\t" \
  "v_cvt_pk_bf16_f32 v32, v32, v33\n\t" \
  "v_mfma_f32_16x16x32_bf16 v[52:55], v[156:159], a[116:119], v[52:55]\n\t" \
  "ds_read_b64_tr_b16 v[202:203], v174 offset:16192\n\t" \
  "v_cvt_pk_bf16_f32 v33, v34, v35\n\t" \
  "v_cvt_pk_bf16_f32 v34, v36, v37\n\t" \
  "v_cvt_pk_bf16_f32 v35, v38, v39\n\t" \
  "v_mfma_f32_16x16x32_bf16 v[56:59], v[144:147], a[72:75], v[56:59]\n\t" \
  "ds_read_b64_tr_b16 v[204:205], v174 offset:21568\n\t" \
  "v_pk_max_i16 v32, v32, 0\n\t" \
  "v_pk_max_i16 v34, v34, 0\n\t" \
  "v_mfma_f32_16x16x32_bf16 v[56:59], v[148:151], a[88:91], v[56:59]\n\t" \
  "s_add_u32 m0, s77, 32768\n\t" \
  "s_mov_b32 s65, 32768\n\t" \
  "v_pk_max_i16 v33, v33, 0\n\t" \
  "v_pk_max_i16 v35, v35, 0\n\t" \
  "v_mfma_f32_16x16x32_bf16 v[56:59], v[152:155], a[104:107], v[56:59]\n\t" \
  "ds_read_b64_tr_b16 v[206:207], v174 offset:23360\n\t" \
  "v_mul_f32 v40, v40, |v40|\n\t" \
  "v_mul_f32 v41, v41, |v41|\n\t" \
  "v_mfma_f32_16x16x32_bf16 v[56:59], v[156:159], a[120:123], v[56:59]\n\t" \
  "buffer_load_dwordx4 v178, s[56:59], s65 offen offset:0 sc1 lds\n\t" \
  "v_mul_f32 v42, v42, |v42|\n\t" \
  "v_mul_f32 v43, v43, |v43|\n\t" \
  "v_mul_f32 v44, v44, |v44|\n\t" \
  "v_mfma_f32_16x16x32_bf16 v[60:63], v[144:147], a[76:79], v[60:63]\n\t" \
  "buffer_load_dwordx4 v178, s[56:59], s65 offen offset:1024 sc1 lds\n\t" \
  "v_mul_f32 v45, v45, |v45|\n\t" \
  "v_mul_f32 v46, v46, |v46|\n\t" \
  "v_mfma_f32_16x16x32_bf16 v[60:63], v[148:151], a[92:95], v[60:63]\n\t" \
  "buffer_load_dwordx4 v178, s[56:59], s65 offen offset:2048 sc1 lds\n\t" \
  "v_mul_f32 v47, v47, |v47|\n\t" \
  "v_cvt_pk_bf16_f32 v40, v40, v41\n\t" \
  "v_mfma_f32_16x16x32_bf16 v[60:63], v[152:155], a[108:111], v[60:63]\n\t" \
  "buffer_load_dwordx4 v178, s[56:59], s65 offen offset:3072 sc1 lds\n\t" \
  "v_cvt_pk_bf16_f32 v41, v42, v43\n\t" \
  "v_cvt_pk_bf16_f32 v42, v44, v45\n\t" \
  "v_mfma_f32_16x16x32_bf16 v[60:63], v[156:159], a[124:127], v[60:63]\n\t" \
  "v_cvt_pk_bf16_f32 v43, v46, v47\n\t" \
  "v_pk_max_i16 v40, v40, 0\n\t" \
  "v_pk_max_i16 v42, v42, 0\n\t" \
  "s_waitcnt lgkmcnt(0)\n\t" \
  "v_mfma_f32_16x16x32_bf16 v[64:67], v[192:195], a[64:67], v[64:67]\n\t" \
  "ds_read_b64_tr_b16 v[144:145], v174 offset:96\n\t" \
  "v_pk_max_i16 v41, v41, 0\n\t" \
  "v_pk_max_i16 v43, v43, 0\n\t" \
  "v_mfma_f32_16x16x32_bf16 v[64:67], v[196:199], a[80:83], v[64:67]\n\t" \
  "v_permlane16_swap_b32 v32, v34\n\t" \
  "v_permlane16_swap_b32 v33, v35\n\t" \
  "v_mfma_f32_16x16x32_bf16 v[64:67], v[200:203], a[96:99], v[64:67]\n\t" \
  "ds_read_b64_tr_b16 v[146:147], v174 offset:1888\n\t" \
  "v_permlane16_swap_b32 v40, v42\n\t" \
  "v_permlane16_swap_b32 v41, v43\n\t" \
  "v_mfma_f32_16x16x32_bf16 v[64:67], v[204:207], a[112:115], v[64:67]\n\t" \
  "ds_read_b64_tr_b16 v[148:149], v174 offset:7264\n\t" \
  "v_mul_f32 v48, v48, |v48|\n\t" \
  "v_mfma_f32_16x16x32_bf16 v[68:71], v[192:195], a[68:71], v[68:71]\n\t" \
  "ds_read_b64_tr_b16 v[150:151], v174 offset:9056\n\t" \
  "v_mul_f32 v49, v49, |v49|\n\t" \
  "v_mul_f32 v50, v50, |v50|\n\t" \
  "v_mfma_f32_16x16x32_bf16 v[68:71], v[196:199], a[84:87], v[68:71]\n\t" \
  "buffer_store_dwordx4 v[32:35], v178, s[52:55], 0 offen offset:0 sc1\n\t" \
  "v_mul_f32 v51, v51, |v51|\n\t" \
  "v_mul_f32 v52, v52, |v52|\n\t" \
  "v_mfma_f32_16x16x32_bf16 v[68:71], v[200:203], a[100:103], v[68:71]\n\t" \
  "ds_read_b64_tr_b16 v[152:153], v174 offset:14432\n\t" \
  "v_mul_f32 v53, v53, |v53|\n\t" \
  "v_mul_f32 v54, v54, |v54|\n\t" \
  "v_mfma_f32_16x16x32_bf16 v[68:71], v[204:207], a[116:119], v[68:71]\n\t" \
  "ds_read_b64_tr_b16 v[154:155], v174 offset:16224\n\t" \
  "v_mul_f32 v55, v55, |v55|\n\t" \
  "v_cvt_pk_bf16_f32 v48, v48, v49\n\t" \
  "v_cvt_pk_bf16_f32 v49, v50, v51\n\t" \
  "v_mfma_f32_16x16x32_bf16 v[72:75], v[192:195], a[72:75], v[72:75]\n\t" \
  "ds_read_b64_tr_b16 v[156:157], v174 offset:21600\n\t" \
  "v_cvt_pk_bf16_f32 v50, v52, v53\n\t" \
  "v_cvt_pk_bf16_f32 v51, v54, v55\n\t" \
  "v_mfma_f32_16x16x32_bf16 v[72:75], v[196:199], a[88:91], v[72:75]\n\t" \
  "buffer_store_dwordx4 v[40:43], v178, s[52:55], s68 offen offset:0 sc1\n\t" \
  "v_pk_max_i16 v48, v48, 0\n\t" \
  "v_pk_max_i16 v50, v50, 0\n\t" \
  "v_mfma_f32_16x16x32_bf16 v[72:75], v[200:203], a[104:107], v[72:75]\n\t" \
  "ds_read_b64_tr_b16 v[158:159], v174 offset:23392\n\t" \
  "v_pk_max_i16 v49, v49, 0\n\t" \
  "v_pk_max_i16 v51, v51, 0\n\t" \
  "v_mfma_f32_16x16x32_bf16 v[72:75], v[204:207], a[120:123], v[72:75]\n\t" \
  "v_mul_f32 v56, v56, |v56|\n\t" \
  "v_mul_f32 v57, v57, |v57|\n\t" \
  "v_mul_f32 v58, v58, |v58|\n\t" \
  "v_mfma_f32_16x16x32_bf16 v[76:79], v[192:195], a[76:79], v[76:79]\n\t" \
  "v_mul_f32 v59, v59, |v59|\n\t" \
  "v_mul_f32 v60, v60, |v60|\n\t" \
  "v_mfma_f32_16x16x32_bf16 v[76:79], v[196:199], a[92:95], v[76:79]\n\t" \
  "v_mul_f32 v61, v61, |v61|\n\t" \
  "v_mul_f32 v62, v62, |v62|\n\t" \
  "v_mfma_f32_16x16x32_bf16 v[76:79], v[200:203], a[108:111], v[76:79]\n\t" \
  "v_mul_f32 v63, v63, |v63|\n\t" \
  "v_cvt_pk_bf16_f32 v56, v56, v57\n\t" \
  "v_mfma_f32_16x16x32_bf16 v[76:79], v[204:207], a[124:127], v[76:79]\n\t" \
  "v_cvt_pk_bf16_f32 v57, v58, v59\n\t" \
  "v_cvt_pk_bf16_f32 v58, v60, v61\n\t" \
  "v_cvt_pk_bf16_f32 v59, v62, v63\n\t" \
  "s_waitcnt lgkmcnt(0)\n\t" \
  "v_mfma_f32_16x16x32_bf16 v[80:83], v[144:147], a[64:67], v[80:83]\n\t" \
  "ds_read_b64_tr_b16 v[192:193], v174 offset:128\n\t" \
  "v_pk_max_i16 v56, v56, 0\n\t" \
  "v_pk_max_i16 v58, v58, 0\n\t" \
  "v_mfma_f32_16x16x32_bf16 v[80:83], v[148:151], a[80:83], v[80:83]\n\t" \
  "v_pk_max_i16 v57, v57, 0\n\t" \
  "v_pk_max_i16 v59, v59, 0\n\t" \
  "v_mfma_f32_16x16x32_bf16 v[80:83], v[152:155], a[96:99], v[80:83]\n\t" \
  "ds_read_b64_tr_b16 v[194:195], v174 offset:1920\n\t" \
  "v_permlane16_swap_b32 v48, v50\n\t" \
  "v_permlane16_swap_b32 v49, v51\n\t" \
  "v_mfma_f32_16x16x32_bf16 v[80:83], v[156:159], a[112:115], v[80:83]\n\t" \
  "ds_read_b64_tr_b16 v[196:197], v174 offset:7296\n\t" \
  "v_permlane16_swap_b32 v56, v58\n\t" \
  "v_permlane16_swap_b32 v57, v59\n\t" \
  "v_mfma_f32_16x16x32_bf16 v[84:87], v[144:147], a[68:71], v[84:87]\n\t" \
  "ds_read_b64_tr_b16 v[198:199], v174 offset:9088\n\t" \
  "v_mul_f32 v64, v64, |v64|\n\t" \
  "v_mfma_f32_16x16x32_bf16 v[84:87], v[148:151], a[84:87], v[84:87]\n\t" \
  "buffer_store_dwordx4 v[48:51], v178, s[52:55], 0 offen offset:1024 sc1\n\t" \
  "v_mul_f32 v65, v65, |v65|\n\t" \
  "v_mul_f32 v66, v66, |v66|\n\t" \
  "v_mfma_f32_16x16x32_bf16 v[84:87], v[152:155], a[100:103], v[84:87]\n\t" \
  "ds_read_b64_tr_b16 v[200:201], v174 offset:14464\n\t" \
  "v_mul_f32 v67, v67, |v67|\n\t" \
  "v_mul_f32 v68, v68, |v68|\n\t" \
  "v_mfma_f32_16x16x32_bf16 v[84:87], v[156:159], a[116:119], v[84:87]\n\t" \
  "ds_read_b64_tr_b16 v[202:203], v174 offset:16256\n\t" \
  "v_mul_f32 v69, v69, |v69|\n\t" \
  "v_mul_f32 v70, v70, |v70|\n\t" \
  "v_mul_f32 v71, v71, |v71|\n\t" \
  "v_mfma_f32_16x16x32_bf16 v[88:91], v[144:147], a[72:75], v[88:91]\n\t" \
  "ds_read_b64_tr_b16 v[204:205], v174 offset:21632\n\t" \
  "v_cvt_pk_bf16_f32 v64, v64, v65\n\t" \
  "v_cvt_pk_bf16_f32 v65, v66, v67\n\t" \
  "v_mfma_f32_16x16x32_bf16 v[88:91], v[148:151], a[88:91], v[88:91]\n\t" \
  "buffer_store_dwordx4 v[56:59], v178, s[52:55], s68 offen offset:1024 sc1\n\t" \
  "v_cvt_pk_bf16_f32 v66, v68, v69\n\t" \
  "v_cvt_pk_bf16_f32 v67, v70, v71\n\t" \
  "v_mfma_f32_16x16x32_bf16 v[88:91], v[152:155], a[104:107], v[88:91]\n\t" \
  "ds_read_b64_tr_b16 v[206:207], v174 offset:23424\n\t" \
  "v_pk_max_i16 v64, v64, 0\n\t" \
  "v_pk_max_i16 v66, v66, 0\n\t" \
  "v_mfma_f32_16x16x32_bf16 v[88:91], v[156:159], a[120:123], v[88:91]\n\t" \
  "v_pk_max_i16 v65, v65, 0\n\t" \
  "v_pk_max_i16 v67, v67, 0\n\t" \
  "v_mul_f32 v72, v72, |v72|\n\t" \
  "v_mfma_f32_16x16x32_bf16 v[92:95], v[144:147], a[76:79], v[92:95]\n\t" \
  "v_mul_f32 v73, v73, |v73|\n\t" \
  "v_mul_f32 v74, v74, |v74|\n\t" \
  "v_mfma_f32_16x16x32_bf16 v[92:95], v[148:151], a[92:95], v[92:95]\n\t" \
  "v_mul_f32 v75, v75, |v75|\n\t" \
  "v_mul_f32 v76, v76, |v76|\n\t" \
  "v_mfma_f32_16x16x32_bf16 v[92:95], v[152:155], a[108:111], v[92:95]\n\t" \
  "v_mul_f32 v77, v77, |v77|\n\t" \
  "v_mul_f32 v78, v78, |v78|\n\t" \
  "v_mfma_f32_16x16x32_bf16 v[92:95], v[156:159], a[124:127], v[92:95]\n\t" \
  "v_mul_f32 v79, v79, |v79|\n\t" \
  "v_cvt_pk_bf16_f32 v72, v72, v73\n\t" \
  "v_cvt_pk_bf16_f32 v73, v74, v75\n\t" \
  "s_waitcnt lgkmcnt(0)\n\t" \
  "v_mfma_f32_16x16x32_bf16 v[96:99], v[192:195], a[64:67], v[96:99]\n\t" \
  "ds_read_b64_tr_b16 v[144:145], v174 offset:160\n\t" \
  "v_cvt_pk_bf16_f32 v74, v76, v77\n\t" \
  "v_cvt_pk_bf16_f32 v75, v78, v79\n\t" \
  "v_mfma_f32_16x16x32_bf16 v[96:99], v[196:199], a[80:83], v[96:99]\n\t" \
  "v_pk_max_i16 v72, v72, 0\n\t" \
  "v_pk_max_i16 v74, v74, 0\n\t" \
  "v_mfma_f32_16x16x32_bf16 v[96:99], v[200:203], a[96:99], v[96:99]\n\t" \
  "ds_read_b64_tr_b16 v[146:147], v174 offset:1952\n\t" \
  "v_pk_max_i16 v73, v73, 0\n\t" \
  "v_pk_max_i16 v75, v75, 0\n\t" \
  "v_mfma_f32_16x16x32_bf16 v[96:99], v[204:207], a[112:115], v[96:99]\n\t" \
  "ds_read_b64_tr_b16 v[148:149], v174 offset:7328\n\t" \
  "v_permlane16_swap_b32 v64, v66\n\t" \
  "v_permlane16_swap_b32 v65, v67\n\t" \
  "v_permlane16_swap_b32 v72, v74\n\t" \
  "v_mfma_f32_16x16x32_bf16 v[100:103], v[192:195], a[68:71], v[100:103]\n\t" \
  "ds_read_b64_tr_b16 v[150:151], v174 offset:9120\n\t" \
  "v_permlane16_swap_b32 v73, v75\n\t" \
  "v_mfma_f32_16x16x32_bf16 v[100:103], v[196:199], a[84:87], v[100:103]\n\t" \
  "buffer_store_dwordx4 v[64:67], v178, s[52:55], 0 offen offset:2048 sc1\n\t" \
  "v_mul_f32 v80, v80, |v80|\n\t" \
  "v_mfma_f32_16x16x32_bf16 v[100:103], v[200:203], a[100:103], v[100:103]\n\t" \
  "ds_read_b64_tr_b16 v[152:153], v174 offset:14496\n\t" \
  "v_mul_f32 v81, v81, |v81|\n\t" \
  "v_mul_f32 v82, v82, |v82|\n\t" \
  "v_mfma_f32_16x16x32_bf16 v[100:103], v[204:207], a[116:119], v[100:103]\n\t" \
  "ds_read_b64_tr_b16 v[154:155], v174 offset:16288\n\t" \
  "v_mul_f32 v83, v83, |v83|\n\t" \
  "v_mul_f32 v84, v84, |v84|\n\t" \
  "v_mul_f32 v85, v85, |v85|\n\t" \
  "v_mfma_f32_16x16x32_bf16 v[104:107], v[192:195], a[72:75], v[104:107]\n\t" \
  "ds_read_b64_tr_b16 v[156:157], v174 offset:21664\n\t" \
  "v_mul_f32 v86, v86, |v86|\n\t" \
  "v_mul_f32 v87, v87, |v87|\n\t" \
  "v_mfma_f32_16x16x32_bf16 v[104:107], v[196:199], a[88:91], v[104:107]\n\t" \
  "buffer_store_dwordx4 v[72:75], v178, s[52:55], s68 offen offset:2048 sc1\n\t" \
  "v_cvt_pk_bf16_f32 v80, v80, v81\n\t" \
  "v_cvt_pk_bf16_f32 v81, v82, v83\n\t" \
  "v_mfma_f32_16x16x32_bf16 v[104:107], v[200:203], a[104:107], v[104:107]\n\t" \
  "ds_read_b64_tr_b16 v[158:159], v174 offset:23456\n\t" \
  "v_cvt_pk_bf16_f32 v82, v84, v85\n\t" \
  "v_cvt_pk_bf16_f32 v83, v86, v87\n\t" \
  "v_mfma_f32_16x16x32_bf16 v[104:107], v[204:207], a[120:123], v[104:107]\n\t" \
  "v_pk_max_i16 v80, v80, 0\n\t" \
  "v_pk_max_i16 v82, v82, 0\n\t" \
  "v_pk_max_i16 v81, v81, 0\n\t" \
  "v_mfma_f32_16x16x32_bf16 v[108:111], v[192:195], a[76:79], v[108:111]\n\t" \
  "v_pk_max_i16 v83, v83, 0\n\t" \
  "v_mul_f32 v88, v88, |v88|\n\t" \
  "v_mfma_f32_16x16x32_bf16 v[108:111], v[196:199], a[92:95], v[108:111]\n\t" \
  "v_mul_f32 v89, v89, |v89|\n\t" \
  "v_mul_f32 v90, v90, |v90|\n\t" \
  "v_mfma_f32_16x16x32_bf16 v[108:111], v[200:203], a[108:111], v[108:111]\n\t" \
  "v_mul_f32 v91, v91, |v91|\n\t" \
  "v_mul_f32 v92, v92, |v92|\n\t" \
  "v_mfma_f32_16x16x32_bf16 v[108:111], v[204:207], a[124:127], v[108:111]\n\t" \
  "v_mul_f32 v93, v93, |v93|\n\t" \
  "v_mul_f32 v94, v94, |v94|\n\t" \
  "v_mul_f32 v95, v95, |v95|\n\t" \
  "s_waitcnt lgkmcnt(0)\n\t" \
  "v_mfma_f32_16x16x32_bf16 v[112:115], v[144:147], a[64:67], v[112:115]\n\t" \
  "ds_read_b64_tr_b16 v[192:193], v174 offset:192\n\t" \
  "v_cvt_pk_bf16_f32 v88, v88, v89\n\t" \
  "v_cvt_pk_bf16_f32 v89, v90, v91\n\t" \
  "v_mfma_f32_16x16x32_bf16 v[112:115], v[148:151], a[80:83], v[112:115]\n\t" \
  "v_cvt_pk_bf16_f32 v90, v92, v93\n\t" \
  "v_cvt_pk_bf16_f32 v91, v94, v95\n\t" \
  "v_mfma_f32_16x16x32_bf16 v[112:115], v[152:155], a[96:99], v[112:115]\n\t" \
  "ds_read_b64_tr_b16 v[194:195], v174 offset:1984\n\t" \
  "v_pk_max_i16 v88, v88, 0\n\t" \
  "v_pk_max_i16 v90, v90, 0\n\t" \
  "v_mfma_f32_16x16x32_bf16 v[112:115], v[156:159], a[112:115], v[112:115]\n\t" \
  "ds_read_b64_tr_b16 v[196:197], v174 offset:7360\n\t" \
  "v_pk_max_i16 v89, v89, 0\n\t" \
  "v_pk_max_i16 v91, v91, 0\n\t" \
  "v_permlane16_swap_b32 v80, v82\n\t" \
  "v_mfma_f32_16x16x32_bf16 v[116:119], v[144:147], a[68:71], v[116:119]\n\t" \
  "ds_read_b64_tr_b16 v[198:199], v174 offset:9152\n\t" \
  "v_permlane16_swap_b32 v81, v83\n\t" \
  "v_permlane16_swap_b32 v88, v90\n\t" \
  "v_mfma_f32_16x16x32_bf16 v[116:119], v[148:151], a[84:87], v[116:119]\n\t" \
  "v_permlane16_swap_b32 v89, v91\n\t" \
  "v_mfma_f32_16x16x32_bf16 v[116:119], v[152:155], a[100:103], v[116:119]\n\t" \
  "ds_read_b64_tr_b16 v[200:201], v174 offset:14528\n\t" \
  "v_mul_f32 v96, v96, |v96|\n\t" \
  "v_mfma_f32_16x16x32_bf16 v[116:119], v[156:159], a[116:119], v[116:119]\n\t" \
  "ds_read_b64_tr_b16 v[202:203], v174 offset:16320\n\t" \
  "v_mul_f32 v97, v97, |v97|\n\t" \
  "v_mul_f32 v98, v98, |v98|\n\t" \
  "v_mul_f32 v99, v99, |v99|\n\t" \
  "v_mfma_f32_16x16x32_bf16 v[120:123], v[144:147], a[72:75], v[120:123]\n\t" \
  "ds_read_b64_tr_b16 v[204:205], v174 offset:21696\n\t" \
  "v_mul_f32 v100, v100, |v100|\n\t" \
  "v_mul_f32 v101, v101, |v101|\n\t" \
  "v_mfma_f32_16x16x32_bf16 v[120:123], v[148:151], a[88:91], v[120:123]\n\t" \
  "buffer_store_dwordx4 v[80:83], v178, s[52:55], 0 offen offset:3072 sc1\n\t" \
  "v_mul_f32 v102, v102, |v102|\n\t" \
  "v_mul_f32 v103, v103, |v103|\n\t" \
  "v_mfma_f32_16x16x32_bf16 v[120:123], v[152:155], a[104:107], v[120:123]\n\t" \
  "ds_read_b64_tr_b16 v[206:207], v174 offset:23488\n\t" \
  "v_cvt_pk_bf16_f32 v96, v96, v97\n\t" \
  "v_cvt_pk_bf16_f32 v97, v98, v99\n\t" \
  "v_mfma_f32_16x16x32_bf16 v[120:123], v[156:159], a[120:123], v[120:123]\n\t" \
  "buffer_store_dwordx4 v[88:91], v178, s[52:55], s68 offen offset:3072 sc1\n\t" \
  "v_cvt_pk_bf16_f32 v98, v100, v101\n\t" \
  "v_cvt_pk_bf16_f32 v99, v102, v103\n\t" \
  "v_pk_max_i16 v96, v96, 0\n\t" \
  "v_mfma_f32_16x16x32_bf16 v[124:127], v[144:147], a[76:79], v[124:127]\n\t" \
  "v_pk_max_i16 v98, v98, 0\n\t" \
  "v_pk_max_i16 v97, v97, 0\n\t" \
  "v_mfma_f32_16x16x32_bf16 v[124:127], v[148:151], a[92:95], v[124:127]\n\t" \
  "v_pk_max_i16 v99, v99, 0\n\t" \
  "v_mul_f32 v104, v104, |v104|\n\t" \
  "v_mfma_f32_16x16x32_bf16 v[124:127], v[152:155], a[108:111], v[124:127]\n\t" \
  "v_mul_f32 v105, v105, |v105|\n\t" \
  "v_mul_f32 v106, v106, |v106|\n\t" \
  "v_mfma_f32_16x16x32_bf16 v[124:127], v[156:159], a[124:127], v[124:127]\n\t" \
  "v_mul_f32 v107, v107, |v107|\n\t" \
  "v_mul_f32 v108, v108, |v108|\n\t" \
  "v_mul_f32 v109, v109, |v109|\n\t" \
  "s_waitcnt lgkmcnt(0)\n\t" \
  "v_mfma_f32_16x16x32_bf16 v[208:211], v[192:195], a[64:67], v[208:211]\n\t" \
  "ds_read_b64_tr_b16 v[144:145], v172 offset:0\n\t" \
  "v_mul_f32 v110, v110, |v110|\n\t" \
  "v_mul_f32 v111, v111, |v111|\n\t" \
  "v_mfma_f32_16x16x32_bf16 v[208:211], v[196:199], a[80:83], v[208:211]\n\t" \
  "v_cvt_pk_bf16_f32 v104, v104, v105\n\t" \
  "v_cvt_pk_bf16_f32 v105, v106, v107\n\t" \
  "v_mfma_f32_16x16x32_bf16 v[208:211], v[200:203], a[96:99], v[208:211]\n\t" \
  "ds_read_b64_tr_b16 v[146:147], v172 offset:512\n\t" \
  "v_cvt_pk_bf16_f32 v106, v108, v109\n\t" \
  "v_cvt_pk_bf16_f32 v107, v110, v111\n\t" \
  "v_mfma_f32_16x16x32_bf16 v[208:211], v[204:207], a[112:115], v[208:211]\n\t" \
  "ds_read_b64_tr_b16 v[148:149], v172 offset:2048\n\t" \
  "v_pk_max_i16 v104, v104, 0\n\t" \
  "v_pk_max_i16 v106, v106, 0\n\t" \
  "v_pk_max_i16 v105, v105, 0\n\t" \
  "v_mfma_f32_16x16x32_bf16 v[212:215], v[192:195], a[68:71], v[212:215]\n\t" \
  "ds_read_b64_tr_b16 v[150:151], v172 offset:2560\n\t" \
  "v_pk_max_i16 v107, v107, 0\n\t" \
  "v_permlane16_swap_b32 v96, v98\n\t" \
  "v_mfma_f32_16x16x32_bf16 v[212:215], v[196:199], a[84:87], v[212:215]\n\t" \
  "v_permlane16_swap_b32 v97, v99\n\t" \
  "v_permlane16_swap_b32 v104, v106\n\t" \
  "v_mfma_f32_16x16x32_bf16 v[212:215], v[200:203], a[100:103], v[212:215]\n\t" \
  "ds_read_b64_tr_b16 v[152:153], v172 offset:4096\n\t" \
  "v_permlane16_swap_b32 v105, v107\n\t" \
  "v_mfma_f32_16x16x32_bf16 v[212:215], v[204:207], a[116:119], v[212:215]\n\t" \
  "ds_read_b64_tr_b16 v[154:155], v172 offset:4608\n\t" \
  "v_mul_f32 v112, v112, |v112|\n\t" \
  "v_mul_f32 v113, v113, |v113|\n\t" \
  "v_mfma_f32_16x16x32_bf16 v[216:219], v[192:195], a[72:75], v[216:219]\n\t" \
  "ds_read_b64_tr_b16 v[156:157], v172 offset:6144\n\t" \
  "v_mul_f32 v114, v114, |v114|\n\t" \
  "v_mul_f32 v115, v115, |v115|\n\t" \
  "v_mfma_f32_16x16x32_bf16 v[216:219], v[196:199], a[88:91], v[216:219]\n\t" \
  "buffer_store_dwordx4 v[96:99], v178, s[52:55], s67 offen offset:0 sc1\n\t" \
  "v_mul_f32 v116, v116, |v116|\n\t" \
  "v_mul_f32 v117, v117, |v117|\n\t" \
  "v_mfma_f32_16x16x32_bf16 v[216:219], v[200:203], a[104:107], v[216:219]\n\t" \
  "ds_read_b64_tr_b16 v[158:159], v172 offset:6656\n\t" \
  "v_mul_f32 v118, v118, |v118|\n\t" \
  "v_mul_f32 v119, v119, |v119|\n\t" \
  "v_mfma_f32_16x16x32_bf16 v[216:219], v[204:207], a[120:123], v[216:219]\n\t" \
  "buffer_store_dwordx4 v[104:107], v178, s[52:55], s69 offen offset:0 sc1\n\t" \
  "v_cvt_pk_bf16_f32 v112, v112, v113\n\t" \
  "v_cvt_pk_bf16_f32 v113, v114, v115\n\t" \
  "v_cvt_pk_bf16_f32 v114, v116, v117\n\t" \
  "v_mfma_f32_16x16x32_bf16 v[220:223], v[192:195], a[76:79], v[220:223]\n\t" \
  "v_cvt_pk_bf16_f32 v115, v118, v119\n\t" \
  "v_pk_max_i16 v112, v112, 0\n\t" \
  "v_mfma_f32_16x16x32_bf16 v[220:223], v[196:199], a[92:95], v[220:223]\n\t" \
  "v_pk_max_i16 v114, v114, 0\n\t" \
  "v_pk_max_i16 v113, v113, 0\n\t" \
  "v_mfma_f32_16x16x32_bf16 v[220:223], v[200:203], a[108:111], v[220:223]\n\t" \
  "v_pk_max_i16 v115, v115, 0\n\t" \
  "v_mul_f32 v120, v120, |v120|\n\t" \
  "v_mfma_f32_16x16x32_bf16 v[220:223], v[204:207], a[124:127], v[220:223]\n\t" \
  "v_mul_f32 v121, v121, |v121|\n\t" \
  "v_mul_f32 v122, v122, |v122|\n\t" \
  "v_mul_f32 v123, v123, |v123|\n\t" \
  "s_waitcnt lgkmcnt(0)\n\t" \
  "v_mfma_f32_16x16x32_bf16 v[0:3], v[144:147], a[64:67], v[0:3]\n\t" \
  "ds_read_b64_tr_b16 v[192:193], v173 offset:0\n\t" \
  "v_mul_f32 v124, v124, |v124|\n\t" \
  "v_mul_f32 v125, v125, |v125|\n\t" \
  "v_mfma_f32_16x16x32_bf16 v[0:3], v[148:151], a[80:83], v[0:3]\n\t" \
  "v_mul_f32 v126, v126, |v126|\n\t" \
  "v_mul_f32 v127, v127, |v127|\n\t" \
  "v_mfma_f32_16x16x32_bf16 v[0:3], v[152:155], a[96:99], v[0:3]\n\t" \
  "ds_read_b64_tr_b16 v[194:195], v173 offset:512\n\t" \
  "v_cvt_pk_bf16_f32 v120, v120, v121\n\t" \
  "v_cvt_pk_bf16_f32 v121, v122, v123\n\t" \
  "v_mfma_f32_16x16x32_bf16 v[0:3], v[156:159], a[112:115], v[0:3]\n\t" \
  "ds_read_b64_tr_b16 v[196:197], v173 offset:2048\n\t" \
  "v_cvt_pk_bf16_f32 v122, v124, v125\n\t" \
  "v_cvt_pk_bf16_f32 v123, v126, v127\n\t" \
  "v_pk_max_i16 v120, v120, 0\n\t" \
  "v_mfma_f32_16x16x32_bf16 v[4:7], v[144:147], a[68:71], v[4:7]\n\t" \
  "ds_read_b64_tr_b16 v[198:199], v173 offset:2560\n\t" \
  "v_pk_max_i16 v122, v122, 0\n\t" \
  "v_pk_max_i16 v121, v121, 0\n\t" \
  "v_mfma_f32_16x16x32_bf16 v[4:7], v[148:151], a[84:87], v[4:7]\n\t" \
  "v_pk_max_i16 v123, v123, 0\n\t" \
  "v_permlane16_swap_b32 v112, v114\n\t" \
  "v_mfma_f32_16x16x32_bf16 v[4:7], v[152:155], a[100:103], v[4:7]\n\t" \
  "ds_read_b64_tr_b16 v[200:201], v173 offset:4096\n\t" \
  "v_permlane16_swap_b32 v113, v115\n\t" \
  "v_permlane16_swap_b32 v120, v122\n\t" \
  "v_mfma_f32_16x16x32_bf16 v[4:7], v[156:159], a[116:119], v[4:7]\n\t" \
  "ds_read_b64_tr_b16 v[202:203], v173 offset:4608\n\t" \
  "v_permlane16_swap_b32 v121, v123\n\t" \
  "v_mfma_f32_16x16x32_bf16 v[8:11], v[144:147], a[72:75], v[8:11]\n\t" \
  "ds_read_b64_tr_b16 v[204:205], v173 offset:6144\n\t" \
  "v_mul_f32 v208, v208, |v208|\n\t" \
  "v_mul_f32 v209, v209, |v209|\n\t" \
  "v_mfma_f32_16x16x32_bf16 v[8:11], v[148:151], a[88:91], v[8:11]\n\t" \
  "buffer_store_dwordx4 v[112:115], v178, s[52:55], s67 offen offset:1024 sc1\n\t" \
  "v_mul_f32 v210, v210, |v210|\n\t" \
  "v_mul_f32 v211, v211, |v211|\n\t" \
  "v_mfma_f32_16x16x32_bf16 v[8:11], v[152:155], a[104:107], v[8:11]\n\t" \
  "ds_read_b64_tr_b16 v[206:207], v173 offset:6656\n\t" \
  "v_mul_f32 v212, v212, |v212|\n\t" \
  "v_mul_f32 v213, v213, |v213|\n\t" \
  "v_mfma_f32_16x16x32_bf16 v[8:11], v[156:159], a[120:123], v[8:11]\n\t" \
  "buffer_store_dwordx4 v[120:123], v178, s[52:55], s69 offen offset:1024 sc1\n\t" \
  "v_mul_f32 v214, v214, |v214|\n\t" \
  "v_mul_f32 v215, v215, |v215|\n\t" \
  "v_cvt_pk_bf16_f32 v208, v208, v209\n\t" \
  "v_mfma_f32_16x16x32_bf16 v[12:15], v[144:147], a[76:79], v[12:15]\n\t" \
  "v_cvt_pk_bf16_f32 v209, v210, v211\n\t" \
  "v_cvt_pk_bf16_f32 v210, v212, v213\n\t" \
  "v_mfma_f32_16x16x32_bf16 v[12:15], v[148:151], a[92:95], v[12:15]\n\t" \
  "v_cvt_pk_bf16_f32 v211, v214, v215\n\t" \
  "v_pk_max_i16 v208, v208, 0\n\t" \
  "v_mfma_f32_16x16x32_bf16 v[12:15], v[152:155], a[108:111], v[12:15]\n\t" \
  "v_pk_max_i16 v210, v210, 0\n\t" \
  "v_pk_max_i16 v209, v209, 0\n\t" \
  "v_mfma_f32_16x16x32_bf16 v[12:15], v[156:159], a[124:127], v[12:15]\n\t" \
  "v_pk_max_i16 v211, v211, 0\n\t" \
  "v_mul_f32 v216, v216, |v216|\n\t" \
  "v_mul_f32 v217, v217, |v217|\n\t" \
  "s_waitcnt lgkmcnt(0)\n\t" \
  "v_mfma_f32_16x16x32_bf16 v[16:19], v[192:195], a[64:67], v[16:19]\n\t" \
  "v_mul_f32 v218, v218, |v218|\n\t" \
  "v_mul_f32 v219, v219, |v219|\n\t" \
  "v_mfma_f32_16x16x32_bf16 v[16:19], v[196:199], a[80:83], v[16:19]\n\t" \
  "v_mul_f32 v220, v220, |v220|\n\t" \
  "v_mul_f32 v221, v221, |v221|\n\t" \
  "v_mfma_f32_16x16x32_bf16 v[16:19], v[200:203], a[96:99], v[16:19]\n\t" \
  "v_mul_f32 v222, v222, |v222|\n\t" \
  "v_mul_f32 v223, v223, |v223|\n\t" \
  "v_mfma_f32_16x16x32_bf16 v[16:19], v[204:207], a[112:115], v[16:19]\n\t" \
  "v_cvt_pk_bf16_f32 v216, v216, v217\n\t" \
  "v_cvt_pk_bf16_f32 v217, v218, v219\n\t" \
  "v_cvt_pk_bf16_f32 v218, v220, v221\n\t" \
  "v_mfma_f32_16x16x32_bf16 v[20:23], v[192:195], a[68:71], v[20:23]\n\t" \
  "v_cvt_pk_bf16_f32 v219, v222, v223\n\t" \
  "v_pk_max_i16 v216, v216, 0\n\t" \
  "v_mfma_f32_16x16x32_bf16 v[20:23], v[196:199], a[84:87], v[20:23]\n\t" \
  "v_pk_max_i16 v218, v218, 0\n\t" \
  "v_pk_max_i16 v217, v217, 0\n\t" \
  "v_mfma_f32_16x16x32_bf16 v[20:23], v[200:203], a[100:103], v[20:23]\n\t" \
  "v_pk_max_i16 v219, v219, 0\n\t" \
  "v_permlane16_swap_b32 v208, v210\n\t" \
  "v_mfma_f32_16x16x32_bf16 v[20:23], v[204:207], a[116:119], v[20:23]\n\t" \
  "v_permlane16_swap_b32 v209, v211\n\t" \
  "v_permlane16_swap_b32 v216, v218\n\t" \
  "v_permlane16_swap_b32 v217, v219\n\t" \
  "v_mfma_f32_16x16x32_bf16 v[24:27], v[192:195], a[72:75], v[24:27]\n\t" \
  "v_mfma_f32_16x16x32_bf16 v[24:27], v[196:199], a[88:91], v[24:27]\n\t" \
  "buffer_store_dwordx4 v[208:211], v178, s[52:55], s67 offen offset:2048 sc1\n\t" \
  "v_mfma_f32_16x16x32_bf16 v[24:27], v[200:203], a[104:107], v[24:27]\n\t" \
  "buffer_store_dwordx4 v[216:219], v178, s[52:55], s69 offen offset:2048 sc1\n\t" \
  "v_mfma_f32_16x16x32_bf16 v[24:27], v[204:207], a[120:123], v[24:27]\n\t" \
  "v_mfma_f32_16x16x32_bf16 v[28:31], v[192:195], a[76:79], v[28:31]\n\t" \
  "v_mfma_f32_16x16x32_bf16 v[28:31], v[196:199], a[92:95], v[28:31]\n\t" \
  "v_mfma_f32_16x16x32_bf16 v[28:31], v[200:203], a[108:111], v[28:31]\n\t" \
  "v_mfma_f32_16x16x32_bf16 v[28:31], v[204:207], a[124:127], v[28:31]\n\t" \
  "s_waitcnt vmcnt(14)\n\t" \
  "s_nop 7\n\t"

#define GAS __attribute__((address_space(1)))
#define RLX_AGENT __ATOMIC_RELAXED, __HIP_MEMORY_SCOPE_AGENT
#define RAWBAR() asm volatile("s_barrier" ::: "memory")
constexpr int P1_STAGE = 36864;
constexpr int KV_OFF = 3 * P1_STAGE;
constexpr unsigned SPIN_CAP = 1u << 18;
DI int fresh_lane() { int l; asm volatile("v_mbcnt_lo_u32_b32 %0, -1, 0\n\tv_mbcnt_hi_u32_b32 %0, -1, %0" : "=v"(l)); return l; }
DI float exp2f_(float x) { return __builtin_amdgcn_exp2f(x); }
DI bf16x8 pack8(const f32x16& s, int o) {
  u32x4 p = {cvtpk(s[o], s[o + 1]), cvtpk(s[o + 2], s[o + 3]), cvtpk(s[o + 4], s[o + 5]), cvtpk(s[o + 6], s[o + 7])};
  return __builtin_bit_cast(bf16x8, p);
}
DI float rowmax16(const f32x16& s) {
  float m = s[0];
#pragma unroll
  for (int i = 1; i < 16; ++i) m = fmaxf(m, s[i]);
  auto r = __builtin_amdgcn_permlane32_swap(__float_as_uint(m), __float_as_uint(m), false, false);
  return fmaxf(__uint_as_float(r[0]), __uint_as_float(r[1]));
}
typedef unsigned long long u64;
DI void echo(char* ws, unsigned l16) {
  u32x4 t;
  asm volatile("global_load_dwordx4 %0, %1, %2 sc1\n\ts_waitcnt vmcnt(0)\n\tglobal_store_dwordx4 %1, %0, %2 offset:1024 sc1\n\ts_nop 1" : "=&v"(t) : "v"(l16), "s"(ws + WS_REQ) : "memory");
}
DI void glb_wait(GAS unsigned* p, unsigned want, bool lead, char* ws, unsigned l16) {
#pragma unroll 1
  for (unsigned it = 0; it < SPIN_CAP; ++it) {
    if (rfl(__hip_atomic_load(p, RLX_AGENT)) >= want) break;
    if (lead) echo(ws, l16);
    __builtin_amdgcn_s_sleep(1);
  }
  asm volatile("" ::: "memory");
}
DI u32x4 mk_rsrc(const void* p, unsigned bytes) { const unsigned long long a = (unsigned long long)p; return (u32x4){(unsigned)a, (unsigned)(a >> 32) & 0xffffu, bytes, 0x00020000u}; }

#define MF32_SLOW(acc, a, b) asm volatile("s_nop 1\n\tv_mfma_f32_32x32x16_bf16 %0, %1, %2, %0\n\ts_nop 15\n\ts_nop 3" : "+v"(acc) : "v"(a), "v"(b))
DI void attn_exact_tile(const char* kv, bf16x8 q0, bf16x8 q1, int lane, f32x16& o) {
  __builtin_amdgcn_fence(__ATOMIC_ACQUIRE, "agent");
  o = (f32x16){}; float m = -INFINITY, l = 0.f;
#pragma unroll 1
  for (int kt = 0; kt < 64; ++kt) {
    const char* p = kv + kt * 4096 + lane * 16;
    const bf16x8 k0 = *(const bf16x8*)p, k1 = *(const bf16x8*)(p + 1024), v0 = *(const bf16x8*)(p + 2048), v1 = *(const bf16x8*)(p + 3072);
    f32x16 s = {};
    MF32_SLOW(s, k0, q0); MF32_SLOW(s, k1, q1);
    const float mn = fmaxf(m, rowmax16(s));
    const float a = exp2f_(m - mn);
    m = mn;
    float ps = 0.f;
#pragma unroll
    for (int i = 0; i < 16; ++i) s[i] = exp2f_(s[i] - m);
    const bf16x8 p0 = pack8(s, 0), p1 = pack8(s, 8);
#pragma unroll
    for (int i = 0; i < 4; ++i) { const unsigned u = __builtin_bit_cast(u32x4, p0)[i], v = __builtin_bit_cast(u32x4, p1)[i]; ps += bf_lo(u) + bf_hi(u) + bf_lo(v) + bf_hi(v); }
    l = l * a + ps;
#pragma unroll
    for (int i = 0; i < 16; ++i) o[i] *= a;
    MF32_SLOW(o, v0, p0); MF32_SLOW(o, v1, p1);
  }
  { auto q = __builtin_amdgcn_permlane32_swap(__float_as_uint(l), __float_as_uint(l), false, false); l = __uint_as_float(q[0]) + __uint_as_float(q[1]); }
  const float r = 1.0f / l;
#pragma unroll
  for (int i = 0; i < 16; ++i) o[i] *= r;
}

struct LaneTab { unsigned v[7][64][4]; };
constexpr int cswap23(int r) { return (r & ~12) | ((r & 4) << 1) | ((r & 8) >> 1); }
constexpr LaneTab make_tab() {
  LaneTab T{};
  for (int lane = 0; lane < 64; ++lane) {
    unsigned t[28] = {};
    const int g = lane >> 4, l15 = lane & 15;
    for (int i = 0; i < 2; ++i) { const int q = i * 64 + lane, rho = q >> 2, cc = (q & 3) ^ (((rho >> 2) & 1) << 1); t[i] = cswap23(rho) * 2048 + cc * 16 - i * 1024; }
    for (int i = 0; i < 7; ++i) { const int q = i * 64 + lane, rho = q / 14, cc = q % 14; t[2 + i] = cswap23(rho) * 7168 + cc * 16 - (i & 3) * 1024; }
    const int rl = 16 * (g >> 1) + 4 * (g & 1) + (l15 >> 2);
    const unsigned qoff0 = rl * 64 + ((g & 1) << 5) + (lane & 3) * 8, hoff = 8192 + rl * 224 + (lane & 3) * 8;
    for (int sl = 0; sl < 3; ++sl) { t[9 + sl * 3] = qoff0 + sl * P1_STAGE; t[10 + sl * 3] = (qoff0 ^ 32) + sl * P1_STAGE; t[11 + sl * 3] = hoff + sl * P1_STAGE; }
    const unsigned vow = (lane >> 3) * 2048 + 16 * ((lane & 7) ^ (((lane >> 4) & 1) << 2));
    for (int i = 0; i < 4; ++i) { t[18 + i] = lane * 16 + i * (XT * 1024); t[22 + i] = vow + i * 15360u; }
    t[26] = (lane < 9 ? lane : 9) * 124; t[27] = (lane & 7) * 256;
    for (int i = 0; i < 28; ++i) T.v[i >> 2][lane][i & 3] = t[i];
  }
  return T;
}
__device__ const LaneTab LANE_TAB __attribute__((aligned(1024))) = make_tab();

__global__ __launch_bounds__(256, 1) void k_main(const u16* __restrict__ x, const u16* __restrict__ wq, const u16* __restrict__ wk, const u16* __restrict__ wv, const u16* __restrict__ wo,
                                                 const u16* __restrict__ w1, const u16* __restrict__ b1, const u16* __restrict__ w2,
                                                 const u16* __restrict__ b2, u16* __restrict__ out, char* __restrict__ ws) {
  __shared__ __attribute__((aligned(16))) char smem[163840];
  LAS char* lds = (LAS char*)smem;
  const unsigned ldsb = (unsigned)(size_t)lds;
  const int tid = threadIdx.x, lane = tid & 63, w = rfl(tid >> 6);
  const int xcd = blockIdx.x & 7, idx = blockIdx.x >> 3;
  const int rp = (xcd & 1) * 4 + (idx & 3), c = (xcd >> 1) * 8 + (idx >> 2);
  const int mt = (xcd & 3) * 4 + (idx & 3), ft = (xcd >> 2) * 8 + (idx >> 2);
  GAS unsigned* gcnt = (GAS unsigned*)(ws + WS_CNT);
  const char* kv = ws + WS_KV;
  const int h = lane >> 5, g = lane >> 4, l15 = lane & 15;
  const unsigned l16 = lane * 16;
  asm volatile("kmain_code_begin:");
#define CODE_PAGE(p) const char* p; { const char* code; unsigned clast; asm volatile("s_getpc_b64 s[60:61]\n.Lkmpc%=:\n\ts_sub_u32 s60, s60, .Lkmpc%=-kmain_code_begin\n\ts_subb_u32 s61, s61, 0\n\ts_and_b32 s60, s60, 0xffffff00\n\ts_mov_b64 %0, s[60:61]\n\ts_mov_b32 %1, kmain_code_end-kmain_code_begin-4096" : "=s"(code), "=s"(clast)::"s60", "s61", "scc"); p = code + min((unsigned)idx * 4096u, clast & ~63u); }
  if ((w >> 1) != (int)(blockIdx.x & 1) && (w & 1) == 0) { CODE_PAGE(cp0); touch4(cp0, lane * 64, 159744 + w * 256); }
  typedef __attribute__((ext_vector_type(16))) unsigned u32x16;
  typedef __attribute__((ext_vector_type(8))) unsigned u32x8;
  constexpr int WKV = 2 * P1_STAGE, RED = WKV + 65536, PART = RED + 512, ARR = PART + 8192, XST = P1_STAGE, NSTEP = 56;
  const int T = blockIdx.x >> 1, which = blockIdx.x & 1;
  *(LAS unsigned*)(lds + ARR) = 0u;
  const bool storer = (w >> 1) == which;
  const unsigned twid = blockIdx.x * 4 + w, pw = blockIdx.x * 2 + (w & 1), sc = 159744 + w * 256;
  const bool lead = twid == 3;
  const bool asker = storer && (w & 1) == 0 && blockIdx.x != 0;
  unsigned nonce = (unsigned)__builtin_amdgcn_s_memrealtime();
  if (lead) __hip_atomic_store(gcnt + (lane & 31) * 64, 0u, RLX_AGENT);
  { unsigned xo = (T * 16 + h) * (DM * 2) + w * 512 + (lane & 31) * 16;
    asm volatile("s_add_u32 m0, %2, 0\n\ts_nop 0\n\tglobal_load_lds_dwordx4 %0, %1\n\ts_add_u32 m0, %2, 1088\n\tv_add_u32 %0, 0x1000, %0\n\tglobal_load_lds_dwordx4 %0, %1\n\ts_add_u32 m0, %2, 2176\n\tv_add_u32 %0, 0x1000, %0\n\tglobal_load_lds_dwordx4 %0, %1\n\ts_add_u32 m0, %2, 3264\n\tv_add_u32 %0, 0x1000, %0\n\tglobal_load_lds_dwordx4 %0, %1\n\ts_add_u32 m0, %2, 4352\n\tv_add_u32 %0, 0x1000, %0\n\tglobal_load_lds_dwordx4 %0, %1\n\ts_add_u32 m0, %2, 5440\n\tv_add_u32 %0, 0x1000, %0\n\tglobal_load_lds_dwordx4 %0, %1\n\ts_add_u32 m0, %2, 6528\n\tv_add_u32 %0, 0x1000, %0\n\tglobal_load_lds_dwordx4 %0, %1\n\ts_add_u32 m0, %2, 7616\n\tv_add_u32 %0, 0x1000, %0\n\tglobal_load_lds_dwordx4 %0, %1" : "+v"(xo) : "s"(x), "s"(XST + w * 8704) : "memory", "scc"); }
  if (asker) asm volatile("s_mov_b32 m0, 161792\n\ts_nop 0\n\tglobal_load_lds_dword %0, %3 sc1\n\tglobal_store_dword %0, %1, %2 sc1" ::"v"(0u), "v"(nonce), "s"(ws + WS_REQ + blockIdx.x * 4), "s"(ws + WS_REQ + 1024 + blockIdx.x * 4) : "memory");
  u32x16 p1A, p1B;
  asm volatile("global_load_dwordx4 v[160:163], %2, %3 offset:-3072\n\tglobal_load_dwordx4 v[164:167], %2, %3 offset:-2048\n\tglobal_load_dwordx4 v[168:171], %2, %3 offset:-1024\n\tglobal_load_dwordx4 v[172:175], %2, %3 offset:0\n\tglobal_load_dwordx4 v[176:179], %2, %3 offset:1024\n\tglobal_load_dwordx4 v[180:183], %2, %3 offset:2048\n\tglobal_load_dwordx4 v[184:187], %2, %3 offset:3072" : "=&{v[160:175]}"(p1A), "=&{v[176:191]}"(p1B) : "v"(l16), "s"((const char*)&LANE_TAB + 3072) : "memory");
  const char* tW = pw < 398 ? (const char*)w1 + pw * 4608 : (const char*)wq + (pw - 398) * 4600;
  const unsigned tWo = min(lane, 36) * 128;
  if (!storer && !lead) {
    touch4(tW, tWo, sc);
    if (w & 1) touch4((const char*)(which ? wv : wk) + (idx & 15) * 4096, (lane & 31) * 128, sc);
  }
  u32x8 p0R;
  { const u32x4 r2 = mk_rsrc((const char*)wq + c * 64, 2097152 - c * 64), r3 = mk_rsrc((const char*)w1 + c * 224, 7340032 - c * 224);
#pragma unroll
    for (int i = 0; i < 4; ++i) { p0R[i] = r2[i]; p0R[4 + i] = r3[i]; } }
#define P1_DMA(st, v, r) asm volatile("s_mov_b32 m0, %0\n\ts_nop 0\n\tbuffer_load_dwordx4 v160, s[88:91], %2 offen offset:0 lds\n\tbuffer_load_dwordx4 v161, s[88:91], %2 offen offset:1024 lds\n\ts_mov_b32 m0, %1\n\ts_nop 0\n\tbuffer_load_dwordx4 v162, s[92:95], %3 offen offset:0 lds\n\tbuffer_load_dwordx4 v163, s[92:95], %3 offen offset:1024 lds\n\tbuffer_load_dwordx4 v164, s[92:95], %3 offen offset:2048 lds\n\tbuffer_load_dwordx4 v165, s[92:95], %3 offen offset:3072 lds\n\ts_add_u32 m0, %1, 4096\n\ts_nop 0\n\tbuffer_load_dwordx4 v166, s[92:95], %3 offen offset:0 lds\n\tbuffer_load_dwordx4 v167, s[92:95], %3 offen offset:1024 lds\n\tbuffer_load_dwordx4 v168, s[92:95], %3 offen offset:2048 lds" ::"s"((st) * P1_STAGE + (v) * 2048), "s"((st) * P1_STAGE + 8192 + (v) * 7168), "s"((unsigned)(r) * 2048u), "s"((unsigned)(r) * 7168u), \
                                      "{v[160:175]}"(p1A), "{s[88:95]}"(p0R) : "memory", "scc")
  const char* w2b = (const char*)w2 + (size_t)w * NSTEP * 16 * 2048;
  const unsigned ring = (w >> 1) * (2 * P1_STAGE) + (w & 1) * 16384;
  u32x2 bq[7];
  {
    u32x4 xr[8];
#define ECHO_LD() asm volatile("s_mov_b32 m0, 160768\n\ts_nop 0\n\tglobal_load_lds_dwordx4 %0, %1 sc1" ::"v"(l16), "s"(ws + WS_REQ) : "memory")
#define ECHO_ST(n) do { u32x4 t_; asm volatile("s_waitcnt vmcnt(" #n ")\n\tds_read_b128 %0, %3\n\ts_waitcnt lgkmcnt(0)\n\tglobal_store_dwordx4 %1, %0, %2 offset:1024 sc1\n\ts_nop 1" \
                                               : "=&v"(t_) : "v"(l16), "s"(ws + WS_REQ), "v"(l16 + 160768u) : "memory"); } while (0)
    if (lead) { __builtin_amdgcn_s_sleep(16); ECHO_LD(); }
    if (asker || (!storer && (w & 1) && !lead)) WAITV(9); else if (storer) WAITV(7); else WAITV(8);
#pragma unroll
    for (int j = 0; j < 8; ++j) xr[j] = *(LAS const u32x4*)(lds + XST + w * 8704 + (l15 >> 1) * 1088 + (l15 & 1) * 512 + j * 64 + g * 16);
    f32x2 xv[32];
#pragma unroll
    for (int j = 0; j < 8; ++j)
#pragma unroll
      for (int e = 0; e < 4; ++e) xv[j * 4 + e] = (f32x2){bf_lo(xr[j][e]), bf_hi(xr[j][e])};
    f32x2 s2[2] = {};
#pragma unroll
    for (int i = 0; i < 32; ++i) s2[i & 1] += xv[i];
    const float mw = xsum16_32((s2[0][0] + s2[0][1]) + (s2[1][0] + s2[1][1])) * (1.0f / 256);
    f32x2 q2[2] = {};
#pragma unroll
    for (int i = 0; i < 32; ++i) { xv[i] -= mw; q2[i & 1] += xv[i] * xv[i]; }
    const float m2w = xsum16_32((q2[0][0] + q2[0][1]) + (q2[1][0] + q2[1][1]));
    LAS f32x2* red = (LAS f32x2*)(lds + RED);
    red[l15 * 4 + w] = (f32x2){mw, m2w};
    if (lead) { ECHO_ST(0); ECHO_LD(); }
    BARRIER();
    if (!storer) {
      unsigned vo_ = l16;
      asm volatile("s_add_u32 m0, %2, 0\n\ts_nop 0\n\tglobal_load_lds_dwordx4 %0, %1 offset:0\n\tglobal_load_lds_dwordx4 %0, %1 offset:1024\n\tglobal_load_lds_dwordx4 %0, %1 offset:2048\n\tglobal_load_lds_dwordx4 %0, %1 offset:3072\n\ts_add_u32 m0, %2, 4096\n\tv_add_u32 %0, 0x1000, %0\n\tglobal_load_lds_dwordx4 %0, %1 offset:0\n\tglobal_load_lds_dwordx4 %0, %1 offset:1024\n\tglobal_load_lds_dwordx4 %0, %1 offset:2048\n\tglobal_load_lds_dwordx4 %0, %1 offset:3072\n\ts_add_u32 m0, %2, 8192\n\tv_add_u32 %0, 0x1000, %0\n\tglobal_load_lds_dwordx4 %0, %1 offset:0\n\tglobal_load_lds_dwordx4 %0, %1 offset:1024\n\tglobal_load_lds_dwordx4 %0, %1 offset:2048\n\tglobal_load_lds_dwordx4 %0, %1 offset:3072\n\ts_add_u32 m0, %2, 12288\n\tv_add_u32 %0, 0x1000, %0\n\tglobal_load_lds_dwordx4 %0, %1 offset:0\n\tglobal_load_lds_dwordx4 %0, %1 offset:1024\n\tglobal_load_lds_dwordx4 %0, %1 offset:2048\n\tglobal_load_lds_dwordx4 %0, %1 offset:3072\n\ts_add_u32 m0, %2, 16384\n\tv_add_u32 %0, 0x1000, %0\n\tglobal_load_lds_dwordx4 %0, %1 offset:0\n\tglobal_load_lds_dwordx4 %0, %1 offset:1024\n\tglobal_load_lds_dwordx4 %0, %1 offset:2048\n\tglobal_load_lds_dwordx4 %0, %1 offset:3072\n\ts_add_u32 m0, %2, 20480\n\tv_add_u32 %0, 0x1000, %0\n\tglobal_load_lds_dwordx4 %0, %1 offset:0\n\tglobal_load_lds_dwordx4 %0, %1 offset:1024\n\tglobal_load_lds_dwordx4 %0, %1 offset:2048\n\tglobal_load_lds_dwordx4 %0, %1 offset:3072\n\ts_add_u32 m0, %2, 24576\n\tv_add_u32 %0, 0x1000, %0\n\tglobal_load_lds_dwordx4 %0, %1 offset:0\n\tglobal_load_lds_dwordx4 %0, %1 offset:1024\n\tglobal_load_lds_dwordx4 %0, %1 offset:2048\n\tglobal_load_lds_dwordx4 %0, %1 offset:3072\n\ts_add_u32 m0, %2, 28672\n\tv_add_u32 %0, 0x1000, %0\n\tglobal_load_lds_dwordx4 %0, %1 offset:0\n\tglobal_load_lds_dwordx4 %0, %1 offset:1024\n\tglobal_load_lds_dwordx4 %0, %1 offset:2048\n\tglobal_load_lds_dwordx4 %0, %1 offset:3072" : "+v"(vo_) : "s"((const char*)(which ? wv : wk) + (w & 1) * 32768), "s"(WKV + (w & 1) * 32768) : "memory", "scc");
    }
    const f32x4 sa4 = *(LAS const f32x4*)(red + l15 * 4), sb4 = *(LAS const f32x4*)(red + l15 * 4 + 2);
    const float mean = 0.25f * ((sa4[0] + sa4[2]) + (sb4[0] + sb4[2]));
    const float d0 = sa4[0] - mean, d1 = sa4[2] - mean, d2 = sb4[0] - mean, d3 = sb4[2] - mean;
    const float rstd = __builtin_amdgcn_rsqf(((sa4[1] + sa4[3]) + (sb4[1] + sb4[3])) * (1.0f / DM) + 0.25f * ((d0 * d0 + d1 * d1) + (d2 * d2 + d3 * d3)) + 1e-5f);
    const float sh = (mw - mean) * rstd;
#pragma unroll
    for (int j = 0; j < 8; ++j)
#pragma unroll
      for (int e = 0; e < 4; ++e) { const f32x2 y = xv[j * 4 + e] * rstd + sh; xr[j][e] = cvtpk(y[0], y[1]); }
    if (storer) {
      const __amdgpu_buffer_rsrc_t rxs = __builtin_amdgcn_make_buffer_rsrc(ws + WS_XN + (T * XT + w * 8) * 1024, 0, 8192, 0x00020000);
#pragma unroll
      for (int j = 0; j < 8; ++j) __builtin_amdgcn_raw_buffer_store_b128(xr[j], rxs, l16 + (j & 3) * 1024, (j & 4) * 1024, 16);
      if (asker) asm volatile("s_mov_b32 m0, 162048\n\ts_nop 0\n\tglobal_load_lds_dword %0, %1 sc1" ::"v"(0u), "s"(ws + WS_REQ + 1024 + blockIdx.x * 4) : "memory");
      WAITV(0);
      const unsigned rold = rfl(*(LAS const unsigned*)(lds + 161792)), rnew = rfl(*(LAS const unsigned*)(lds + 162048));
      if (asker && __builtin_expect(rnew != nonce || rold == nonce, 0)) {
        GAS unsigned* req = (GAS unsigned*)(ws + WS_REQ + blockIdx.x * 4);
        if (rold == nonce) { ++nonce; __hip_atomic_store(req, nonce, RLX_AGENT); }
#pragma unroll 1
        for (unsigned it = 0; it < SPIN_CAP; ++it) { if (rfl(__hip_atomic_load(req + 256, RLX_AGENT)) == nonce) break; __builtin_amdgcn_s_sleep(1); }
      }
      unsigned ta = ARR, tb = 1;
      asm volatile("s_mov_b64 exec, 1\n\tds_add_rtn_u32 %0, %0, %1\n\ts_waitcnt lgkmcnt(0)\n\tv_cmp_eq_u32 vcc, 1, %0\n\ts_cbranch_vccz .Lnp%=\n\tv_mov_b32 %0, 0\n\tglobal_atomic_add %0, %1, %2\n.Lnp%=:\n\ts_mov_b64 exec, -1"
                   : "+v"(ta) : "v"(tb), "s"(ws + WS_CNT + (16 + (T >> 4)) * 256) : "memory", "vcc");
    } else {
      WAITV(32);
      if (lead) ECHO_ST(32);
      P1_DMA(0, w, w * 32); P1_DMA(0, w ^ 2, (w ^ 2) * 32);
      WAITV(18);
    }
#pragma unroll
    for (int i = 0; i < 7; ++i) asm volatile("global_load_dwordx2 %0, %1, %2 offset:%3" : "=v"(bq[i]) : "v"(8 * g), "s"(b1 + c * 112), "i"(i * 32) : "memory");
    RAWBAR();
    P1_DMA(1, w, 128 + w * 32);
    if (!storer) touch4(tW + (pw < 398 ? 256 * 7168 : 256 * 2048), tWo, sc);
    if (lead) ECHO_LD();
    f32x4 ka[2] = {};
    LAS const char* wp = lds + WKV + w * 16384 + (8 * g + (l15 >> 2)) * 64 + (lane & 3) * 8;
    bf16x8 wf[8][2];
#pragma unroll
    for (int j = 0; j < 8; ++j)
#pragma unroll
      for (int nt = 0; nt < 2; ++nt) wf[j][nt] = cat8(trread(wp + (32 * j) * 64 + nt * 32), trread(wp + (32 * j + 4) * 64 + nt * 32));
    if (which == 0) {
#pragma unroll
      for (int j = 0; j < 8; ++j) { ka[0] = MFMA16(wf[j][0], __builtin_bit_cast(bf16x8, xr[j]), ka[0]); ka[1] = MFMA16(wf[j][1], __builtin_bit_cast(bf16x8, xr[j]), ka[1]); }
    } else {
#pragma unroll
      for (int j = 0; j < 8; ++j) { ka[0] = MFMA16(__builtin_bit_cast(bf16x8, xr[j]), wf[j][0], ka[0]); ka[1] = MFMA16(__builtin_bit_cast(bf16x8, xr[j]), wf[j][1], ka[1]); }
    }
    *(LAS f32x4*)(lds + PART + w * 2048 + l16) = ka[0]; *(LAS f32x4*)(lds + PART + w * 2048 + 1024 + l16) = ka[1];
  }
  BARRIER();
  if (w == 1) {
    const unsigned a0 = (which == 0 ? (g >> 1) * 64 + (g & 1) * 32 : (g & 1) * 64 + h * 16) * 16 + l15 * 16, a1 = a0 + (which == 0 ? 256 : 512);
    f32x4 lo = {}, hi = {};
#pragma unroll
    for (int i = 0; i < 4; ++i) { lo += *(LAS const f32x4*)(lds + PART + i * 2048 + a0); hi += *(LAS const f32x4*)(lds + PART + i * 2048 + a1); }
    const u32x4 pk = {cvtpk(lo[0], lo[1]), cvtpk(lo[2], lo[3]), cvtpk(hi[0], hi[1]), cvtpk(hi[2], hi[3])};
    const unsigned ko = which == 0 ? g * 512 + l15 * 16 : l16;
    const __amdgpu_buffer_rsrc_t rks = __builtin_amdgcn_make_buffer_rsrc(ws + WS_KV, 0, 262144, 0x00020000);
    __builtin_amdgcn_raw_buffer_store_b128(pk, rks, ko, (T >> 1) * 4096 + (which == 0 ? (T & 1) * 256 : 2048 + (T & 1) * 1024), 16);
  }
  u32x16 p1R; u32x8 p1P, p1H; u32x4 p1T;
  {
    const unsigned xso = (rp * 16 + w * 4) * XT * 1024, hso = ((rp * 8 + w * 2) * HT + 7 * c) * 1024;
    const u32x4 r0 = mk_rsrc(ws + WS_XN + xso, 4 * XT * 1024), r1 = mk_rsrc(w2b + ft * 128, NSTEP * 16 * 2048 - ft * 128);
    const u32x4 r2 = mk_rsrc((const char*)wq + c * 64 + w * 32 * 2048, 2097152 - c * 64 - w * 32 * 2048), r3 = mk_rsrc((const char*)w1 + c * 224 + w * 32 * 7168, 7340032 - c * 224 - w * 32 * 7168);
    const u32x4 r4 = mk_rsrc(ws + WS_HF + hso, (HT + 7) * 1024), r5 = mk_rsrc(kv + w * 4096, 262144 - w * 4096);
#pragma unroll
    for (int i = 0; i < 4; ++i) { p1R[i] = r0[i]; p1R[4 + i] = r1[i]; p1R[8 + i] = r2[i]; p1R[12 + i] = r3[i]; p1H[i] = r4[i]; p1H[4 + i] = r5[i]; }
    const u64 cp = (u64)(ws + WS_CNT + 24 * 256);
    p1P = (u32x8){ring, (unsigned)w * 2048u, 8192u + w * 7168u, sc, twid, KV_OFF + w * 4096u, (unsigned)cp, (unsigned)(cp >> 32)};
    const u64 tp = (u64)(twid < 796 ? (const char*)w1 + 512 * 7168 + twid * 1152 : (const char*)wq + 512 * 2048 + (twid - 796) * 1148);
    p1T = (u32x4){(unsigned)tp, (unsigned)(tp >> 32), twid < 796 ? 128u * 7168u : 128u * 2048u, 0u};
  }
  f32x16 acc[2], bia[2];
  asm volatile("s_waitcnt vmcnt(9)" : "+v"(bq[0]), "+v"(bq[1]), "+v"(bq[2]), "+v"(bq[3]), "+v"(bq[4]), "+v"(bq[5]), "+v"(bq[6]));
#pragma unroll
  for (int e = 0; e < 32; ++e) bia[e >> 4][e & 15] = e >= 28 ? 0.f : (e & 1) ? bf_hi(bq[e >= 28 ? 0 : e >> 2][(e >> 1) & 1]) : bf_lo(bq[e >= 28 ? 0 : e >> 2][(e >> 1) & 1]);
  asm volatile("" : "+{v[224:239]}"(bia[0]), "+{v[240:255]}"(bia[1]), "+{s[80:95]}"(p1R), "+{s[72:79]}"(p1P), "+{s[52:59]}"(p1H), "+{s[48:51]}"(p1T));
  if (lead) ECHO_ST(0);
  glb_wait(gcnt + (16 + rp) * 64, 32, lead, ws, l16);
  WAITV(0);
  if (w == 1) asm volatile("s_mov_b64 exec, 1\n\tglobal_atomic_add %0, %1, %2\n\ts_mov_b64 exec, -1" ::"v"(0u), "v"(1u), "s"(ws + WS_CNT + (24 + (T >> 4)) * 256) : "memory");
#define P2_SCLOB "s60", "s61", "s62", "s63", "s64", "s65", "s66", "s67", "s68", "s69", "s70", "s71"
  CODE_PAGE(cp1);
  asm volatile(P1_ASM
               : "=&{v[0:15]}"(acc[0]), "=&{v[16:31]}"(acc[1]), "+{v[176:191]}"(p1B), "+{s[48:51]}"(p1T)
               : "{v[160:175]}"(p1A), "{s[72:79]}"(p1P), "{s[80:95]}"(p1R), "{s[52:59]}"(p1H), "{s[46:47]}"(cp1 + w * 960), "{v[224:239]}"(bia[0]), "{v[240:255]}"(bia[1])
               : "memory", "scc", "vcc", P2_SCLOB, "v128", "v129", "v130", "v131", "v32", "v33", "v34", "v35", "v36", "v37", "v38", "v39", "v40", "v41", "v42", "v43", "v44", "v45", "v46", "v47", "v48", "v49", "v50", "v51", "v52", "v53", "v54", "v55", "v56", "v57", "v58", "v59", "v60", "v61", "v62", "v63", "v64", "v65", "v66", "v67", "v68", "v69", "v70", "v71", "v72", "v73", "v74", "v75", "v76", "v77", "v78", "v79", "v80", "v81", "v82", "v83", "v84", "v85", "v86", "v87", "v88", "v89", "v90", "v91", "v92", "v93", "v94", "v95", "v96", "v97", "v98", "v99", "v100", "v101", "v102", "v103", "v104", "v105", "v106", "v107", "v108", "v109", "v110", "v111", "v112", "v113", "v114", "v115", "v116", "v117", "v118", "v119", "v120", "v121", "v122", "v123", "v124", "v125", "v126", "v127", "v192", "v193", "v194", "v195", "v196", "v197", "v198", "v199", "v200", "v201", "v202", "v203", "v204", "v205", "v206", "v207", "v208", "v209", "v210", "v211", "v212", "v213", "v214", "v215", "v216", "v217", "v218", "v219", "v220", "v221", "v222", "v223", "v144", "v145", "v146", "v147", "v148", "v149", "v150", "v151", "v152", "v153", "v154", "v155", "v156", "v157", "v158", "v159", "a0", "a1", "a2", "a3", "a4", "a5", "a6", "a7", "a8", "a9", "a10", "a11", "a12", "a13", "a14", "a15", "a16", "a17", "a18", "a19", "a20", "a21", "a22", "a23", "a24", "a25", "a26", "a27", "a28", "a29", "a30", "a31", "a32", "a33", "a34", "a35", "a36", "a37", "a38", "a39", "a40", "a41", "a42", "a43", "a44", "a45", "a46", "a47", "a48", "a49", "a50", "a51", "a52", "a53", "a54", "a55", "a56", "a57", "a58", "a59", "a60", "a61", "a62", "a63", "a64", "a65", "a66", "a67", "a68", "a69", "a70", "a71", "a72", "a73", "a74", "a75", "a76", "a77", "a78", "a79", "a80", "a81", "a82", "a83", "a84", "a85", "a86", "a87", "a88", "a89", "a90", "a91", "a92", "a93", "a94", "a95", "a96", "a97", "a98", "a99", "a100", "a101", "a102", "a103", "a104", "a105", "a106", "a107", "a108", "a109", "a110", "a111", "a112", "a113", "a114", "a115", "a116", "a117", "a118", "a119", "a120", "a121", "a122", "a123", "a124", "a125", "a126", "a127");
  const int laneB = fresh_lane(); const unsigned l16B = laneB * 16; const int hB = laneB >> 5, q4B = (laneB & 15) >> 2;
  const unsigned vowB = (laneB >> 3) * 2048 + (ft * 64 + 8 * ((laneB & 7) ^ (((laneB >> 4) & 1) << 2))) * 2;
  const float qscale = 0.17677669529663687f * 1.4426950408889634f;
  bf16x8 qf[2][2];
#pragma unroll
  for (int i = 0; i < 2; ++i)
#pragma unroll
    for (int tp = 0; tp < 2; ++tp) {
      const unsigned x0 = cvtpk(acc[i][8 * tp] * qscale, acc[i][8 * tp + 1] * qscale), x1 = cvtpk(acc[i][8 * tp + 2] * qscale, acc[i][8 * tp + 3] * qscale);
      const unsigned y0 = cvtpk(acc[i][8 * tp + 4] * qscale, acc[i][8 * tp + 5] * qscale), y1 = cvtpk(acc[i][8 * tp + 6] * qscale, acc[i][8 * tp + 7] * qscale);
      auto s0 = __builtin_amdgcn_permlane16_swap(x0, y0, false, false);
      auto s1 = __builtin_amdgcn_permlane16_swap(x1, y1, false, false);
      qf[tp][i] = __builtin_bit_cast(bf16x8, (u32x4){s0[0], s1[0], s0[1], s1[1]});
    }
  RAWBAR();
  f32x16 tA[2], tB; u32x16 tQ, tK;
  {
    const int par = (laneB >> 2) & 1;
    const u32x4 kf0 = *(LAS const u32x4*)(lds + KV_OFF + l16B), kf1 = *(LAS const u32x4*)(lds + KV_OFF + 1024 + l16B);
    f32x16 s0_[2];
    asm volatile("s_nop 1\n\tv_mfma_f32_32x32x16_bf16 %0, %2, %4, 0\n\tv_mfma_f32_32x32x16_bf16 %1, %2, %6, 0\n\tv_mfma_f32_32x32x16_bf16 %0, %3, %5, %0\n\tv_mfma_f32_32x32x16_bf16 %1, %3, %7, %1\n\ts_nop 11"
                 : "=&v"(s0_[0]), "=&v"(s0_[1]) : "v"(kf0), "v"(kf1), "v"(qf[0][0]), "v"(qf[0][1]), "v"(qf[1][0]), "v"(qf[1][1]));
#pragma unroll
    for (int t = 0; t < 2; ++t) {
      const f32x16& s = s0_[t];
      const float nm = -rowmax16(s) - 40.0f;
      const unsigned sv = ((laneB >> 1) & 1) == t ? 0x3f803f80u : 0u;
#pragma unroll
      for (int i = 0; i < 16; ++i) { tA[t][i] = nm; if (t == 0) tB[i] = s[i] + nm; }
#pragma unroll
      for (int i = 0; i < 4; ++i) { tQ[t * 8 + i] = __builtin_bit_cast(u32x4, qf[t][0])[i]; tQ[t * 8 + 4 + i] = __builtin_bit_cast(u32x4, qf[t][1])[i]; tK[t * 4 + i] = (i >> 1) == par ? sv : 0u; }
    }
#pragma unroll
    for (int i = 0; i < 4; ++i) { tK[8 + i] = kf0[i]; tK[12 + i] = kf1[i]; }
  }
  const unsigned gw0 = ring + (8 * (laneB >> 4) + q4B) * 128 + 64 * ((q4B >> 1) & 1) + 32 * ((laneB >> 4) & 1) + 16 * (laneB & 1) + 8 * ((laneB >> 1) & 1);
  const unsigned la = (laneB >> 5) * 1024 + ((laneB >> 4) & 1) * 512 + (laneB & 15) * 16;
  u32x4 vW = {vowB, (vowB + 15360u) ^ 32u, vowB + 30720u, (vowB + 46080u) ^ 32u};
  u32x8 vX = {l16B, ldsb + KV_OFF + l16B, gw0, gw0 ^ 32u, la + (unsigned)(0 ^ w) * (HT * 1024), la + (unsigned)(1 ^ w) * (HT * 1024), la + (unsigned)(2 ^ w) * (HT * 1024), la + (unsigned)(3 ^ w) * (HT * 1024)};
  u32x16 sR; u32x8 sP;
  {
    const unsigned aso = (mt * 4 * HT + w * NSTEP) * 1024;
    const u32x4 r0 = mk_rsrc(ws + WS_HF + aso, HF_B - aso), r1 = mk_rsrc(w2b, NSTEP * 16 * 2048), r2 = mk_rsrc(kv + w * 4096, 262144 - w * 4096), r3 = mk_rsrc((const char*)wo + w * 65536, 2097152 - w * 65536);
#pragma unroll
    for (int i = 0; i < 4; ++i) { sR[i] = r0[i]; sR[4 + i] = r1[i]; sR[8 + i] = r2[i]; sR[12 + i] = r3[i]; }
    const unsigned long long pub = (unsigned long long)(ws + WS_CNT + rp * 256), pol = (unsigned long long)(ws + WS_CNT + (mt >> 1) * 256);
    sP = (u32x8){ring, ldsb + KV_OFF + w * 4096, ldsb, (unsigned)w, (unsigned)pub, (unsigned)(pub >> 32), (unsigned)pol, (unsigned)(pol >> 32)};
  }
  const u32x4 vE = {(unsigned)(laneB & 15) * 128u, 8u * hB, (laneB & 1) ? 0xeeeeu : 0x4444u, gw0 ^ 64u};
  const u32x4 sE = mk_rsrc(b2 + ft * 64, 128);
  u32x4 sT; CODE_PAGE(cp2);
  { const unsigned tw = blockIdx.x * 4 + w;
    const u64 ta = (u64)((const char*)w2 + (tw >> 8) * 1835008u + min((tw & 255u) * 1792u, 458752u - 2048u)), tb = (u64)((const char*)wo + tw * 2048u);
    sT = (u32x4){(unsigned)ta, (unsigned)(ta >> 32), (unsigned)tb, (unsigned)(tb >> 32)}; }
  u32x16 eb, ex;
  u32x16 ar[4];
#define ALL_AGPR "a0", "a1", "a2", "a3", "a4", "a5", "a6", "a7", "a8", "a9", "a10", "a11", "a12", "a13", "a14", "a15", "a16", "a17", "a18", "a19", "a20", "a21", "a22", "a23", "a24", "a25", "a26", "a27", "a28", "a29", "a30", "a31", "a32", "a33", "a34", "a35", "a36", "a37", "a38", "a39", "a40", "a41", "a42", "a43", "a44", "a45", "a46", "a47", "a48", "a49", "a50", "a51", "a52", "a53", "a54", "a55", "a56", "a57", "a58", "a59", "a60", "a61", "a62", "a63", "a64", "a65", "a66", "a67", "a68", "a69", "a70", "a71", "a72", "a73", "a74", "a75", "a76", "a77", "a78", "a79", "a80", "a81", "a82", "a83", "a84", "a85", "a86", "a87", "a88", "a89", "a90", "a91", "a92", "a93", "a94", "a95", "a96", "a97", "a98", "a99", "a100", "a101", "a102", "a103", "a104", "a105", "a106", "a107", "a108", "a109", "a110", "a111", "a112", "a113", "a114", "a115", "a116", "a117", "a118", "a119", "a120", "a121", "a122", "a123", "a124", "a125", "a126", "a127", "a128", "a129", "a130", "a131", "a132", "a133", "a134", "a135", "a136", "a137", "a138", "a139", "a140", "a141", "a142", "a143", "a144", "a145", "a146", "a147", "a148", "a149", "a150", "a151", "a152", "a153", "a154", "a155", "a156", "a157", "a158", "a159", "a160", "a161", "a162", "a163", "a164", "a165", "a166", "a167", "a168", "a169", "a170", "a171", "a172", "a173", "a174", "a175", "a176", "a177", "a178", "a179", "a180", "a181", "a182", "a183", "a184", "a185", "a186", "a187", "a188", "a189", "a190", "a191", "a192", "a193", "a194", "a195", "a196", "a197", "a198", "a199", "a200", "a201", "a202", "a203", "a204", "a205", "a206", "a207", "a208", "a209", "a210", "a211", "a212", "a213", "a214", "a215", "a216", "a217", "a218", "a219", "a220", "a221", "a222", "a223", "a224", "a225", "a226", "a227", "a228", "a229", "a230", "a231", "a232", "a233", "a234", "a235", "a236", "a237", "a238", "a239", "a240", "a241", "a242", "a243", "a244", "a245", "a246", "a247", "a248", "a249", "a250", "a251", "a252", "a253", "a254", "a255"
#define P2_VCLOB "v96", "v97", "v98", "v99", "v100", "v101", "v102", "v103", "v104", "v105", "v106", "v107", "v108", "v109", "v110", "v111", "v112", "v113", "v114", "v115", "v116", "v117", "v118", "v119", "v120", "v121", "v122", "v123", "v124", "v125", "v126", "v127", "v128", "v129", "v130", "v131", "v132", "v133", "v134", "v135", "v136", "v137"
  asm volatile(P2_ASM
               : "=&{v[160:175]}"(ar[0]), "=&{v[176:191]}"(ar[1]), "=&{v[192:207]}"(ar[2]), "=&{v[208:223]}"(ar[3]), "=&{v[224:239]}"(eb), "=&{v[240:255]}"(ex),
                 "+{v[0:15]}"(tA[0]), "+{v[16:31]}"(tA[1]), "+{v[32:47]}"(tB), "+{v[80:95]}"(tK), "+{v[140:143]}"(vW), "+{v[144:151]}"(vX), "+{v[64:79]}"(tQ), "+{s[48:51]}"(sT)
               : "{v[152:155]}"(vE), "{s[80:95]}"(sR), "{s[72:79]}"(sP), "{s[56:59]}"(sE), "{s[52:53]}"(cp2 + (w & 1) * 2048)
               : "memory", "scc", P2_VCLOB, P2_SCLOB, "v156", "v157", "v158", "v159", "v48", "v49", "v50", "v51", "v52", "v53", "v54", "v55", "v56", "v57", "v58", "v59", "v60", "v61", "v62", "v63", ALL_AGPR);
  const int laneC = fresh_lane(); const unsigned l16C = laneC * 16; const int hC = laneC >> 5, q4C = (laneC & 15) >> 2;
  const __amdgpu_buffer_rsrc_t ro = __builtin_amdgcn_make_buffer_rsrc(ws + WS_OF, 0, OF_B, 0x00020000);
#pragma unroll
  for (int t = 0; t < 2; ++t) {
    const float l = tB[2 * t] + tB[2 * t + 1];
    float am = 0.f;
#pragma unroll
    for (int i = 0; i < 16; ++i) am += fabsf(tA[t][i]);
    const bool ok = (l > 1e-30f) && (l < 1e30f) && (am < 1e37f);
    f32x16 y;
    if (__builtin_expect(__all(ok), 1)) {
      const float r = 1.0f / l;
#pragma unroll
      for (int i = 0; i < 16; ++i) y[i] = tA[t][i] * r;
    } else {
      { const u32x4 q0 = {tQ[t * 8], tQ[t * 8 + 1], tQ[t * 8 + 2], tQ[t * 8 + 3]}, q1 = {tQ[t * 8 + 4], tQ[t * 8 + 5], tQ[t * 8 + 6], tQ[t * 8 + 7]};
        attn_exact_tile(kv, __builtin_bit_cast(bf16x8, q0), __builtin_bit_cast(bf16x8, q1), laneC, y); }
    }
    const unsigned so2 = ((rp * 8 + w * 2 + t) * OT + 2 * c) * 1024;
    __builtin_amdgcn_raw_buffer_store_b128(__builtin_bit_cast(u32x4, pack8(y, 0)), ro, l16C, so2, 16);
    __builtin_amdgcn_raw_buffer_store_b128(__builtin_bit_cast(u32x4, pack8(y, 8)), ro, l16C, so2 + 1024, 16);
  }
  f32x16 sa[2], res[2];
  {
    const unsigned oso = (mt * 4 + w) * OT * 1024;
    const u32x4 rso = mk_rsrc(ws + WS_OF + oso, OF_B - oso);
#pragma unroll
    for (int i = 0; i < 4; ++i) sR[8 + i] = rso[i];
    const unsigned long long pub = (unsigned long long)(ws + WS_CNT + (8 + rp) * 256), pol = (unsigned long long)(ws + WS_CNT + (8 + (mt >> 1)) * 256);
    sP[4] = (unsigned)pub; sP[5] = (unsigned)(pub >> 32); sP[6] = (unsigned)pol; sP[7] = (unsigned)(pol >> 32);
    const unsigned wo0 = ldsb + (4 * hC + q4C) * 128 + 64 * ((q4C >> 1) & 1) + 32 * ((laneC >> 4) & 1) + 8 * (laneC & 3);
    u32x8 wb = {wo0, wo0 + 65536u, wo0 + 131072u, wo0 ^ 64u, (wo0 ^ 64u) + 65536u, (wo0 ^ 64u) + 131072u, 8u * hC, 0u};
    const int w3 = w ^ 3;
    const u32x4 ra = {ring + l16C, (ring ^ 16384u) + l16C, ldsb + (w3 >> 1) * (2 * P1_STAGE) + (w3 & 1) * 16384 + l16C, (unsigned)(((mt * 128 + w * 32 + (laneC & 31)) * DM + ft * 64 + 8 * hC) * 2)};
    const u32x4 rsx = mk_rsrc(x, 4u << 20);
    asm volatile(TAIL_ASM
                 : "=&{v[0:15]}"(sa[0]), "=&{v[16:31]}"(sa[1]), "=&{v[72:87]}"(res[0]), "=&{v[88:103]}"(res[1]), "+{v[160:175]}"(ar[0]), "+{v[176:191]}"(ar[1]), "+{v[192:207]}"(ar[2]), "+{v[208:223]}"(ar[3]), "+{v[240:255]}"(ex), "+{v[224:239]}"(eb)
                 , "+{v[64:71]}"(wb)
                 : "{v[140:143]}"(vW), "{v[144:151]}"(vX), "{v[152:155]}"(ra), "{s[80:95]}"(sR), "{s[72:79]}"(sP), "{s[52:55]}"(rsx)
                 : "memory", "scc", P2_SCLOB, "v32", "v33", "v34", "v35", "v36", "v37", "v38", "v39", "v40", "v41", "v42", "v43", "v44", "v45", "v46", "v47", "v48", "v49", "v50", "v51", "v52", "v53", "v54", "v55", "v56", "v57", "v58", "v59", "v60", "v61", "v62", "v63", "v104", "v105", "v106", "v107", "v108", "v109", "v110", "v111", "v112", "v113", "v114", "v115", "v116", "v117", "v118", "v119", "v120", "v121", "v122", "v123", "v124", "v125", "v126", "v127", "v128", "v129", "v130", "v131", "v132", "v133", "v134", "v135", "v136", "v137", "v156", "v157", "v158", "v159", ALL_AGPR);
  }
  const int laneD = fresh_lane(), hD = laneD >> 5;
  LAS char* ost = lds + ((w ^ 3) >> 1) * (2 * P1_STAGE) + ((w ^ 3) & 1) * 16384;
#pragma unroll
  for (int i = 0; i < 4; ++i) {
    unsigned pk[4];
#pragma unroll
    for (int e = 0; e < 4; ++e) {
      const int k = (8 * i + 2 * e) & 15;
      pk[e] = cvtpk(__uint_as_float(ar[i >> 1][k]) + sa[i >> 1][k] * res[i >> 1][k], __uint_as_float(ar[i >> 1][k + 1]) + sa[i >> 1][k + 1] * res[i >> 1][k + 1]);
    }
    auto s0 = __builtin_amdgcn_permlane32_swap(pk[0], pk[2], false, false);
    auto s1 = __builtin_amdgcn_permlane32_swap(pk[1], pk[3], false, false);
    *(LAS u32x4*)(ost + (laneD & 31) * 144 + i * 32 + hD * 16) = (u32x4){s0[0], s1[0], s0[1], s1[1]};
  }
  {
    const unsigned oo_ = ((mt * 128 + w * 32 + (laneD >> 3)) * DM + ft * 64) * 2 + (laneD & 7) * 16;
    const __amdgpu_buffer_rsrc_t rout = __builtin_amdgcn_make_buffer_rsrc(out, 0, 4u << 20, 0x00020000);
    u32x4 ov[4];
#pragma unroll
    for (int j = 0; j < 4; ++j) ov[j] = *(LAS const u32x4*)(ost + (j * 8 + (laneD >> 3)) * 144 + (laneD & 7) * 16);
#pragma unroll
    for (int j = 0; j < 4; ++j) __builtin_amdgcn_raw_buffer_store_b128(ov[j], rout, oo_, j * 8 * 2048, 2);
  }
  asm volatile("kmain_code_end:" ::: "memory");
}

extern "C" void kernel_launch(void* const* d_in, const int* in_sizes, int n_in, void* d_out, int out_size, void* d_ws, size_t ws_size,
                              hipStream_t stream) {
  const u16* x = (const u16*)d_in[0];
  const u16* wq = (const u16*)d_in[1];
  const u16* wk = (const u16*)d_in[2];
  const u16* wv = (const u16*)d_in[3];
  const u16* wo = (const u16*)d_in[4];
  const u16* w1 = (const u16*)d_in[6];
  const u16* b1 = (const u16*)d_in[7];
  const u16* w2 = (const u16*)d_in[8];
  const u16* b2 = (const u16*)d_in[9];
  char* ws = (char*)d_ws;
  k_main<<<256, 256, 0, stream>>>(x, wq, wk, wv, wo, w1, b1, w2, b2, (u16*)d_out, ws);
}
```
